```python
import torch

def _softmax(x, axis=-1):
    return torch.softmax(x, dim=axis)
def _gelu(x):
    return torch.nn.functional.gelu(x, approximate="tanh")
def _silu(x):
    return torch.nn.functional.silu(x)
def _sigmoid(x):
    return torch.sigmoid(x)
def _layernorm(x, axis=-1, eps=1e-5):
    m = x.mean(dim=axis, keepdim=True)
    v = x.var(dim=axis, keepdim=True, unbiased=False)
    return (x - m) / torch.sqrt(v + eps)
def _scatter_add(tgt, idx, src, axis=0):
    return tgt.clone().index_add_(axis, idx, src)
def _segment_sum(src, seg, n):
    out = torch.zeros((n,) + src.shape[1:], dtype=src.dtype)
    return out.index_add_(0, seg, src)
def _pad(x, pw):
    flat = tuple(v for pair in reversed(pw) for v in pair)
    return torch.nn.functional.pad(x, flat)

kernel_name = 'gated_mix__ssm_conv_s5__ssm_conv_h3__prompt'

def setup_inputs():

    _w = (lambda a: (lambda t: t.to(torch.bfloat16) if t.is_floating_point() else t)(torch.as_tensor(a)))
    def _A_setup():
        B, S, D = 1, 2048, 1024
        P = D
        Lambda_re = -0.5 * (lambda _x: torch.exp(torch.as_tensor(_x)))((lambda *s: torch.randn(*s, dtype=torch.bfloat16) * 0.2)(P))
        Lambda_im = (lambda *s: torch.randn(*s, dtype=torch.bfloat16) * 0.4)(P) * 1.0
        B_re = (lambda *s: torch.randn(*s, dtype=torch.bfloat16) * 0.2)(P, D) * (D ** -0.5)
        B_im = (lambda *s: torch.randn(*s, dtype=torch.bfloat16) * 0.2)(P, D) * (D ** -0.5)
        C_re = (lambda *s: torch.randn(*s, dtype=torch.bfloat16) * 0.2)(D, P) * (P ** -0.5)
        C_im = (lambda *s: torch.randn(*s, dtype=torch.bfloat16) * 0.2)(D, P) * (P ** -0.5)
        log_dt = (lambda *s: torch.randn(*s, dtype=torch.bfloat16) * 0.2)(P) * 0.3 - 2.0
        Dskip = (lambda *s: torch.randn(*s, dtype=torch.bfloat16) * 0.05)(D)
        return {
            "x": _w((lambda *s: torch.randn(*s, dtype=torch.bfloat16) * 0.05)(B, S, D)),
            "Lambda_re": _w(Lambda_re),
            "Lambda_im": _w(Lambda_im),
            "B_re": _w(B_re),
            "B_im": _w(B_im),
            "C_re": _w(C_re),
            "C_im": _w(C_im),
            "log_dt": _w(log_dt),
            "Dskip": _w(Dskip),
        }
    _A_in = _A_setup()

    def _B_setup():
        B, S, D = 1, 2048, 1024
        ks = 4
        return {
            "q": _w((lambda *s: torch.randn(*s, dtype=torch.bfloat16) * 0.05)(B, S, D)),
            "k": _w((lambda *s: torch.randn(*s, dtype=torch.bfloat16) * 0.4)(B, S, D)),
            "v": _w((lambda *s: torch.randn(*s, dtype=torch.bfloat16) * 0.2)(B, S, D)),
            "shift_k": _w((lambda *s: torch.randn(*s, dtype=torch.bfloat16) * 0.4)(D, ks) * 0.5),
            "ssm_k": _w((lambda *s: torch.randn(*s, dtype=torch.bfloat16) * 0.4)(D, S) * (1.0 / S ** 0.5)),
            "ssm_D": _w((lambda *s: torch.randn(*s, dtype=torch.bfloat16) * 0.2)(D)),
        }
    _B_in = _B_setup()

    return {
        "A_x": _A_in["x"],
        "A_Lambda_re": _A_in["Lambda_re"],
        "A_Lambda_im": _A_in["Lambda_im"],
        "A_B_re": _A_in["B_re"],
        "A_B_im": _A_in["B_im"],
        "A_C_re": _A_in["C_re"],
        "A_C_im": _A_in["C_im"],
        "A_log_dt": _A_in["log_dt"],
        "A_Dskip": _A_in["Dskip"],
        "B_q": _B_in["q"],
        "B_k": _B_in["k"],
        "B_v": _B_in["v"],
        "B_shift_k": _B_in["shift_k"],
        "B_ssm_k": _B_in["ssm_k"],
        "B_ssm_D": _B_in["ssm_D"],
        "gate_w": _w((lambda *s: torch.randn(*s, dtype=torch.bfloat16) * 0.05)(1024) * 16.0),
    }

def reference(A_x, A_Lambda_re, A_Lambda_im, A_B_re, A_B_im, A_C_re, A_C_im, A_log_dt, A_Dskip, B_q, B_k, B_v, B_shift_k, B_ssm_k, B_ssm_D, gate_w):
    def _A_ref(x, Lambda_re, Lambda_im, B_re, B_im, C_re, C_im, log_dt, Dskip):
        Bsz, L, H = x.shape
        Lam = Lambda_re + 1j * Lambda_im
        Bmat = B_re + 1j * B_im
        Cmat = C_re + 1j * C_im
        dt = torch.exp(log_dt)
        Lam_bar = torch.exp(dt * Lam)
        B_bar = ((Lam_bar - 1.0) / Lam)[:, None] * Bmat
        Bu = torch.einsum("bld,pd->blp", x + 0j, B_bar)
        h = torch.zeros_like(Bu[:, 0, :])
        hs = []
        for t in range(L):
            h = Lam_bar[None, :] * h + Bu[:, t, :]
            hs.append(h)
        hseq = torch.stack(hs, 1)
        y = 2.0 * torch.einsum("blp,dp->bld", hseq, Cmat).real + x * Dskip[None, None, :]
        return y

    def _B_ref(q, k, v, shift_k, ssm_k, ssm_D):
        B, S, D = q.shape
        ks = shift_k.shape[-1]
        k_pad = _pad(k, ((0, 0), (ks - 1, 0), (0, 0)))
        k_shift = torch.zeros_like(k)
        for t in range(ks):
            k_shift = k_shift + k_pad[:, t:t + S, :] * shift_k[:, ks - 1 - t][None, None, :]
        kv = k_shift * v
        L = 2 * S
        kv_pad = _pad(kv, ((0, 0), (0, S), (0, 0)))
        kf_pad = _pad(ssm_k, ((0, 0), (0, S)))
        kv_f = (lambda x, axes=(-2,-1): torch.fft.rfft2(x.float(), dim=axes))(kv_pad[:, None, :, :], axes=(1, 2))
        kf_f = (lambda x, axes=(-2,-1): torch.fft.rfft2(x.float(), dim=axes))(kf_pad[:, None, :], axes=(1, 2))
        kf_f = (lambda x, *d: x.permute(*d))(kf_f, 1, 2, 0)
        y_f = kv_f * kf_f[None, :, :, :]
        y = (lambda x, s, axes=(-2,-1): torch.fft.irfft2(x, s=s, dim=axes).to(torch.bfloat16))(y_f, (1, L), axes=(1, 2))[:, 0, :S, :]
        y = y + kv * ssm_D[None, None, :]
        return q * y

    x = A_x
    g = _sigmoid((x * gate_w).sum(-1, keepdims=True))
    a = _A_ref(x, A_Lambda_re, A_Lambda_im, A_B_re, A_B_im, A_C_re, A_C_im, A_log_dt, A_Dskip)
    b = _B_ref(x, B_k, B_v, B_shift_k, B_ssm_k, B_ssm_D)
    return g * a + (1 - g) * b
```

```cpp
#if defined(__HIP_DEVICE_COMPILE__)
#pragma clang attribute push(__attribute__((target("no-packed-fp32-ops"))), apply_to = function)
#endif
#include <hip/hip_runtime.h>
#include <hip/hip_bf16.h>
#include <stdint.h>

#define TIDX ((int)__builtin_amdgcn_workitem_id_x())
#define BIDX ((int)__builtin_amdgcn_workgroup_id_x())
typedef unsigned short u16;
typedef __attribute__((ext_vector_type(8))) short bf16x8;
typedef __attribute__((ext_vector_type(4))) float f32x4;
typedef __attribute__((ext_vector_type(2))) float f32x2;
typedef __attribute__((ext_vector_type(2))) __bf16 bf16x2;
typedef __attribute__((ext_vector_type(4))) unsigned u32x4;
typedef __attribute__((ext_vector_type(2))) unsigned u32x2;
typedef __attribute__((address_space(1))) unsigned long long gu64;
typedef __attribute__((address_space(1))) unsigned gu32;
typedef __attribute__((address_space(3))) void lds_void;
typedef __attribute__((address_space(3))) unsigned lds_u32;
typedef __attribute__((ext_vector_type(4))) short s16x4;
typedef __attribute__((address_space(3))) s16x4 lds_s16x4;
#define TRD(a) __builtin_amdgcn_ds_read_tr16_b64_v4i16((lds_s16x4*)(size_t)(a))

#define DBGON 0
#define STAMPT(buf, k, t) do { } while (0)
#define CODE_LINES 292
#define SWP 0
#define SEQ 2048
#define DM 1024
#define SPIN_MAX (1u << 18)
#define PRIO_G 2
#define PSLEEP 4
#define PRIO_L 3
#define CPSLEEP 4
#define WT 16

__device__ __forceinline__ float bf2f(u16 v) { return __uint_as_float(((unsigned)v) << 16); }
__device__ __forceinline__ u16 f2bf(float f) { __bf16 b = (__bf16)f; return __builtin_bit_cast(u16, b); }
__device__ __forceinline__ float rbf(float f) { return bf2f(f2bf(f)); }
__device__ __forceinline__ unsigned pkbf(float lo, float hi) { return __builtin_bit_cast(unsigned, __builtin_convertvector((f32x2){lo, hi}, bf16x2)); }
__device__ __forceinline__ float lo16(unsigned u) { return __uint_as_float(u << 16); }
__device__ __forceinline__ float hi16(unsigned u) { return __uint_as_float(u & 0xffff0000u); }

struct cf { float r, i; };
__device__ __forceinline__ cf cmul(cf a, cf b) { return {a.r * b.r - a.i * b.i, a.r * b.i + a.i * b.r}; }
__device__ __forceinline__ cf cfma(cf a, cf b, cf c) {
    return {fmaf(a.r, b.r, fmaf(-a.i, b.i, c.r)), fmaf(a.r, b.i, fmaf(a.i, b.r, c.i))};
}
__device__ __forceinline__ f32x4 splat4(float v) { return (f32x4){v, v, v, v}; }
__device__ __forceinline__ f32x4 fma4(f32x4 a, f32x4 b, f32x4 c) { return __builtin_elementwise_fma(a, b, c); }
__device__ __forceinline__ f32x4 mul4(f32x4 a, f32x4 b) { return a * b; }

__device__ __forceinline__ float red32(float a, float b) {
    const auto r = __builtin_amdgcn_permlane32_swap(__float_as_uint(a), __float_as_uint(b), false, false);
    return __uint_as_float(r[0]) + __uint_as_float(r[1]);
}
__device__ __forceinline__ float red16(float x) {
    const auto r = __builtin_amdgcn_permlane16_swap(__float_as_uint(x), __float_as_uint(x), false, false);
    return __uint_as_float(r[0]) + __uint_as_float(r[1]);
}
__device__ __forceinline__ int fresh_lane() {
    int l;
    asm volatile("v_mbcnt_lo_u32_b32 %0, -1, 0\n\tv_mbcnt_hi_u32_b32 %0, -1, %0" : "=v"(l));
    return l;
}
#define BAR() __builtin_amdgcn_s_barrier()
#define WAIT_VM(n) asm volatile("s_waitcnt vmcnt(" #n ")" ::: "memory")
#define WAIT_LGKM0() __builtin_amdgcn_s_waitcnt(0xC07F)
#define SCHED() __builtin_amdgcn_sched_barrier(0)
#define MF(c, a, b) asm volatile("v_mfma_f32_16x16x32_bf16 %0, %1, %2, %0" : "+v"(c) : "v"(a), "v"(b))
#define PAD8(c0, c1, c2, c3) asm volatile("s_nop 7" : "+v"(c0), "+v"(c1), "+v"(c2), "+v"(c3))
#define PIN4(c0, c1, c2, c3) asm volatile("" : "+v"(c0), "+v"(c1), "+v"(c2), "+v"(c3))
#define GLDS_S(sbase, voff, sl, IMM) \
    asm volatile("s_add_u32 m0, %2, %3\n\ts_nop 0\n\tglobal_load_lds_dwordx4 %0, %1" \
                 :: "v"(voff), "s"(sbase), "s"(sl), "i"(IMM) : "memory", "scc")
#define GLDS_K(sbase, voff, sl, IMM) \
    asm volatile("s_add_u32 m0, %2, %3\n\ts_nop 0\n\tglobal_load_lds_dwordx4 %0, %1 nt" \
                 :: "v"(voff), "s"(sbase), "s"(sl), "i"(IMM) : "memory", "scc")
#define RLX_AGENT __ATOMIC_RELAXED, __HIP_MEMORY_SCOPE_AGENT

#define UNITS_A 0x0756075607560560ull
#define UNITS_B 0x1432143214321432ull
#define RA(t) ((int)((UNITS_A >> (4 * (t))) & 15) * 16384)
#define RB(t) ((int)((UNITS_B >> (4 * (t))) & 15) * 16384)
#define LA_OFF 0
#define GA_OFF 32768
#define CV_COPY 4160
#define CV_W (4 * CV_COPY)
#define KC_OFF 65536
#define ST_OFF 114688
#define ST_V 8832
#define ST_SZ (ST_V + 8 * 1056)
#define U_OFF (KC_OFF + 16 * CV_COPY)
#define HT_OFF (U_OFF + 256 + 16384)
#define XCH_OFF (HT_OFF + 8192)
#define GW_OFF (XCH_OFF + 2048)
#define TAG_OFF (GW_OFF + 2048)
#define UFLAG_OFF (TAG_OFF + 16)
#define G1D_OFF (UFLAG_OFF + 16)
#define MISC_OFF (G1D_OFF + 16)
#define YFLAG_OFF (MISC_OFF + 16)
#define RF_OFF (YFLAG_OFF + 16)
#define EF_OFF (RF_OFF + 64)
#define COK_OFF (EF_OFF + 64)
#define NFLAGW 52
#define DUMMY_OFF (COK_OFF + 16)
#define K1_LDS (DUMMY_OFF + 256)
#define WS_GRAN 0
#define WS_KVFLAG (256u << 10)
#define WS_ARRIVE (WS_KVFLAG + 32768)
#define WS_EPOCH (WS_ARRIVE + 32768)
#define WS_CFLAG (WS_EPOCH + 4096)
#define WS_HFLAG (384u << 10)
#define WS_GATE (512u << 10)

#define RD128(a, off) (*(const __attribute__((address_space(3))) bf16x8*)(size_t)(unsigned)((a) + (off)))
#define RDA(dst, A, F) do { const s16x4 lo_ = TRD((A) + 512 * (F) + 8); SCHED(); const s16x4 hi_ = TRD((A) + 512 * (F)); SCHED(); \
                            dst = __builtin_shufflevector(lo_, hi_, 0, 1, 2, 3, 4, 5, 6, 7); } while (0)
#define TOUCH(sbase, voff) asm volatile("s_mov_b32 m0, %2\n\ts_nop 0\n\tglobal_load_lds_dword %0, %1" :: "v"(voff), "s"(sbase), "i"(DUMMY_OFF) : "memory")
#define FLAGD(off) do { unsigned long long pc_; asm volatile("s_getpc_b64 %0\n\ts_mov_b32 m0, %2\n\ts_mov_b64 exec, 1\n\tglobal_load_lds_dword %1, %0\n\ts_mov_b64 exec, -1" \
                                                             : "=&s"(pc_) : "v"(0u), "s"(__builtin_amdgcn_readfirstlane(off)) : "memory"); } while (0)
#define GLDS_SC1(sbase, voff, sl, IMM) \
    asm volatile("s_add_u32 m0, %2, %3\n\ts_nop 0\n\tglobal_load_lds_dwordx4 %0, %1 sc1" \
                 :: "v"(voff), "s"(sbase), "s"(sl), "i"(IMM) : "memory", "scc")
__device__ __forceinline__ unsigned mv(unsigned v) { unsigned r; asm("v_mov_b32 %0, %1" : "=v"(r) : "v"(v)); return r; }
__device__ __forceinline__ unsigned al(unsigned hi, unsigned lo) { unsigned r; asm volatile("v_alignbit_b32 %0, %1, %2, 16" : "=v"(r) : "v"(hi), "v"(lo)); return r; }
__device__ __forceinline__ u32x2 pred2(const u32x4& a, unsigned f0, unsigned f1) {
    return (u32x2){(unsigned)__builtin_amdgcn_update_dpp((int)f0, (int)a.z, 0x138, 0xf, 0xf, false), (unsigned)__builtin_amdgcn_update_dpp((int)f1, (int)a.w, 0x138, 0xf, 0xf, false)};
}
#define RL63(v) ((unsigned)__builtin_amdgcn_readlane((int)(v), 63))
__device__ __forceinline__ void wait_lds4(char* smem, int off) {
    const unsigned a = (unsigned)(size_t)(lds_void*)smem + off;
    unsigned spins = 0;
    for (;;) {
        u32x4 f;
        asm volatile("ds_read_b128 %0, %1\n\ts_waitcnt lgkmcnt(0)" : "=&v"(f) : "v"(a) : "memory");
        if (__builtin_amdgcn_readfirstlane((f.x != 0u) & (f.y != 0u) & (f.z != 0u) & (f.w != 0u))) break;
        if (++spins > SPIN_MAX * 16u) break;
        __builtin_amdgcn_s_sleep(1);
    }
}
__device__ __forceinline__ unsigned lds_word(char* smem, int off) {
    const unsigned a = (unsigned)(size_t)(lds_void*)smem + off;
    unsigned f;
    asm volatile("ds_read_b32 %0, %1\n\ts_waitcnt lgkmcnt(0)" : "=&v"(f) : "v"(a) : "memory");
    return __builtin_amdgcn_readfirstlane(f);
}
__device__ __forceinline__ void wait_lds1(char* smem, int off) {
    unsigned spins = 0;
    while (lds_word(smem, off) == 0u) {
        if (++spins > SPIN_MAX * 16u) break;
        __builtin_amdgcn_s_sleep(1);
    }
}
#define SETW(off, i, v) __hip_atomic_store((lds_u32*)(smem + (off)) + (i), (v), __ATOMIC_RELAXED, __HIP_MEMORY_SCOPE_WORKGROUP)

#define PRIO_CV 1
#define PRIO_V0 3
#define PRIO_KV 3
#define CS0 1
#define CS1 3
#define CS2 5
#define XE 4
#define YPOLL 32
#define ZF 59
template <int I> struct IC { static constexpr int value = I; };
template <int N, int I = 0, class F> __device__ __forceinline__ void unroll(F& f) { if constexpr (I < N) { f(IC<I>{}); unroll<N, I + 1>(f); } }
#define MF0(c, a, b) asm volatile("v_mfma_f32_16x16x32_bf16 %0, %1, %2, 0" : "=&v"(c) : "v"(a), "v"(b))
#define DEPTH 6
#define PRIO2_G 0
#define PRIO2_L 1
__device__ __forceinline__ void both(cf x, cf* o) {
    const auto r = __builtin_amdgcn_permlane32_swap(__float_as_uint(x.r), __float_as_uint(x.r), false, false);
    const auto i = __builtin_amdgcn_permlane32_swap(__float_as_uint(x.i), __float_as_uint(x.i), false, false);
    o[0] = {__uint_as_float(r[0]), __uint_as_float(i[0])};
    o[1] = {__uint_as_float(r[1]), __uint_as_float(i[1])};
}
__device__ __forceinline__ cf pick4(int q, cf a1, cf a2, cf a3) {
    return {q == 0 ? 1.f : q == 1 ? a1.r : q == 2 ? a2.r : a3.r, q == 0 ? 0.f : q == 1 ? a1.i : q == 2 ? a2.i : a3.i};
}
#define PINC(c) asm volatile("" : "+v"((c).r), "+v"((c).i))

extern "C" __global__ __launch_bounds__(512, 2) void k1_kernel(
        const u16* __restrict__ x, const u16* __restrict__ Bre, const u16* __restrict__ Bim,
        const u16* __restrict__ kin, const u16* __restrict__ vin, const u16* __restrict__ shiftk, const u16* __restrict__ gatew, char* ws,
        const u16* __restrict__ lre, const u16* __restrict__ lim, const u16* __restrict__ logdt,
        const u16* __restrict__ ssmk, const u16* __restrict__ ssmD,
        const u16* __restrict__ Cre, const u16* __restrict__ Cim, const u16* __restrict__ dskip, float* __restrict__ out) {
    extern __shared__ __attribute__((aligned(16))) char smem[];
#define h ((u16*)(ws + (1u << 20)))
#define kvT ((u16*)(ws + (9u << 20)))
#define y2T ((u16*)(ws + (13u << 20)))
#define dbg ((unsigned long long*)(ws + (64u << 20)))
    const int tid = TIDX;
#define CTN 40
    if (tid < CTN && tid + ((BIDX >> 3) & 7) * CTN < CODE_LINES) {
        const unsigned vo = (unsigned)(tid + ((BIDX >> 3) & 7) * CTN) * 128u;
        asm volatile("s_getpc_b64 s[98:99]\n\ts_add_u32 s98, s98, k1_kernel@rel32@lo+4\n\ts_addc_u32 s99, s99, k1_kernel@rel32@hi+12\n\t"
                     "s_mov_b32 m0, %1\n\ts_nop 0\n\tglobal_load_lds_dword %0, s[98:99]"
                     :: "v"(vo), "i"(DUMMY_OFF) : "s98", "s99", "scc", "memory");
    }
    const int lane = tid & 63;
    int wid = __builtin_amdgcn_readfirstlane(tid >> 6);
    const int b = BIDX;
    const int xcd = b & 7, idx = b >> 3;
    const int tb = ((xcd >> 1) ^ (xcd >> 2)) * 4 + (idx & 3), nb = (xcd & 1) * 8 + (idx >> 2);
    const int cb = xcd * 32 + idx;
    const int t_base = tb * 128, p_base = nb * 64, d_base = nb * 64;
    const int l15 = lane & 15, q4 = lane >> 4;
    unsigned stv = 0, sth = 0;
    STAMPT(dbg, 0, 0);
    const unsigned lbase = (unsigned)(size_t)(lds_void*)smem;
#define ROT1 ((xcd >> 1) * 4 + (xcd & 1) * 8 + (idx >> 2))
    const int rot1 = ROT1;
    unsigned voA[4] = {0u, 0u, 0u, 0u}, voB[2] = {0u, 0u}, lw0 = 0;
    unsigned long long pt0 = 0, pt1 = 0, pt2 = 0;
    if (wid == 4 || wid == 5) {
        __builtin_amdgcn_s_setprio(PRIO_L);
        const int lh = wid - 4;
        const unsigned ch = (unsigned)(((lane & 7) ^ (lane >> 4)) * 16);
        voA[0] = (unsigned)(t_base + lh * 64 + (lane >> 5) * 16 + ((lane >> 3) & 3)) * 2048u + ch;
        voA[1] = (voA[0] ^ 64u) + (65536u - 1024u);
        voA[2] = voA[0] + 6144u; voA[3] = voA[1] + 6144u;
        voB[0] = (unsigned)(p_base + lh * 32 + (lane >> 3) * 2) * 2048u + ch;
        voB[1] = (voB[0] ^ 64u) + (32768u - 1024u);
        PIN4(voA[0], voA[1], voA[2], voA[3]); asm volatile("" : "+v"(voB[0]), "+v"(voB[1]));
#define GLDS4(sbase, v0, v1, v2, v3, sl, IMM) \
            asm volatile("s_add_u32 m0, %5, %6\n\ts_nop 0\n\tglobal_load_lds_dwordx4 %0, %4\n\tglobal_load_lds_dwordx4 %1, %4 offset:1024\n\t" \
                         "global_load_lds_dwordx4 %2, %4 offset:2048\n\tglobal_load_lds_dwordx4 %3, %4 offset:3072" \
                         :: "v"(v0), "v"(v1), "v"(v2), "v"(v3), "s"(sbase), "s"(sl), "i"(IMM) : "memory", "scc")
#define STAGE_A(kt, sl) do { const char* sa_ = (const char*)x + (((kt) + rot1) & 15) * 128; \
            GLDS4(sa_, voA[0], voA[1], voA[2], voA[3], sl, 0); GLDS4(sa_ + 16384, voA[0], voA[1], voA[2], voA[3], sl, 4096); } while (0)
#define STAGE_B(kt, sl) do { const int kr_ = (((kt) + rot1) & 15) * 128; const char* sb_ = (const char*)Bre + kr_; const char* sc_ = (const char*)Bim + kr_; \
            GLDS4(sb_, voB[0], voB[1], voB[0], voB[1], sl, 0); GLDS4(sc_, voB[0], voB[1], voB[0], voB[1], sl, 4096); } while (0)
        lw0 = lbase + lh * 8192;
        STAGE_A(0, lw0 + RA(0)); STAGE_B(0, lw0 + RB(0)); STAGE_A(1, lw0 + RA(1)); STAGE_B(1, lw0 + RB(1));
    }
    const int sblk = b >> 3, dblk = b & 7;
    u32x2 tw[4];
    unsigned epoch;
    asm volatile("" : "=v"(tw[0]), "=v"(tw[1]), "=v"(tw[2]), "=v"(tw[3]), "=v"(epoch));
    if (wid >= 6) {
            __builtin_amdgcn_s_setprio(PRIO_V0);
            const int vw = wid - 6;
            const int dch = dblk * 128 + vw * 64 + l15;
            const unsigned lk = lbase + ST_OFF + vw * ST_SZ;
            const unsigned vk = (unsigned)((lane >> 3) * DM * 2 + (((lane & 7) ^ ((lane >> 4) & 1)) * 16));
            const char* kb = (const char*)kin + (size_t)(sblk * 64 - 4) * (DM * 2) + dblk * 256 + vw * 128;
#define KLD(n) GLDS_K(kb + (n) * 8 * DM * 2, vk, lk, (n) * 1024 + ((n) >> 1) * 32)
#define VLD(n) GLDS_K(vb + (((n) >> 1) * 16 + ((n) & 1) * 2) * DM * 2, vv_, lk, ST_V + (n) * 1056)
            if (vw == 1) {
                const unsigned vg = (unsigned)((lane * 16 + rot1 * 128) & 2047), lg = lbase + GW_OFF;
                GLDS_S(gatew, vg, lg, 0); GLDS_S(gatew, vg ^ 1024u, lg, 1024);
            }
            asm volatile("global_load_dwordx2 %0, %5, %7\n\tglobal_load_dwordx2 %1, %5, %7 offset:128\n\tglobal_load_dwordx2 %2, %5, %7 offset:256\n\t"
                         "global_load_dwordx2 %3, %5, %7 offset:384\n\tglobal_load_dword %4, %6, %8 sc1"
                         : "+v"(tw[0]), "+v"(tw[1]), "+v"(tw[2]), "+v"(tw[3]), "+v"(epoch) : "v"((unsigned)(dch * 8)), "v"((unsigned)WS_EPOCH), "s"(shiftk), "s"(ws) : "memory");
            if (sblk == 0 && lane < 32) *(u32x4*)(smem + ST_OFF + vw * ST_SZ + lane * 16) = (u32x4){0u, 0u, 0u, 0u};
            if (sblk != 0 || lane >= 32) KLD(0);
            KLD(1); KLD(2); KLD(3); KLD(4); KLD(5); KLD(6); KLD(7);
            if (lane < 32) KLD(8);
            __builtin_amdgcn_s_setprio(0);
    }
    asm volatile("" : "+s"(wid));

#define WSL(w) ((((w) >> 2) ^ (nb >> 3)) * 4 + ((w) & 3))
#define TQ 0
    const int tq = TQ;
#define CSRC(w, ti) ((const char*)((((ti) ^ tq) & 1) ? Cim : Cre) + (2 * WSL(w) + (((ti) ^ tq) >> 1)) * 128)
    bf16x8 cf_[8][4];
    bf16x8 af[DEPTH];
    bool bumped = false;
    int cra, crb;
#define CT_SETUP() const unsigned vB0 = (unsigned)((d_base + (lane >> 3) * 4) * 2048 + (((lane & 7) ^ (lane >> 4)) * 16)), vB1 = (vB0 ^ 64u) + 65536u - 1024u
#define CTILE(w, ti, dst) do { const char* sb_ = CSRC(w, ti); const unsigned ds_ = (dst); \
                               GLDS4(sb_, vB0, vB1, vB0, vB1, ds_, 0); GLDS4(sb_ + 4096, vB0, vB1, vB0, vB1, ds_, 4096); } while (0)
#define CREAD(ti, a0_, a1_, off) do { _Pragma("unroll") for (int n_ = 0; n_ < 4; ++n_) { \
                               cf_[2 * (ti)][n_] = RD128(a0_, (off) + n_ * 2048); cf_[2 * (ti) + 1][n_] = RD128(a1_, (off) + n_ * 2048); } } while (0)
#define CPIN(k) PIN4(cf_[k][0], cf_[k][1], cf_[k][2], cf_[k][3])
    const int rot2 = (idx & 16) >> 2;
#define JB(u) (((((u) ^ (rot2 >> 2)) & 1) << 2) | ((wid - ((u) >> 1) + rot2) & 3))
    const __amdgpu_buffer_rsrc_t hs = __builtin_amdgcn_make_buffer_rsrc((char*)h + (size_t)tb * 524288 + WSL(wid) * 8192, 0, 524288, 0x00020000);
#define LOADF(f) do { af[(f) % DEPTH] = __builtin_bit_cast(bf16x8, __builtin_amdgcn_raw_buffer_load_b128(hs, vh + ((f) & 1) * 1024, JB((f) >> 3) * 65536 + ((((f) >> 1) & 3) ^ tq) * 2048, 16)); SCHED(); } while (0)
#define HPOLL_SETUP() gu32* hfp = (gu32*)(ws + WS_HFLAG) + (tb * 16 + 2 * WSL(wid) + (lane & 1)) * 32
#define HWAIT() do { unsigned spins = 0; \
            while (!__all(__hip_atomic_load(hfp, RLX_AGENT) == tag)) { if (++spins > SPIN_MAX) break; __builtin_amdgcn_s_sleep(PSLEEP); } asm volatile("" ::: "memory"); } while (0)

    const __amdgpu_buffer_rsrc_t gs = __builtin_amdgcn_make_buffer_rsrc(ws + WS_GATE, 0, 32768, 0x00020000);
#define GV(q4) ((unsigned)(((q4) >> 1) * 128 + ((q4) & 1) * 32))
#define GSO(r) (tb * 2048 + JB(6 + (r)) * 256 + (wid >> 2) * 16)
#define TR(r) (t_base + (JB(6 + (r)) >> 2) * 64 + (JB(6 + (r)) & 3) * 4 + (wid >> 2) * 2)
    u32x2 xx[2][2], dk2;
    u32x4 gv[2];
#define XLOAD() do { \
        const __amdgpu_buffer_rsrc_t xs = __builtin_amdgcn_make_buffer_rsrc((void*)x, 0, SEQ * DM * 2, 0x00020000); \
        const __amdgpu_buffer_rsrc_t ks = __builtin_amdgcn_make_buffer_rsrc((void*)dskip, 0, DM * 2, 0x00020000); \
        dk2 = __builtin_amdgcn_raw_buffer_load_b64(ks, (unsigned)(l15 * 8), d_base * 2, 0); \
        _Pragma("unroll") for (int q = 0; q < 4; ++q) \
            xx[q >> 1][q & 1] = __builtin_amdgcn_raw_buffer_load_b64(xs, (unsigned)(q4 * 16 * DM * 2 + l15 * 8 + (q & 1) * DM * 2), (TR(q >> 1) * DM + d_base) * 2, 0); \
        _Pragma("unroll") for (int r = 0; r < 2; ++r) gv[r] = __builtin_amdgcn_raw_buffer_load_b128(gs, GV(q4), GSO(r), 16); \
        SCHED(); } while (0)
#define XPIN() do { PIN4(xx[0][0], xx[0][1], xx[1][0], xx[1][1]); asm volatile("" : "+v"(dk2), "+v"(gv[0]), "+v"(gv[1])); } while (0)

    if (wid < 4) {
        const int wr = wid >> 1, wc = wid & 1;
        typedef __attribute__((ext_vector_type(16))) unsigned u32x16;
        typedef const __attribute__((address_space(4))) u32x16 cu16;
        const int po = (p_base + wc * 32) * 2;
        u32x16 s0 = *(cu16*)((size_t)logdt + po), s1 = *(cu16*)((size_t)lre + po), s2 = *(cu16*)((size_t)lim + po);
        SCHED();
        const int pq = p_base + wc * 32 + (q4 >> 1) * 16 + l15;
        unsigned goff[8], gmask[8];
#pragma unroll
        for (int i = 0; i < 8; ++i) {
            goff[i] = (unsigned)(((tb - 1 - (q4 & 1) - 2 * i) * 1024 + pq) * 16);
            gmask[i] = (tb - 1 - (q4 & 1) - 2 * i >= 0) ? ~0u : 0u;
            asm volatile("" : "+v"(goff[i]), "+v"(gmask[i]));
        }
        unsigned gpub = (unsigned)((tb * 1024 + pq) * 16), xch = lbase + XCH_OFF + wc * 1024 + lane * 16;
        unsigned hp = (unsigned)(tb * 524288 + wr * 262144 + (nb * 4 + wc) * 1024 + lane * 16);
        unsigned wa = (unsigned)(HT_OFF + wid * 2048 + (q4 * 32 + (l15 ^ ((q4 & 1) * 2 + (q4 >> 1) * 8))) * 8);
        unsigned ra0 = (unsigned)(HT_OFF + wid * 2048 + ((l15 & 3) * 32 + ((l15 >> 2) & 1) * 16 + ((4 * q4 + (l15 >> 3)) ^ ((l15 & 1) * 2 + ((l15 >> 1) & 1) * 8))) * 8), ra1 = ra0 ^ 16u;
        cra = GA_OFF + wid * 8192 + l15 * 128 + ((q4 ^ ((l15 >> 1) & 7)) * 16); crb = cra ^ 64;
        unsigned vB0 = (unsigned)((d_base + (lane >> 3) * 4) * 2048 + (((lane & 7) ^ (lane >> 4)) * 16)), vB1 = (vB0 ^ 64u) + 65536u - 1024u;
        PIN4(gpub, xch, hp, wa); PIN4(ra0, ra1, cra, crb); asm volatile("" : "+v"(vB0), "+v"(vB1));
        f32x4 acc[4][4];
#pragma unroll
        for (int m = 0; m < 4; ++m) {
#pragma unroll
            for (int c = 0; c < 4; ++c) acc[m][c] = (f32x4){0.f, 0.f, 0.f, 0.f};
            PIN4(acc[m][0], acc[m][1], acc[m][2], acc[m][3]);
        }
        SCHED();
        cf L1[2], Lq3[2], cc[2], l16[2], cLq[2], L64s, Lp, L256;
        {
            unsigned w0 = 0, w1 = 0, w2 = 0;
#pragma unroll
            for (int i = 0; i < 16; ++i) {
                asm("v_writelane_b32 %0, %3, %6\n\tv_writelane_b32 %1, %4, %6\n\tv_writelane_b32 %2, %5, %6" : "+v"(w0), "+v"(w1), "+v"(w2) : "s"(s0[i]), "s"(s1[i]), "s"(s2[i]), "i"(i));
            }
            asm("s_nop 1" : "+v"(w0), "+v"(w1), "+v"(w2));
            auto spread = [&](unsigned w) {
                const unsigned a = __builtin_amdgcn_permlane16_swap(w, w, false, false)[0];
                const unsigned c = __builtin_amdgcn_permlane32_swap(a, a, false, false)[0];
                return (q4 & 2) ? hi16(c) : lo16(c);
            };
            const float ldt = spread(w0), re = spread(w1), im = spread(w2);
            if (wr == 0) *(u32x4*)(smem + XCH_OFF + wc * 1024 + lane * 16) = (u32x4){0u, 0u, 0u, 0u};
            if (wid == 3 && lane < NFLAGW) *(unsigned*)(smem + UFLAG_OFF + lane * 4) = 0u;
            const float dt = rbf(expf(ldt));
            const float ea = expf(dt * re);
            float sn, cs;
            sincosf(dt * im, &sn, &cs);
            const cf T1 = {ea * cs, ea * sn};
            const float nr = T1.r - 1.0f, ni = T1.i, den = re * re + im * im;
            const cf c2 = {2.f * ((nr * re + ni * im) / den), 2.f * ((ni * re - nr * im) / den)};
            const cf T2 = cmul(T1, T1), T4 = cmul(T2, T2), L8 = cmul(T4, T4);
            const cf T16 = cmul(L8, L8), L32 = cmul(T16, T16), L48 = cmul(L32, T16);
            L64s = cmul(L32, L32);
            const cf L128 = cmul(L64s, L64s);
            L256 = cmul(L128, L128);
            Lp = {(q4 & 1) ? L128.r : 1.f, (q4 & 1) ? L128.i : 0.f};
            cf l32[2], l48[2];
            both(T1, L1); both(c2, cc); both(T16, l16); both(L32, l32); both(L48, l48);
#pragma unroll
            for (int sub = 0; sub < 2; ++sub) {
                Lq3[sub] = pick4(3 - q4, l16[sub], l32[sub], l48[sub]);
                cLq[sub] = cmul(cc[sub], pick4(q4, l16[sub], l32[sub], l48[sub]));
                PINC(L1[sub]); PINC(Lq3[sub]); PINC(cc[sub]); PINC(l16[sub]); PINC(cLq[sub]);
            }
            PINC(L64s); PINC(Lp); PINC(L256);
        }
        __builtin_amdgcn_s_setprio(PRIO_G);
        unsigned long long c_a = 0, r_a = 0;
        unsigned tagv;
        cf S[2];
        STAMPT(dbg, 22, 0);
        const int swz = (l15 >> 1) & 7;
        {
            const int a0 = RA(0) + (wr * 64 + l15) * 128 + ((q4 ^ swz) * 16);
            const int b0 = RB(0) + (wc * 64 + l15) * 128 + ((q4 ^ swz) * 16);
            WAIT_LGKM0();
            BAR();
            asm volatile("" ::: "memory");
            STAMPT(dbg, 19, 64);
            bf16x8 FA[2][4], FB[2][4];
            int pa[2] = {a0, a0 ^ 64}, pb[2] = {b0, b0 ^ 64};
            PIN4(pa[0], pa[1], pb[0], pb[1]);
#define LGW(n) __builtin_amdgcn_s_waitcnt(0xC07F | ((n) << 8))
#define RA_(j, mq) FA[((j) ^ ((j) >> 1)) & 1][(mq) + ((j) >> 1)] = RD128(pa[((j) ^ ((j) >> 1)) & 1], ((mq) + ((j) >> 1)) * 2048)
#define RB_(j, nq) FB[(j) & 1][(nq) + ((j) >> 1)] = RD128(pb[(j) & 1], ((nq) + ((j) >> 1)) * 2048)
#define G1_Q(Q, RD, FILL) do { \
            _Pragma("unroll") for (int i = 0; i < 8; ++i) { \
                const int p = i >> 1, dn = p >> 1, dm = (p ^ dn) & 1, ks = (i ^ p) & 1, m = ((Q) >> 1) * 2 + dm, n = ((Q) & 1) * 2 + dn; \
                if ((Q) == 0) { if (i < 2) LGW(3); if (i == 4) LGW(4); } \
                if ((Q) == 1) { if (i == 0) LGW(7); if (i == 1) LGW(6); if (i == 4) LGW(4); } \
                SCHED(); \
                MF(acc[m][n], FA[ks][m], FB[ks][n]); SCHED(); \
                if ((Q) == 0) { if (i < 4) RB_(i, 2); else RA_(i - 4, 2); } \
                if ((Q) == 2 && (RD) && i < 4) RA_(i, 0); \
                if ((Q) == 3 && (RD) && i < 4) RB_(i, 0); \
                FILL(((Q) - 2) * 8 + i); \
                SCHED(); \
            } } while (0)
#define NOF(g) do { } while (0)
#pragma unroll
            for (int j = 0; j < 4; ++j) { RA_(j, 0); SCHED(); }
#pragma unroll
            for (int j = 0; j < 4; ++j) { RB_(j, 0); SCHED(); }
            unsigned long long ua_ = UNITS_A;
            int sb = RB(0), sa = RA(0);
#pragma unroll 1
            for (int t3 = 0; t3 < 5; ++t3) {
#pragma unroll
                for (int t = 0; t < 3; ++t) {
                    int nx, na;
#define ADV(g) do { if ((g) == -8) { ua_ >>= 4; na = (int)((unsigned)ua_ & 15u) << 14; asm volatile("" : "+s"(na), "+s"(ua_)); } \
                    if ((g) == -7) { const int o_ = sa; sa = na; na -= o_; asm volatile("" : "+s"(na), "+s"(sa)); } \
                    if ((g) == -6) { pa[0] += na; pa[1] += na; asm volatile("" : "+v"(pa[0]), "+v"(pa[1])); } \
                    if ((g) == -5) { nx = (sb == 65536) ? -49152 : 16384; sb += nx; asm volatile("" : "+s"(nx), "+s"(sb)); } \
                    if ((g) == -3) { pb[0] += nx; pb[1] += nx; asm volatile("" : "+v"(pb[0]), "+v"(pb[1])); } } while (0)
                    G1_Q(0, true, NOF); G1_Q(1, true, ADV);
                    WAIT_LGKM0();
                    asm volatile("" ::: "memory");
                    BAR();
                    asm volatile("" ::: "memory");
                    G1_Q(2, true, NOF); G1_Q(3, true, NOF);
                }
            }
#define LASTF(g) do { if ((g) == -4) SETW(G1D_OFF, wid, 1u);        \
                      if ((g) == -3) tagv = *(volatile const lds_u32*)(size_t)(unsigned)TAG_OFF; } while (0)
            G1_Q(0, true, NOF);
            asm volatile("" ::: "memory");
            G1_Q(1, true, LASTF);
            asm volatile("s_nop 1");
#define AGG(g) do { if ((g) >= 2) { const int t = (g) >> 1, sub = (g) & 1; \
                        if (t == 1) { asm volatile("" : "+v"(acc[0][sub]), "+v"(acc[0][2 + sub])); S[sub] = {acc[0][sub][0], acc[0][2 + sub][0]}; } \
                        if (t == 4) asm volatile("" : "+v"(acc[1][sub]), "+v"(acc[1][2 + sub])); \
                        S[sub] = cfma(L1[sub], S[sub], cf{acc[t >> 2][sub][t & 3], acc[t >> 2][2 + sub][t & 3]}); PINC(S[sub]); } } while (0)
            G1_Q(2, false, AGG); G1_Q(3, false, AGG);
            PAD8(acc[2][0], acc[2][1], acc[2][2], acc[2][3]); PIN4(acc[3][0], acc[3][1], acc[3][2], acc[3][3]);
        }
        STAMPT(dbg, 1, 0);
        if (0 && TIDX == 128) { unsigned long long c_b = __builtin_amdgcn_s_memtime(), r_b = __builtin_amdgcn_s_memrealtime(); dbg[BIDX * 64 + 23] = (c_b - c_a) | (1ull << 62); dbg[BIDX * 64 + 24] = (r_b - r_a) | (1ull << 62); }
        cf ag0 = {0.f, 0.f};
        const unsigned tag = __builtin_amdgcn_readfirstlane(tagv);
        const __amdgpu_buffer_rsrc_t grs = __builtin_amdgcn_make_buffer_rsrc(ws, 0, 15 * 16384, 0x00020000);
        {
#pragma unroll
            for (int t = 8; t < 16; ++t)
#pragma unroll
                for (int sub = 0; sub < 2; ++sub) S[sub] = cfma(L1[sub], S[sub], cf{acc[t >> 2][sub][t & 3], acc[t >> 2][2 + sub][t & 3]});
            const cf W0 = cmul(S[0], Lq3[0]), W1 = cmul(S[1], Lq3[1]);
            const float er = red16(red32(W0.r, W1.r)), ei = red16(red32(W0.i, W1.i));
            if (wr == 0) {
                const u32x4 ev = {__float_as_uint(er), 1u, __float_as_uint(ei), 1u};
                asm volatile("ds_write_b128 %0, %1" :: "v"(xch), "v"(ev) : "memory");
            } else {
                u32x4 a0;
                unsigned spins = 0;
                for (;;) {
                    asm volatile("ds_read_b128 %0, %1\n\ts_waitcnt lgkmcnt(0)" : "=&v"(a0) : "v"(xch) : "memory");
                    if (__all(a0.y == 1u && a0.w == 1u)) break;
                    if (++spins > SPIN_MAX) break;
                }
                ag0 = {__uint_as_float(a0.x), __uint_as_float(a0.z)};
                const cf E = cfma(L64s, ag0, cf{er, ei});
                if ((q4 & 1) == 0) {
                    const u32x4 gv = {__float_as_uint(E.r), tag, __float_as_uint(E.i), tag};
                    __builtin_amdgcn_raw_buffer_store_b128(gv, grs, gpub, 0, 16);
                }
            }
        }
        SCHED();
        STAMPT(dbg, 2, 0);
        const unsigned cg = lbase + GA_OFF + wid * 8192;
#define GORD 0
#define GFIRST() do { wait_lds1(smem, COK_OFF + wid * 4); CREAD(0, cra, crb, 0); WAIT_LGKM0(); CPIN(0); CPIN(1); CTILE(wid, 1, cg); } while (0)
        if (GORD == 0) GFIRST();
#pragma unroll
        for (int sub = 0; sub < 2; ++sub)
#pragma unroll
            for (int m = 0; m < 4; ++m) {
                const f32x4 R = acc[m][sub], I = acc[m][2 + sub];
                acc[m][sub] = fma4(splat4(cc[sub].r), R, mul4(splat4(-cc[sub].i), I));
                acc[m][2 + sub] = fma4(splat4(-cc[sub].r), I, mul4(splat4(-cc[sub].i), R));
                asm volatile("" : "+v"(acc[m][sub]), "+v"(acc[m][2 + sub]));
                if (GORD == 1 && sub == 0 && m == 3) { SCHED(); GFIRST(); SCHED(); }
            }
        u32x4 gq[8];
        SCHED();
#pragma unroll
        for (int i = 0; i < 8; ++i) asm volatile("buffer_load_dwordx4 %0, %1, %2, 0 offen sc1" : "=&v"(gq[i]) : "v"(goff[i]), "s"(grs) : "memory");
        SCHED();
        if (b == 0 && wid == 0) {
            bool ok = true;
#pragma unroll
            for (int q = 0; q < 4; ++q) ok &= (__hip_atomic_load((gu32*)(ws + WS_ARRIVE) + (lane + q * 64) * 32, RLX_AGENT) == tag);
            bumped = __all(ok);
            if (bumped && lane == 0) __hip_atomic_store((gu32*)(ws + WS_EPOCH), tag, RLX_AGENT);
        }
        cf Ec[2];
#pragma unroll
        for (int sub = 0; sub < 2; ++sub) {
            const auto sr = __builtin_amdgcn_permlane16_swap(__float_as_uint(S[sub].r), __float_as_uint(S[sub].r), false, false);
            const auto si = __builtin_amdgcn_permlane16_swap(__float_as_uint(S[sub].i), __float_as_uint(S[sub].i), false, false);
            const cf Se = {__uint_as_float(sr[0]), __uint_as_float(si[0])}, So = {__uint_as_float(sr[1]), __uint_as_float(si[1])};
            const cf A = cfma(l16[sub], Se, So);
            const auto ar = __builtin_amdgcn_permlane32_swap(__float_as_uint(A.r), __float_as_uint(A.r), false, false);
            const auto ai = __builtin_amdgcn_permlane32_swap(__float_as_uint(A.i), __float_as_uint(A.i), false, false);
            const cf A01 = {(q4 & 2) ? __uint_as_float(ar[0]) : 0.f, (q4 & 2) ? __uint_as_float(ai[0]) : 0.f};
            const cf kq = {(q4 & 1) ? l16[sub].r : 1.f, (q4 & 1) ? l16[sub].i : 0.f};
            const cf E = cfma(kq, A01, cf{(q4 & 1) ? Se.r : 0.f, (q4 & 1) ? Se.i : 0.f});
            Ec[sub] = cmul(cc[sub], E);
            PINC(Ec[sub]);
        }
#define G2L 0
#define GSECOND() do { CREAD(1, cra, crb, 0); WAIT_LGKM0(); CPIN(2); CPIN(3); CTILE(wid, 2, cg); } while (0)
        WAIT_VM(8);
        if (G2L == 0) GSECOND();
        asm volatile("s_waitcnt vmcnt(%8)" : "+v"(gq[0]), "+v"(gq[1]), "+v"(gq[2]), "+v"(gq[3]), "+v"(gq[4]), "+v"(gq[5]), "+v"(gq[6]), "+v"(gq[7]) : "i"(G2L ? 0 : 8) : "memory");
        STAMPT(dbg, 27, 0);
        cf carry[2];
        {
            unsigned spins = 0;
            for (;;) {
                unsigned bad = 0;
#pragma unroll
                for (int i = 0; i < 8; ++i) bad |= ((gq[i].y ^ tag) | (gq[i].w ^ tag)) & gmask[i];
                if (__all(bad == 0u)) break;
                if (++spins > SPIN_MAX) break;
#pragma unroll
                for (int i = 0; i < 8; ++i) asm volatile("buffer_load_dwordx4 %0, %1, %2, 0 offen sc1" : "+v"(gq[i]) : "v"(goff[i]), "s"(grs) : "memory");
                asm volatile("s_waitcnt vmcnt(0)" : "+v"(gq[0]), "+v"(gq[1]), "+v"(gq[2]), "+v"(gq[3]), "+v"(gq[4]), "+v"(gq[5]), "+v"(gq[6]), "+v"(gq[7]) :: "memory");
            }
            cf E = {__uint_as_float(gq[7].x), __uint_as_float(gq[7].z)};
#pragma unroll
            for (int i = 6; i >= 0; --i) E = cfma(L256, E, cf{__uint_as_float(gq[i].x), __uint_as_float(gq[i].z)});
            E = cmul(E, Lp);
            cf Cy = {red16(E.r), red16(E.i)};
            if (wr == 1) Cy = cfma(L64s, Cy, ag0);
            both(Cy, carry);
        }
        STAMPT(dbg, 3, 0);
        if (G2L == 1) GSECOND();
        {
            const __amdgpu_buffer_rsrc_t hrs = __builtin_amdgcn_make_buffer_rsrc(h, 0, SEQ * 2048 * 2, 0x00020000);
            float hr[2], hn[2];
#pragma unroll
            for (int sub = 0; sub < 2; ++sub) {
                const cf e = cfma(cLq[sub], carry[sub], Ec[sub]);
                hr[sub] = e.r; hn[sub] = -e.i;
            }
            s16x4 fr[2][4];
#define HSTORE(m) do { _Pragma("unroll") for (int c = 0; c < 2; ++c) \
                __builtin_amdgcn_raw_buffer_store_b128(__builtin_bit_cast(u32x4, __builtin_shufflevector(fr[(m) & 1][2 * c], fr[(m) & 1][2 * c + 1], 0, 1, 2, 3, 4, 5, 6, 7)), hrs, hp + (unsigned)(c * 2048), (m) * 65536, WT); } while (0)
#pragma unroll
            for (int m = 0; m < 4; ++m) {
#pragma unroll
                for (int j = 0; j < 4; ++j)
#pragma unroll
                    for (int sub = 0; sub < 2; ++sub) {
                        const float nr = fmaf(L1[sub].r, hr[sub], fmaf(L1[sub].i, hn[sub], acc[m][sub][j]));
                        const float nn = fmaf(L1[sub].r, hn[sub], fmaf(-L1[sub].i, hr[sub], acc[m][2 + sub][j]));
                        hr[sub] = nr; hn[sub] = nn;
                        acc[m][sub][j] = nr; acc[m][2 + sub][j] = nn;
                    }
                u32x2 pk[4];
#pragma unroll
                for (int c = 0; c < 4; ++c) pk[c] = (u32x2){pkbf(acc[m][c][0], acc[m][c][1]), pkbf(acc[m][c][2], acc[m][c][3])};
                PIN4(pk[0], pk[1], pk[2], pk[3]);
                SCHED();
#pragma unroll
                for (int c = 0; c < 4; ++c) { *(volatile __attribute__((address_space(3))) u32x2*)(size_t)(wa + (c >> 1) * 1024 + (c & 1) * 128) = pk[c]; SCHED(); }
                asm volatile("" ::: "memory");
#pragma unroll
                for (int c = 0; c < 2; ++c) { fr[m & 1][2 * c] = TRD(ra0 + c * 1024); SCHED(); fr[m & 1][2 * c + 1] = TRD(ra1 + c * 1024); SCHED(); }
                asm volatile("" ::: "memory");
                if (m > 0) HSTORE(m - 1);
                SCHED();
            }
            HSTORE(3);
        }
        STAMPT(dbg, 4, 0);
        {
            WAIT_VM(8);
            CREAD(2, cra, crb, 0);
            WAIT_VM(0);
            {
                unsigned n;
                asm volatile("s_mov_b64 exec, 1\n\tds_add_rtn_u32 %0, %1, %2\n\ts_mov_b64 exec, -1\n\ts_waitcnt lgkmcnt(0)" : "=&v"(n) : "v"(MISC_OFF), "v"(1u) : "memory");
                if (__builtin_amdgcn_readfirstlane(n) == 3u) {
                    if (lane == 0) __hip_atomic_store((gu32*)(ws + WS_HFLAG) + (tb * 16 + nb) * 32, tag, RLX_AGENT);
                    if (DBGON && lane == 0) dbg[BIDX * 64 + 20] = __builtin_amdgcn_s_memrealtime();
                }
            }
            STAMPT(dbg, 12, 0);
            __builtin_amdgcn_s_setprio(0);
            WAIT_LGKM0();
            CPIN(4); CPIN(5);
            CTILE(wid, 3, cg);
            XLOAD();
            HPOLL_SETUP();
            HWAIT();
            STAMPT(dbg, 8, 0);
            const unsigned vh = (unsigned)(lane * 16);
#pragma unroll
            for (int f = 0; f < DEPTH; ++f) LOADF(f);
            __builtin_amdgcn_s_setprio(PRIO2_G);
        }
    } else {
        const int lw = wid - 4;
        const int d = cb * 4 + lw;
        const char* Krow = (const char*)(ssmk + (size_t)d * SEQ);
        cra = LA_OFF + lw * 8192 + l15 * 128 + ((q4 ^ ((l15 >> 1) & 7)) * 16); crb = cra ^ 64;
        const unsigned cst0 = lbase + LA_OFF + lw * 8192;
        CT_SETUP();
        asm volatile("" : "+v"(cra), "+v"(crb));
        u32x4 KA[4], KS[4][3];
        unsigned draw;
        auto shifts = [&](int q0, int q1) __attribute__((always_inline)) {
            if (q0 == 0 && lane == 0) KA[0].x = (KA[0].x & 0xffff0000u) | f2bf(lo16(KA[0].x) + bf2f((u16)draw));
#pragma unroll
            for (int q = q0; q < q1; ++q) {
                const u32x2 p = q ? pred2(KA[q], RL63(KA[q ? q - 1 : 0].z), RL63(KA[q ? q - 1 : 0].w)) : pred2(KA[0], 0u, 0u);
                const u32x4 a = KA[q];
                KS[q][0] = (u32x4){al(a.x, p.y), al(a.y, a.x), al(a.z, a.y), al(a.w, a.z)};
                KS[q][1] = (u32x4){p.y, mv(a.x), mv(a.y), mv(a.z)};
                KS[q][2] = (u32x4){al(p.y, p.x), mv(KS[q][0].x), mv(KS[q][0].y), mv(KS[q][0].z)};
                PIN4(KA[q], KS[q][0], KS[q][1], KS[q][2]);
            }
        };
        auto copies = [&]() __attribute__((always_inline)) {
            char* cp = smem + KC_OFF + lw * CV_W + 64 + lane * 16;
#pragma unroll
            for (int q = 3; q >= 0; --q) {
                *(u32x4*)(cp + q * 1024) = KA[q];
#pragma unroll
                for (int c = 0; c < 3; ++c) *(u32x4*)(cp + q * 1024 + (c + 1) * CV_COPY) = KS[q][c];
            }
            if (lane < 16) *(u32x4*)(smem + KC_OFF + lw * CV_W + (lane >> 2) * CV_COPY + (lane & 3) * 16) = (u32x4){0u, 0u, 0u, 0u};
            SCHED();
        };
        unsigned tag;
        float gacc = 0.f;
#define KLOAD() asm volatile("global_load_dwordx4 %0, %5, %7 nt\n\tglobal_load_dwordx4 %1, %5, %7 offset:1024 nt\n\tglobal_load_dwordx4 %2, %5, %7 offset:2048 nt\n\t" \
                             "global_load_dwordx4 %3, %5, %7 offset:3072 nt\n\tglobal_load_ushort %4, %6, %8" \
                             : "=&v"(KA[0]), "=&v"(KA[1]), "=&v"(KA[2]), "=&v"(KA[3]), "=&v"(draw) : "v"((unsigned)(lane * 16)), "v"((unsigned)(d * 2)), "s"(Krow), "s"(ssmD) : "memory")
#define KWAIT(n) asm volatile("s_waitcnt vmcnt(%5)" : "+v"(KA[0]), "+v"(KA[1]), "+v"(KA[2]), "+v"(KA[3]), "+v"(draw) : "i"(n) : "memory")
        if (wid < 6) {
#define LT0() do { } while (0)
#define LT1() do { } while (0)
#define LWB(n) WAIT_VM(n)
            STAGE_A(2, lw0 + RA(2));
#define PRE 6
            if (PRE >= 6) STAGE_B(2, lw0 + RB(2));
            if (PRE >= 7) STAGE_B(3, lw0 + RB(3));
            if (PRE == 5) WAIT_VM(24); else if (PRE == 6) WAIT_VM(32); else WAIT_VM(40);
            STAMPT(dbg, 17, 320);
            BAR();
            LT0();
            if (PRE < 6) STAGE_B(2, lw0 + RB(2));
            LT1();
            if (PRE < 7) STAGE_B(3, lw0 + RB(3));
            unsigned long long ua_ = UNITS_A >> 12;
            unsigned sla = lw0 + RA(3), slb = lw0 + RB(4);
            int ka = ((3 + rot1) & 15) * 128, kb_ = ka;
            const char* sa = (const char*)x + ka;
            asm volatile("" : "+s"(sa), "+s"(sla), "+s"(ka), "+s"(ua_));
#define NEXT_A() do { ka = (ka + 128) & 2047; sa = (const char*)x + ka; ua_ >>= 4; sla = lw0 + (((unsigned)ua_ & 15u) << 14); asm volatile("" : "+s"(sa), "+s"(sla), "+s"(ka), "+s"(ua_)); } while (0)
#define LSTEP(n) do { \
                LWB(n); \
                BAR(); \
                GLDS4(sa, voA[0], voA[1], voA[2], voA[3], sla, 0); \
                LT0(); \
                GLDS4(sa + 16384, voA[0], voA[1], voA[2], voA[3], sla, 4096); \
                kb_ = (kb_ + 128) & 2047; \
                GLDS4((const char*)Bre + kb_, voB[0], voB[1], voB[0], voB[1], slb, 0); \
                LT1(); \
                NEXT_A(); \
                const unsigned slb0 = slb; \
                slb = (slb == lw0 + 65536) ? lw0 + 16384 : slb + 16384; \
                asm volatile("" : "+s"(slb), "+s"(kb_), "+s"(t)); \
                GLDS4((const char*)Bim + kb_, voB[0], voB[1], voB[0], voB[1], slb0, 4096); } while (0)
#pragma unroll 1
            for (int t = 1; t < 5; ++t) LSTEP(24);
            GLDS4(sa, voA[0], voA[1], voA[2], voA[3], sla, 0); GLDS4(sa + 16384, voA[0], voA[1], voA[2], voA[3], sla, 4096);
            NEXT_A();
#pragma unroll 1
            for (int t = 5; t < 13; ++t) LSTEP(32);
#define LK12 1
#define LC 14
#define LCD 4
#define LSH 0
            if (LK12) { KLOAD(); LWB(37); } else LWB(32);
            BAR();
            LT0();
            if (!LK12) KLOAD();
            __builtin_amdgcn_s_setprio(0);
            if (LC == 13) { __builtin_amdgcn_s_sleep(LCD); CTILE(lw ^ SWP, 0, (cst0 ^ (SWP * 8192)) + GA_OFF); FLAGD(COK_OFF + (lw ^ SWP) * 4); }
            LT1();
            if (LC == 13) LWB(30); else LWB(21);
            BAR();
            LT0();
            if (LC == 14) { CTILE(lw ^ SWP, 0, (cst0 ^ (SWP * 8192)) + GA_OFF); FLAGD(COK_OFF + (lw ^ SWP) * 4); }
            if (LSH) { KWAIT(9); shifts(0, LSH); }
            LT1();
            LWB(14);
            BAR();
            LT0();
            KWAIT(9);
            shifts(LSH, 4);
            STAMPT(dbg, 14, 320);
            copies();
            if (lane == 0) SETW(UFLAG_OFF, lw, 1u);
            LT1();
            tag = lds_word(smem, TAG_OFF);
        } else {
            const int vw = wid - 6;
            const int dch = dblk * 128 + vw * 64 + l15;
            const unsigned lk = lbase + ST_OFF + vw * ST_SZ;
            const unsigned vv_ = (unsigned)(((lane >> 4) * 4 + ((lane >> 3) & 1)) * DM * 2 + (lane & 7) * 16);
            const char* vb = (const char*)vin + (size_t)(sblk * 64) * (DM * 2) + dblk * 256 + vw * 128;
            const int grow = nb * 8 + vw * 4 + q4;
            unsigned gx0 = (unsigned)(grow * 128 + (((l15 >> 1) ^ ((grow >> 1) & 7)) * 16) + (l15 & 1) * 8), gx1 = gx0 + RA(2), gwa = (unsigned)(GW_OFF + l15 * 8);
            asm volatile("" : "+v"(gx0), "+v"(gx1), "+v"(gwa));
            u32x2 gxv, gwv;
            typedef const volatile __attribute__((address_space(3))) u32x2* l2p;
#define XA() do { } while (0)
#define XR() do { } while (0)
#define XS() do { } while (0)
#define VBAR(t) do { SCHED(); XA(); asm volatile("" ::: "memory"); BAR(); asm volatile("" ::: "memory"); XR(); \
                     gxv = *(l2p)(size_t)(RA(t) == 0 ? gx0 : gx1 + (RA(t) - RA(2))); gwv = *(l2p)(size_t)(gwa + (t) * 128); SCHED(); } while (0)
#define GATE() do { SCHED(); gacc = fmaf(lo16(gxv.x), lo16(gwv.x), gacc); gacc = fmaf(hi16(gxv.x), hi16(gwv.x), gacc); \
                    gacc = fmaf(lo16(gxv.y), lo16(gwv.y), gacc); gacc = fmaf(hi16(gxv.y), hi16(gwv.y), gacc); asm volatile("" : "+v"(gacc)); XS(); SCHED(); } while (0)
            unsigned ka[4][2], sel[4];
            const int R0 = 4 + (l15 >> 2) * 16 + (l15 & 3) - (q4 >> 1);
            unsigned kr0 = (unsigned)(2 * R0), kr1 = (unsigned)(ST_OFF + vw * ST_SZ + R0 * 128 + (((q4 ^ (R0 >> 1)) & 1) * 16));
            asm volatile("" : "+v"(kr0), "+v"(kr1));
#pragma unroll
            for (int i = 0; i < 4; ++i)
#pragma unroll
                for (int tp = 0; tp < 2; ++tp) {
                    ka[i][tp] = (kr1 ^ (tp * 16)) + ((kr0 + 8 * i - 4 * tp) & 0xe0) + (4 * i - 2 * tp) * 128;
                    asm volatile("" : "+v"(ka[i][tp]));
                }
#pragma unroll
            for (int r = 0; r < 4; ++r) {
                const unsigned pick = (q4 & 2) ? 0x0302u : 0x0100u;
                sel[r] = ((l15 >> 1) == (q4 & 1) * 4 + r) ? ((l15 & 1) ? (pick << 16) | 0x0c0cu : 0x0c0c0000u | pick) : 0x0c0c0c0cu;
            }
            unsigned va_ = (unsigned)(ST_OFF + vw * ST_SZ + ST_V + (q4 * 2 + (l15 >> 3)) * 1056 + ((l15 >> 2) & 1) * 128 + (l15 & 3) * 8);
            unsigned so = (unsigned)(((dch >> 2) * 32 + sblk) * 512 + q4 * 64 + (dch & 3) * 16);
            PIN4(sel[0], sel[1], sel[2], sel[3]); asm volatile("" : "+v"(so), "+v"(va_));
#define VALL() do { VLD(0); VLD(1); VLD(2); VLD(3); VLD(4); VLD(5); VLD(6); VLD(7); } while (0)
            asm volatile("s_waitcnt vmcnt(9)" : "+v"(tw[0]), "+v"(tw[1]), "+v"(tw[2]), "+v"(tw[3]), "+v"(epoch) :: "memory");
#define VP 2
            if (VP == 0) VALL();
            STAMPT(dbg, 18, 384);
            VBAR(0);
            if (VP == 1) VALL();
            bf16x8 wf[4][2];
#pragma unroll
            for (int ps = 0; ps < 4; ++ps)
#pragma unroll
                for (int tp = 0; tp < 2; ++tp) {
                    u32x4 w;
#pragma unroll
                    for (int r = 0; r < 4; ++r) w[r] = __builtin_amdgcn_perm(0u, tw[ps][tp], sel[r]);
                    wf[ps][tp] = __builtin_bit_cast(bf16x8, w);
                    asm volatile("" : "+v"(wf[ps][tp]));
                }
            SCHED();
            if (VP == 2) VALL();
            GATE(); VBAR(1);
            f32x4 ks[4][4];
            auto sconv = [&](int i0) __attribute__((always_inline)) {
                bf16x8 kf[2][4][2];
#pragma unroll
                for (int q = 0; q < 16; ++q) { kf[q >> 3][q & 3][(q >> 2) & 1] = RD128(ka[i0 + (q >> 3)][(q >> 2) & 1], (q & 3) * 32); SCHED(); }
                GATE();
                __builtin_amdgcn_s_setprio(PRIO_KV);
                WAIT_LGKM0();
                SCHED();
#pragma unroll
                for (int i = 0; i < 2; ++i) {
#pragma unroll
                    for (int ps = 0; ps < 4; ++ps) MF0(ks[ps][i0 + i], kf[i][ps][0], wf[ps][0]);
#pragma unroll
                    for (int ps = 0; ps < 4; ++ps) MF(ks[ps][i0 + i], kf[i][ps][1], wf[ps][1]);
                }
                __builtin_amdgcn_s_setprio(0);
                SCHED();
            };
            WAIT_VM(8);
            STAMPT(dbg, 21, 384);
            sconv(0); VBAR(2);
            sconv(2); VBAR(3);
            WAIT_VM(0);
            u32x2 vq[4][4];
#pragma unroll
            for (int ps = 0; ps < 4; ++ps)
#pragma unroll
                for (int j = 0; j < 4; ++j) vq[ps][j] = __builtin_bit_cast(u32x2, TRD(va_ + j * 256 + ps * 32));
            const __amdgpu_buffer_rsrc_t rs = __builtin_amdgcn_make_buffer_rsrc(kvT, 0, DM * SEQ * 2, 0x00020000);
            u32x4 o[8];
            auto kvout = [&](int n) __attribute__((always_inline)) {
                const int hh = n >> 2, ps = n & 3;
                asm volatile("" : "+v"(ks[ps][2 * hh]), "+v"(ks[ps][2 * hh + 1]));
#pragma unroll
                for (int e = 0; e < 4; ++e) {
                    const unsigned vv = vq[ps][2 * hh + (e >> 1)][e & 1];
                    o[n][e] = pkbf(ks[ps][2 * hh + (e >> 1)][2 * (e & 1)] * lo16(vv), ks[ps][2 * hh + (e >> 1)][2 * (e & 1) + 1] * hi16(vv));
                }
                asm volatile("" : "+v"(o[n]));
                SCHED();
            };
            GATE(); kvout(0); kvout(1);
            WAIT_LGKM0();
            VBAR(4);
            kvout(2); kvout(3); GATE(); VBAR(5);
            kvout(4); kvout(5); GATE(); VBAR(6);
            kvout(6); kvout(7); GATE(); VBAR(7);
            if (vw == 1 && lane == 0) *(unsigned*)(smem + TAG_OFF) = (epoch + 1u) ? epoch + 1u : 1u;
#define NG 1
#define GRP (idx % NG)
            const int grp = GRP;
            const unsigned av = (unsigned)(WS_ARRIVE + b * 128), tg = (epoch + 1u) ? epoch + 1u : 1u;
#define BURST(g) do { \
                asm volatile("s_cmp_lg_u32 %13, %14\n\ts_cbranch_scc1 1f\n\t" \
                    "buffer_store_dwordx4 %0, %8, %9, 0 offen sc1\n\tbuffer_store_dwordx4 %4, %8, %9, 0 offen offset:256 sc1\n\t" \
                    "buffer_store_dwordx4 %1, %8, %9, %10 offen sc1\n\tbuffer_store_dwordx4 %5, %8, %9, %10 offen offset:256 sc1\n\t" \
                    "buffer_store_dwordx4 %2, %8, %9, %11 offen sc1\n\tbuffer_store_dwordx4 %6, %8, %9, %11 offen offset:256 sc1\n\t" \
                    "buffer_store_dwordx4 %3, %8, %9, %12 offen sc1\n\tbuffer_store_dwordx4 %7, %8, %9, %12 offen offset:256 sc1\n1:" \
                    :: "v"(o[0]), "v"(o[1]), "v"(o[2]), "v"(o[3]), "v"(o[4]), "v"(o[5]), "v"(o[6]), "v"(o[7]), "v"(so), "s"(rs), "s"(65536), "s"(131072), "s"(196608), "s"(grp), "i"(g) : "memory", "scc"); \
                if (grp == (g) && vw == 1 && lane == 0) asm volatile("global_store_dword %0, %1, %2 sc1" :: "v"(av), "v"(tg), "s"(ws) : "memory"); } while (0)
            const char* tsrc = (const char*)(vw ? Cim : Cre) + (size_t)b * 8192;
#define TP 10
            BURST(0); GATE(); VBAR(8);
            if (NG > 1) { BURST(1); GATE(); VBAR(9); }
            if (NG > 2) { BURST(2); GATE(); VBAR(10); }
#define FD 0
            if (FD) { GATE(); VBAR(8 + NG); }
            GATE();
#define FP (8 + NG + FD)
            WAIT_VM(0);
            VBAR(FP);
            tag = __builtin_amdgcn_readfirstlane(*(const unsigned*)(smem + TAG_OFF));
            if (vw == 0 && lane == 0) __hip_atomic_store((gu32*)(ws + WS_KVFLAG) + (dblk * 32 + sblk) * 32, tag, RLX_AGENT);
            if (vw == 1 && lane < 4) *(u32x4*)(smem + U_OFF + 192 + lane * 16) = (u32x4){0u, 0u, 0u, 0u};
            gu32* kfp = (gu32*)(ws + WS_KVFLAG) + ((cb >> 5) * 32 + vw * 16 + l15) * 32;
            const char* ub = (const char*)kvT + (size_t)__builtin_amdgcn_readfirstlane(cb * 16384 + vw * 8192);
            const unsigned vu = (unsigned)((lane >> 5) * 512 + ((lane >> 2) & 1) * 256 + ((lane >> 3) & 3) * 64 + ((lane - (lane >> 4)) & 3) * 16);
            const unsigned lu = lbase + U_OFF + 256 + vw * 8192;
#define U_DMA1(o) asm volatile("s_add_u32 m0, %2, %3\n\ts_nop 0\n\tglobal_load_lds_dwordx4 %0, %1 sc1\n\tglobal_load_lds_dwordx4 %0, %1 offset:1024 sc1\n\t" \
                             "global_load_lds_dwordx4 %0, %1 offset:2048 sc1\n\tglobal_load_lds_dwordx4 %0, %1 offset:3072 sc1" \
                             :: "v"(vu), "s"(ub + (o)), "s"(lu), "i"(o) : "memory", "scc")
#define U_DMA() do { U_DMA1(0); U_DMA1(4096); } while (0)
#define UP 13
#define PP (UP - 2)
            unsigned pv = 0;
            bool done = false;
#define VKD 4
#define VSTEP(t) do { \
                if ((t) == UP) { asm volatile("s_waitcnt vmcnt(%1)" : "+v"(pv) : "i"(VK12 ? 5 : 0) : "memory"); done = __all(pv == tag); if (!VK12) KLOAD(); if (done) U_DMA(); } \
                if ((t) == PP) asm volatile("global_load_dword %0, %1, off sc1" : "=&v"(pv) : "v"(kfp) : "memory"); \
                if ((t) == TP) TOUCH(tsrc, (unsigned)(lane * 128)); \
                if ((t) == VC) { CTILE(lw ^ SWP, 0, (cst0 ^ (SWP * 8192)) + GA_OFF); FLAGD(COK_OFF + (lw ^ SWP) * 4); } \
                GATE(); \
                if ((t) == 12 && VK12) { __builtin_amdgcn_s_sleep(VKD); KLOAD(); } \
                VBAR((t) + 1); } while (0)
#define VK12 0
#define VC 15
            if (FP <= 9) VSTEP(9);
            if (FP <= 10) VSTEP(10);
            if (FP <= 11) VSTEP(11);
            VSTEP(12); VSTEP(13); VSTEP(14);
            if (DBGON && TIDX == 384) dbg[BIDX * 64 + 13] = (done ? 100ull : 0ull) | (1ull << 62);
            GATE();
            if (VC == 15) { CTILE(lw ^ SWP, 0, (cst0 ^ (SWP * 8192)) + GA_OFF); FLAGD(COK_OFF + (lw ^ SWP) * 4); }
            if (VC == 16) { if (!done) WAIT_VM(0); KWAIT(8); } else { if (!done) WAIT_VM(9); KWAIT(17); }
            shifts(0, 4);
            copies();
            if (VC == 16) { WAIT_LGKM0(); CTILE(lw ^ SWP, 0, (cst0 ^ (SWP * 8192)) + GA_OFF); FLAGD(COK_OFF + (lw ^ SWP) * 4); }
            WAIT_VM(9);
            if (!done) {
                unsigned spins = 0;
                for (;;) {
                    const unsigned v = __hip_atomic_load(kfp, RLX_AGENT);
                    if (__all(v == tag)) break;
                    if (++spins > SPIN_MAX) break;
                    __builtin_amdgcn_s_sleep(1);
                }
                U_DMA();
                WAIT_VM(0);
            }
            if (lane == 0) SETW(UFLAG_OFF, lw, 1u);
        }
        STAMPT(dbg, 26, 320);
        wait_lds4(smem, G1D_OFF);
        wait_lds4(smem, UFLAG_OFF);
#define C0Q 4
        if (C0Q < 0) CTILE(wid, 0, cst0);
        STAMPT(dbg, 9, 256);
        __builtin_amdgcn_s_setprio(PRIO_CV);
        STAMPT(dbg, 10, 384);
        f32x4 cv[8];
#pragma unroll
        for (int g = 0; g < 8; ++g) cv[g] = (f32x4){0.f, 0.f, 0.f, 0.f};
        PIN4(cv[0], cv[1], cv[2], cv[3]); PIN4(cv[4], cv[5], cv[6], cv[7]);
        const int uz = U_OFF + 192;
        const int ua0 = U_OFF + 256 + (2 * l15 + q4 - 2) * 64, rv0 = (2 * l15 + q4 - 2) * 4 + lw * 16;
        const unsigned ka0 = (unsigned)((l15 >> 2) * CV_COPY - 16 * q4 + 8 * (l15 & 3) - 8);
        int ua, rv, pu, pz;
        unsigned adr;
#define UNEXT1() do { pu = ua | (rv & 0x30); asm volatile("" : "+v"(pu)); } while (0)
#define UNEXT2() do { pz = (ua > uz ? ua : uz) | (rv & 0x30); asm volatile("" : "+v"(pz)); } while (0)
#define UNEXT3() do { ua -= 256; rv -= 16; asm volatile("" : "+v"(ua), "+v"(rv)); } while (0)
#define ANEXT() do { adr += 64; asm volatile("" : "+v"(adr)); } while (0)
#define RDU(dst, c) do { dst = (c) ? RD128(pu, (c) * 2048) : RD128(pz, 0); SCHED(); } while (0)
        HPOLL_SETUP();
        unsigned hpv = 0;
        {
            bf16x8 A[8], UB[8];
            ua = ua0; rv = rv0; adr = lbase + KC_OFF + lw * CV_W + ka0 + 96;
            asm volatile("" : "+v"(adr));
            UNEXT1(); UNEXT2(); UNEXT3();
#pragma unroll
            for (int i = 0; i < 8; ++i) { RDA(A[7 - i], adr, 7 - i); RDU(UB[i], i); }
            unsigned gb = lw >= 2 ? (unsigned)((tb * 16 + nb) * 128 + (lw - 2) * 32) : 0x80000000u;
            asm volatile("" : "+s"(gb));
            const unsigned gso = (l15 == 0) ? gb + (unsigned)(q4 * 8) : 0x80000000u;
            float gt = 0.f;
            unsigned vxo = 0, vdo = 0, vgo = 0;
            auto gate_out = [&](int i) __attribute__((always_inline)) {
                if (i == 0) gacc += __uint_as_float(__builtin_amdgcn_update_dpp(0u, __float_as_uint(gacc), 0xb1, 0xf, 0xf, false));
                if (i == 1) gacc += __uint_as_float(__builtin_amdgcn_update_dpp(0u, __float_as_uint(gacc), 0x4e, 0xf, 0xf, false));
                if (i == 2) gacc += __uint_as_float(__builtin_amdgcn_update_dpp(0u, __float_as_uint(gacc), 0x141, 0xf, 0xf, false));
                if (i == 3) gacc += __uint_as_float(__builtin_amdgcn_update_dpp(0u, __float_as_uint(gacc), 0x140, 0xf, 0xf, false));
                if (i == 4) gacc = rbf(gacc);
                if (i == 5) gacc = __builtin_amdgcn_exp2f(gacc * -1.44269504088896f);
                if (i == 6) gacc = __builtin_amdgcn_rcpf(1.0f + gacc);
                if (i == 7) gacc = rbf(gacc);
                if (i == 8) gt = __uint_as_float(pkbf(gacc, 1.0f - gacc));
                if (i == 9) __builtin_amdgcn_raw_buffer_store_b64((u32x2){__float_as_uint(gt), tag}, gs, gso, 0, WT);
                asm volatile("" : "+v"(gacc), "+v"(gt));
            };
            auto step = [&](auto E) __attribute__((always_inline)) {
                constexpr int e = decltype(E)::value;
                constexpr int ti = (e == CS0) ? 0 : (e == CS1) ? 1 : (e == CS2) ? 2 : -1, tj = ti < 0 ? 0 : ti;
                constexpr unsigned char GA[36] = {0x66, 0x77, 0x76, 0x44, 0x55, 0x54, 0x65, 0x64, 0x75, 0x74, 0x22, 0x33, 0x32, 0x43, 0x42, 0x53, 0x52, 0x63, 0x62, 0x73, 0x72,
                                                  0x00, 0x11, 0x10, 0x21, 0x20, 0x31, 0x30, 0x41, 0x40, 0x51, 0x50, 0x61, 0x60, 0x71, 0x70};
                const char* sb_ = CSRC(wid, tj + 1);
#pragma unroll
                for (int q = 0; q < 36; ++q) {
                    const int g = GA[q] >> 4, a = GA[q] & 15;
                    MF(cv[g], A[a], UB[g - a]); SCHED();
                    if (e < 7) {
                        if (a == 1) RDU(UB[g - 1], g - 1);
                        if (q == 35) RDU(UB[7], 7);
                        if (g == 7) RDA(A[a], adr + 64 * (e + 1), a);
                        if (q == 11) UNEXT1();
                        if (q == 13) UNEXT2();
                        if (q == 15) UNEXT3();
                    }
                    if (ti >= 0) {
                        const int n = (q >= 3 && q <= 7) ? q - 3 : q == 10 ? 5 : q == 12 ? 6 : q == 14 ? 7 : -1;
                        if (q == 3) { if (ti == 0 && C0Q < 18) WAIT_VM(1); else if (e == XE + 1) WAIT_VM(7); else WAIT_VM(0); }
                        if (n >= 0) cf_[2 * tj + (n >> 2)][n & 3] = RD128((n >> 2) ? crb : cra, (n & 3) * 2048);
                        if (q == 18) { CPIN(2 * tj); CPIN(2 * tj + 1); }
                        if (q == 18 || q == 23) asm volatile("s_add_u32 m0, %3, %4\n\ts_nop 0\n\tglobal_load_lds_dwordx4 %0, %2\n\tglobal_load_lds_dwordx4 %1, %2 offset:1024"
                                                             :: "v"(vB0), "v"(vB1), "s"(sb_ + (q == 18 ? 0 : 4096)), "s"(cst0), "i"(q == 18 ? 0 : 4096) : "memory", "scc");
                        if (q == 21 || q == 25) asm volatile("global_load_lds_dwordx4 %0, %2 offset:2048\n\tglobal_load_lds_dwordx4 %1, %2 offset:3072"
                                                             :: "v"(vB0), "v"(vB1), "s"(sb_ + (q == 21 ? 0 : 4096)) : "memory");
                    }
                    if (e == 0 && q >= 3 && q <= 7) gate_out(q - 3);
                    if (e == 0 && q >= 10 && q <= 18 && !(q & 1)) gate_out(5 + (q - 10) / 2);
                    if (e == 0 && q == C0Q) CTILE(wid, 0, cst0);
                    if (e == XE && q >= 23 && q <= 35 && (q & 1)) {
                        const int i = (q - 23) / 2;
                        const __amdgpu_buffer_rsrc_t xs = __builtin_amdgcn_make_buffer_rsrc((void*)x, 0, SEQ * DM * 2, 0x00020000);
                        const __amdgpu_buffer_rsrc_t ks = __builtin_amdgcn_make_buffer_rsrc((void*)dskip, 0, DM * 2, 0x00020000);
                        if (i < 4) asm volatile("buffer_load_dwordx2 %0, %1, %2, %3 offen offset:%4" : "=&v"(xx[(i >> 1) & 1][i & 1]) : "v"(vxo), "s"(xs), "s"((TR((i >> 1) & 1) * DM + d_base) * 2), "i"((i & 1) * 2048) : "memory");
                        if (i == 4) asm volatile("buffer_load_dwordx2 %0, %1, %2, %3 offen" : "=&v"(dk2) : "v"(vdo), "s"(ks), "s"(d_base * 2) : "memory");
                        if (i >= 5) asm volatile("buffer_load_dwordx4 %0, %1, %2, %3 offen sc1" : "=&v"(gv[(i - 5) & 1]) : "v"(vgo), "s"(gs), "s"(GSO((i - 5) & 1)) : "memory");
                    }
                    if (e == XE && q == 16) { vxo = (unsigned)(q4 * 16 * DM * 2 + l15 * 8); vdo = (unsigned)(l15 * 8); asm volatile("" : "+v"(vxo), "+v"(vdo)); }
                    if (e == XE && q == 17) { vgo = GV(q4); asm volatile("" : "+v"(vgo)); }
                    if (e == 6 && q == 30) hpv = __hip_atomic_load(hfp, RLX_AGENT);
                    SCHED();
                }
            };
            unroll<8>(step);
            WAIT_VM(0);
            XPIN();
        }
        PAD8(cv[0], cv[1], cv[2], cv[3]); PIN4(cv[4], cv[5], cv[6], cv[7]);
        STAMPT(dbg, 11, 384);
        const unsigned vh = (unsigned)((lane ^ 2) * 16);
        if (!__all(hpv == tag)) HWAIT();
        asm volatile("" ::: "memory");
#pragma unroll
        for (int f = 0; f < DEPTH; ++f) LOADF(f);
        STAMPT(dbg, 25, 384);
        {
            const __amdgpu_buffer_rsrc_t yrs = __builtin_amdgcn_make_buffer_rsrc(y2T, 0, DM * SEQ * 2, 0x00020000);
            const unsigned yo = (unsigned)(d * 4096 + (q4 & 1) * 512 + (l15 >> 3) * 256 + (q4 >> 1) * 128 + (l15 & 7) * 16);
#pragma unroll
            for (int g = 0; g < 8; g += 2) {
                const auto r0 = __builtin_amdgcn_permlane16_swap(pkbf(cv[g][0], cv[g][1]), pkbf(cv[g + 1][0], cv[g + 1][1]), false, false);
                const auto r1 = __builtin_amdgcn_permlane16_swap(pkbf(cv[g][2], cv[g][3]), pkbf(cv[g + 1][2], cv[g + 1][3]), false, false);
                __builtin_amdgcn_raw_buffer_store_b128((u32x4){r0[0], r1[0], r0[1], r1[1]}, yrs, yo + (unsigned)(g * 512), 0, WT);
            }
        }
        asm volatile("" ::: "memory");
        STAMPT(dbg, 15, 384);
        __builtin_amdgcn_s_setprio(PRIO2_L);
    }
    {
        const int lane = fresh_lane(), l15 = lane & 15, q4 = lane >> 4;
        const int ring = wid >> 2;
        const unsigned va = (unsigned)((lane ^ (ring * 2)) * 16), vh = va;
        const unsigned tag = lds_word(smem, TAG_OFF);
        const int cb0 = ring ? KC_OFF : GA_OFF, cst = ring ? CV_W : 8192;
        f32x4 ac[2][4];
        unsigned ca[2], fa[2], fl = 0, fv = 0;
#define CHAIN(u) do { const int c_ = (wid - ((u) >> 1)) & 3; ca[(u) & 1] = va + (unsigned)(cb0 + c_ * cst + ((u) & 1) * 4096); \
                      fa[(u) & 1] = (unsigned)(RF_OFF + ring * 32 + ((u) & 1) * 16 + c_ * 4); asm volatile("" : "+v"(ca[(u) & 1]), "+v"(fa[(u) & 1])); } while (0)
#define RDIN(dst, a, f_) asm volatile("ds_read_b32 %0, %5\n\tds_read_b128 %1, %6\n\tds_read_b128 %2, %6 offset:1024\n\tds_read_b128 %3, %6 offset:2048\n\tds_read_b128 %4, %6 offset:3072" \
                     : "=&v"(fl), "=&v"(dst[0]), "=&v"(dst[1]), "=&v"(dst[2]), "=&v"(dst[3]) : "v"(f_), "v"(a) : "memory")
#define LWAIT(dst) asm volatile("s_waitcnt lgkmcnt(0)" : "+v"(fl), "+v"(dst[0]), "+v"(dst[1]), "+v"(dst[2]), "+v"(dst[3]) :: "memory")
        CHAIN(0); CHAIN(1);
        WAIT_LGKM0();
#pragma unroll
        for (int k = 0; k < 6; ++k) CPIN(k);
        CREAD(3, cra, crb, 0);
        int tr[2] = {TR(0), TR(1)};
        unsigned gw[2][2];
        {
            unsigned spins = 0;
            while (__builtin_expect(!__all((gv[0].y == tag) & (gv[0].w == tag) & (gv[1].y == tag) & (gv[1].w == tag)), 0)) {
                if (++spins > SPIN_MAX) break;
#pragma unroll
                for (int r = 0; r < 2; ++r) asm volatile("s_nop 4\n\tbuffer_load_dwordx4 %0, %1, %2, %3 offen sc1\n\ts_waitcnt vmcnt(0)" : "=&v"(gv[r]) : "v"(GV(q4)), "s"(gs), "s"(__builtin_amdgcn_readfirstlane(GSO(r))) : "memory");
            }
            gw[0][0] = gv[0].x; gw[0][1] = gv[0].z; gw[1][0] = gv[1].x; gw[1][1] = gv[1].z;
            PIN4(gw[0][0], gw[0][1], gw[1][0], gw[1][1]);
        }
        unsigned cpv = 0, yy[2][4];
        f32x2 zz[2][4];
        unsigned exo = 0, exi = 0, efo = 0, efi = 0;
        auto send = [&](int r) __attribute__((always_inline)) {
            const unsigned one = 1u;
#pragma unroll
            for (int n = 0; n < 4; ++n) { const f32x2 v = {ac[r][n][2], ac[r][n][3]}; asm volatile("ds_write_b64 %0, %1 offset:%2" :: "v"(exo), "v"(v), "i"(r * 4096 + n * 512) : "memory"); }
            asm volatile("ds_write_b32 %0, %1 offset:%2" :: "v"(efo), "v"(one), "i"(r * 4) : "memory");
        };
        STAMPT(dbg, 5, 0);
        auto body = [&](auto F) __attribute__((always_inline)) {
            constexpr int f = decltype(F)::value;
            constexpr int u = f >> 3, k = f & 7;
            f32x4* an = ac[u & 1];
            f32x4* ao = ac[(u + 1) & 1];
#pragma unroll
            for (int n = 0; n < 4; ++n) {
                if (k == 0 && u < 2) MF0(an[n], af[f % DEPTH], cf_[k][n]); else MF(an[n], af[f % DEPTH], cf_[k][n]);
                SCHED();
                const int g = k * 4 + n;
                if (u >= 1 && u < 7) {
                    if (g == 1) { fv = ((u - 1) >> 1) + 1; asm volatile("" : "+v"(fv)); }
                    if (g == 4 || g == 6 || g == 8 || g == 10) asm volatile("ds_write_b128 %0, %1 offset:%2" :: "v"(ca[(u + 1) & 1]), "v"(ao[(g - 4) / 2]), "i"((g - 4) * 512) : "memory");
                    if (g == 12) asm volatile("ds_write_b32 %0, %1" :: "v"(fa[(u + 1) & 1]), "v"(fv) : "memory");
                    if (g == 18) CHAIN(u + 1);
                    if (g == 20) asm volatile("ds_read_b32 %0, %1" : "=&v"(fl) : "v"(fa[(u + 1) & 1]) : "memory");
                    if (g > 20 && g <= 24) asm volatile("ds_read_b128 %0, %1 offset:%2" : "=&v"(ao[g - 21]) : "v"(ca[(u + 1) & 1]), "i"((g - 21) * 1024) : "memory");
                    if (g == 30) {
                        LWAIT(ao);
                        if (__builtin_expect(__builtin_amdgcn_readfirstlane(fl) != (unsigned)((u + 1) >> 1), 0)) {
                            unsigned spins = 0;
                            do {
                                __builtin_amdgcn_s_sleep(1);
                                RDIN(ao, ca[(u + 1) & 1], fa[(u + 1) & 1]); LWAIT(ao);
                                if (++spins > SPIN_MAX * 16u) break;
                            } while (__builtin_amdgcn_readfirstlane(fl) != (unsigned)((u + 1) >> 1));
                        }
                    }
                }
                if (u == 7 && g == 6) send(0);
                if (f >= ZF && f < ZF + 4) {
                    constexpr int r = ((f - ZF) >> 1) & 1, j = (f - ZF) & 1;
                    const unsigned xw = xx[r][j][n >> 1], dw = dk2[n >> 1];
                    const float xf = (n & 1) ? hi16(xw) : lo16(xw), df = (n & 1) ? hi16(dw) : lo16(dw), yf = j ? hi16(yy[r][n]) : lo16(yy[r][n]);
                    zz[r][n][j] = xf * fmaf(hi16(gw[r][j]), yf, lo16(gw[r][j]) * df);
                    asm volatile("" : "+v"(zz[r][n]));
                }
                if (f == ZF + 4) {
                    zz[0][n] = (f32x2){fmaf(lo16(gw[0][0]), ac[0][n][0], zz[0][n][0]), fmaf(lo16(gw[0][1]), ac[0][n][1], zz[0][n][1])};
                    asm volatile("" : "+v"(zz[0][n]));
                }
                SCHED();
            }
            if (f + DEPTH < 64) LOADF(f + DEPTH);
            if (f == DEPTH + 1 && ring) {
                WAIT_VM(6);
                if (lane == 0) __hip_atomic_store((gu32*)(ws + WS_CFLAG) + cb * 32 + (wid & 3), tag, RLX_AGENT);
                SCHED();
            }
            if (f == YPOLL && !ring) { const int l15 = fresh_lane() & 15; cpv = __hip_atomic_load((gu32*)(ws + WS_CFLAG) + (nb * 16 + (wid & 3) * 4 + (l15 >> 2)) * 32 + (l15 & 3), RLX_AGENT); SCHED(); }
            if (f == YPOLL + DEPTH && !ring) {
                const int ln = fresh_lane(), l15 = ln & 15, q4 = ln >> 4;
                if (__builtin_expect(!__all(cpv == tag), 0)) {
                    unsigned spins = 0;
                    while (!__all(__hip_atomic_load((gu32*)(ws + WS_CFLAG) + (nb * 16 + (wid & 3) * 4 + (l15 >> 2)) * 32 + (l15 & 3), RLX_AGENT) == tag)) {
                        if (++spins > SPIN_MAX) break;
                        __builtin_amdgcn_s_sleep(1);
                    }
                }
                wait_lds1(smem, RF_OFF + 32 + wid * 4);
                const unsigned lx = lbase + LA_OFF + (wid & 3) * 8192;
#pragma unroll
                for (int i = 0; i < 4; ++i) {
                    const unsigned vo = (unsigned)((d_base + (wid & 3) * 16 + i * 4 + q4) * 4096 + tb * 256 + ((l15 ^ ((wid & 3) * 4 + i)) * 16));
                    GLDS_SC1(y2T, vo, lx, i * 1024);
                }
                SCHED();
            }
            if (f == YPOLL + 2 * DEPTH + 1 && !ring) {
                WAIT_VM(6);
                SETW(YFLAG_OFF, wid & 3, 1u);
                SCHED();
            }
            if (f == 55) {
                const int ln = fresh_lane(), c_ = (wid - 3) & 3;
                const int eg = GA_OFF + c_ * 8192, el = KC_OFF + c_ * CV_W;
                exo = (unsigned)((ring ? el : eg) + ln * 8);
                exi = (unsigned)((ring ? eg : el) + ln * 8);
                efo = (unsigned)(EF_OFF + wid * 8); efi = (unsigned)(EF_OFF + (wid ^ 4) * 8);
                PIN4(exo, exi, efo, efi);
                SCHED();
            }
            if (f == ZF - 2) {
                const int ln = fresh_lane(), l15 = ln & 15, q4 = ln >> 4;
                wait_lds4(smem, YFLAG_OFF);
#pragma unroll
                for (int r = 0; r < 2; ++r) {
                    const int jb = JB(6 + r);
                    const int ya = LA_OFF + (l15 >> 2) * 8192 + (l15 & 3) * 1024 + (((((jb >> 1) & 1) * 8 + (jb >> 2) * 4 + q4) ^ l15) * 16) + (jb & 1) * 8 + ring * 4;
#pragma unroll
                    for (int e = 0; e < 4; ++e) yy[r][e] = *(const unsigned*)(smem + ya + e * 256);
                }
                SCHED();
            }
        };
        unroll<64>(body);
        PAD8(ac[1][0], ac[1][1], ac[1][2], ac[1][3]);
        send(1);
        STAMPT(dbg, 6, 0);
        const __amdgpu_buffer_rsrc_t ors = __builtin_amdgcn_make_buffer_rsrc(out, 0, SEQ * DM * 4, 0x00020000);
        const unsigned ov = (unsigned)((lane >> 4) * (16 * DM * 4) + (lane & 15) * 16);
#pragma unroll
        for (int r = 0; r < 2; ++r) {
            f32x2 in[4];
#define EXRD() asm volatile("ds_read_b32 %0, %5 offset:%7\n\tds_read_b64 %1, %6 offset:%8\n\tds_read_b64 %2, %6 offset:%9\n\tds_read_b64 %3, %6 offset:%10\n\tds_read_b64 %4, %6 offset:%11" \
                             : "=&v"(fl), "=&v"(in[0]), "=&v"(in[1]), "=&v"(in[2]), "=&v"(in[3]) \
                             : "v"(efi), "v"(exi), "i"(r * 4), "i"(r * 4096), "i"(r * 4096 + 512), "i"(r * 4096 + 1024), "i"(r * 4096 + 1536) : "memory")
            EXRD();
            if (r == 0) {
#pragma unroll
                for (int n = 0; n < 4; ++n) {
                    zz[1][n] = (f32x2){fmaf(lo16(gw[1][0]), ac[1][n][0], zz[1][n][0]), fmaf(lo16(gw[1][1]), ac[1][n][1], zz[1][n][1])};
                    asm volatile("" : "+v"(zz[1][n]));
                }
            }
            LWAIT(in);
            if (__builtin_expect(__builtin_amdgcn_readfirstlane(fl) == 0u, 0)) {
                unsigned spins = 0;
                do {
                    EXRD(); LWAIT(in);
                    if (++spins > SPIN_MAX * 16u) break;
                } while (__builtin_amdgcn_readfirstlane(fl) == 0u);
            }
            f32x4 o[2];
#pragma unroll
            for (int j = 0; j < 2; ++j)
#pragma unroll
                for (int e = 0; e < 4; ++e) o[j][e] = fmaf(lo16(gw[r][j]), in[e][j], zz[r][e][j]);
#pragma unroll
            for (int j = 0; j < 2; ++j)
                __builtin_amdgcn_raw_buffer_store_b128(__builtin_bit_cast(u32x4, o[j]), ors, ov + (unsigned)(((tr[r] + j) * DM + d_base) * 4), 0, WT);
        }
        STAMPT(dbg, 7, 0);
        STAMPT(dbg, 16, 384);
        if (b == 0 && wid == 0 && !bumped) {
            gu32* ap = (gu32*)(ws + WS_ARRIVE) + lane * 32;
            unsigned spins = 0;
            for (;;) {
                bool ok = true;
#pragma unroll
                for (int q = 0; q < 4; ++q) ok &= (__hip_atomic_load(ap + q * 64 * 32, RLX_AGENT) == tag);
                if (__all(ok)) break;
                if (++spins > SPIN_MAX) break;
                __builtin_amdgcn_s_sleep(1);
            }
            if (lane == 0) __hip_atomic_store((gu32*)(ws + WS_EPOCH), tag, RLX_AGENT);
        }
    }
}

extern "C" void kernel_launch(void* const* d_in, const int* in_sizes, int n_in,
                              void* d_out, int out_size, void* d_ws, size_t ws_size,
                              hipStream_t stream) {
    const u16* x = (const u16*)d_in[0];
    const u16* lre = (const u16*)d_in[1];
    const u16* lim = (const u16*)d_in[2];
    const u16* Bre = (const u16*)d_in[3];
    const u16* Bim = (const u16*)d_in[4];
    const u16* Cre = (const u16*)d_in[5];
    const u16* Cim = (const u16*)d_in[6];
    const u16* logdt = (const u16*)d_in[7];
    const u16* dskip = (const u16*)d_in[8];
    const u16* kin = (const u16*)d_in[10];
    const u16* vin = (const u16*)d_in[11];
    const u16* shiftk = (const u16*)d_in[12];
    const u16* ssmk = (const u16*)d_in[13];
    const u16* ssmD = (const u16*)d_in[14];
    const u16* gatew = (const u16*)d_in[15];
    float* out = (float*)d_out;

    char* ws = (char*)d_ws;

    hipFuncSetAttribute((const void*)k1_kernel, hipFuncAttributeMaxDynamicSharedMemorySize, K1_LDS);

    k1_kernel<<<256, 512, K1_LDS, stream>>>(x, Bre, Bim, kin, vin, shiftk, gatew, ws, lre, lim, logdt, ssmk, ssmD, Cre, Cim, dskip, out);
}
#if defined(__HIP_DEVICE_COMPILE__)
#pragma clang attribute pop
#endif
```

```cpp
#if defined(__HIP_DEVICE_COMPILE__)
#pragma clang attribute push(__attribute__((target("no-packed-fp32-ops"))), apply_to = function)
#endif
#include <hip/hip_runtime.h>
#include <hip/hip_bf16.h>
#include <stdint.h>

#define TIDX ((int)__builtin_amdgcn_workitem_id_x())
#define BIDX ((int)__builtin_amdgcn_workgroup_id_x())
typedef unsigned short u16;
typedef __attribute__((ext_vector_type(8))) short bf16x8;
typedef __attribute__((ext_vector_type(4))) float f32x4;
typedef __attribute__((ext_vector_type(2))) float f32x2;
typedef __attribute__((ext_vector_type(2))) __bf16 bf16x2;
typedef __attribute__((ext_vector_type(4))) unsigned u32x4;
typedef __attribute__((ext_vector_type(2))) unsigned u32x2;
typedef __attribute__((address_space(1))) unsigned long long gu64;
typedef __attribute__((address_space(1))) unsigned gu32;
typedef __attribute__((address_space(3))) void lds_void;
typedef __attribute__((address_space(3))) unsigned lds_u32;
typedef __attribute__((ext_vector_type(4))) short s16x4;
typedef __attribute__((address_space(3))) s16x4 lds_s16x4;
#define TRD(a) __builtin_amdgcn_ds_read_tr16_b64_v4i16((lds_s16x4*)(size_t)(a))

#define DBGON 0
#define STAMPT(buf, k, t) do { } while (0)
#define CODE_LINES 285
#define SEQ 2048
#define DM 1024
#define SPIN_MAX (1u << 18)
#define PRIO_G 2
#define PSLEEP 4
#define PRIO_L 3
#define WT 16

__device__ __forceinline__ float bf2f(u16 v) { return __uint_as_float(((unsigned)v) << 16); }
__device__ __forceinline__ u16 f2bf(float f) { __bf16 b = (__bf16)f; return __builtin_bit_cast(u16, b); }
__device__ __forceinline__ float rbf(float f) { return bf2f(f2bf(f)); }
__device__ __forceinline__ unsigned pkbf(float lo, float hi) { return __builtin_bit_cast(unsigned, __builtin_convertvector((f32x2){lo, hi}, bf16x2)); }
__device__ __forceinline__ float lo16(unsigned u) { return __uint_as_float(u << 16); }
__device__ __forceinline__ float hi16(unsigned u) { return __uint_as_float(u & 0xffff0000u); }

struct cf { float r, i; };
__device__ __forceinline__ cf cmul(cf a, cf b) { return {a.r * b.r - a.i * b.i, a.r * b.i + a.i * b.r}; }
__device__ __forceinline__ cf cfma(cf a, cf b, cf c) {
    return {fmaf(a.r, b.r, fmaf(-a.i, b.i, c.r)), fmaf(a.r, b.i, fmaf(a.i, b.r, c.i))};
}
__device__ __forceinline__ f32x4 splat4(float v) { return (f32x4){v, v, v, v}; }
__device__ __forceinline__ f32x4 fma4(f32x4 a, f32x4 b, f32x4 c) { return __builtin_elementwise_fma(a, b, c); }
__device__ __forceinline__ f32x4 mul4(f32x4 a, f32x4 b) { return a * b; }

__device__ __forceinline__ float red32(float a, float b) {
    const auto r = __builtin_amdgcn_permlane32_swap(__float_as_uint(a), __float_as_uint(b), false, false);
    return __uint_as_float(r[0]) + __uint_as_float(r[1]);
}
__device__ __forceinline__ float red16(float x) {
    const auto r = __builtin_amdgcn_permlane16_swap(__float_as_uint(x), __float_as_uint(x), false, false);
    return __uint_as_float(r[0]) + __uint_as_float(r[1]);
}
__device__ __forceinline__ int fresh_lane() {
    int l;
    asm volatile("v_mbcnt_lo_u32_b32 %0, -1, 0\n\tv_mbcnt_hi_u32_b32 %0, -1, %0" : "=v"(l));
    return l;
}
#define BAR() __builtin_amdgcn_s_barrier()
#define WAIT_VM(n) asm volatile("s_waitcnt vmcnt(" #n ")" ::: "memory")
#define WAIT_LGKM0() __builtin_amdgcn_s_waitcnt(0xC07F)
#define SCHED() __builtin_amdgcn_sched_barrier(0)
#define MF(c, a, b) asm volatile("v_mfma_f32_16x16x32_bf16 %0, %1, %2, %0" : "+v"(c) : "v"(a), "v"(b))
#define PAD8(c0, c1, c2, c3) asm volatile("s_nop 7" : "+v"(c0), "+v"(c1), "+v"(c2), "+v"(c3))
#define PIN4(c0, c1, c2, c3) asm volatile("" : "+v"(c0), "+v"(c1), "+v"(c2), "+v"(c3))
#define GLDS_S(sbase, voff, sl, IMM) \
    asm volatile("s_add_u32 m0, %2, %3\n\ts_nop 0\n\tglobal_load_lds_dwordx4 %0, %1" \
                 :: "v"(voff), "s"(sbase), "s"(sl), "i"(IMM) : "memory", "scc")
#define GLDS_K(sbase, voff, sl, IMM) \
    asm volatile("s_add_u32 m0, %2, %3\n\ts_nop 0\n\tglobal_load_lds_dwordx4 %0, %1 nt" \
                 :: "v"(voff), "s"(sbase), "s"(sl), "i"(IMM) : "memory", "scc")
#define RLX_AGENT __ATOMIC_RELAXED, __HIP_MEMORY_SCOPE_AGENT

#define UNITS_A 0x0756075607560560ull
#define UNITS_B 0x1432143214321432ull
#define RA(t) ((int)((UNITS_A >> (4 * (t))) & 15) * 16384)
#define RB(t) ((int)((UNITS_B >> (4 * (t))) & 15) * 16384)
#define LA_OFF 0
#define GA_OFF 32768
#define CV_COPY 4160
#define CV_W (4 * CV_COPY)
#define KC_OFF 65536
#define ST_OFF 114688
#define ST_V 8832
#define ST_SZ (ST_V + 8 * 1056)
#define U_OFF (KC_OFF + 16 * CV_COPY)
#define HT_OFF (U_OFF + 256 + 16384)
#define XCH_OFF (HT_OFF + 8192)
#define GW_OFF (XCH_OFF + 2048)
#define TAG_OFF (GW_OFF + 2048)
#define UFLAG_OFF (TAG_OFF + 16)
#define G1D_OFF (UFLAG_OFF + 16)
#define MISC_OFF (G1D_OFF + 16)
#define YFLAG_OFF (MISC_OFF + 16)
#define RF_OFF (YFLAG_OFF + 16)
#define EF_OFF (RF_OFF + 64)
#define COK_OFF (EF_OFF + 64)
#define NFLAGW 52
#define DUMMY_OFF (COK_OFF + 16)
#define K1_LDS (DUMMY_OFF + 256)
#define WS_GRAN 0
#define WS_KVFLAG (256u << 10)
#define WS_ARRIVE (WS_KVFLAG + 32768)
#define WS_EPOCH (WS_ARRIVE + 32768)
#define WS_CFLAG (WS_EPOCH + 4096)
#define WS_HFLAG (384u << 10)
#define WS_GATE (512u << 10)

#define RD128(a, off) (*(const __attribute__((address_space(3))) bf16x8*)(size_t)(unsigned)((a) + (off)))
#define RDA(dst, A, F) do { const s16x4 lo_ = TRD((A) + 512 * (F) + 8); SCHED(); const s16x4 hi_ = TRD((A) + 512 * (F)); SCHED(); \
                            dst = __builtin_shufflevector(lo_, hi_, 0, 1, 2, 3, 4, 5, 6, 7); } while (0)
#define TOUCH(sbase, voff) asm volatile("s_mov_b32 m0, %2\n\ts_nop 0\n\tglobal_load_lds_dword %0, %1" :: "v"(voff), "s"(sbase), "i"(DUMMY_OFF) : "memory")
#define FLAGD(off) do { unsigned long long pc_; asm volatile("s_getpc_b64 %0\n\ts_mov_b32 m0, %2\n\ts_mov_b64 exec, 1\n\tglobal_load_lds_dword %1, %0\n\ts_mov_b64 exec, -1" \
                                                             : "=&s"(pc_) : "v"(0u), "s"(__builtin_amdgcn_readfirstlane(off)) : "memory"); } while (0)
#define GLDS_SC1(sbase, voff, sl, IMM) \
    asm volatile("s_add_u32 m0, %2, %3\n\ts_nop 0\n\tglobal_load_lds_dwordx4 %0, %1 sc1" \
                 :: "v"(voff), "s"(sbase), "s"(sl), "i"(IMM) : "memory", "scc")
__device__ __forceinline__ unsigned mv(unsigned v) { unsigned r; asm("v_mov_b32 %0, %1" : "=v"(r) : "v"(v)); return r; }
__device__ __forceinline__ unsigned al(unsigned hi, unsigned lo) { unsigned r; asm volatile("v_alignbit_b32 %0, %1, %2, 16" : "=v"(r) : "v"(hi), "v"(lo)); return r; }
__device__ __forceinline__ u32x2 pred2(const u32x4& a, unsigned f0, unsigned f1) {
    return (u32x2){(unsigned)__builtin_amdgcn_update_dpp((int)f0, (int)a.z, 0x138, 0xf, 0xf, false), (unsigned)__builtin_amdgcn_update_dpp((int)f1, (int)a.w, 0x138, 0xf, 0xf, false)};
}
#define RL63(v) ((unsigned)__builtin_amdgcn_readlane((int)(v), 63))
__device__ __forceinline__ void wait_lds4(char* smem, int off) {
    const unsigned a = (unsigned)(size_t)(lds_void*)smem + off;
    unsigned spins = 0;
    for (;;) {
        u32x4 f;
        asm volatile("ds_read_b128 %0, %1\n\ts_waitcnt lgkmcnt(0)" : "=&v"(f) : "v"(a) : "memory");
        if (__builtin_amdgcn_readfirstlane((f.x != 0u) & (f.y != 0u) & (f.z != 0u) & (f.w != 0u))) break;
        if (++spins > SPIN_MAX * 16u) break;
        __builtin_amdgcn_s_sleep(1);
    }
}
__device__ __forceinline__ unsigned lds_word(char* smem, int off) {
    const unsigned a = (unsigned)(size_t)(lds_void*)smem + off;
    unsigned f;
    asm volatile("ds_read_b32 %0, %1\n\ts_waitcnt lgkmcnt(0)" : "=&v"(f) : "v"(a) : "memory");
    return __builtin_amdgcn_readfirstlane(f);
}
__device__ __forceinline__ void wait_lds1(char* smem, int off) {
    unsigned spins = 0;
    while (lds_word(smem, off) == 0u) {
        if (++spins > SPIN_MAX * 16u) break;
        __builtin_amdgcn_s_sleep(1);
    }
}
#define SETW(off, i, v) __hip_atomic_store((lds_u32*)(smem + (off)) + (i), (v), __ATOMIC_RELAXED, __HIP_MEMORY_SCOPE_WORKGROUP)

#define PRIO_CV 1
#define PRIO_KV 3
#define CS0 1
#define CS1 3
#define CS2 5
#define XE 4
#define YPOLL 32
#define ZF 59
template <int I> struct IC { static constexpr int value = I; };
template <int N, int I = 0, class F> __device__ __forceinline__ void unroll(F& f) { if constexpr (I < N) { f(IC<I>{}); unroll<N, I + 1>(f); } }
#define MF0(c, a, b) asm volatile("v_mfma_f32_16x16x32_bf16 %0, %1, %2, 0" : "=&v"(c) : "v"(a), "v"(b))
#define DEPTH 6
#define PRIO2_G 0
#define PRIO2_L 1
__device__ __forceinline__ void both(cf x, cf* o) {
    const auto r = __builtin_amdgcn_permlane32_swap(__float_as_uint(x.r), __float_as_uint(x.r), false, false);
    const auto i = __builtin_amdgcn_permlane32_swap(__float_as_uint(x.i), __float_as_uint(x.i), false, false);
    o[0] = {__uint_as_float(r[0]), __uint_as_float(i[0])};
    o[1] = {__uint_as_float(r[1]), __uint_as_float(i[1])};
}
__device__ __forceinline__ cf pick4(int q, cf a1, cf a2, cf a3) {
    return {q == 0 ? 1.f : q == 1 ? a1.r : q == 2 ? a2.r : a3.r, q == 0 ? 0.f : q == 1 ? a1.i : q == 2 ? a2.i : a3.i};
}
#define PINC(c) asm volatile("" : "+v"((c).r), "+v"((c).i))

extern "C" __global__ __launch_bounds__(512, 2) void k1_kernel(
        const u16* __restrict__ x, const u16* __restrict__ Bre, const u16* __restrict__ Bim,
        const u16* __restrict__ kin, const u16* __restrict__ vin, const u16* __restrict__ shiftk, const u16* __restrict__ gatew, char* ws,
        const u16* __restrict__ lre, const u16* __restrict__ lim, const u16* __restrict__ logdt,
        const u16* __restrict__ ssmk, const u16* __restrict__ ssmD,
        const u16* __restrict__ Cre, const u16* __restrict__ Cim, const u16* __restrict__ dskip, float* __restrict__ out) {
    extern __shared__ __attribute__((aligned(16))) char smem[];
#define h ((u16*)(ws + (1u << 20)))
#define kvT ((u16*)(ws + (9u << 20)))
#define y2T ((u16*)(ws + (13u << 20)))
#define dbg ((unsigned long long*)(ws + (64u << 20)))
    const int tid = TIDX;
    if (tid < 40 && tid + ((BIDX >> 3) & 7) * 40 < CODE_LINES) {
        const unsigned vo = (unsigned)(tid + ((BIDX >> 3) & 7) * 40) * 128u;
        asm volatile("s_getpc_b64 s[98:99]\n\ts_add_u32 s98, s98, k1_kernel@rel32@lo+4\n\ts_addc_u32 s99, s99, k1_kernel@rel32@hi+12\n\t"
                     "s_mov_b32 m0, %1\n\ts_nop 0\n\tglobal_load_lds_dword %0, s[98:99]"
                     :: "v"(vo), "i"(DUMMY_OFF) : "s98", "s99", "scc", "memory");
    }
    const int lane = tid & 63;
    int wid = __builtin_amdgcn_readfirstlane(tid >> 6);
    const int b = BIDX;
    const int xcd = b & 7, idx = b >> 3;
    const int tb = ((xcd >> 1) ^ (xcd >> 2)) * 4 + (idx & 3), nb = (xcd & 1) * 8 + (idx >> 2);
    const int cb = xcd * 32 + idx;
    const int t_base = tb * 128, p_base = nb * 64, d_base = nb * 64;
    const int l15 = lane & 15, q4 = lane >> 4;
    unsigned stv = 0, sth = 0;
    STAMPT(dbg, 0, 0);
    const unsigned lbase = (unsigned)(size_t)(lds_void*)smem;
    const int rot1 = (xcd >> 1) * 4 + (xcd & 1) * 8 + (idx >> 2);
    unsigned voA[4] = {0u, 0u, 0u, 0u}, voB[2] = {0u, 0u}, lw0 = 0;
    if (wid == 4 || wid == 5) {
        __builtin_amdgcn_s_setprio(PRIO_L);
        const int lh = wid - 4;
        const unsigned ch = (unsigned)(((lane & 7) ^ (lane >> 4)) * 16);
        voA[0] = (unsigned)(t_base + lh * 64 + (lane >> 5) * 16 + ((lane >> 3) & 3)) * 2048u + ch;
        voA[1] = (voA[0] ^ 64u) + (65536u - 1024u);
        voA[2] = voA[0] + 6144u; voA[3] = voA[1] + 6144u;
        voB[0] = (unsigned)(p_base + lh * 32 + (lane >> 3) * 2) * 2048u + ch;
        voB[1] = (voB[0] ^ 64u) + (32768u - 1024u);
        PIN4(voA[0], voA[1], voA[2], voA[3]); asm volatile("" : "+v"(voB[0]), "+v"(voB[1]));
#define GLDS4(sbase, v0, v1, v2, v3, sl, IMM) \
            asm volatile("s_add_u32 m0, %5, %6\n\ts_nop 0\n\tglobal_load_lds_dwordx4 %0, %4\n\tglobal_load_lds_dwordx4 %1, %4 offset:1024\n\t" \
                         "global_load_lds_dwordx4 %2, %4 offset:2048\n\tglobal_load_lds_dwordx4 %3, %4 offset:3072" \
                         :: "v"(v0), "v"(v1), "v"(v2), "v"(v3), "s"(sbase), "s"(sl), "i"(IMM) : "memory", "scc")
#define STAGE_A(kt, sl) do { const char* sa_ = (const char*)x + (((kt) + rot1) & 15) * 128; \
            GLDS4(sa_, voA[0], voA[1], voA[2], voA[3], sl, 0); GLDS4(sa_ + 16384, voA[0], voA[1], voA[2], voA[3], sl, 4096); } while (0)
#define STAGE_B(kt, sl) do { const int kr_ = (((kt) + rot1) & 15) * 128; const char* sb_ = (const char*)Bre + kr_; const char* sc_ = (const char*)Bim + kr_; \
            GLDS4(sb_, voB[0], voB[1], voB[0], voB[1], sl, 0); GLDS4(sc_, voB[0], voB[1], voB[0], voB[1], sl, 4096); } while (0)
        lw0 = lbase + lh * 8192;
        STAGE_A(0, lw0 + RA(0)); STAGE_B(0, lw0 + RB(0)); STAGE_A(1, lw0 + RA(1)); STAGE_B(1, lw0 + RB(1));
    }
    const int sblk = b >> 3, dblk = b & 7;
    u32x2 tw[4];
    unsigned epoch;
    asm volatile("" : "=v"(tw[0]), "=v"(tw[1]), "=v"(tw[2]), "=v"(tw[3]), "=v"(epoch));
    if (wid >= 6) {
        __builtin_amdgcn_s_setprio(PRIO_L);
        const int vw = wid - 6;
        const int dch = dblk * 128 + vw * 64 + l15;
        const unsigned lk = lbase + ST_OFF + vw * ST_SZ;
        const unsigned vk = (unsigned)((lane >> 3) * DM * 2 + (((lane & 7) ^ ((lane >> 4) & 1)) * 16));
        const char* kb = (const char*)kin + (size_t)(sblk * 64 - 4) * (DM * 2) + dblk * 256 + vw * 128;
#define KLD(n) GLDS_K(kb + (n) * 8 * DM * 2, vk, lk, (n) * 1024 + ((n) >> 1) * 32)
#define VLD(n) GLDS_K(vb + (((n) >> 1) * 16 + ((n) & 1) * 2) * DM * 2, vv_, lk, ST_V + (n) * 1056)
        if (vw == 1) {
            const unsigned vg = (unsigned)((lane * 16 + rot1 * 128) & 2047), lg = lbase + GW_OFF;
            GLDS_S(gatew, vg, lg, 0); GLDS_S(gatew, vg ^ 1024u, lg, 1024);
        }
        asm volatile("global_load_dwordx2 %0, %5, %7\n\tglobal_load_dwordx2 %1, %5, %7 offset:128\n\tglobal_load_dwordx2 %2, %5, %7 offset:256\n\t"
                     "global_load_dwordx2 %3, %5, %7 offset:384\n\tglobal_load_dword %4, %6, %8 sc1"
                     : "+v"(tw[0]), "+v"(tw[1]), "+v"(tw[2]), "+v"(tw[3]), "+v"(epoch) : "v"((unsigned)(dch * 8)), "v"((unsigned)WS_EPOCH), "s"(shiftk), "s"(ws) : "memory");
        if (sblk == 0 && lane < 32) *(u32x4*)(smem + ST_OFF + vw * ST_SZ + lane * 16) = (u32x4){0u, 0u, 0u, 0u};
        if (sblk != 0 || lane >= 32) KLD(0);
        KLD(1); KLD(2); KLD(3); KLD(4); KLD(5); KLD(6); KLD(7);
        if (lane < 32) KLD(8);
        __builtin_amdgcn_s_setprio(0);
    }
    asm volatile("" : "+s"(wid));

#define WSL(w) ((((w) >> 2) ^ (nb >> 3)) * 4 + ((w) & 3))
#define CSRC(w, ti) ((const char*)(((ti) & 1) ? Cim : Cre) + (2 * WSL(w) + ((ti) >> 1)) * 128)
    bf16x8 cf_[8][4];
    bf16x8 af[DEPTH];
    bool bumped = false;
    int cra, crb;
#define CT_SETUP() const unsigned vB0 = (unsigned)((d_base + (lane >> 3) * 4) * 2048 + (((lane & 7) ^ (lane >> 4)) * 16)), vB1 = (vB0 ^ 64u) + 65536u - 1024u
#define CTILE(w, ti, dst) do { const char* sb_ = CSRC(w, ti); const unsigned ds_ = (dst); \
                               GLDS4(sb_, vB0, vB1, vB0, vB1, ds_, 0); GLDS4(sb_ + 4096, vB0, vB1, vB0, vB1, ds_, 4096); } while (0)
#define CREAD(ti, a0_, a1_, off) do { _Pragma("unroll") for (int n_ = 0; n_ < 4; ++n_) { \
                               cf_[2 * (ti)][n_] = RD128(a0_, (off) + n_ * 2048); cf_[2 * (ti) + 1][n_] = RD128(a1_, (off) + n_ * 2048); } } while (0)
#define CPIN(k) PIN4(cf_[k][0], cf_[k][1], cf_[k][2], cf_[k][3])
    const int rot2 = (idx & 16) >> 2;
#define JB(u) (((((u) ^ (rot2 >> 2)) & 1) << 2) | ((wid - ((u) >> 1) + rot2) & 3))
    const __amdgpu_buffer_rsrc_t hs = __builtin_amdgcn_make_buffer_rsrc((char*)h + (size_t)tb * 524288 + WSL(wid) * 8192, 0, 524288, 0x00020000);
#define LOADF(f) do { af[(f) % DEPTH] = __builtin_bit_cast(bf16x8, __builtin_amdgcn_raw_buffer_load_b128(hs, vh + ((f) & 3) * 1024, JB((f) >> 3) * 65536 + ((f) & 4) * 1024, 16)); SCHED(); } while (0)
#define HPOLL_SETUP() gu32* hfp = (gu32*)(ws + WS_HFLAG) + (tb * 16 + 2 * WSL(wid) + (lane & 1)) * 32
#define HWAIT() do { unsigned spins = 0; \
            while (!__all(__hip_atomic_load(hfp, RLX_AGENT) == tag)) { if (++spins > SPIN_MAX) break; __builtin_amdgcn_s_sleep(PSLEEP); } asm volatile("" ::: "memory"); } while (0)

    const __amdgpu_buffer_rsrc_t gs = __builtin_amdgcn_make_buffer_rsrc(ws + WS_GATE, 0, 32768, 0x00020000);
#define GV(q4) ((unsigned)(((q4) >> 1) * 128 + ((q4) & 1) * 32))
#define GSO(r) (tb * 2048 + JB(6 + (r)) * 256 + (wid >> 2) * 16)
#define TR(r) (t_base + (JB(6 + (r)) >> 2) * 64 + (JB(6 + (r)) & 3) * 4 + (wid >> 2) * 2)
    u32x2 xx[2][2], dk2;
    u32x4 gv[2];
#define XLOAD() do { \
        const __amdgpu_buffer_rsrc_t xs = __builtin_amdgcn_make_buffer_rsrc((void*)x, 0, SEQ * DM * 2, 0x00020000); \
        const __amdgpu_buffer_rsrc_t ks = __builtin_amdgcn_make_buffer_rsrc((void*)dskip, 0, DM * 2, 0x00020000); \
        dk2 = __builtin_amdgcn_raw_buffer_load_b64(ks, (unsigned)(l15 * 8), d_base * 2, 0); \
        _Pragma("unroll") for (int q = 0; q < 4; ++q) \
            xx[q >> 1][q & 1] = __builtin_amdgcn_raw_buffer_load_b64(xs, (unsigned)(q4 * 16 * DM * 2 + l15 * 8 + (q & 1) * DM * 2), (TR(q >> 1) * DM + d_base) * 2, 0); \
        _Pragma("unroll") for (int r = 0; r < 2; ++r) gv[r] = __builtin_amdgcn_raw_buffer_load_b128(gs, GV(q4), GSO(r), 16); \
        SCHED(); } while (0)
#define XPIN() do { PIN4(xx[0][0], xx[0][1], xx[1][0], xx[1][1]); asm volatile("" : "+v"(dk2), "+v"(gv[0]), "+v"(gv[1])); } while (0)

    if (wid < 4) {
        const int wr = wid >> 1, wc = wid & 1;
        typedef __attribute__((ext_vector_type(16))) unsigned u32x16;
        typedef const __attribute__((address_space(4))) u32x16 cu16;
        const int po = (p_base + wc * 32) * 2;
        u32x16 s0 = *(cu16*)((size_t)logdt + po), s1 = *(cu16*)((size_t)lre + po), s2 = *(cu16*)((size_t)lim + po);
        SCHED();
        const int pq = p_base + wc * 32 + (q4 >> 1) * 16 + l15;
        unsigned goff[8], gmask[8];
#pragma unroll
        for (int i = 0; i < 8; ++i) {
            goff[i] = (unsigned)(((tb - 1 - (q4 & 1) - 2 * i) * 1024 + pq) * 16);
            gmask[i] = (tb - 1 - (q4 & 1) - 2 * i >= 0) ? ~0u : 0u;
            asm volatile("" : "+v"(goff[i]), "+v"(gmask[i]));
        }
        unsigned gpub = (unsigned)((tb * 1024 + pq) * 16), xch = lbase + XCH_OFF + wc * 1024 + lane * 16;
        unsigned hp = (unsigned)(tb * 524288 + wr * 262144 + (nb * 4 + wc) * 1024 + lane * 16);
        unsigned wa = (unsigned)(HT_OFF + wid * 2048 + (q4 * 32 + (l15 ^ ((q4 & 1) * 2 + (q4 >> 1) * 8))) * 8);
        unsigned ra0 = (unsigned)(HT_OFF + wid * 2048 + ((l15 & 3) * 32 + ((l15 >> 2) & 1) * 16 + ((4 * q4 + (l15 >> 3)) ^ ((l15 & 1) * 2 + ((l15 >> 1) & 1) * 8))) * 8), ra1 = ra0 ^ 16u;
        cra = GA_OFF + wid * 8192 + l15 * 128 + ((q4 ^ ((l15 >> 1) & 7)) * 16); crb = cra ^ 64;
        unsigned vB0 = (unsigned)((d_base + (lane >> 3) * 4) * 2048 + (((lane & 7) ^ (lane >> 4)) * 16)), vB1 = (vB0 ^ 64u) + 65536u - 1024u;
        PIN4(gpub, xch, hp, wa); PIN4(ra0, ra1, cra, crb); asm volatile("" : "+v"(vB0), "+v"(vB1));
        f32x4 acc[4][4];
#pragma unroll
        for (int m = 0; m < 4; ++m) {
#pragma unroll
            for (int c = 0; c < 4; ++c) acc[m][c] = (f32x4){0.f, 0.f, 0.f, 0.f};
            PIN4(acc[m][0], acc[m][1], acc[m][2], acc[m][3]);
        }
        SCHED();
        cf L1[2], Lq3[2], cc[2], l16[2], cLq[2], L64s, Lp, L256;
        {
            unsigned w0 = 0, w1 = 0, w2 = 0;
#pragma unroll
            for (int i = 0; i < 16; ++i) {
                asm("v_writelane_b32 %0, %3, %6\n\tv_writelane_b32 %1, %4, %6\n\tv_writelane_b32 %2, %5, %6" : "+v"(w0), "+v"(w1), "+v"(w2) : "s"(s0[i]), "s"(s1[i]), "s"(s2[i]), "i"(i));
            }
            asm("s_nop 1" : "+v"(w0), "+v"(w1), "+v"(w2));
            auto spread = [&](unsigned w) {
                const unsigned a = __builtin_amdgcn_permlane16_swap(w, w, false, false)[0];
                const unsigned c = __builtin_amdgcn_permlane32_swap(a, a, false, false)[0];
                return (q4 & 2) ? hi16(c) : lo16(c);
            };
            const float ldt = spread(w0), re = spread(w1), im = spread(w2);
            if (wr == 0) *(u32x4*)(smem + XCH_OFF + wc * 1024 + lane * 16) = (u32x4){0u, 0u, 0u, 0u};
            if (wid == 3 && lane < NFLAGW) *(unsigned*)(smem + UFLAG_OFF + lane * 4) = 0u;
            const float dt = rbf(expf(ldt));
            const float ea = expf(dt * re);
            float sn, cs;
            sincosf(dt * im, &sn, &cs);
            const cf T1 = {ea * cs, ea * sn};
            const float nr = T1.r - 1.0f, ni = T1.i, den = re * re + im * im;
            const cf c2 = {2.f * ((nr * re + ni * im) / den), 2.f * ((ni * re - nr * im) / den)};
            const cf T2 = cmul(T1, T1), T4 = cmul(T2, T2), L8 = cmul(T4, T4);
            const cf T16 = cmul(L8, L8), L32 = cmul(T16, T16), L48 = cmul(L32, T16);
            L64s = cmul(L32, L32);
            const cf L128 = cmul(L64s, L64s);
            L256 = cmul(L128, L128);
            Lp = {(q4 & 1) ? L128.r : 1.f, (q4 & 1) ? L128.i : 0.f};
            cf l32[2], l48[2];
            both(T1, L1); both(c2, cc); both(T16, l16); both(L32, l32); both(L48, l48);
#pragma unroll
            for (int sub = 0; sub < 2; ++sub) {
                Lq3[sub] = pick4(3 - q4, l16[sub], l32[sub], l48[sub]);
                cLq[sub] = cmul(cc[sub], pick4(q4, l16[sub], l32[sub], l48[sub]));
                PINC(L1[sub]); PINC(Lq3[sub]); PINC(cc[sub]); PINC(l16[sub]); PINC(cLq[sub]);
            }
            PINC(L64s); PINC(Lp); PINC(L256);
        }
        __builtin_amdgcn_s_setprio(PRIO_G);
        unsigned long long c_a = 0, r_a = 0;
        unsigned tagv;
        cf S[2];
        STAMPT(dbg, 22, 0);
        const int swz = (l15 >> 1) & 7;
        {
            const int a0 = RA(0) + (wr * 64 + l15) * 128 + ((q4 ^ swz) * 16);
            const int b0 = RB(0) + (wc * 64 + l15) * 128 + ((q4 ^ swz) * 16);
            WAIT_LGKM0();
            BAR();
            asm volatile("" ::: "memory");
            STAMPT(dbg, 19, 64);
            bf16x8 FA[2][4], FB[2][4];
            int pa[2] = {a0, a0 ^ 64}, pb[2] = {b0, b0 ^ 64};
            PIN4(pa[0], pa[1], pb[0], pb[1]);
#define LGW(n) __builtin_amdgcn_s_waitcnt(0xC07F | ((n) << 8))
#define RA_(j, mq) FA[((j) ^ ((j) >> 1)) & 1][(mq) + ((j) >> 1)] = RD128(pa[((j) ^ ((j) >> 1)) & 1], ((mq) + ((j) >> 1)) * 2048)
#define RB_(j, nq) FB[(j) & 1][(nq) + ((j) >> 1)] = RD128(pb[(j) & 1], ((nq) + ((j) >> 1)) * 2048)
#define G1_Q(Q, RD, FILL) do { \
            _Pragma("unroll") for (int i = 0; i < 8; ++i) { \
                const int p = i >> 1, dn = p >> 1, dm = (p ^ dn) & 1, ks = (i ^ p) & 1, m = ((Q) >> 1) * 2 + dm, n = ((Q) & 1) * 2 + dn; \
                if ((Q) == 0) { if (i < 2) LGW(3); if (i == 4) LGW(4); } \
                if ((Q) == 1) { if (i == 0) LGW(7); if (i == 1) LGW(6); if (i == 4) LGW(4); } \
                SCHED(); \
                MF(acc[m][n], FA[ks][m], FB[ks][n]); SCHED(); \
                if ((Q) == 0) { if (i < 4) RB_(i, 2); else RA_(i - 4, 2); } \
                if ((Q) == 2 && (RD) && i < 4) RA_(i, 0); \
                if ((Q) == 3 && (RD) && i < 4) RB_(i, 0); \
                FILL(((Q) - 2) * 8 + i); \
                SCHED(); \
            } } while (0)
#define NOF(g) do { } while (0)
#pragma unroll
            for (int j = 0; j < 4; ++j) { RA_(j, 0); SCHED(); }
#pragma unroll
            for (int j = 0; j < 4; ++j) { RB_(j, 0); SCHED(); }
            unsigned long long ua_ = UNITS_A;
            int sb = RB(0), sa = RA(0);
#pragma unroll 1
            for (int t3 = 0; t3 < 5; ++t3) {
#pragma unroll
                for (int t = 0; t < 3; ++t) {
                    int nx, na;
#define ADV(g) do { if ((g) == -8) { ua_ >>= 4; na = (int)((unsigned)ua_ & 15u) << 14; asm volatile("" : "+s"(na), "+s"(ua_)); } \
                    if ((g) == -7) { const int o_ = sa; sa = na; na -= o_; asm volatile("" : "+s"(na), "+s"(sa)); } \
                    if ((g) == -6) { pa[0] += na; pa[1] += na; asm volatile("" : "+v"(pa[0]), "+v"(pa[1])); } \
                    if ((g) == -5) { nx = (sb == 65536) ? -49152 : 16384; sb += nx; asm volatile("" : "+s"(nx), "+s"(sb)); } \
                    if ((g) == -3) { pb[0] += nx; pb[1] += nx; asm volatile("" : "+v"(pb[0]), "+v"(pb[1])); } } while (0)
                    G1_Q(0, true, NOF); G1_Q(1, true, ADV);
                    WAIT_LGKM0();
                    asm volatile("" ::: "memory");
                    BAR();
                    asm volatile("" ::: "memory");
                    G1_Q(2, true, NOF); G1_Q(3, true, NOF);
                }
            }
#define LASTF(g) do { if ((g) == -4) SETW(G1D_OFF, wid, 1u);        \
                      if ((g) == -3) tagv = *(volatile const lds_u32*)(size_t)(unsigned)TAG_OFF; } while (0)
            G1_Q(0, true, NOF);
            asm volatile("" ::: "memory");
            G1_Q(1, true, LASTF);
            asm volatile("s_nop 1");
#define AGG(g) do { if ((g) >= 2) { const int t = (g) >> 1, sub = (g) & 1; \
                        if (t == 1) { asm volatile("" : "+v"(acc[0][sub]), "+v"(acc[0][2 + sub])); S[sub] = {acc[0][sub][0], acc[0][2 + sub][0]}; } \
                        if (t == 4) asm volatile("" : "+v"(acc[1][sub]), "+v"(acc[1][2 + sub])); \
                        S[sub] = cfma(L1[sub], S[sub], cf{acc[t >> 2][sub][t & 3], acc[t >> 2][2 + sub][t & 3]}); PINC(S[sub]); } } while (0)
            G1_Q(2, false, AGG); G1_Q(3, false, AGG);
            PAD8(acc[2][0], acc[2][1], acc[2][2], acc[2][3]); PIN4(acc[3][0], acc[3][1], acc[3][2], acc[3][3]);
        }
        STAMPT(dbg, 1, 0);
        if (0 && TIDX == 128) { unsigned long long c_b = __builtin_amdgcn_s_memtime(), r_b = __builtin_amdgcn_s_memrealtime(); dbg[BIDX * 64 + 23] = (c_b - c_a) | (1ull << 62); dbg[BIDX * 64 + 24] = (r_b - r_a) | (1ull << 62); }
        cf ag0 = {0.f, 0.f};
        const unsigned tag = __builtin_amdgcn_readfirstlane(tagv);
        const __amdgpu_buffer_rsrc_t grs = __builtin_amdgcn_make_buffer_rsrc(ws, 0, 15 * 16384, 0x00020000);
        {
#pragma unroll
            for (int t = 8; t < 16; ++t)
#pragma unroll
                for (int sub = 0; sub < 2; ++sub) S[sub] = cfma(L1[sub], S[sub], cf{acc[t >> 2][sub][t & 3], acc[t >> 2][2 + sub][t & 3]});
            const cf W0 = cmul(S[0], Lq3[0]), W1 = cmul(S[1], Lq3[1]);
            const float er = red16(red32(W0.r, W1.r)), ei = red16(red32(W0.i, W1.i));
            if (wr == 0) {
                const u32x4 ev = {__float_as_uint(er), 1u, __float_as_uint(ei), 1u};
                asm volatile("ds_write_b128 %0, %1" :: "v"(xch), "v"(ev) : "memory");
            } else {
                u32x4 a0;
                unsigned spins = 0;
                for (;;) {
                    asm volatile("ds_read_b128 %0, %1\n\ts_waitcnt lgkmcnt(0)" : "=&v"(a0) : "v"(xch) : "memory");
                    if (__all(a0.y == 1u && a0.w == 1u)) break;
                    if (++spins > SPIN_MAX) break;
                }
                ag0 = {__uint_as_float(a0.x), __uint_as_float(a0.z)};
                const cf E = cfma(L64s, ag0, cf{er, ei});
                if ((q4 & 1) == 0) {
                    const u32x4 gv = {__float_as_uint(E.r), tag, __float_as_uint(E.i), tag};
                    __builtin_amdgcn_raw_buffer_store_b128(gv, grs, gpub, 0, 16);
                }
            }
        }
        SCHED();
        STAMPT(dbg, 2, 0);
        const unsigned cg = lbase + GA_OFF + wid * 8192;
        wait_lds1(smem, COK_OFF + wid * 4);
        CREAD(0, cra, crb, 0);
        WAIT_LGKM0();
        CPIN(0); CPIN(1);
        CTILE(wid, 1, cg);
#pragma unroll
        for (int sub = 0; sub < 2; ++sub)
#pragma unroll
            for (int m = 0; m < 4; ++m) {
                const f32x4 R = acc[m][sub], I = acc[m][2 + sub];
                acc[m][sub] = fma4(splat4(cc[sub].r), R, mul4(splat4(-cc[sub].i), I));
                acc[m][2 + sub] = fma4(splat4(-cc[sub].r), I, mul4(splat4(-cc[sub].i), R));
                asm volatile("" : "+v"(acc[m][sub]), "+v"(acc[m][2 + sub]));
            }
        u32x4 gq[8];
        SCHED();
#pragma unroll
        for (int i = 0; i < 8; ++i) asm volatile("buffer_load_dwordx4 %0, %1, %2, 0 offen sc1" : "=&v"(gq[i]) : "v"(goff[i]), "s"(grs) : "memory");
        SCHED();
        if (b == 0 && wid == 0) {
            bool ok = true;
#pragma unroll
            for (int q = 0; q < 4; ++q) ok &= (__hip_atomic_load((gu32*)(ws + WS_ARRIVE) + (lane + q * 64) * 32, RLX_AGENT) == tag);
            bumped = __all(ok);
            if (bumped && lane == 0) __hip_atomic_store((gu32*)(ws + WS_EPOCH), tag, RLX_AGENT);
        }
        cf Ec[2];
#pragma unroll
        for (int sub = 0; sub < 2; ++sub) {
            const auto sr = __builtin_amdgcn_permlane16_swap(__float_as_uint(S[sub].r), __float_as_uint(S[sub].r), false, false);
            const auto si = __builtin_amdgcn_permlane16_swap(__float_as_uint(S[sub].i), __float_as_uint(S[sub].i), false, false);
            const cf Se = {__uint_as_float(sr[0]), __uint_as_float(si[0])}, So = {__uint_as_float(sr[1]), __uint_as_float(si[1])};
            const cf A = cfma(l16[sub], Se, So);
            const auto ar = __builtin_amdgcn_permlane32_swap(__float_as_uint(A.r), __float_as_uint(A.r), false, false);
            const auto ai = __builtin_amdgcn_permlane32_swap(__float_as_uint(A.i), __float_as_uint(A.i), false, false);
            const cf A01 = {(q4 & 2) ? __uint_as_float(ar[0]) : 0.f, (q4 & 2) ? __uint_as_float(ai[0]) : 0.f};
            const cf kq = {(q4 & 1) ? l16[sub].r : 1.f, (q4 & 1) ? l16[sub].i : 0.f};
            const cf E = cfma(kq, A01, cf{(q4 & 1) ? Se.r : 0.f, (q4 & 1) ? Se.i : 0.f});
            Ec[sub] = cmul(cc[sub], E);
            PINC(Ec[sub]);
        }
        WAIT_VM(8);
        CREAD(1, cra, crb, 0);
        WAIT_LGKM0();
        CPIN(2); CPIN(3);
        CTILE(wid, 2, cg);
        asm volatile("s_waitcnt vmcnt(8)" : "+v"(gq[0]), "+v"(gq[1]), "+v"(gq[2]), "+v"(gq[3]), "+v"(gq[4]), "+v"(gq[5]), "+v"(gq[6]), "+v"(gq[7]) :: "memory");
        STAMPT(dbg, 27, 0);
        cf carry[2];
        {
            unsigned spins = 0;
            for (;;) {
                unsigned bad = 0;
#pragma unroll
                for (int i = 0; i < 8; ++i) bad |= ((gq[i].y ^ tag) | (gq[i].w ^ tag)) & gmask[i];
                if (__all(bad == 0u)) break;
                if (++spins > SPIN_MAX) break;
#pragma unroll
                for (int i = 0; i < 8; ++i) asm volatile("buffer_load_dwordx4 %0, %1, %2, 0 offen sc1" : "+v"(gq[i]) : "v"(goff[i]), "s"(grs) : "memory");
                asm volatile("s_waitcnt vmcnt(0)" : "+v"(gq[0]), "+v"(gq[1]), "+v"(gq[2]), "+v"(gq[3]), "+v"(gq[4]), "+v"(gq[5]), "+v"(gq[6]), "+v"(gq[7]) :: "memory");
            }
            cf E = {__uint_as_float(gq[7].x), __uint_as_float(gq[7].z)};
#pragma unroll
            for (int i = 6; i >= 0; --i) E = cfma(L256, E, cf{__uint_as_float(gq[i].x), __uint_as_float(gq[i].z)});
            E = cmul(E, Lp);
            cf Cy = {red16(E.r), red16(E.i)};
            if (wr == 1) Cy = cfma(L64s, Cy, ag0);
            both(Cy, carry);
        }
        STAMPT(dbg, 3, 0);
        {
            const __amdgpu_buffer_rsrc_t hrs = __builtin_amdgcn_make_buffer_rsrc(h, 0, SEQ * 2048 * 2, 0x00020000);
            float hr[2], hn[2];
#pragma unroll
            for (int sub = 0; sub < 2; ++sub) {
                const cf e = cfma(cLq[sub], carry[sub], Ec[sub]);
                hr[sub] = e.r; hn[sub] = -e.i;
            }
            s16x4 fr[2][4];
#define HSTORE(m) do { _Pragma("unroll") for (int c = 0; c < 2; ++c) \
                __builtin_amdgcn_raw_buffer_store_b128(__builtin_bit_cast(u32x4, __builtin_shufflevector(fr[(m) & 1][2 * c], fr[(m) & 1][2 * c + 1], 0, 1, 2, 3, 4, 5, 6, 7)), hrs, hp + (unsigned)(c * 2048), (m) * 65536, WT); } while (0)
#pragma unroll
            for (int m = 0; m < 4; ++m) {
#pragma unroll
                for (int j = 0; j < 4; ++j)
#pragma unroll
                    for (int sub = 0; sub < 2; ++sub) {
                        const float nr = fmaf(L1[sub].r, hr[sub], fmaf(L1[sub].i, hn[sub], acc[m][sub][j]));
                        const float nn = fmaf(L1[sub].r, hn[sub], fmaf(-L1[sub].i, hr[sub], acc[m][2 + sub][j]));
                        hr[sub] = nr; hn[sub] = nn;
                        acc[m][sub][j] = nr; acc[m][2 + sub][j] = nn;
                    }
                u32x2 pk[4];
#pragma unroll
                for (int c = 0; c < 4; ++c) pk[c] = (u32x2){pkbf(acc[m][c][0], acc[m][c][1]), pkbf(acc[m][c][2], acc[m][c][3])};
                PIN4(pk[0], pk[1], pk[2], pk[3]);
                SCHED();
#pragma unroll
                for (int c = 0; c < 4; ++c) { *(volatile __attribute__((address_space(3))) u32x2*)(size_t)(wa + (c >> 1) * 1024 + (c & 1) * 128) = pk[c]; SCHED(); }
                asm volatile("" ::: "memory");
#pragma unroll
                for (int c = 0; c < 2; ++c) { fr[m & 1][2 * c] = TRD(ra0 + c * 1024); SCHED(); fr[m & 1][2 * c + 1] = TRD(ra1 + c * 1024); SCHED(); }
                asm volatile("" ::: "memory");
                if (m > 0) HSTORE(m - 1);
                SCHED();
            }
            HSTORE(3);
        }
        STAMPT(dbg, 4, 0);
        {
            WAIT_VM(8);
            CREAD(2, cra, crb, 0);
            WAIT_VM(0);
            {
                unsigned n;
                asm volatile("s_mov_b64 exec, 1\n\tds_add_rtn_u32 %0, %1, %2\n\ts_mov_b64 exec, -1\n\ts_waitcnt lgkmcnt(0)" : "=&v"(n) : "v"(MISC_OFF), "v"(1u) : "memory");
                if (__builtin_amdgcn_readfirstlane(n) == 3u) {
                    if (lane == 0) __hip_atomic_store((gu32*)(ws + WS_HFLAG) + (tb * 16 + nb) * 32, tag, RLX_AGENT);
                    if (DBGON && lane == 0) dbg[BIDX * 64 + 20] = __builtin_amdgcn_s_memrealtime();
                }
            }
            STAMPT(dbg, 12, 0);
            __builtin_amdgcn_s_setprio(0);
            WAIT_LGKM0();
            CPIN(4); CPIN(5);
            CTILE(wid, 3, cg);
            XLOAD();
            HPOLL_SETUP();
            HWAIT();
            STAMPT(dbg, 8, 0);
            const unsigned vh = (unsigned)(lane * 16);
#pragma unroll
            for (int f = 0; f < DEPTH; ++f) LOADF(f);
            __builtin_amdgcn_s_setprio(PRIO2_G);
        }
    } else {
        const int lw = wid - 4;
        const int d = cb * 4 + lw;
        const char* Krow = (const char*)(ssmk + (size_t)d * SEQ);
        cra = LA_OFF + lw * 8192 + l15 * 128 + ((q4 ^ ((l15 >> 1) & 7)) * 16); crb = cra ^ 64;
        const unsigned cst0 = lbase + LA_OFF + lw * 8192;
        CT_SETUP();
        asm volatile("" : "+v"(cra), "+v"(crb));
        u32x4 KA[4];
        unsigned draw;
        unsigned tag;
        float gacc = 0.f;
#define KLOAD() asm volatile("global_load_dwordx4 %0, %5, %7 nt\n\tglobal_load_dwordx4 %1, %5, %7 offset:1024 nt\n\tglobal_load_dwordx4 %2, %5, %7 offset:2048 nt\n\t" \
                             "global_load_dwordx4 %3, %5, %7 offset:3072 nt\n\tglobal_load_ushort %4, %6, %8" \
                             : "=&v"(KA[0]), "=&v"(KA[1]), "=&v"(KA[2]), "=&v"(KA[3]), "=&v"(draw) : "v"((unsigned)(lane * 16)), "v"((unsigned)(d * 2)), "s"(Krow), "s"(ssmD) : "memory")
#define KWAIT(n) asm volatile("s_waitcnt vmcnt(%5)" : "+v"(KA[0]), "+v"(KA[1]), "+v"(KA[2]), "+v"(KA[3]), "+v"(draw) : "i"(n) : "memory")
        if (wid < 6) {
            STAGE_A(2, lw0 + RA(2)); STAGE_B(2, lw0 + RB(2));
            WAIT_VM(32);
            STAMPT(dbg, 17, 320);
            BAR();
            STAGE_B(3, lw0 + RB(3));
            unsigned long long ua_ = UNITS_A >> 12;
            unsigned sla = lw0 + RA(3), slb = lw0 + RB(4);
            int ka = ((3 + rot1) & 15) * 128, kb_ = ka;
            const char* sa = (const char*)x + ka;
            asm volatile("" : "+s"(sa), "+s"(sla), "+s"(ka), "+s"(ua_));
#define NEXT_A() do { ka = (ka + 128) & 2047; sa = (const char*)x + ka; ua_ >>= 4; sla = lw0 + (((unsigned)ua_ & 15u) << 14); asm volatile("" : "+s"(sa), "+s"(sla), "+s"(ka), "+s"(ua_)); } while (0)
#define LSTEP(n) do { \
                WAIT_VM(n); \
                BAR(); \
                GLDS4(sa, voA[0], voA[1], voA[2], voA[3], sla, 0); \
                GLDS4(sa + 16384, voA[0], voA[1], voA[2], voA[3], sla, 4096); \
                kb_ = (kb_ + 128) & 2047; \
                GLDS4((const char*)Bre + kb_, voB[0], voB[1], voB[0], voB[1], slb, 0); \
                NEXT_A(); \
                const unsigned slb0 = slb; \
                slb = (slb == lw0 + 65536) ? lw0 + 16384 : slb + 16384; \
                asm volatile("" : "+s"(slb), "+s"(kb_), "+s"(t)); \
                GLDS4((const char*)Bim + kb_, voB[0], voB[1], voB[0], voB[1], slb0, 4096); } while (0)
#pragma unroll 1
            for (int t = 1; t < 5; ++t) LSTEP(24);
            GLDS4(sa, voA[0], voA[1], voA[2], voA[3], sla, 0); GLDS4(sa + 16384, voA[0], voA[1], voA[2], voA[3], sla, 4096);
            NEXT_A();
#pragma unroll 1
            for (int t = 5; t < 13; ++t) LSTEP(32);
            KLOAD();
            WAIT_VM(37);
            BAR();
            __builtin_amdgcn_s_setprio(0);
            tag = lds_word(smem, TAG_OFF);
            WAIT_VM(21);
            BAR();
            CTILE(lw, 0, cst0 + GA_OFF);
            FLAGD(COK_OFF + lw * 4);
            WAIT_VM(14);
            BAR();
            WAIT_VM(9);
        } else {
            const int vw = wid - 6;
            const int dch = dblk * 128 + vw * 64 + l15;
            const unsigned lk = lbase + ST_OFF + vw * ST_SZ;
            const unsigned vv_ = (unsigned)(((lane >> 4) * 4 + ((lane >> 3) & 1)) * DM * 2 + (lane & 7) * 16);
            const char* vb = (const char*)vin + (size_t)(sblk * 64) * (DM * 2) + dblk * 256 + vw * 128;
            const int grow = nb * 8 + vw * 4 + q4;
            unsigned gx0 = (unsigned)(grow * 128 + (((l15 >> 1) ^ ((grow >> 1) & 7)) * 16) + (l15 & 1) * 8), gx1 = gx0 + RA(2), gwa = (unsigned)(GW_OFF + l15 * 8);
            asm volatile("" : "+v"(gx0), "+v"(gx1), "+v"(gwa));
            u32x2 gxv, gwv;
            typedef const volatile __attribute__((address_space(3))) u32x2* l2p;
#define VBAR(t) do { SCHED(); asm volatile("" ::: "memory"); BAR(); asm volatile("" ::: "memory"); \
                     gxv = *(l2p)(size_t)(RA(t) == 0 ? gx0 : gx1 + (RA(t) - RA(2))); gwv = *(l2p)(size_t)(gwa + (t) * 128); SCHED(); } while (0)
#define GATE() do { SCHED(); gacc = fmaf(lo16(gxv.x), lo16(gwv.x), gacc); gacc = fmaf(hi16(gxv.x), hi16(gwv.x), gacc); \
                    gacc = fmaf(lo16(gxv.y), lo16(gwv.y), gacc); gacc = fmaf(hi16(gxv.y), hi16(gwv.y), gacc); asm volatile("" : "+v"(gacc)); SCHED(); } while (0)
            unsigned ka[4][2], sel[4];
            const int R0 = 4 + (l15 >> 2) * 16 + (l15 & 3) - (q4 >> 1);
            unsigned kr0 = (unsigned)(2 * R0), kr1 = (unsigned)(ST_OFF + vw * ST_SZ + R0 * 128 + (((q4 ^ (R0 >> 1)) & 1) * 16));
            asm volatile("" : "+v"(kr0), "+v"(kr1));
#pragma unroll
            for (int i = 0; i < 4; ++i)
#pragma unroll
                for (int tp = 0; tp < 2; ++tp) {
                    ka[i][tp] = (kr1 ^ (tp * 16)) + ((kr0 + 8 * i - 4 * tp) & 0xe0) + (4 * i - 2 * tp) * 128;
                    asm volatile("" : "+v"(ka[i][tp]));
                }
#pragma unroll
            for (int r = 0; r < 4; ++r) {
                const unsigned pick = (q4 & 2) ? 0x0302u : 0x0100u;
                sel[r] = ((l15 >> 1) == (q4 & 1) * 4 + r) ? ((l15 & 1) ? (pick << 16) | 0x0c0cu : 0x0c0c0000u | pick) : 0x0c0c0c0cu;
            }
            unsigned va_ = (unsigned)(ST_OFF + vw * ST_SZ + ST_V + (q4 * 2 + (l15 >> 3)) * 1056 + ((l15 >> 2) & 1) * 128 + (l15 & 3) * 8);
            unsigned so = (unsigned)(((dch >> 2) * 32 + sblk) * 512 + q4 * 64 + (dch & 3) * 16);
            PIN4(sel[0], sel[1], sel[2], sel[3]); asm volatile("" : "+v"(so), "+v"(va_));
            asm volatile("s_waitcnt vmcnt(9)" : "+v"(tw[0]), "+v"(tw[1]), "+v"(tw[2]), "+v"(tw[3]), "+v"(epoch) :: "memory");
            STAMPT(dbg, 18, 384);
            VBAR(0);
            bf16x8 wf[4][2];
#pragma unroll
            for (int ps = 0; ps < 4; ++ps)
#pragma unroll
                for (int tp = 0; tp < 2; ++tp) {
                    u32x4 w;
#pragma unroll
                    for (int r = 0; r < 4; ++r) w[r] = __builtin_amdgcn_perm(0u, tw[ps][tp], sel[r]);
                    wf[ps][tp] = __builtin_bit_cast(bf16x8, w);
                    asm volatile("" : "+v"(wf[ps][tp]));
                }
            SCHED();
            VLD(0); VLD(1); VLD(2); VLD(3); VLD(4); VLD(5); VLD(6); VLD(7);
            GATE(); VBAR(1);
            f32x4 ks[4][4];
            auto sconv = [&](int i0) __attribute__((always_inline)) {
                bf16x8 kf[2][4][2];
#pragma unroll
                for (int q = 0; q < 16; ++q) { kf[q >> 3][q & 3][(q >> 2) & 1] = RD128(ka[i0 + (q >> 3)][(q >> 2) & 1], (q & 3) * 32); SCHED(); }
                GATE();
                __builtin_amdgcn_s_setprio(PRIO_KV);
                WAIT_LGKM0();
                SCHED();
#pragma unroll
                for (int i = 0; i < 2; ++i) {
#pragma unroll
                    for (int ps = 0; ps < 4; ++ps) MF0(ks[ps][i0 + i], kf[i][ps][0], wf[ps][0]);
#pragma unroll
                    for (int ps = 0; ps < 4; ++ps) MF(ks[ps][i0 + i], kf[i][ps][1], wf[ps][1]);
                }
                __builtin_amdgcn_s_setprio(0);
                SCHED();
            };
            WAIT_VM(8);
            STAMPT(dbg, 21, 384);
            sconv(0); VBAR(2);
            sconv(2); VBAR(3);
            WAIT_VM(0);
            u32x2 vq[4][4];
#pragma unroll
            for (int ps = 0; ps < 4; ++ps)
#pragma unroll
                for (int j = 0; j < 4; ++j) vq[ps][j] = __builtin_bit_cast(u32x2, TRD(va_ + j * 256 + ps * 32));
            const __amdgpu_buffer_rsrc_t rs = __builtin_amdgcn_make_buffer_rsrc(kvT, 0, DM * SEQ * 2, 0x00020000);
            u32x4 o[8];
            auto kvout = [&](int n) __attribute__((always_inline)) {
                const int hh = n >> 2, ps = n & 3;
                asm volatile("" : "+v"(ks[ps][2 * hh]), "+v"(ks[ps][2 * hh + 1]));
#pragma unroll
                for (int e = 0; e < 4; ++e) {
                    const unsigned vv = vq[ps][2 * hh + (e >> 1)][e & 1];
                    o[n][e] = pkbf(ks[ps][2 * hh + (e >> 1)][2 * (e & 1)] * lo16(vv), ks[ps][2 * hh + (e >> 1)][2 * (e & 1) + 1] * hi16(vv));
                }
                asm volatile("" : "+v"(o[n]));
                SCHED();
            };
            GATE(); kvout(0); kvout(1);
            WAIT_LGKM0();
            VBAR(4);
            kvout(2); kvout(3); GATE(); VBAR(5);
            kvout(4); kvout(5); GATE(); VBAR(6);
            kvout(6); kvout(7); GATE(); VBAR(7);
#pragma unroll
            for (int n = 0; n < 8; ++n) __builtin_amdgcn_raw_buffer_store_b128(o[(n >> 1) + (n & 1) * 4], rs, so + (unsigned)((n & 1) * 256), (n >> 1) * 65536, WT);
            if (vw == 1 && lane == 0) {
                const unsigned tg = (epoch + 1u) ? epoch + 1u : 1u;
                *(unsigned*)(smem + TAG_OFF) = tg;
                __hip_atomic_store((gu32*)(ws + WS_ARRIVE) + b * 32, tg, RLX_AGENT);
            }
            GATE(); VBAR(8);
            GATE();
            WAIT_VM(0);
            VBAR(9);
            tag = __builtin_amdgcn_readfirstlane(*(const unsigned*)(smem + TAG_OFF));
            if (vw == 0 && lane == 0) __hip_atomic_store((gu32*)(ws + WS_KVFLAG) + (dblk * 32 + sblk) * 32, tag, RLX_AGENT);
            if (vw == 1 && lane < 4) *(u32x4*)(smem + U_OFF + 192 + lane * 16) = (u32x4){0u, 0u, 0u, 0u};
            gu32* kfp = (gu32*)(ws + WS_KVFLAG) + ((cb >> 5) * 32 + vw * 16 + l15) * 32;
            const char* ub = (const char*)kvT + (size_t)__builtin_amdgcn_readfirstlane(cb * 16384 + vw * 8192);
            const unsigned vu = (unsigned)((lane >> 5) * 512 + ((lane >> 2) & 1) * 256 + ((lane >> 3) & 3) * 64 + ((lane - (lane >> 4)) & 3) * 16);
            const unsigned lu = lbase + U_OFF + 256 + vw * 8192;
#define U_DMA1(o) asm volatile("s_add_u32 m0, %2, %3\n\ts_nop 0\n\tglobal_load_lds_dwordx4 %0, %1 sc1\n\tglobal_load_lds_dwordx4 %0, %1 offset:1024 sc1\n\t" \
                             "global_load_lds_dwordx4 %0, %1 offset:2048 sc1\n\tglobal_load_lds_dwordx4 %0, %1 offset:3072 sc1" \
                             :: "v"(vu), "s"(ub + (o)), "s"(lu), "i"(o) : "memory", "scc")
#define U_DMA() do { U_DMA1(0); U_DMA1(4096); FLAGD(UFLAG_OFF + lw * 4); } while (0)
            GATE(); VBAR(10);
            TOUCH((const char*)(vw ? Cim : Cre) + (size_t)b * 8192, (unsigned)(lane * 128));
            GATE(); VBAR(11);
            unsigned pv;
            asm volatile("global_load_dword %0, %1, off sc1" : "=&v"(pv) : "v"(kfp) : "memory");
            GATE(); VBAR(12);
            GATE(); VBAR(13);
            asm volatile("s_waitcnt vmcnt(0)" : "+v"(pv) :: "memory");
            const bool done = __all(pv == tag);
            KLOAD();
            if (done) U_DMA();
            GATE(); VBAR(14);
            GATE(); VBAR(15);
            if (DBGON && TIDX == 384) dbg[BIDX * 64 + 13] = (done ? 100ull : 0ull) | (1ull << 62);
            GATE();
            if (lane == 0) SETW(UFLAG_OFF, vw, 1u);
            CTILE(lw, 0, cst0 + GA_OFF);
            FLAGD(COK_OFF + lw * 4);
            if (!done) {
                unsigned spins = 0;
                for (;;) {
                    const unsigned v = __hip_atomic_load(kfp, RLX_AGENT);
                    if (__all(v == tag)) break;
                    if (++spins > SPIN_MAX) break;
                    __builtin_amdgcn_s_sleep(1);
                }
                U_DMA();
            }
        }
        KWAIT(18);
        STAMPT(dbg, 14, 320);
        __builtin_amdgcn_s_sleep(6);
        {
            const unsigned cp = (unsigned)(KC_OFF + lw * CV_W + 64 + lane * 16);
            if (lane == 0) KA[0].x = (KA[0].x & 0xffff0000u) | f2bf(lo16(KA[0].x) + bf2f((u16)draw));
#define WR128(v, off) asm volatile("ds_write_b128 %0, %1 offset:%2" :: "v"(cp), "v"(v), "i"(off) : "memory")
#pragma unroll
            for (int q = 3; q >= 0; --q) WR128(KA[q], q * 1024);
#pragma unroll
            for (int q = 3; q >= 0; --q) {
                const u32x2 p = q ? pred2(KA[q], RL63(KA[q ? q - 1 : 0].z), RL63(KA[q ? q - 1 : 0].w)) : pred2(KA[0], 0u, 0u);
                const u32x4 a = KA[q];
                const u32x4 s1 = {al(a.x, p.y), al(a.y, a.x), al(a.z, a.y), al(a.w, a.z)};
                WR128(s1, q * 1024 + CV_COPY);
                const u32x4 s2 = {p.y, mv(a.x), mv(a.y), mv(a.z)};
                WR128(s2, q * 1024 + 2 * CV_COPY);
                const u32x4 s3 = {al(p.y, p.x), mv(s1.x), mv(s1.y), mv(s1.z)};
                WR128(s3, q * 1024 + 3 * CV_COPY);
            }
            if (lane < 16) *(u32x4*)(smem + KC_OFF + lw * CV_W + (lane >> 2) * CV_COPY + (lane & 3) * 16) = (u32x4){0u, 0u, 0u, 0u};
            SCHED();
        }
        STAMPT(dbg, 26, 320);
        wait_lds4(smem, G1D_OFF);
        wait_lds4(smem, UFLAG_OFF);
        STAMPT(dbg, 9, 256);
        __builtin_amdgcn_s_setprio(PRIO_CV);
        STAMPT(dbg, 10, 384);
        f32x4 cv[8];
#pragma unroll
        for (int g = 0; g < 8; ++g) cv[g] = (f32x4){0.f, 0.f, 0.f, 0.f};
        PIN4(cv[0], cv[1], cv[2], cv[3]); PIN4(cv[4], cv[5], cv[6], cv[7]);
        const int uz = U_OFF + 192;
        const int ua0 = U_OFF + 256 + (2 * l15 + q4 - 2) * 64, rv0 = (2 * l15 + q4 - 2) * 4 + lw * 16;
        const unsigned ka0 = (unsigned)((l15 >> 2) * CV_COPY - 16 * q4 + 8 * (l15 & 3) - 8);
        int ua, rv, pu, pz;
        unsigned adr;
#define UNEXT1() do { pu = ua | (rv & 0x30); asm volatile("" : "+v"(pu)); } while (0)
#define UNEXT2() do { pz = (ua > uz ? ua : uz) | (rv & 0x30); asm volatile("" : "+v"(pz)); } while (0)
#define UNEXT3() do { ua -= 256; rv -= 16; asm volatile("" : "+v"(ua), "+v"(rv)); } while (0)
#define ANEXT() do { adr += 64; asm volatile("" : "+v"(adr)); } while (0)
#define RDU(dst, c) do { dst = (c) ? RD128(pu, (c) * 2048) : RD128(pz, 0); SCHED(); } while (0)
        HPOLL_SETUP();
        unsigned hpv = 0;
        {
            bf16x8 A[8], UB[8];
            ua = ua0; rv = rv0; adr = lbase + KC_OFF + lw * CV_W + ka0 + 96;
            asm volatile("" : "+v"(adr));
            UNEXT1(); UNEXT2(); UNEXT3();
#pragma unroll
            for (int i = 0; i < 8; ++i) { RDA(A[7 - i], adr, 7 - i); RDU(UB[i], i); }
            unsigned gb = lw >= 2 ? (unsigned)((tb * 16 + nb) * 128 + (lw - 2) * 32) : 0x80000000u;
            asm volatile("" : "+s"(gb));
            const unsigned gso = (l15 == 0) ? gb + (unsigned)(q4 * 8) : 0x80000000u;
            float gt = 0.f;
            unsigned vxo = 0, vdo = 0, vgo = 0;
            auto gate_out = [&](int i) __attribute__((always_inline)) {
                if (i == 0) gacc += __uint_as_float(__builtin_amdgcn_update_dpp(0u, __float_as_uint(gacc), 0xb1, 0xf, 0xf, false));
                if (i == 1) gacc += __uint_as_float(__builtin_amdgcn_update_dpp(0u, __float_as_uint(gacc), 0x4e, 0xf, 0xf, false));
                if (i == 2) gacc += __uint_as_float(__builtin_amdgcn_update_dpp(0u, __float_as_uint(gacc), 0x141, 0xf, 0xf, false));
                if (i == 3) gacc += __uint_as_float(__builtin_amdgcn_update_dpp(0u, __float_as_uint(gacc), 0x140, 0xf, 0xf, false));
                if (i == 4) gacc = rbf(gacc);
                if (i == 5) gacc = __builtin_amdgcn_exp2f(gacc * -1.44269504088896f);
                if (i == 6) gacc = __builtin_amdgcn_rcpf(1.0f + gacc);
                if (i == 7) gacc = rbf(gacc);
                if (i == 8) gt = __uint_as_float(pkbf(gacc, 1.0f - gacc));
                if (i == 9) __builtin_amdgcn_raw_buffer_store_b64((u32x2){__float_as_uint(gt), tag}, gs, gso, 0, WT);
                asm volatile("" : "+v"(gacc), "+v"(gt));
            };
            auto step = [&](auto E) __attribute__((always_inline)) {
                constexpr int e = decltype(E)::value;
                constexpr int ti = (e == CS0) ? 0 : (e == CS1) ? 1 : (e == CS2) ? 2 : -1, tj = ti < 0 ? 0 : ti;
                constexpr unsigned char GA[36] = {0x66, 0x77, 0x76, 0x44, 0x55, 0x54, 0x65, 0x64, 0x75, 0x74, 0x22, 0x33, 0x32, 0x43, 0x42, 0x53, 0x52, 0x63, 0x62, 0x73, 0x72,
                                                  0x00, 0x11, 0x10, 0x21, 0x20, 0x31, 0x30, 0x41, 0x40, 0x51, 0x50, 0x61, 0x60, 0x71, 0x70};
                const char* sb_ = CSRC(wid, tj + 1);
#pragma unroll
                for (int q = 0; q < 36; ++q) {
                    const int g = GA[q] >> 4, a = GA[q] & 15;
                    MF(cv[g], A[a], UB[g - a]); SCHED();
                    if (e < 7) {
                        if (a == 1) RDU(UB[g - 1], g - 1);
                        if (q == 35) RDU(UB[7], 7);
                        if (g == 7) RDA(A[a], adr + 64 * (e + 1), a);
                        if (q == 11) UNEXT1();
                        if (q == 13) UNEXT2();
                        if (q == 15) UNEXT3();
                    }
                    if (ti >= 0) {
                        const int n = (q >= 3 && q <= 7) ? q - 3 : q == 10 ? 5 : q == 12 ? 6 : q == 14 ? 7 : -1;
                        if (q == 3) { if (ti == 0) WAIT_VM(1); else if (e == XE + 1) WAIT_VM(7); else WAIT_VM(0); }
                        if (n >= 0) cf_[2 * tj + (n >> 2)][n & 3] = RD128((n >> 2) ? crb : cra, (n & 3) * 2048);
                        if (q == 18) { CPIN(2 * tj); CPIN(2 * tj + 1); }
                        if (q == 18 || q == 23) asm volatile("s_add_u32 m0, %3, %4\n\ts_nop 0\n\tglobal_load_lds_dwordx4 %0, %2\n\tglobal_load_lds_dwordx4 %1, %2 offset:1024"
                                                             :: "v"(vB0), "v"(vB1), "s"(sb_ + (q == 18 ? 0 : 4096)), "s"(cst0), "i"(q == 18 ? 0 : 4096) : "memory", "scc");
                        if (q == 21 || q == 25) asm volatile("global_load_lds_dwordx4 %0, %2 offset:2048\n\tglobal_load_lds_dwordx4 %1, %2 offset:3072"
                                                             :: "v"(vB0), "v"(vB1), "s"(sb_ + (q == 21 ? 0 : 4096)) : "memory");
                    }
                    if (e == 0 && q >= 3 && q <= 7) gate_out(q - 3);
                    if (e == 0 && q >= 10 && q <= 18 && !(q & 1)) gate_out(5 + (q - 10) / 2);
                    if (e == 0 && q == 4) CTILE(wid, 0, cst0);
                    if (e == XE && q >= 23 && q <= 35 && (q & 1)) {
                        const int i = (q - 23) / 2;
                        const __amdgpu_buffer_rsrc_t xs = __builtin_amdgcn_make_buffer_rsrc((void*)x, 0, SEQ * DM * 2, 0x00020000);
                        const __amdgpu_buffer_rsrc_t ks = __builtin_amdgcn_make_buffer_rsrc((void*)dskip, 0, DM * 2, 0x00020000);
                        if (i < 4) asm volatile("buffer_load_dwordx2 %0, %1, %2, %3 offen offset:%4" : "=&v"(xx[(i >> 1) & 1][i & 1]) : "v"(vxo), "s"(xs), "s"((TR((i >> 1) & 1) * DM + d_base) * 2), "i"((i & 1) * 2048) : "memory");
                        if (i == 4) asm volatile("buffer_load_dwordx2 %0, %1, %2, %3 offen" : "=&v"(dk2) : "v"(vdo), "s"(ks), "s"(d_base * 2) : "memory");
                        if (i >= 5) asm volatile("buffer_load_dwordx4 %0, %1, %2, %3 offen sc1" : "=&v"(gv[(i - 5) & 1]) : "v"(vgo), "s"(gs), "s"(GSO((i - 5) & 1)) : "memory");
                    }
                    if (e == XE && q == 16) { vxo = (unsigned)(q4 * 16 * DM * 2 + l15 * 8); vdo = (unsigned)(l15 * 8); asm volatile("" : "+v"(vxo), "+v"(vdo)); }
                    if (e == XE && q == 17) { vgo = GV(q4); asm volatile("" : "+v"(vgo)); }
                    if (e == 6 && q == 30) hpv = __hip_atomic_load(hfp, RLX_AGENT);
                    SCHED();
                }
            };
            unroll<8>(step);
            WAIT_VM(0);
            XPIN();
        }
        PAD8(cv[0], cv[1], cv[2], cv[3]); PIN4(cv[4], cv[5], cv[6], cv[7]);
        STAMPT(dbg, 11, 384);
        const unsigned vh = (unsigned)((lane ^ 2) * 16);
        if (!__all(hpv == tag)) HWAIT();
        asm volatile("" ::: "memory");
#pragma unroll
        for (int f = 0; f < DEPTH; ++f) LOADF(f);
        STAMPT(dbg, 25, 384);
        {
            const __amdgpu_buffer_rsrc_t yrs = __builtin_amdgcn_make_buffer_rsrc(y2T, 0, DM * SEQ * 2, 0x00020000);
            const unsigned yo = (unsigned)(d * 4096 + (q4 & 1) * 512 + (l15 >> 3) * 256 + (q4 >> 1) * 128 + (l15 & 7) * 16);
#pragma unroll
            for (int g = 0; g < 8; g += 2) {
                const auto r0 = __builtin_amdgcn_permlane16_swap(pkbf(cv[g][0], cv[g][1]), pkbf(cv[g + 1][0], cv[g + 1][1]), false, false);
                const auto r1 = __builtin_amdgcn_permlane16_swap(pkbf(cv[g][2], cv[g][3]), pkbf(cv[g + 1][2], cv[g + 1][3]), false, false);
                __builtin_amdgcn_raw_buffer_store_b128((u32x4){r0[0], r1[0], r0[1], r1[1]}, yrs, yo + (unsigned)(g * 512), 0, WT);
            }
        }
        asm volatile("" ::: "memory");
        STAMPT(dbg, 15, 384);
        __builtin_amdgcn_s_setprio(PRIO2_L);
    }
    {
        const int lane = fresh_lane(), l15 = lane & 15, q4 = lane >> 4;
        const int ring = wid >> 2;
        const unsigned va = (unsigned)((lane ^ (ring * 2)) * 16), vh = va;
        const unsigned tag = lds_word(smem, TAG_OFF);
        const int cb0 = ring ? KC_OFF : GA_OFF, cst = ring ? CV_W : 8192;
        f32x4 ac[2][4];
        unsigned ca[2], fa[2], fl = 0, fv = 0;
#define CHAIN(u) do { const int c_ = (wid - ((u) >> 1)) & 3; ca[(u) & 1] = va + (unsigned)(cb0 + c_ * cst + ((u) & 1) * 4096); \
                      fa[(u) & 1] = (unsigned)(RF_OFF + ring * 32 + ((u) & 1) * 16 + c_ * 4); asm volatile("" : "+v"(ca[(u) & 1]), "+v"(fa[(u) & 1])); } while (0)
#define RDIN(dst, a, f_) asm volatile("ds_read_b32 %0, %5\n\tds_read_b128 %1, %6\n\tds_read_b128 %2, %6 offset:1024\n\tds_read_b128 %3, %6 offset:2048\n\tds_read_b128 %4, %6 offset:3072" \
                     : "=&v"(fl), "=&v"(dst[0]), "=&v"(dst[1]), "=&v"(dst[2]), "=&v"(dst[3]) : "v"(f_), "v"(a) : "memory")
#define LWAIT(dst) asm volatile("s_waitcnt lgkmcnt(0)" : "+v"(fl), "+v"(dst[0]), "+v"(dst[1]), "+v"(dst[2]), "+v"(dst[3]) :: "memory")
        CHAIN(0); CHAIN(1);
        WAIT_LGKM0();
#pragma unroll
        for (int k = 0; k < 6; ++k) CPIN(k);
        CREAD(3, cra, crb, 0);
        int tr[2] = {TR(0), TR(1)};
        unsigned gw[2][2];
        {
            unsigned spins = 0;
            while (__builtin_expect(!__all((gv[0].y == tag) & (gv[0].w == tag) & (gv[1].y == tag) & (gv[1].w == tag)), 0)) {
                if (++spins > SPIN_MAX) break;
#pragma unroll
                for (int r = 0; r < 2; ++r) asm volatile("s_nop 4\n\tbuffer_load_dwordx4 %0, %1, %2, %3 offen sc1\n\ts_waitcnt vmcnt(0)" : "=&v"(gv[r]) : "v"(GV(q4)), "s"(gs), "s"(__builtin_amdgcn_readfirstlane(GSO(r))) : "memory");
            }
            gw[0][0] = gv[0].x; gw[0][1] = gv[0].z; gw[1][0] = gv[1].x; gw[1][1] = gv[1].z;
            PIN4(gw[0][0], gw[0][1], gw[1][0], gw[1][1]);
        }
        unsigned cpv = 0, yy[2][4];
        f32x2 zz[2][4];
        unsigned exo = 0, exi = 0, efo = 0, efi = 0;
        auto send = [&](int r) __attribute__((always_inline)) {
            const unsigned one = 1u;
#pragma unroll
            for (int n = 0; n < 4; ++n) { const f32x2 v = {ac[r][n][2], ac[r][n][3]}; asm volatile("ds_write_b64 %0, %1 offset:%2" :: "v"(exo), "v"(v), "i"(r * 4096 + n * 512) : "memory"); }
            asm volatile("ds_write_b32 %0, %1 offset:%2" :: "v"(efo), "v"(one), "i"(r * 4) : "memory");
        };
        STAMPT(dbg, 5, 0);
        auto body = [&](auto F) __attribute__((always_inline)) {
            constexpr int f = decltype(F)::value;
            constexpr int u = f >> 3, k = f & 7;
            f32x4* an = ac[u & 1];
            f32x4* ao = ac[(u + 1) & 1];
#pragma unroll
            for (int n = 0; n < 4; ++n) {
                if (k == 0 && u < 2) MF0(an[n], af[f % DEPTH], cf_[k][n]); else MF(an[n], af[f % DEPTH], cf_[k][n]);
                SCHED();
                const int g = k * 4 + n;
                if (u >= 1 && u < 7) {
                    if (g == 1) { fv = ((u - 1) >> 1) + 1; asm volatile("" : "+v"(fv)); }
                    if (g == 4 || g == 6 || g == 8 || g == 10) asm volatile("ds_write_b128 %0, %1 offset:%2" :: "v"(ca[(u + 1) & 1]), "v"(ao[(g - 4) / 2]), "i"((g - 4) * 512) : "memory");
                    if (g == 12) asm volatile("ds_write_b32 %0, %1" :: "v"(fa[(u + 1) & 1]), "v"(fv) : "memory");
                    if (g == 18) CHAIN(u + 1);
                    if (g == 20) asm volatile("ds_read_b32 %0, %1" : "=&v"(fl) : "v"(fa[(u + 1) & 1]) : "memory");
                    if (g > 20 && g <= 24) asm volatile("ds_read_b128 %0, %1 offset:%2" : "=&v"(ao[g - 21]) : "v"(ca[(u + 1) & 1]), "i"((g - 21) * 1024) : "memory");
                    if (g == 30) {
                        LWAIT(ao);
                        if (__builtin_expect(__builtin_amdgcn_readfirstlane(fl) != (unsigned)((u + 1) >> 1), 0)) {
                            unsigned spins = 0;
                            do {
                                __builtin_amdgcn_s_sleep(1);
                                RDIN(ao, ca[(u + 1) & 1], fa[(u + 1) & 1]); LWAIT(ao);
                                if (++spins > SPIN_MAX * 16u) break;
                            } while (__builtin_amdgcn_readfirstlane(fl) != (unsigned)((u + 1) >> 1));
                        }
                    }
                }
                if (u == 7 && g == 6) send(0);
                if (f >= ZF && f < ZF + 4) {
                    constexpr int r = ((f - ZF) >> 1) & 1, j = (f - ZF) & 1;
                    const unsigned xw = xx[r][j][n >> 1], dw = dk2[n >> 1];
                    const float xf = (n & 1) ? hi16(xw) : lo16(xw), df = (n & 1) ? hi16(dw) : lo16(dw), yf = j ? hi16(yy[r][n]) : lo16(yy[r][n]);
                    zz[r][n][j] = xf * fmaf(hi16(gw[r][j]), yf, lo16(gw[r][j]) * df);
                    asm volatile("" : "+v"(zz[r][n]));
                }
                if (f == ZF + 4) {
                    zz[0][n] = (f32x2){fmaf(lo16(gw[0][0]), ac[0][n][0], zz[0][n][0]), fmaf(lo16(gw[0][1]), ac[0][n][1], zz[0][n][1])};
                    asm volatile("" : "+v"(zz[0][n]));
                }
                SCHED();
            }
            if (f + DEPTH < 64) LOADF(f + DEPTH);
            if (f == DEPTH + 1 && ring) {
                WAIT_VM(6);
                if (lane == 0) __hip_atomic_store((gu32*)(ws + WS_CFLAG) + cb * 32 + (wid & 3), tag, RLX_AGENT);
                SCHED();
            }
            if (f == YPOLL && !ring) { const int l15 = fresh_lane() & 15; cpv = __hip_atomic_load((gu32*)(ws + WS_CFLAG) + (nb * 16 + (wid & 3) * 4 + (l15 >> 2)) * 32 + (l15 & 3), RLX_AGENT); SCHED(); }
            if (f == YPOLL + DEPTH && !ring) {
                const int ln = fresh_lane(), l15 = ln & 15, q4 = ln >> 4;
                if (__builtin_expect(!__all(cpv == tag), 0)) {
                    unsigned spins = 0;
                    while (!__all(__hip_atomic_load((gu32*)(ws + WS_CFLAG) + (nb * 16 + (wid & 3) * 4 + (l15 >> 2)) * 32 + (l15 & 3), RLX_AGENT) == tag)) {
                        if (++spins > SPIN_MAX) break;
                        __builtin_amdgcn_s_sleep(1);
                    }
                }
                wait_lds1(smem, RF_OFF + 32 + wid * 4);
                const unsigned lx = lbase + LA_OFF + (wid & 3) * 8192;
#pragma unroll
                for (int i = 0; i < 4; ++i) {
                    const unsigned vo = (unsigned)((d_base + (wid & 3) * 16 + i * 4 + q4) * 4096 + tb * 256 + ((l15 ^ ((wid & 3) * 4 + i)) * 16));
                    GLDS_SC1(y2T, vo, lx, i * 1024);
                }
                SCHED();
            }
            if (f == YPOLL + 2 * DEPTH + 1 && !ring) {
                WAIT_VM(6);
                SETW(YFLAG_OFF, wid & 3, 1u);
                SCHED();
            }
            if (f == 55) {
                const int ln = fresh_lane(), c_ = (wid - 3) & 3;
                const int eg = GA_OFF + c_ * 8192, el = KC_OFF + c_ * CV_W;
                exo = (unsigned)((ring ? el : eg) + ln * 8);
                exi = (unsigned)((ring ? eg : el) + ln * 8);
                efo = (unsigned)(EF_OFF + wid * 8); efi = (unsigned)(EF_OFF + (wid ^ 4) * 8);
                PIN4(exo, exi, efo, efi);
                SCHED();
            }
            if (f == ZF - 2) {
                const int ln = fresh_lane(), l15 = ln & 15, q4 = ln >> 4;
                wait_lds4(smem, YFLAG_OFF);
#pragma unroll
                for (int r = 0; r < 2; ++r) {
                    const int jb = JB(6 + r);
                    const int ya = LA_OFF + (l15 >> 2) * 8192 + (l15 & 3) * 1024 + (((((jb >> 1) & 1) * 8 + (jb >> 2) * 4 + q4) ^ l15) * 16) + (jb & 1) * 8 + ring * 4;
#pragma unroll
                    for (int e = 0; e < 4; ++e) yy[r][e] = *(const unsigned*)(smem + ya + e * 256);
                }
                SCHED();
            }
        };
        unroll<64>(body);
        PAD8(ac[1][0], ac[1][1], ac[1][2], ac[1][3]);
        send(1);
        STAMPT(dbg, 6, 0);
        const __amdgpu_buffer_rsrc_t ors = __builtin_amdgcn_make_buffer_rsrc(out, 0, SEQ * DM * 4, 0x00020000);
        const unsigned ov = (unsigned)((lane >> 4) * (16 * DM * 4) + (lane & 15) * 16);
#pragma unroll
        for (int r = 0; r < 2; ++r) {
            f32x2 in[4];
#define EXRD() asm volatile("ds_read_b32 %0, %5 offset:%7\n\tds_read_b64 %1, %6 offset:%8\n\tds_read_b64 %2, %6 offset:%9\n\tds_read_b64 %3, %6 offset:%10\n\tds_read_b64 %4, %6 offset:%11" \
                             : "=&v"(fl), "=&v"(in[0]), "=&v"(in[1]), "=&v"(in[2]), "=&v"(in[3]) \
                             : "v"(efi), "v"(exi), "i"(r * 4), "i"(r * 4096), "i"(r * 4096 + 512), "i"(r * 4096 + 1024), "i"(r * 4096 + 1536) : "memory")
            EXRD();
            if (r == 0) {
#pragma unroll
                for (int n = 0; n < 4; ++n) {
                    zz[1][n] = (f32x2){fmaf(lo16(gw[1][0]), ac[1][n][0], zz[1][n][0]), fmaf(lo16(gw[1][1]), ac[1][n][1], zz[1][n][1])};
                    asm volatile("" : "+v"(zz[1][n]));
                }
            }
            LWAIT(in);
            if (__builtin_expect(__builtin_amdgcn_readfirstlane(fl) == 0u, 0)) {
                unsigned spins = 0;
                do {
                    EXRD(); LWAIT(in);
                    if (++spins > SPIN_MAX * 16u) break;
                } while (__builtin_amdgcn_readfirstlane(fl) == 0u);
            }
            f32x4 o[2];
#pragma unroll
            for (int j = 0; j < 2; ++j)
#pragma unroll
                for (int e = 0; e < 4; ++e) o[j][e] = fmaf(lo16(gw[r][j]), in[e][j], zz[r][e][j]);
#pragma unroll
            for (int j = 0; j < 2; ++j)
                __builtin_amdgcn_raw_buffer_store_b128(__builtin_bit_cast(u32x4, o[j]), ors, ov + (unsigned)(((tr[r] + j) * DM + d_base) * 4), 0, WT);
        }
        STAMPT(dbg, 7, 0);
        STAMPT(dbg, 16, 384);
        if (b == 0 && wid == 0 && !bumped) {
            gu32* ap = (gu32*)(ws + WS_ARRIVE) + lane * 32;
            unsigned spins = 0;
            for (;;) {
                bool ok = true;
#pragma unroll
                for (int q = 0; q < 4; ++q) ok &= (__hip_atomic_load(ap + q * 64 * 32, RLX_AGENT) == tag);
                if (__all(ok)) break;
                if (++spins > SPIN_MAX) break;
                __builtin_amdgcn_s_sleep(1);
            }
            if (lane == 0) __hip_atomic_store((gu32*)(ws + WS_EPOCH), tag, RLX_AGENT);
        }
    }
}

extern "C" void kernel_launch(void* const* d_in, const int* in_sizes, int n_in,
                              void* d_out, int out_size, void* d_ws, size_t ws_size,
                              hipStream_t stream) {
    const u16* x = (const u16*)d_in[0];
    const u16* lre = (const u16*)d_in[1];
    const u16* lim = (const u16*)d_in[2];
    const u16* Bre = (const u16*)d_in[3];
    const u16* Bim = (const u16*)d_in[4];
    const u16* Cre = (const u16*)d_in[5];
    const u16* Cim = (const u16*)d_in[6];
    const u16* logdt = (const u16*)d_in[7];
    const u16* dskip = (const u16*)d_in[8];
    const u16* kin = (const u16*)d_in[10];
    const u16* vin = (const u16*)d_in[11];
    const u16* shiftk = (const u16*)d_in[12];
    const u16* ssmk = (const u16*)d_in[13];
    const u16* ssmD = (const u16*)d_in[14];
    const u16* gatew = (const u16*)d_in[15];
    float* out = (float*)d_out;

    char* ws = (char*)d_ws;

    hipFuncSetAttribute((const void*)k1_kernel, hipFuncAttributeMaxDynamicSharedMemorySize, K1_LDS);

    k1_kernel<<<256, 512, K1_LDS, stream>>>(x, Bre, Bim, kin, vin, shiftk, gatew, ws, lre, lim, logdt, ssmk, ssmD, Cre, Cim, dskip, out);
}
#if defined(__HIP_DEVICE_COMPILE__)
#pragma clang attribute pop
#endif
```

```cpp
#if defined(__HIP_DEVICE_COMPILE__)
#pragma clang attribute push(__attribute__((target("no-packed-fp32-ops"))), apply_to = function)
#endif
#include <hip/hip_runtime.h>
#include <hip/hip_bf16.h>
#include <stdint.h>

#define TIDX ((int)__builtin_amdgcn_workitem_id_x())
#define BIDX ((int)__builtin_amdgcn_workgroup_id_x())
typedef unsigned short u16;
typedef __attribute__((ext_vector_type(8))) short bf16x8;
typedef __attribute__((ext_vector_type(4))) float f32x4;
typedef __attribute__((ext_vector_type(2))) float f32x2;
typedef __attribute__((ext_vector_type(2))) __bf16 bf16x2;
typedef __attribute__((ext_vector_type(4))) unsigned u32x4;
typedef __attribute__((ext_vector_type(2))) unsigned u32x2;
typedef __attribute__((address_space(1))) unsigned long long gu64;
typedef __attribute__((address_space(1))) unsigned gu32;
typedef __attribute__((address_space(3))) void lds_void;
typedef __attribute__((address_space(3))) unsigned lds_u32;
typedef __attribute__((ext_vector_type(4))) short s16x4;
typedef __attribute__((address_space(3))) s16x4 lds_s16x4;
#define TRD(a) __builtin_amdgcn_ds_read_tr16_b64_v4i16((lds_s16x4*)(size_t)(a))

#define DBGON 0
#define STAMPT(buf, k, t) do { } while (0)
#define CODE_LINES 274
#define SEQ 2048
#define DM 1024
#define SPIN_MAX (1u << 18)
#define PRIO_G 2
#define PSLEEP 4
#define PRIO_L 3
#define WT 16

__device__ __forceinline__ float bf2f(u16 v) { return __uint_as_float(((unsigned)v) << 16); }
__device__ __forceinline__ u16 f2bf(float f) { __bf16 b = (__bf16)f; return __builtin_bit_cast(u16, b); }
__device__ __forceinline__ float rbf(float f) { return bf2f(f2bf(f)); }
__device__ __forceinline__ unsigned pkbf(float lo, float hi) { return __builtin_bit_cast(unsigned, __builtin_convertvector((f32x2){lo, hi}, bf16x2)); }
__device__ __forceinline__ float lo16(unsigned u) { return __uint_as_float(u << 16); }
__device__ __forceinline__ float hi16(unsigned u) { return __uint_as_float(u & 0xffff0000u); }

struct cf { float r, i; };
__device__ __forceinline__ cf cmul(cf a, cf b) { return {a.r * b.r - a.i * b.i, a.r * b.i + a.i * b.r}; }
__device__ __forceinline__ cf cfma(cf a, cf b, cf c) {
    return {fmaf(a.r, b.r, fmaf(-a.i, b.i, c.r)), fmaf(a.r, b.i, fmaf(a.i, b.r, c.i))};
}
__device__ __forceinline__ f32x4 splat4(float v) { return (f32x4){v, v, v, v}; }
__device__ __forceinline__ f32x4 fma4(f32x4 a, f32x4 b, f32x4 c) { return __builtin_elementwise_fma(a, b, c); }
__device__ __forceinline__ f32x4 mul4(f32x4 a, f32x4 b) { return a * b; }

__device__ __forceinline__ float red32(float a, float b) {
    const auto r = __builtin_amdgcn_permlane32_swap(__float_as_uint(a), __float_as_uint(b), false, false);
    return __uint_as_float(r[0]) + __uint_as_float(r[1]);
}
__device__ __forceinline__ float red16(float x) {
    const auto r = __builtin_amdgcn_permlane16_swap(__float_as_uint(x), __float_as_uint(x), false, false);
    return __uint_as_float(r[0]) + __uint_as_float(r[1]);
}
__device__ __forceinline__ int fresh_lane() {
    int l;
    asm volatile("v_mbcnt_lo_u32_b32 %0, -1, 0\n\tv_mbcnt_hi_u32_b32 %0, -1, %0" : "=v"(l));
    return l;
}
#define BAR() __builtin_amdgcn_s_barrier()
#define WAIT_VM(n) asm volatile("s_waitcnt vmcnt(" #n ")" ::: "memory")
#define WAIT_LGKM0() __builtin_amdgcn_s_waitcnt(0xC07F)
#define SCHED() __builtin_amdgcn_sched_barrier(0)
#define MF(c, a, b) asm volatile("v_mfma_f32_16x16x32_bf16 %0, %1, %2, %0" : "+v"(c) : "v"(a), "v"(b))
#define PAD8(c0, c1, c2, c3) asm volatile("s_nop 7" : "+v"(c0), "+v"(c1), "+v"(c2), "+v"(c3))
#define PIN4(c0, c1, c2, c3) asm volatile("" : "+v"(c0), "+v"(c1), "+v"(c2), "+v"(c3))
#define GLDS_S(sbase, voff, sl, IMM) \
    asm volatile("s_add_u32 m0, %2, %3\n\ts_nop 0\n\tglobal_load_lds_dwordx4 %0, %1" \
                 :: "v"(voff), "s"(sbase), "s"(sl), "i"(IMM) : "memory", "scc")
#define GLDS_K(sbase, voff, sl, IMM) \
    asm volatile("s_add_u32 m0, %2, %3\n\ts_nop 0\n\tglobal_load_lds_dwordx4 %0, %1 nt" \
                 :: "v"(voff), "s"(sbase), "s"(sl), "i"(IMM) : "memory", "scc")
#define RLX_AGENT __ATOMIC_RELAXED, __HIP_MEMORY_SCOPE_AGENT

#define UNITS_A 0x0756075607560560ull
#define UNITS_B 0x1432143214321432ull
#define RA(t) ((int)((UNITS_A >> (4 * (t))) & 15) * 16384)
#define RB(t) ((int)((UNITS_B >> (4 * (t))) & 15) * 16384)
#define LA_OFF 0
#define GA_OFF 32768
#define CV_COPY 4160
#define CV_W (4 * CV_COPY)
#define KC_OFF 65536
#define ST_OFF 114688
#define ST_V 8832
#define ST_SZ (ST_V + 8 * 1056)
#define U_OFF (KC_OFF + 16 * CV_COPY)
#define HT_OFF (U_OFF + 256 + 16384)
#define GW_OFF (HT_OFF + 8192)
#define TAG_OFF (GW_OFF + 2048)
#define UFLAG_OFF (TAG_OFF + 16)
#define G1D_OFF (UFLAG_OFF + 16)
#define MISC_OFF (G1D_OFF + 16)
#define YFLAG_OFF (MISC_OFF + 16)
#define RF_OFF (YFLAG_OFF + 16)
#define EF_OFF (RF_OFF + 64)
#define COK_OFF (EF_OFF + 64)
#define NFLAGW 52
#define DUMMY_OFF (COK_OFF + 16)
#define K1_LDS (DUMMY_OFF + 256)
#define WS_GRAN 0
#define WS_KVFLAG (256u << 10)
#define WS_EPOCH (WS_KVFLAG + 65536)
#define WS_CFLAG (WS_EPOCH + 4096)
#define WS_HFLAG (384u << 10)
#define WS_GATE (512u << 10)

#define RD128(a, off) (*(const __attribute__((address_space(3))) bf16x8*)(size_t)(unsigned)((a) + (off)))
#define RDA(dst, A, F) do { const s16x4 lo_ = TRD((A) + 512 * (F) + 8); SCHED(); const s16x4 hi_ = TRD((A) + 512 * (F)); SCHED(); \
                            dst = __builtin_shufflevector(lo_, hi_, 0, 1, 2, 3, 4, 5, 6, 7); } while (0)
#define TOUCH(sbase, voff) asm volatile("s_mov_b32 m0, %2\n\ts_nop 0\n\tglobal_load_lds_dword %0, %1" :: "v"(voff), "s"(sbase), "i"(DUMMY_OFF) : "memory")
#define FLAGD(off) do { unsigned long long pc_; asm volatile("s_getpc_b64 %0\n\ts_mov_b32 m0, %2\n\ts_mov_b64 exec, 1\n\tglobal_load_lds_dword %1, %0\n\ts_mov_b64 exec, -1" \
                                                             : "=&s"(pc_) : "v"(0u), "s"(__builtin_amdgcn_readfirstlane(off)) : "memory"); } while (0)
#define GLDS_SC1(sbase, voff, sl, IMM) \
    asm volatile("s_add_u32 m0, %2, %3\n\ts_nop 0\n\tglobal_load_lds_dwordx4 %0, %1 sc1" \
                 :: "v"(voff), "s"(sbase), "s"(sl), "i"(IMM) : "memory", "scc")
__device__ __forceinline__ unsigned mv(unsigned v) { unsigned r; asm("v_mov_b32 %0, %1" : "=v"(r) : "v"(v)); return r; }
__device__ __forceinline__ unsigned al(unsigned hi, unsigned lo) { unsigned r; asm volatile("v_alignbit_b32 %0, %1, %2, 16" : "=v"(r) : "v"(hi), "v"(lo)); return r; }
__device__ __forceinline__ u32x2 pred2(const u32x4& a, unsigned f0, unsigned f1) {
    return (u32x2){(unsigned)__builtin_amdgcn_update_dpp((int)f0, (int)a.z, 0x138, 0xf, 0xf, false), (unsigned)__builtin_amdgcn_update_dpp((int)f1, (int)a.w, 0x138, 0xf, 0xf, false)};
}
#define RL63(v) ((unsigned)__builtin_amdgcn_readlane((int)(v), 63))
__device__ __forceinline__ void wait_lds4(char* smem, int off) {
    const unsigned a = (unsigned)(size_t)(lds_void*)smem + off;
    unsigned spins = 0;
    for (;;) {
        u32x4 f;
        asm volatile("ds_read_b128 %0, %1\n\ts_waitcnt lgkmcnt(0)" : "=&v"(f) : "v"(a) : "memory");
        if (__builtin_amdgcn_readfirstlane((f.x != 0u) & (f.y != 0u) & (f.z != 0u) & (f.w != 0u))) break;
        if (++spins > SPIN_MAX * 16u) break;
        __builtin_amdgcn_s_sleep(1);
    }
}
__device__ __forceinline__ unsigned lds_word(char* smem, int off) {
    const unsigned a = (unsigned)(size_t)(lds_void*)smem + off;
    unsigned f;
    asm volatile("ds_read_b32 %0, %1\n\ts_waitcnt lgkmcnt(0)" : "=&v"(f) : "v"(a) : "memory");
    return __builtin_amdgcn_readfirstlane(f);
}
__device__ __forceinline__ void wait_lds1(char* smem, int off) {
    unsigned spins = 0;
    while (lds_word(smem, off) == 0u) {
        if (++spins > SPIN_MAX * 16u) break;
        __builtin_amdgcn_s_sleep(1);
    }
}
#define SETW(off, i, v) __hip_atomic_store((lds_u32*)(smem + (off)) + (i), (v), __ATOMIC_RELAXED, __HIP_MEMORY_SCOPE_WORKGROUP)

#define PRIO_CV 1
#define PRIO_KV 3
#define CS0 1
#define CS1 3
#define CS2 5
#define XE 4
#define YPOLL 32
#define ZF 59
template <int I> struct IC { static constexpr int value = I; };
template <int N, int I = 0, class F> __device__ __forceinline__ void unroll(F& f) { if constexpr (I < N) { f(IC<I>{}); unroll<N, I + 1>(f); } }
#define MF0(c, a, b) asm volatile("v_mfma_f32_16x16x32_bf16 %0, %1, %2, 0" : "=&v"(c) : "v"(a), "v"(b))
#define DEPTH 6
#define PRIO2_G 0
#define PRIO2_L 1
__device__ __forceinline__ cf pick4(int q, cf a1, cf a2, cf a3) {
    return {q == 0 ? 1.f : q == 1 ? a1.r : q == 2 ? a2.r : a3.r, q == 0 ? 0.f : q == 1 ? a1.i : q == 2 ? a2.i : a3.i};
}
#define PINC(c) asm volatile("" : "+v"((c).r), "+v"((c).i))

extern "C" __global__ __launch_bounds__(512, 2) void k1_kernel(
        const u16* __restrict__ x, const u16* __restrict__ Bre, const u16* __restrict__ Bim,
        const u16* __restrict__ kin, const u16* __restrict__ vin, const u16* __restrict__ shiftk, const u16* __restrict__ gatew, char* ws,
        const u16* __restrict__ lre, const u16* __restrict__ lim, const u16* __restrict__ logdt,
        const u16* __restrict__ ssmk, const u16* __restrict__ ssmD,
        const u16* __restrict__ Cre, const u16* __restrict__ Cim, const u16* __restrict__ dskip, float* __restrict__ out) {
    extern __shared__ __attribute__((aligned(16))) char smem[];
#define h ((u16*)(ws + (1u << 20)))
#define kvT ((u16*)(ws + (9u << 20)))
#define y2T ((u16*)(ws + (13u << 20)))
#define dbg ((unsigned long long*)(ws + (64u << 20)))
    const int tid = TIDX;
    if (tid < 40 && tid + ((BIDX >> 3) & 7) * 40 < CODE_LINES) {
        const unsigned vo = (unsigned)(tid + ((BIDX >> 3) & 7) * 40) * 128u;
        asm volatile("s_getpc_b64 s[98:99]\n\ts_add_u32 s98, s98, k1_kernel@rel32@lo+4\n\ts_addc_u32 s99, s99, k1_kernel@rel32@hi+12\n\t"
                     "s_mov_b32 m0, %1\n\ts_nop 0\n\tglobal_load_lds_dword %0, s[98:99]"
                     :: "v"(vo), "i"(DUMMY_OFF) : "s98", "s99", "scc", "memory");
    }
    const int lane = tid & 63;
    int wid = __builtin_amdgcn_readfirstlane(tid >> 6);
    const int b = BIDX;
    const int xcd = b & 7, idx = b >> 3;
    const int tb = ((xcd >> 1) ^ (xcd >> 2)) * 4 + (idx & 3), nb = (xcd & 1) * 8 + (idx >> 2);
    const int cb = xcd * 32 + idx;
    const int t_base = tb * 128, p_base = nb * 64, d_base = nb * 64;
    const int l15 = lane & 15, q4 = lane >> 4;
    unsigned stv = 0, sth = 0;
    STAMPT(dbg, 0, 0);
    const unsigned lbase = (unsigned)(size_t)(lds_void*)smem;
    const int rot1 = (xcd >> 1) * 4 + (xcd & 1) * 8 + (idx >> 2);
    unsigned voA[4] = {0u, 0u, 0u, 0u}, voB[4] = {0u, 0u, 0u, 0u}, lw0 = 0;
    if (wid == 4 || wid == 5) {
        __builtin_amdgcn_s_setprio(PRIO_L);
        const int lh = wid - 4;
        const unsigned ch = (unsigned)(((lane & 7) ^ (lane >> 4)) * 16);
        voA[0] = (unsigned)(t_base + lh * 16 + (lane >> 5) * 32 + ((lane >> 3) & 3)) * 2048u + ch;
        voA[1] = (voA[0] ^ 64u) + (131072u - 1024u);
        voA[2] = voA[0] + 6144u; voA[3] = voA[1] + 6144u;
        voB[0] = (unsigned)(p_base + lh * 32 + (lane >> 3)) * 2048u + ch;
        voB[1] = (voB[0] ^ 64u) + (16384u - 1024u);
        voB[2] = voB[0] + 30720u; voB[3] = voB[1] + 30720u;
        PIN4(voA[0], voA[1], voA[2], voA[3]); PIN4(voB[0], voB[1], voB[2], voB[3]);
#define GLDS4(sbase, v0, v1, v2, v3, sl, IMM) \
            asm volatile("s_add_u32 m0, %5, %6\n\ts_nop 0\n\tglobal_load_lds_dwordx4 %0, %4\n\tglobal_load_lds_dwordx4 %1, %4 offset:1024\n\t" \
                         "global_load_lds_dwordx4 %2, %4 offset:2048\n\tglobal_load_lds_dwordx4 %3, %4 offset:3072" \
                         :: "v"(v0), "v"(v1), "v"(v2), "v"(v3), "s"(sbase), "s"(sl), "i"(IMM) : "memory", "scc")
#define STAGE_A(kt, sl) do { const char* sa_ = (const char*)x + (((kt) + rot1) & 15) * 128; \
            GLDS4(sa_, voA[0], voA[1], voA[2], voA[3], sl, 0); GLDS4(sa_ + 16384, voA[0], voA[1], voA[2], voA[3], sl, 4096); } while (0)
#define STAGE_B(kt, sl) do { const int kr_ = (((kt) + rot1) & 15) * 128; const char* sb_ = (const char*)Bre + kr_; const char* sc_ = (const char*)Bim + kr_; \
            GLDS4(sb_, voB[0], voB[1], voB[2], voB[3], sl, 0); GLDS4(sc_, voB[0], voB[1], voB[2], voB[3], sl, 4096); } while (0)
        lw0 = lbase + lh * 8192;
        STAGE_A(0, lw0 + RA(0)); STAGE_B(0, lw0 + RB(0)); STAGE_A(1, lw0 + RA(1)); STAGE_B(1, lw0 + RB(1));
    }
    const int sblk = b >> 3, dblk = b & 7;
    u32x2 tw[4];
    unsigned epoch;
    asm volatile("" : "=v"(tw[0]), "=v"(tw[1]), "=v"(tw[2]), "=v"(tw[3]), "=v"(epoch));
    if (wid >= 6) {
        __builtin_amdgcn_s_setprio(PRIO_L);
        const int vw = wid - 6;
        const int dch = dblk * 128 + vw * 64 + l15;
        const unsigned lk = lbase + ST_OFF + vw * ST_SZ;
        const unsigned vk = (unsigned)((lane >> 3) * DM * 2 + (((lane & 7) ^ ((lane >> 4) & 1)) * 16));
        const char* kb = (const char*)kin + (size_t)(sblk * 64 - 4) * (DM * 2) + dblk * 256 + vw * 128;
#define KLD(n) GLDS_K(kb + (n) * 8 * DM * 2, vk, lk, (n) * 1024 + ((n) >> 1) * 32)
#define VLD(n) GLDS_K(vb + (((n) >> 1) * 16 + ((n) & 1) * 2) * DM * 2, vv_, lk, ST_V + (n) * 1056)
        if (vw == 1) {
            const unsigned vg = (unsigned)((lane * 16 + rot1 * 128) & 2047), lg = lbase + GW_OFF;
            GLDS_S(gatew, vg, lg, 0); GLDS_S(gatew, vg ^ 1024u, lg, 1024);
        }
        asm volatile("global_load_dwordx2 %0, %5, %7\n\tglobal_load_dwordx2 %1, %5, %7 offset:128\n\tglobal_load_dwordx2 %2, %5, %7 offset:256\n\t"
                     "global_load_dwordx2 %3, %5, %7 offset:384\n\tglobal_load_dword %4, %6, %8 sc1"
                     : "+v"(tw[0]), "+v"(tw[1]), "+v"(tw[2]), "+v"(tw[3]), "+v"(epoch) : "v"((unsigned)(dch * 8)), "v"((unsigned)WS_EPOCH), "s"(shiftk), "s"(ws) : "memory");
        if (sblk == 0 && lane < 32) *(u32x4*)(smem + ST_OFF + vw * ST_SZ + lane * 16) = (u32x4){0u, 0u, 0u, 0u};
        if (sblk != 0 || lane >= 32) KLD(0);
        KLD(1); KLD(2); KLD(3); KLD(4); KLD(5); KLD(6); KLD(7);
        if (lane < 32) KLD(8);
        __builtin_amdgcn_s_setprio(0);
    }
    asm volatile("" : "+s"(wid));

#define WSL(w) ((((w) >> 2) ^ (nb >> 3)) * 4 + ((w) & 3))
#define COFF(w, ti) ((2 * WSL(w) + ((ti) >> 1)) * 128 + ((ti) & 1) * 4096)
    bf16x8 cf_[8][4];
    bf16x8 af[DEPTH];
    int cra, crb;
#define CT_SETUP() const unsigned vB0 = (unsigned)((d_base + (lane >> 3) * 4) * 2048 + (((lane & 7) ^ (lane >> 4)) * 16)), vB1 = (vB0 ^ 64u) + 65536u - 1024u
#define CTILE(w, ti, dst) do { const int co_ = COFF(w, ti); const unsigned ds_ = (dst); \
                               GLDS4((const char*)Cre + co_, vB0, vB1, vB0, vB1, ds_, 0); GLDS4((const char*)Cim + co_, vB0, vB1, vB0, vB1, ds_, 4096); } while (0)
#define CF(ti, n) cf_[4 * ((ti) >> 1) + ((n) >> 1)][2 * ((ti) & 1) + ((n) & 1)]
#define CRD(ti, n) CF(ti, n) = RD128((((n) & 4) ? crb : cra) ^ (((n) & 2) * 16), ((n) & 1) * 2048)
#define CREAD(ti) do { _Pragma("unroll") for (int n_ = 0; n_ < 8; ++n_) CRD(ti, n_); } while (0)
#define CPIN(ti) do { PIN4(CF(ti, 0), CF(ti, 1), CF(ti, 2), CF(ti, 3)); PIN4(CF(ti, 4), CF(ti, 5), CF(ti, 6), CF(ti, 7)); } while (0)
#define CRA(base) ((base) + (q4 >> 1) * 4096 + l15 * 128 + (((q4 & 1) ^ ((l15 >> 1) & 7)) * 16))
    const int rot2 = (idx & 16) >> 2;
#define JB(u) (((((u) ^ (rot2 >> 2)) & 1) << 2) | ((wid - ((u) >> 1) + rot2) & 3))
    const __amdgpu_buffer_rsrc_t hs = __builtin_amdgcn_make_buffer_rsrc((char*)h + (size_t)tb * 524288 + WSL(wid) * 8192, 0, 524288, 0x00020000);
#define LOADF(f) do { af[(f) % DEPTH] = __builtin_bit_cast(bf16x8, __builtin_amdgcn_raw_buffer_load_b128(hs, vh + ((f) & 3) * 1024, JB((f) >> 3) * 65536 + ((f) & 4) * 1024, 16)); SCHED(); } while (0)
#define HPOLL_SETUP() gu32* hfp = (gu32*)(ws + WS_HFLAG) + (tb * 16 + 2 * WSL(wid) + (lane & 1)) * 32
#define HWAIT() do { unsigned spins = 0; \
            while (!__all(__hip_atomic_load(hfp, RLX_AGENT) == tag)) { if (++spins > SPIN_MAX) break; __builtin_amdgcn_s_sleep(PSLEEP); } asm volatile("" ::: "memory"); } while (0)

    const __amdgpu_buffer_rsrc_t gs = __builtin_amdgcn_make_buffer_rsrc(ws + WS_GATE, 0, 32768, 0x00020000);
#define GV(q4) ((unsigned)(((q4) >> 1) * 128 + ((q4) & 1) * 32))
#define GSO(r) (tb * 2048 + JB(6 + (r)) * 256 + (wid >> 2) * 16)
#define TR(r) (t_base + JB(6 + (r)) * 4 + (wid >> 2) * 2)
    u32x2 xx[2][2], dk2;
    u32x4 gv[2];
#define XLOAD() do { \
        const __amdgpu_buffer_rsrc_t xs = __builtin_amdgcn_make_buffer_rsrc((void*)x, 0, SEQ * DM * 2, 0x00020000); \
        const __amdgpu_buffer_rsrc_t ks = __builtin_amdgcn_make_buffer_rsrc((void*)dskip, 0, DM * 2, 0x00020000); \
        dk2 = __builtin_amdgcn_raw_buffer_load_b64(ks, (unsigned)(l15 * 8), d_base * 2, 0); \
        _Pragma("unroll") for (int q = 0; q < 4; ++q) \
            xx[q >> 1][q & 1] = __builtin_amdgcn_raw_buffer_load_b64(xs, (unsigned)(q4 * 32 * DM * 2 + l15 * 8 + (q & 1) * DM * 2), (TR(q >> 1) * DM + d_base) * 2, 0); \
        _Pragma("unroll") for (int r = 0; r < 2; ++r) gv[r] = __builtin_amdgcn_raw_buffer_load_b128(gs, GV(q4), GSO(r), 16); \
        SCHED(); } while (0)
#define XPIN() do { PIN4(xx[0][0], xx[0][1], xx[1][0], xx[1][1]); asm volatile("" : "+v"(dk2), "+v"(gv[0]), "+v"(gv[1])); } while (0)

    if (wid < 4) {
        typedef __attribute__((ext_vector_type(8))) unsigned u32x8;
        typedef const __attribute__((address_space(4))) u32x8 cu8;
        const int po = (p_base + wid * 16) * 2;
        u32x8 s0 = *(cu8*)((size_t)logdt + po), s1 = *(cu8*)((size_t)lre + po), s2 = *(cu8*)((size_t)lim + po);
        SCHED();
        const int pq = p_base + wid * 16 + l15;
        unsigned goff[4], gmask[4];
#pragma unroll
        for (int i = 0; i < 4; ++i) {
            goff[i] = (unsigned)(((tb - 1 - q4 - 4 * i) * 1024 + pq) * 16);
            gmask[i] = (tb - 1 - q4 - 4 * i >= 0) ? ~0u : 0u;
            asm volatile("" : "+v"(goff[i]), "+v"(gmask[i]));
        }
        unsigned gpub = q4 == 0 ? (unsigned)((tb * 1024 + pq) * 16) : 0x80000000u;
        unsigned hp = (unsigned)(tb * 524288 + (nb * 4 + wid) * 1024 + lane * 16);
        unsigned wa = (unsigned)(HT_OFF + wid * 2048 + (q4 * 16 + (l15 ^ ((q4 >> 1) * 4))) * 8);
        unsigned ra0 = (unsigned)(HT_OFF + wid * 2048 + ((q4 >> 1) * 64 + (l15 & 3) * 16 + (((q4 & 1) * 8 + (l15 >> 2)) ^ ((l15 & 2) * 2))) * 8), ra1 = ra0 ^ 32u;
        cra = CRA(GA_OFF + wid * 8192); crb = cra ^ 64;
        unsigned vB0 = (unsigned)((d_base + (lane >> 3) * 4) * 2048 + (((lane & 7) ^ (lane >> 4)) * 16)), vB1 = (vB0 ^ 64u) + 65536u - 1024u;
        PIN4(gpub, vB0, hp, wa); PIN4(ra0, ra1, cra, crb); asm volatile("" : "+v"(vB1));
        f32x4 acc[8][2];
#pragma unroll
        for (int m = 0; m < 8; m += 2) {
#pragma unroll
            for (int c = 0; c < 4; ++c) acc[m + (c >> 1)][c & 1] = (f32x4){0.f, 0.f, 0.f, 0.f};
            PIN4(acc[m][0], acc[m][1], acc[m + 1][0], acc[m + 1][1]);
        }
        SCHED();
        cf L1, Lq3, cc, L32, cLq, Lp, L512;
        {
            unsigned w0 = 0, w1 = 0, w2 = 0;
#pragma unroll
            for (int i = 0; i < 8; ++i) {
                asm("v_writelane_b32 %0, %3, %6\n\tv_writelane_b32 %1, %4, %6\n\tv_writelane_b32 %2, %5, %6" : "+v"(w0), "+v"(w1), "+v"(w2) : "s"(s0[i]), "s"(s1[i]), "s"(s2[i]), "i"(2 * i));
            }
            asm("s_nop 1" : "+v"(w0), "+v"(w1), "+v"(w2));
            auto spread = [&](unsigned w) {
                const unsigned e = (unsigned)__builtin_amdgcn_update_dpp(0, (int)w, 0xa0, 0xf, 0xf, false);
                const unsigned a = __builtin_amdgcn_permlane16_swap(e, e, false, false)[0];
                const unsigned c = __builtin_amdgcn_permlane32_swap(a, a, false, false)[0];
                return (l15 & 1) ? hi16(c) : lo16(c);
            };
            const float ldt = spread(w0), re = spread(w1), im = spread(w2);
            if (wid == 3 && lane < NFLAGW) *(unsigned*)(smem + UFLAG_OFF + lane * 4) = 0u;
            const float dt = rbf(expf(ldt));
            const float ea = expf(dt * re);
            float sn, cs;
            sincosf(dt * im, &sn, &cs);
            L1 = {ea * cs, ea * sn};
            const float nr = L1.r - 1.0f, ni = L1.i, den = re * re + im * im;
            cc = {2.f * ((nr * re + ni * im) / den), 2.f * ((ni * re - nr * im) / den)};
            const cf T2 = cmul(L1, L1), T4 = cmul(T2, T2), L8 = cmul(T4, T4), L16 = cmul(L8, L8);
            L32 = cmul(L16, L16);
            const cf L64 = cmul(L32, L32), L96 = cmul(L64, L32), L128 = cmul(L64, L64), L256 = cmul(L128, L128), L384 = cmul(L256, L128);
            L512 = cmul(L256, L256);
            Lp = pick4(q4, L128, L256, L384);
            Lq3 = pick4(3 - q4, L32, L64, L96);
            cLq = cmul(cc, pick4(q4, L32, L64, L96));
            PINC(L1); PINC(Lq3); PINC(cc); PINC(L32); PINC(cLq); PINC(Lp); PINC(L512);
        }
        __builtin_amdgcn_s_setprio(PRIO_G);
        unsigned tagv;
        cf S;
        STAMPT(dbg, 22, 0);
        const int swz = (l15 >> 1) & 7;
        {
            const int a0 = RA(0) + l15 * 128 + ((q4 ^ swz) * 16);
            const int b0 = RB(0) + ((wid >> 1) * 64 + (wid & 1) * 16 + l15) * 128 + ((q4 ^ swz) * 16);
            WAIT_LGKM0();
            BAR();
            asm volatile("" ::: "memory");
            STAMPT(dbg, 19, 64);
            bf16x8 FA[8][2], FB[2][2][2];
            int pa[2] = {a0, a0 ^ 64}, pb[2] = {b0, b0 ^ 64};
            PIN4(pa[0], pa[1], pb[0], pb[1]);
#define LGW(n) __builtin_amdgcn_s_waitcnt(0xC07F | ((n) << 8))
#define RA_(j) FA[(j) >> 1][(j) & 1] = RD128(pa[(j) & 1], ((j) >> 1) * 2048)
#define RB_(bu, j) FB[bu][(j) >> 1][((j) ^ ((j) >> 1)) & 1] = RD128(pb[((j) ^ ((j) >> 1)) & 1], ((j) >> 1) * 4096)
#define RN_(bu, i) do { if ((i) == 0) RB_(bu, 0); else if ((i) < 3) RA_((i) - 1); else if ((i) < 6) RB_(bu, (i) - 2); else RA_((i) - 4); } while (0)
#define G1_H(H, bu, RD, FILL) do { \
            _Pragma("unroll") for (int i = 0; i < 16; ++i) { \
                const int m = (H) * 4 + (i >> 2), n = ((i >> 1) ^ m) & 1, ks = (i ^ (i >> 1)) & 1; \
                if ((H) == 0) { if (i == 0) LGW(6); if (i == 4) LGW(8); if (i == 8) LGW(10); if (i == 12) LGW(8); } \
                SCHED(); \
                MF(acc[m][n], FA[m][ks], FB[bu][n][ks]); SCHED(); \
                if ((H) == 0 && i < 8) RA_(8 + i); \
                if ((H) == 1 && (RD) && i < 12) RN_((bu) ^ 1, i); \
                FILL((H) * 16 + i); \
                SCHED(); \
            } } while (0)
#define NOF(g) do { } while (0)
#pragma unroll
            for (int j = 0; j < 12; ++j) { RN_(0, j); SCHED(); }
            unsigned long long ua_ = UNITS_A;
            int sb = RB(0), sa = RA(0), nx, na;
#define ADV(g) do { if ((g) == 8) { ua_ >>= 4; na = (int)((unsigned)ua_ & 15u) << 14; asm volatile("" : "+s"(na), "+s"(ua_)); } \
                    if ((g) == 9) { const int o_ = sa; sa = na; na -= o_; asm volatile("" : "+s"(na), "+s"(sa)); } \
                    if ((g) == 10) { pa[0] += na; pa[1] += na; asm volatile("" : "+v"(pa[0]), "+v"(pa[1])); } \
                    if ((g) == 11) { nx = (sb == 65536) ? -49152 : 16384; sb += nx; asm volatile("" : "+s"(nx), "+s"(sb)); } \
                    if ((g) == 13) { pb[0] += nx; pb[1] += nx; asm volatile("" : "+v"(pb[0]), "+v"(pb[1])); } } while (0)
#define G1_T(bu) do { \
                G1_H(0, bu, true, ADV); \
                WAIT_LGKM0(); \
                asm volatile("" ::: "memory"); \
                BAR();                     \
                asm volatile("" ::: "memory"); \
                G1_H(1, bu, true, NOF); } while (0)
#pragma unroll 1
            for (int t2 = 0; t2 < 7; ++t2) { G1_T(0); G1_T(1); }
            G1_T(0);
#define AGG(g) do { if ((g) == 8) { asm volatile("" ::: "memory"); SETW(G1D_OFF, wid, 1u); }        \
                    if ((g) == 9) tagv = *(volatile const lds_u32*)(size_t)(unsigned)TAG_OFF; \
                    if ((g) >= 7) { const int t = (g) - 6; \
                        if (t == 1) { asm volatile("s_nop 1" : "+v"(acc[0][0]), "+v"(acc[0][1])); S = {acc[0][0][0], acc[0][1][0]}; } \
                        if ((t & 3) == 0) asm volatile("" : "+v"(acc[t >> 2][0]), "+v"(acc[t >> 2][1])); \
                        S = cfma(L1, S, cf{acc[t >> 2][0][t & 3], acc[t >> 2][1][t & 3]}); PINC(S); } } while (0)
            G1_H(0, 1, false, AGG);
            G1_H(1, 1, false, AGG);
        }
        STAMPT(dbg, 1, 0);
        const unsigned tag = __builtin_amdgcn_readfirstlane(tagv);
        const __amdgpu_buffer_rsrc_t grs = __builtin_amdgcn_make_buffer_rsrc(ws, 0, 15 * 16384, 0x00020000);
        {
#pragma unroll
            for (int t = 26; t < 32; ++t) {
                if (t == 28) { PINC(S); asm volatile("" : "+v"(acc[7][0]), "+v"(acc[7][1])); }
                S = cfma(L1, S, cf{acc[t >> 2][0][t & 3], acc[t >> 2][1][t & 3]});
            }
            const cf W = cmul(S, Lq3);
            const float e2 = red16(red32(W.r, W.i));
            const auto eo = __builtin_amdgcn_permlane32_swap(__float_as_uint(e2), __float_as_uint(e2), false, false);
            __builtin_amdgcn_raw_buffer_store_b128((u32x4){eo[0], tag, eo[1], tag}, grs, gpub, 0, 16);
        }
        SCHED();
        STAMPT(dbg, 2, 0);
        const unsigned cg = lbase + GA_OFF + wid * 8192;
        wait_lds1(smem, COK_OFF + wid * 4);
        CREAD(0);
        WAIT_LGKM0();
        CPIN(0);
        CTILE(wid, 1, cg);
        u32x4 gq[4];
#pragma unroll
        for (int m = 0; m < 8; ++m) {
            const f32x4 R = acc[m][0], I = acc[m][1];
            acc[m][0] = fma4(splat4(cc.r), R, mul4(splat4(-cc.i), I));
            acc[m][1] = fma4(splat4(-cc.r), I, mul4(splat4(-cc.i), R));
            asm volatile("" : "+v"(acc[m][0]), "+v"(acc[m][1]));
            if (m == 3) {
                SCHED();
#pragma unroll
                for (int i = 0; i < 4; ++i) asm volatile("buffer_load_dwordx4 %0, %1, %2, 0 offen sc1" : "=&v"(gq[i]) : "v"(goff[i]), "s"(grs) : "memory");
                SCHED();
            }
        }
        cf Ec;
        {
            const auto sr = __builtin_amdgcn_permlane16_swap(__float_as_uint(S.r), __float_as_uint(S.r), false, false);
            const auto si = __builtin_amdgcn_permlane16_swap(__float_as_uint(S.i), __float_as_uint(S.i), false, false);
            const cf Se = {__uint_as_float(sr[0]), __uint_as_float(si[0])}, So = {__uint_as_float(sr[1]), __uint_as_float(si[1])};
            const cf A = cfma(L32, Se, So);
            const auto ar = __builtin_amdgcn_permlane32_swap(__float_as_uint(A.r), __float_as_uint(A.r), false, false);
            const auto ai = __builtin_amdgcn_permlane32_swap(__float_as_uint(A.i), __float_as_uint(A.i), false, false);
            const cf A01 = {(q4 & 2) ? __uint_as_float(ar[0]) : 0.f, (q4 & 2) ? __uint_as_float(ai[0]) : 0.f};
            const cf kq = {(q4 & 1) ? L32.r : 1.f, (q4 & 1) ? L32.i : 0.f};
            const cf E = cfma(kq, A01, cf{(q4 & 1) ? Se.r : 0.f, (q4 & 1) ? Se.i : 0.f});
            Ec = cmul(cc, E);
            PINC(Ec);
        }
        WAIT_VM(4);
        CREAD(1);
        WAIT_LGKM0();
        CPIN(1);
        CTILE(wid, 2, cg);
        asm volatile("s_waitcnt vmcnt(8)" : "+v"(gq[0]), "+v"(gq[1]), "+v"(gq[2]), "+v"(gq[3]) :: "memory");
        STAMPT(dbg, 27, 0);
        cf carry;
        {
            unsigned spins = 0;
            for (;;) {
                unsigned bad = 0;
#pragma unroll
                for (int i = 0; i < 4; ++i) bad |= ((gq[i].y ^ tag) | (gq[i].w ^ tag)) & gmask[i];
                if (__all(bad == 0u)) break;
                if (++spins > SPIN_MAX) break;
#pragma unroll
                for (int i = 0; i < 4; ++i) asm volatile("buffer_load_dwordx4 %0, %1, %2, 0 offen sc1" : "+v"(gq[i]) : "v"(goff[i]), "s"(grs) : "memory");
                asm volatile("s_waitcnt vmcnt(0)" : "+v"(gq[0]), "+v"(gq[1]), "+v"(gq[2]), "+v"(gq[3]) :: "memory");
            }
            cf E = {__uint_as_float(gq[3].x), __uint_as_float(gq[3].z)};
#pragma unroll
            for (int i = 2; i >= 0; --i) E = cfma(L512, E, cf{__uint_as_float(gq[i].x), __uint_as_float(gq[i].z)});
            E = cmul(E, Lp);
            const float e2 = red16(red32(E.r, E.i));
            const auto eo = __builtin_amdgcn_permlane32_swap(__float_as_uint(e2), __float_as_uint(e2), false, false);
            carry = {__uint_as_float(eo[0]), __uint_as_float(eo[1])};
        }
        STAMPT(dbg, 3, 0);
        {
            const __amdgpu_buffer_rsrc_t hrs = __builtin_amdgcn_make_buffer_rsrc(h, 0, SEQ * 2048 * 2, 0x00020000);
            const cf e = cfma(cLq, carry, Ec);
            float hr = e.r, hn = -e.i;
            s16x4 fr[2][4];
#define HSTORE(m) do { _Pragma("unroll") for (int c = 0; c < 2; ++c) \
                __builtin_amdgcn_raw_buffer_store_b128(__builtin_bit_cast(u32x4, __builtin_shufflevector(fr[(m) & 1][2 * c], fr[(m) & 1][2 * c + 1], 0, 1, 2, 3, 4, 5, 6, 7)), hrs, hp, (2 * (m) + c) * 65536, WT); } while (0)
#pragma unroll
            for (int m = 0; m < 4; ++m) {
#pragma unroll
                for (int j = 0; j < 8; ++j) {
                    const float nr = fmaf(L1.r, hr, fmaf(L1.i, hn, acc[2 * m + (j >> 2)][0][j & 3]));
                    const float nn = fmaf(L1.r, hn, fmaf(-L1.i, hr, acc[2 * m + (j >> 2)][1][j & 3]));
                    hr = nr; hn = nn;
                    acc[2 * m + (j >> 2)][0][j & 3] = nr; acc[2 * m + (j >> 2)][1][j & 3] = nn;
                }
                u32x2 pk[4];
#pragma unroll
                for (int c = 0; c < 4; ++c) { const f32x4 v = acc[2 * m + (c >> 1)][c & 1]; pk[c] = (u32x2){pkbf(v[0], v[1]), pkbf(v[2], v[3])}; }
                PIN4(pk[0], pk[1], pk[2], pk[3]);
                SCHED();
#pragma unroll
                for (int c = 0; c < 4; ++c) { *(volatile __attribute__((address_space(3))) u32x2*)(size_t)(wa + c * 512) = pk[c]; SCHED(); }
                asm volatile("" ::: "memory");
#pragma unroll
                for (int c = 0; c < 2; ++c) { fr[m & 1][2 * c] = TRD(ra0 + c * 1024); SCHED(); fr[m & 1][2 * c + 1] = TRD(ra1 + c * 1024); SCHED(); }
                asm volatile("" ::: "memory");
                if (m > 0) HSTORE(m - 1);
                SCHED();
            }
            HSTORE(3);
        }
        STAMPT(dbg, 4, 0);
        {
            WAIT_VM(8);
            CREAD(2);
            WAIT_VM(0);
            {
                unsigned n;
                asm volatile("s_mov_b64 exec, 1\n\tds_add_rtn_u32 %0, %1, %2\n\ts_mov_b64 exec, -1\n\ts_waitcnt lgkmcnt(0)" : "=&v"(n) : "v"(MISC_OFF), "v"(1u) : "memory");
                if (__builtin_amdgcn_readfirstlane(n) == 3u) {
                    if (lane == 0) __hip_atomic_store((gu32*)(ws + WS_HFLAG) + (tb * 16 + nb) * 32, tag, RLX_AGENT);
                    if (DBGON && lane == 0) dbg[BIDX * 64 + 20] = __builtin_amdgcn_s_memrealtime();
                }
            }
            STAMPT(dbg, 12, 0);
            __builtin_amdgcn_s_setprio(0);
            WAIT_LGKM0();
            CPIN(2);
            CTILE(wid, 3, cg);
            XLOAD();
            HPOLL_SETUP();
            HWAIT();
            STAMPT(dbg, 8, 0);
            const unsigned vh = (unsigned)(lane * 16);
#pragma unroll
            for (int f = 0; f < DEPTH; ++f) LOADF(f);
            __builtin_amdgcn_s_setprio(PRIO2_G);
        }
    } else {
        const int lw = wid - 4;
        const int d = cb * 4 + lw;
        const char* Krow = (const char*)(ssmk + (size_t)d * SEQ);
        cra = CRA(LA_OFF + lw * 8192); crb = cra ^ 64;
        const unsigned cst0 = lbase + LA_OFF + lw * 8192;
        CT_SETUP();
        asm volatile("" : "+v"(cra), "+v"(crb));
        u32x4 KA[4];
        unsigned draw;
        unsigned tag;
        float gacc = 0.f;
#define KLOAD() asm volatile("global_load_dwordx4 %0, %5, %7 nt\n\tglobal_load_dwordx4 %1, %5, %7 offset:1024 nt\n\tglobal_load_dwordx4 %2, %5, %7 offset:2048 nt\n\t" \
                             "global_load_dwordx4 %3, %5, %7 offset:3072 nt\n\tglobal_load_ushort %4, %6, %8" \
                             : "=&v"(KA[0]), "=&v"(KA[1]), "=&v"(KA[2]), "=&v"(KA[3]), "=&v"(draw) : "v"((unsigned)(lane * 16)), "v"((unsigned)(d * 2)), "s"(Krow), "s"(ssmD) : "memory")
#define KWAIT(n) asm volatile("s_waitcnt vmcnt(%5)" : "+v"(KA[0]), "+v"(KA[1]), "+v"(KA[2]), "+v"(KA[3]), "+v"(draw) : "i"(n) : "memory")
        if (wid < 6) {
            STAGE_A(2, lw0 + RA(2)); STAGE_B(2, lw0 + RB(2));
            WAIT_VM(32);
            STAMPT(dbg, 17, 320);
            BAR();
            STAGE_B(3, lw0 + RB(3));
            unsigned long long ua_ = UNITS_A >> 12;
            unsigned sla = lw0 + RA(3), slb = lw0 + RB(4);
            int ka = ((3 + rot1) & 15) * 128, kb_ = ka;
            const char* sa = (const char*)x + ka;
            asm volatile("" : "+s"(sa), "+s"(sla), "+s"(ka), "+s"(ua_));
#define NEXT_A() do { ka = (ka + 128) & 2047; sa = (const char*)x + ka; ua_ >>= 4; sla = lw0 + (((unsigned)ua_ & 15u) << 14); asm volatile("" : "+s"(sa), "+s"(sla), "+s"(ka), "+s"(ua_)); } while (0)
#define LSTEP(n) do { \
                WAIT_VM(n); \
                BAR(); \
                GLDS4(sa, voA[0], voA[1], voA[2], voA[3], sla, 0); \
                GLDS4(sa + 16384, voA[0], voA[1], voA[2], voA[3], sla, 4096); \
                kb_ = (kb_ + 128) & 2047; \
                GLDS4((const char*)Bre + kb_, voB[0], voB[1], voB[2], voB[3], slb, 0); \
                NEXT_A(); \
                const unsigned slb0 = slb; \
                slb = (slb == lw0 + 65536) ? lw0 + 16384 : slb + 16384; \
                asm volatile("" : "+s"(slb), "+s"(kb_), "+s"(t)); \
                GLDS4((const char*)Bim + kb_, voB[0], voB[1], voB[2], voB[3], slb0, 4096); } while (0)
#pragma unroll 1
            for (int t = 1; t < 5; ++t) LSTEP(24);
            GLDS4(sa, voA[0], voA[1], voA[2], voA[3], sla, 0); GLDS4(sa + 16384, voA[0], voA[1], voA[2], voA[3], sla, 4096);
            NEXT_A();
#pragma unroll 1
            for (int t = 5; t < 13; ++t) LSTEP(32);
            KLOAD();
            WAIT_VM(37);
            BAR();
            __builtin_amdgcn_s_setprio(0);
            tag = lds_word(smem, TAG_OFF);
            WAIT_VM(21);
            BAR();
            CTILE(lw, 0, cst0 + GA_OFF);
            FLAGD(COK_OFF + lw * 4);
            WAIT_VM(14);
            BAR();
            WAIT_VM(9);
        } else {
            const int vw = wid - 6;
            const int dch = dblk * 128 + vw * 64 + l15;
            const unsigned lk = lbase + ST_OFF + vw * ST_SZ;
            const unsigned vv_ = (unsigned)(((lane >> 4) * 4 + ((lane >> 3) & 1)) * DM * 2 + (lane & 7) * 16);
            const char* vb = (const char*)vin + (size_t)(sblk * 64) * (DM * 2) + dblk * 256 + vw * 128;
            const int grow = nb * 8 + vw * 4 + q4;
            unsigned gx0 = (unsigned)(grow * 128 + (((l15 >> 1) ^ ((grow >> 1) & 7)) * 16) + (l15 & 1) * 8), gx1 = gx0 + RA(2), gwa = (unsigned)(GW_OFF + l15 * 8);
            asm volatile("" : "+v"(gx0), "+v"(gx1), "+v"(gwa));
            u32x2 gxv, gwv;
            typedef const volatile __attribute__((address_space(3))) u32x2* l2p;
#define VBAR(t) do { SCHED(); asm volatile("" ::: "memory"); BAR(); asm volatile("" ::: "memory"); \
                     gxv = *(l2p)(size_t)(RA(t) == 0 ? gx0 : gx1 + (RA(t) - RA(2))); gwv = *(l2p)(size_t)(gwa + (t) * 128); SCHED(); } while (0)
#define GATE() do { SCHED(); gacc = fmaf(lo16(gxv.x), lo16(gwv.x), gacc); gacc = fmaf(hi16(gxv.x), hi16(gwv.x), gacc); \
                    gacc = fmaf(lo16(gxv.y), lo16(gwv.y), gacc); gacc = fmaf(hi16(gxv.y), hi16(gwv.y), gacc); asm volatile("" : "+v"(gacc)); SCHED(); } while (0)
            unsigned ka[4][2], sel[4];
            const int R0 = 4 + (l15 >> 2) * 16 + (l15 & 3) - (q4 >> 1);
            unsigned kr0 = (unsigned)(2 * R0), kr1 = (unsigned)(ST_OFF + vw * ST_SZ + R0 * 128 + (((q4 ^ (R0 >> 1)) & 1) * 16));
            asm volatile("" : "+v"(kr0), "+v"(kr1));
#pragma unroll
            for (int i = 0; i < 4; ++i)
#pragma unroll
                for (int tp = 0; tp < 2; ++tp) {
                    ka[i][tp] = (kr1 ^ (tp * 16)) + ((kr0 + 8 * i - 4 * tp) & 0xe0) + (4 * i - 2 * tp) * 128;
                    asm volatile("" : "+v"(ka[i][tp]));
                }
#pragma unroll
            for (int r = 0; r < 4; ++r) {
                const unsigned pick = (q4 & 2) ? 0x0302u : 0x0100u;
                sel[r] = ((l15 >> 1) == (q4 & 1) * 4 + r) ? ((l15 & 1) ? (pick << 16) | 0x0c0cu : 0x0c0c0000u | pick) : 0x0c0c0c0cu;
            }
            unsigned va_ = (unsigned)(ST_OFF + vw * ST_SZ + ST_V + (q4 * 2 + (l15 >> 3)) * 1056 + ((l15 >> 2) & 1) * 128 + (l15 & 3) * 8);
            unsigned so = (unsigned)(((dch >> 2) * 32 + sblk) * 512 + q4 * 64 + (dch & 3) * 16);
            PIN4(sel[0], sel[1], sel[2], sel[3]); asm volatile("" : "+v"(so), "+v"(va_));
            asm volatile("s_waitcnt vmcnt(9)" : "+v"(tw[0]), "+v"(tw[1]), "+v"(tw[2]), "+v"(tw[3]), "+v"(epoch) :: "memory");
            STAMPT(dbg, 18, 384);
            VBAR(0);
            bf16x8 wf[4][2];
#pragma unroll
            for (int ps = 0; ps < 4; ++ps)
#pragma unroll
                for (int tp = 0; tp < 2; ++tp) {
                    u32x4 w;
#pragma unroll
                    for (int r = 0; r < 4; ++r) w[r] = __builtin_amdgcn_perm(0u, tw[ps][tp], sel[r]);
                    wf[ps][tp] = __builtin_bit_cast(bf16x8, w);
                    asm volatile("" : "+v"(wf[ps][tp]));
                }
            SCHED();
            VLD(0); VLD(1); VLD(2); VLD(3); VLD(4); VLD(5); VLD(6); VLD(7);
            GATE(); VBAR(1);
            f32x4 ks[4][4];
            auto sconv = [&](int i0) __attribute__((always_inline)) {
                bf16x8 kf[2][4][2];
#pragma unroll
                for (int q = 0; q < 16; ++q) { kf[q >> 3][q & 3][(q >> 2) & 1] = RD128(ka[i0 + (q >> 3)][(q >> 2) & 1], (q & 3) * 32); SCHED(); }
                GATE();
                __builtin_amdgcn_s_setprio(PRIO_KV);
                WAIT_LGKM0();
                SCHED();
#pragma unroll
                for (int i = 0; i < 2; ++i) {
#pragma unroll
                    for (int ps = 0; ps < 4; ++ps) MF0(ks[ps][i0 + i], kf[i][ps][0], wf[ps][0]);
#pragma unroll
                    for (int ps = 0; ps < 4; ++ps) MF(ks[ps][i0 + i], kf[i][ps][1], wf[ps][1]);
                }
                __builtin_amdgcn_s_setprio(0);
                SCHED();
            };
            WAIT_VM(8);
            STAMPT(dbg, 21, 384);
            sconv(0); VBAR(2);
            sconv(2); VBAR(3);
            WAIT_VM(0);
            u32x2 vq[4][4];
#pragma unroll
            for (int ps = 0; ps < 4; ++ps)
#pragma unroll
                for (int j = 0; j < 4; ++j) vq[ps][j] = __builtin_bit_cast(u32x2, TRD(va_ + j * 256 + ps * 32));
            const __amdgpu_buffer_rsrc_t rs = __builtin_amdgcn_make_buffer_rsrc(kvT, 0, DM * SEQ * 2, 0x00020000);
            u32x4 o[8];
            auto kvout = [&](int n) __attribute__((always_inline)) {
                const int hh = n >> 2, ps = n & 3;
                asm volatile("" : "+v"(ks[ps][2 * hh]), "+v"(ks[ps][2 * hh + 1]));
#pragma unroll
                for (int e = 0; e < 4; ++e) {
                    const unsigned vv = vq[ps][2 * hh + (e >> 1)][e & 1];
                    o[n][e] = pkbf(ks[ps][2 * hh + (e >> 1)][2 * (e & 1)] * lo16(vv), ks[ps][2 * hh + (e >> 1)][2 * (e & 1) + 1] * hi16(vv));
                }
                asm volatile("" : "+v"(o[n]));
                SCHED();
            };
            GATE(); kvout(0); kvout(1);
            WAIT_LGKM0();
            VBAR(4);
            kvout(2); kvout(3); GATE(); VBAR(5);
            kvout(4); kvout(5); GATE(); VBAR(6);
            kvout(6); kvout(7); GATE(); VBAR(7);
#pragma unroll
            for (int n = 0; n < 8; ++n) __builtin_amdgcn_raw_buffer_store_b128(o[(n >> 1) + (n & 1) * 4], rs, so + (unsigned)((n & 1) * 256), (n >> 1) * 65536, WT);
            SCHED();
            asm volatile("s_nop 1" ::: "memory");
            GATE(); VBAR(8);
            if (vw == 1 && lane == 0) *(unsigned*)(smem + TAG_OFF) = (epoch + 1u) ? epoch + 1u : 1u;
            GATE();
            WAIT_LGKM0();
            WAIT_VM(0);
            VBAR(9);
            tag = __builtin_amdgcn_readfirstlane(*(const unsigned*)(smem + TAG_OFF));
            if (vw == 0 && lane == 0) __hip_atomic_store((gu32*)(ws + WS_KVFLAG) + (dblk * 32 + sblk) * 32, tag, RLX_AGENT);
            if (vw == 1 && lane < 4) *(u32x4*)(smem + U_OFF + 192 + lane * 16) = (u32x4){0u, 0u, 0u, 0u};
            gu32* kfp = (gu32*)(ws + WS_KVFLAG) + ((cb >> 5) * 32 + vw * 16 + l15) * 32;
            const char* ub = (const char*)kvT + (size_t)__builtin_amdgcn_readfirstlane(cb * 16384 + vw * 8192);
            const unsigned vu = (unsigned)((lane >> 5) * 512 + ((lane >> 2) & 1) * 256 + ((lane >> 3) & 3) * 64 + ((lane - (lane >> 4)) & 3) * 16);
            const unsigned lu = lbase + U_OFF + 256 + vw * 8192;
#define U_DMA1(o) asm volatile("s_add_u32 m0, %2, %3\n\ts_nop 0\n\tglobal_load_lds_dwordx4 %0, %1 sc1\n\tglobal_load_lds_dwordx4 %0, %1 offset:1024 sc1\n\t" \
                             "global_load_lds_dwordx4 %0, %1 offset:2048 sc1\n\tglobal_load_lds_dwordx4 %0, %1 offset:3072 sc1" \
                             :: "v"(vu), "s"(ub + (o)), "s"(lu), "i"(o) : "memory", "scc")
#define U_DMA() do { U_DMA1(0); U_DMA1(4096); FLAGD(UFLAG_OFF + lw * 4); } while (0)
            GATE(); VBAR(10);
            TOUCH((const char*)(vw ? Cim : Cre) + (size_t)b * 8192, (unsigned)(lane * 128));
            GATE(); VBAR(11);
            unsigned pv;
            asm volatile("global_load_dword %0, %1, off sc1" : "=&v"(pv) : "v"(kfp) : "memory");
            GATE(); VBAR(12);
            GATE(); VBAR(13);
            asm volatile("s_waitcnt vmcnt(0)" : "+v"(pv) :: "memory");
            const bool done = __all(pv == tag);
            KLOAD();
            if (done) U_DMA();
            GATE(); VBAR(14);
            GATE(); VBAR(15);
            if (DBGON && TIDX == 384) dbg[BIDX * 64 + 13] = (done ? 100ull : 0ull) | (1ull << 62);
            GATE();
            if (lane == 0) SETW(UFLAG_OFF, vw, 1u);
            CTILE(lw, 0, cst0 + GA_OFF);
            FLAGD(COK_OFF + lw * 4);
            if (!done) {
                unsigned spins = 0;
                for (;;) {
                    const unsigned v = __hip_atomic_load(kfp, RLX_AGENT);
                    if (__all(v == tag)) break;
                    if (++spins > SPIN_MAX) break;
                    __builtin_amdgcn_s_sleep(1);
                }
                U_DMA();
            }
        }
        KWAIT(18);
        STAMPT(dbg, 14, 320);
        __builtin_amdgcn_s_sleep(6);
        {
            const unsigned cp = (unsigned)(KC_OFF + lw * CV_W + 64 + lane * 16);
            if (lane == 0) KA[0].x = (KA[0].x & 0xffff0000u) | f2bf(lo16(KA[0].x) + bf2f((u16)draw));
#define WR128(v, off) asm volatile("ds_write_b128 %0, %1 offset:%2" :: "v"(cp), "v"(v), "i"(off) : "memory")
#pragma unroll
            for (int q = 3; q >= 0; --q) WR128(KA[q], q * 1024);
#pragma unroll
            for (int q = 3; q >= 0; --q) {
                const u32x2 p = q ? pred2(KA[q], RL63(KA[q ? q - 1 : 0].z), RL63(KA[q ? q - 1 : 0].w)) : pred2(KA[0], 0u, 0u);
                const u32x4 a = KA[q];
                const u32x4 s1 = {al(a.x, p.y), al(a.y, a.x), al(a.z, a.y), al(a.w, a.z)};
                WR128(s1, q * 1024 + CV_COPY);
                const u32x4 s2 = {p.y, mv(a.x), mv(a.y), mv(a.z)};
                WR128(s2, q * 1024 + 2 * CV_COPY);
                const u32x4 s3 = {al(p.y, p.x), mv(s1.x), mv(s1.y), mv(s1.z)};
                WR128(s3, q * 1024 + 3 * CV_COPY);
            }
            if (lane < 16) *(u32x4*)(smem + KC_OFF + lw * CV_W + (lane >> 2) * CV_COPY + (lane & 3) * 16) = (u32x4){0u, 0u, 0u, 0u};
            SCHED();
        }
        STAMPT(dbg, 26, 320);
        wait_lds4(smem, G1D_OFF);
        wait_lds4(smem, UFLAG_OFF);
        STAMPT(dbg, 9, 256);
        __builtin_amdgcn_s_setprio(PRIO_CV);
        STAMPT(dbg, 10, 384);
        f32x4 cv[8];
#pragma unroll
        for (int g = 0; g < 8; ++g) cv[g] = (f32x4){0.f, 0.f, 0.f, 0.f};
        PIN4(cv[0], cv[1], cv[2], cv[3]); PIN4(cv[4], cv[5], cv[6], cv[7]);
        const int uz = U_OFF + 192;
        const int ua0 = U_OFF + 256 + (2 * l15 + q4 - 2) * 64, rv0 = (2 * l15 + q4 - 2) * 4 + lw * 16;
        const unsigned ka0 = (unsigned)((l15 >> 2) * CV_COPY - 16 * q4 + 8 * (l15 & 3) - 8);
        int ua, rv, pu, pz;
        unsigned adr;
#define UNEXT1() do { pu = ua | (rv & 0x30); asm volatile("" : "+v"(pu)); } while (0)
#define UNEXT2() do { pz = (ua > uz ? ua : uz) | (rv & 0x30); asm volatile("" : "+v"(pz)); } while (0)
#define UNEXT3() do { ua -= 256; rv -= 16; asm volatile("" : "+v"(ua), "+v"(rv)); } while (0)
#define ANEXT() do { adr += 64; asm volatile("" : "+v"(adr)); } while (0)
#define RDU(dst, c) do { dst = (c) ? RD128(pu, (c) * 2048) : RD128(pz, 0); SCHED(); } while (0)
        HPOLL_SETUP();
        unsigned hpv = 0;
        {
            bf16x8 A[8], UB[8];
            ua = ua0; rv = rv0; adr = lbase + KC_OFF + lw * CV_W + ka0 + 96;
            asm volatile("" : "+v"(adr));
            UNEXT1(); UNEXT2(); UNEXT3();
#pragma unroll
            for (int i = 0; i < 8; ++i) { RDA(A[7 - i], adr, 7 - i); RDU(UB[i], i); }
            unsigned gb = lw >= 2 ? (unsigned)((tb * 16 + nb) * 128 + (lw - 2) * 32) : 0x80000000u;
            asm volatile("" : "+s"(gb));
            const unsigned gso = (l15 == 0) ? gb + (unsigned)(q4 * 8) : 0x80000000u;
            float gt = 0.f;
            unsigned vxo = 0, vdo = 0, vgo = 0;
            auto gate_out = [&](int i) __attribute__((always_inline)) {
                if (i == 0) gacc += __uint_as_float(__builtin_amdgcn_update_dpp(0u, __float_as_uint(gacc), 0xb1, 0xf, 0xf, false));
                if (i == 1) gacc += __uint_as_float(__builtin_amdgcn_update_dpp(0u, __float_as_uint(gacc), 0x4e, 0xf, 0xf, false));
                if (i == 2) gacc += __uint_as_float(__builtin_amdgcn_update_dpp(0u, __float_as_uint(gacc), 0x141, 0xf, 0xf, false));
                if (i == 3) gacc += __uint_as_float(__builtin_amdgcn_update_dpp(0u, __float_as_uint(gacc), 0x140, 0xf, 0xf, false));
                if (i == 4) gacc = rbf(gacc);
                if (i == 5) gacc = __builtin_amdgcn_exp2f(gacc * -1.44269504088896f);
                if (i == 6) gacc = __builtin_amdgcn_rcpf(1.0f + gacc);
                if (i == 7) gacc = rbf(gacc);
                if (i == 8) gt = __uint_as_float(pkbf(gacc, 1.0f - gacc));
                if (i == 9) __builtin_amdgcn_raw_buffer_store_b64((u32x2){__float_as_uint(gt), tag}, gs, gso, 0, WT);
                asm volatile("" : "+v"(gacc), "+v"(gt));
            };
            auto step = [&](auto E) __attribute__((always_inline)) {
                constexpr int e = decltype(E)::value;
                constexpr int ti = (e == CS0) ? 0 : (e == CS1) ? 1 : (e == CS2) ? 2 : -1, tj = ti < 0 ? 0 : ti;
                constexpr unsigned char GA[36] = {0x66, 0x77, 0x76, 0x44, 0x55, 0x54, 0x65, 0x64, 0x75, 0x74, 0x22, 0x33, 0x32, 0x43, 0x42, 0x53, 0x52, 0x63, 0x62, 0x73, 0x72,
                                                  0x00, 0x11, 0x10, 0x21, 0x20, 0x31, 0x30, 0x41, 0x40, 0x51, 0x50, 0x61, 0x60, 0x71, 0x70};
                const char* sb_ = (const char*)Cre + COFF(wid, tj + 1);
                const char* sc_ = (const char*)Cim + COFF(wid, tj + 1);
#pragma unroll
                for (int q = 0; q < 36; ++q) {
                    const int g = GA[q] >> 4, a = GA[q] & 15;
                    MF(cv[g], A[a], UB[g - a]); SCHED();
                    if (e < 7) {
                        if (a == 1) RDU(UB[g - 1], g - 1);
                        if (q == 35) RDU(UB[7], 7);
                        if (g == 7) RDA(A[a], adr + 64 * (e + 1), a);
                        if (q == 11) UNEXT1();
                        if (q == 13) UNEXT2();
                        if (q == 15) UNEXT3();
                    }
                    if (ti >= 0) {
                        const int n = (q >= 3 && q <= 7) ? q - 3 : q == 10 ? 5 : q == 12 ? 6 : q == 14 ? 7 : -1;
                        if (q == 3) { if (ti == 0) WAIT_VM(1); else if (e == XE + 1) WAIT_VM(7); else WAIT_VM(0); }
                        if (n >= 0) CRD(tj, n & 7);
                        if (q == 18) CPIN(tj);
                        if (q == 18 || q == 23) asm volatile("s_add_u32 m0, %3, %4\n\ts_nop 0\n\tglobal_load_lds_dwordx4 %0, %2\n\tglobal_load_lds_dwordx4 %1, %2 offset:1024"
                                                             :: "v"(vB0), "v"(vB1), "s"(q == 18 ? sb_ : sc_), "s"(cst0), "i"(q == 18 ? 0 : 4096) : "memory", "scc");
                        if (q == 21 || q == 25) asm volatile("global_load_lds_dwordx4 %0, %2 offset:2048\n\tglobal_load_lds_dwordx4 %1, %2 offset:3072"
                                                             :: "v"(vB0), "v"(vB1), "s"(q == 21 ? sb_ : sc_) : "memory");
                    }
                    if (e == 0 && q >= 3 && q <= 7) gate_out(q - 3);
                    if (e == 0 && q >= 10 && q <= 18 && !(q & 1)) gate_out(5 + (q - 10) / 2);
                    if (e == 0 && q == 4) CTILE(wid, 0, cst0);
                    if (e == XE && q >= 23 && q <= 35 && (q & 1)) {
                        const int i = (q - 23) / 2;
                        const __amdgpu_buffer_rsrc_t xs = __builtin_amdgcn_make_buffer_rsrc((void*)x, 0, SEQ * DM * 2, 0x00020000);
                        const __amdgpu_buffer_rsrc_t ks = __builtin_amdgcn_make_buffer_rsrc((void*)dskip, 0, DM * 2, 0x00020000);
                        if (i < 4) asm volatile("buffer_load_dwordx2 %0, %1, %2, %3 offen offset:%4" : "=&v"(xx[(i >> 1) & 1][i & 1]) : "v"(vxo), "s"(xs), "s"((TR((i >> 1) & 1) * DM + d_base) * 2), "i"((i & 1) * 2048) : "memory");
                        if (i == 4) asm volatile("buffer_load_dwordx2 %0, %1, %2, %3 offen" : "=&v"(dk2) : "v"(vdo), "s"(ks), "s"(d_base * 2) : "memory");
                        if (i >= 5) asm volatile("buffer_load_dwordx4 %0, %1, %2, %3 offen sc1" : "=&v"(gv[(i - 5) & 1]) : "v"(vgo), "s"(gs), "s"(GSO((i - 5) & 1)) : "memory");
                    }
                    if (e == XE && q == 16) { vxo = (unsigned)(q4 * 32 * DM * 2 + l15 * 8); vdo = (unsigned)(l15 * 8); asm volatile("" : "+v"(vxo), "+v"(vdo)); }
                    if (e == XE && q == 17) { vgo = GV(q4); asm volatile("" : "+v"(vgo)); }
                    if (e == 6 && q == 30) hpv = __hip_atomic_load(hfp, RLX_AGENT);
                    SCHED();
                }
            };
            unroll<8>(step);
            WAIT_VM(0);
            XPIN();
        }
        PAD8(cv[0], cv[1], cv[2], cv[3]); PIN4(cv[4], cv[5], cv[6], cv[7]);
        STAMPT(dbg, 11, 384);
        const unsigned vh = (unsigned)((lane ^ 2) * 16);
        if (!__all(hpv == tag)) HWAIT();
        asm volatile("" ::: "memory");
#pragma unroll
        for (int f = 0; f < DEPTH; ++f) LOADF(f);
        STAMPT(dbg, 25, 384);
        {
            const __amdgpu_buffer_rsrc_t yrs = __builtin_amdgcn_make_buffer_rsrc(y2T, 0, DM * SEQ * 2, 0x00020000);
            const unsigned yo = (unsigned)(d * 4096 + (q4 & 1) * 512 + (l15 >> 3) * 256 + (q4 >> 1) * 128 + (l15 & 7) * 16);
#pragma unroll
            for (int g = 0; g < 8; g += 2) {
                const auto r0 = __builtin_amdgcn_permlane16_swap(pkbf(cv[g][0], cv[g][1]), pkbf(cv[g + 1][0], cv[g + 1][1]), false, false);
                const auto r1 = __builtin_amdgcn_permlane16_swap(pkbf(cv[g][2], cv[g][3]), pkbf(cv[g + 1][2], cv[g + 1][3]), false, false);
                __builtin_amdgcn_raw_buffer_store_b128((u32x4){r0[0], r1[0], r0[1], r1[1]}, yrs, yo + (unsigned)(g * 512), 0, WT);
            }
        }
        asm volatile("" ::: "memory");
        STAMPT(dbg, 15, 384);
        __builtin_amdgcn_s_setprio(PRIO2_L);
    }
    {
        const int lane = fresh_lane(), l15 = lane & 15, q4 = lane >> 4;
        const int ring = wid >> 2;
        const unsigned va = (unsigned)((lane ^ (ring * 2)) * 16), vh = va;
        const unsigned tag = lds_word(smem, TAG_OFF);
        const int cb0 = ring ? KC_OFF : GA_OFF, cst = ring ? CV_W : 8192;
        f32x4 ac[2][4];
        unsigned ca[2], fa[2], fl = 0, fv = 0;
#define CHAIN(u) do { const int c_ = (wid - ((u) >> 1)) & 3; ca[(u) & 1] = va + (unsigned)(cb0 + c_ * cst + ((u) & 1) * 4096); \
                      fa[(u) & 1] = (unsigned)(RF_OFF + ring * 32 + ((u) & 1) * 16 + c_ * 4); asm volatile("" : "+v"(ca[(u) & 1]), "+v"(fa[(u) & 1])); } while (0)
#define RDIN(dst, a, f_) asm volatile("ds_read_b32 %0, %5\n\tds_read_b128 %1, %6\n\tds_read_b128 %2, %6 offset:1024\n\tds_read_b128 %3, %6 offset:2048\n\tds_read_b128 %4, %6 offset:3072" \
                     : "=&v"(fl), "=&v"(dst[0]), "=&v"(dst[1]), "=&v"(dst[2]), "=&v"(dst[3]) : "v"(f_), "v"(a) : "memory")
#define LWAIT(dst) asm volatile("s_waitcnt lgkmcnt(0)" : "+v"(fl), "+v"(dst[0]), "+v"(dst[1]), "+v"(dst[2]), "+v"(dst[3]) :: "memory")
        CHAIN(0); CHAIN(1);
        WAIT_LGKM0();
        CPIN(0); CPIN(1); CPIN(2);
        CREAD(3);
        int tr[2] = {TR(0), TR(1)};
        unsigned gw[2][2];
        {
            unsigned spins = 0;
            while (__builtin_expect(!__all((gv[0].y == tag) & (gv[0].w == tag) & (gv[1].y == tag) & (gv[1].w == tag)), 0)) {
                if (++spins > SPIN_MAX) break;
#pragma unroll
                for (int r = 0; r < 2; ++r) asm volatile("s_nop 4\n\tbuffer_load_dwordx4 %0, %1, %2, %3 offen sc1\n\ts_waitcnt vmcnt(0)" : "=&v"(gv[r]) : "v"(GV(q4)), "s"(gs), "s"(__builtin_amdgcn_readfirstlane(GSO(r))) : "memory");
            }
            gw[0][0] = gv[0].x; gw[0][1] = gv[0].z; gw[1][0] = gv[1].x; gw[1][1] = gv[1].z;
            PIN4(gw[0][0], gw[0][1], gw[1][0], gw[1][1]);
        }
        unsigned cpv = 0, yy[2][4];
        f32x2 zz[2][4];
        unsigned exo = 0, exi = 0, efo = 0, efi = 0;
        auto send = [&](int r) __attribute__((always_inline)) {
            const unsigned one = 1u;
#pragma unroll
            for (int n = 0; n < 4; ++n) { const f32x2 v = {ac[r][n][2], ac[r][n][3]}; asm volatile("ds_write_b64 %0, %1 offset:%2" :: "v"(exo), "v"(v), "i"(r * 4096 + n * 512) : "memory"); }
            asm volatile("ds_write_b32 %0, %1 offset:%2" :: "v"(efo), "v"(one), "i"(r * 4) : "memory");
        };
        STAMPT(dbg, 5, 0);
        auto body = [&](auto F) __attribute__((always_inline)) {
            constexpr int f = decltype(F)::value;
            constexpr int u = f >> 3, k = f & 7;
            f32x4* an = ac[u & 1];
            f32x4* ao = ac[(u + 1) & 1];
#pragma unroll
            for (int n = 0; n < 4; ++n) {
                if (k == 0 && u < 2) MF0(an[n], af[f % DEPTH], cf_[k][n]); else MF(an[n], af[f % DEPTH], cf_[k][n]);
                SCHED();
                const int g = k * 4 + n;
                if (u >= 1 && u < 7) {
                    if (g == 1) { fv = ((u - 1) >> 1) + 1; asm volatile("" : "+v"(fv)); }
                    if (g == 4 || g == 6 || g == 8 || g == 10) asm volatile("ds_write_b128 %0, %1 offset:%2" :: "v"(ca[(u + 1) & 1]), "v"(ao[(g - 4) / 2]), "i"((g - 4) * 512) : "memory");
                    if (g == 12) asm volatile("ds_write_b32 %0, %1" :: "v"(fa[(u + 1) & 1]), "v"(fv) : "memory");
                    if (g == 18) CHAIN(u + 1);
                    if (g == 20) asm volatile("ds_read_b32 %0, %1" : "=&v"(fl) : "v"(fa[(u + 1) & 1]) : "memory");
                    if (g > 20 && g <= 24) asm volatile("ds_read_b128 %0, %1 offset:%2" : "=&v"(ao[g - 21]) : "v"(ca[(u + 1) & 1]), "i"((g - 21) * 1024) : "memory");
                    if (g == 30) {
                        LWAIT(ao);
                        if (__builtin_expect(__builtin_amdgcn_readfirstlane(fl) != (unsigned)((u + 1) >> 1), 0)) {
                            unsigned spins = 0;
                            do {
                                __builtin_amdgcn_s_sleep(1);
                                RDIN(ao, ca[(u + 1) & 1], fa[(u + 1) & 1]); LWAIT(ao);
                                if (++spins > SPIN_MAX * 16u) break;
                            } while (__builtin_amdgcn_readfirstlane(fl) != (unsigned)((u + 1) >> 1));
                        }
                    }
                }
                if (u == 7 && g == 6) send(0);
                if (f >= ZF && f < ZF + 4) {
                    constexpr int r = ((f - ZF) >> 1) & 1, j = (f - ZF) & 1;
                    const unsigned xw = xx[r][j][n >> 1], dw = dk2[n >> 1];
                    const float xf = (n & 1) ? hi16(xw) : lo16(xw), df = (n & 1) ? hi16(dw) : lo16(dw), yf = j ? hi16(yy[r][n]) : lo16(yy[r][n]);
                    zz[r][n][j] = xf * fmaf(hi16(gw[r][j]), yf, lo16(gw[r][j]) * df);
                    asm volatile("" : "+v"(zz[r][n]));
                }
                if (f == ZF + 4) {
                    zz[0][n] = (f32x2){fmaf(lo16(gw[0][0]), ac[0][n][0], zz[0][n][0]), fmaf(lo16(gw[0][1]), ac[0][n][1], zz[0][n][1])};
                    asm volatile("" : "+v"(zz[0][n]));
                }
                SCHED();
            }
            if (f + DEPTH < 64) LOADF(f + DEPTH);
            if (f == DEPTH + 1 && ring) {
                WAIT_VM(6);
                if (lane == 0) __hip_atomic_store((gu32*)(ws + WS_CFLAG) + cb * 32 + (wid & 3), tag, RLX_AGENT);
                SCHED();
            }
            if (f == YPOLL && ring) { const int l15 = fresh_lane() & 15; cpv = __hip_atomic_load((gu32*)(ws + WS_CFLAG) + (nb * 16 + (wid & 3) * 4 + (l15 >> 2)) * 32 + (l15 & 3), RLX_AGENT); SCHED(); }
            if (f == YPOLL + DEPTH && ring) {
                const int ln = fresh_lane(), l15 = ln & 15, q4 = ln >> 4;
                if (__builtin_expect(!__all(cpv == tag), 0)) {
                    unsigned spins = 0;
                    while (!__all(__hip_atomic_load((gu32*)(ws + WS_CFLAG) + (nb * 16 + (wid & 3) * 4 + (l15 >> 2)) * 32 + (l15 & 3), RLX_AGENT) == tag)) {
                        if (++spins > SPIN_MAX) break;
                        __builtin_amdgcn_s_sleep(1);
                    }
                }
                const unsigned lx = lbase + LA_OFF + (wid & 3) * 8192;
#pragma unroll
                for (int i = 0; i < 4; ++i) {
                    const unsigned vo = (unsigned)((d_base + (wid & 3) * 16 + i * 4 + q4) * 4096 + tb * 256 + ((l15 ^ ((wid & 3) * 4 + i)) * 16));
                    GLDS_SC1(y2T, vo, lx, i * 1024);
                }
                SCHED();
            }
            if (f == YPOLL + 2 * DEPTH + 1 && ring) {
                WAIT_VM(6);
                SETW(YFLAG_OFF, wid & 3, 1u);
                SCHED();
            }
            if (f == 55) {
                const int ln = fresh_lane(), c_ = (wid - 3) & 3;
                const int eg = GA_OFF + c_ * 8192, el = KC_OFF + c_ * CV_W;
                exo = (unsigned)((ring ? el : eg) + ln * 8);
                exi = (unsigned)((ring ? eg : el) + ln * 8);
                efo = (unsigned)(EF_OFF + wid * 8); efi = (unsigned)(EF_OFF + (wid ^ 4) * 8);
                PIN4(exo, exi, efo, efi);
                SCHED();
            }
            if (f == ZF - 2) {
                const int ln = fresh_lane(), l15 = ln & 15, q4 = ln >> 4;
                wait_lds4(smem, YFLAG_OFF);
#pragma unroll
                for (int r = 0; r < 2; ++r) {
                    const int jb = JB(6 + r);
                    const int ya = LA_OFF + (l15 >> 2) * 8192 + (l15 & 3) * 1024 + (((((jb >> 1) & 1) * 8 + (jb >> 2) + q4 * 2) ^ l15) * 16) + (jb & 1) * 8 + ring * 4;
#pragma unroll
                    for (int e = 0; e < 4; ++e) yy[r][e] = *(const unsigned*)(smem + ya + e * 256);
                }
                SCHED();
            }
        };
        unroll<64>(body);
        PAD8(ac[1][0], ac[1][1], ac[1][2], ac[1][3]);
        send(1);
        STAMPT(dbg, 6, 0);
        const __amdgpu_buffer_rsrc_t ors = __builtin_amdgcn_make_buffer_rsrc(out, 0, SEQ * DM * 4, 0x00020000);
        const unsigned ov = (unsigned)((lane >> 4) * (32 * DM * 4) + (lane & 15) * 16);
#pragma unroll
        for (int r = 0; r < 2; ++r) {
            f32x2 in[4];
#define EXRD() asm volatile("ds_read_b32 %0, %5 offset:%7\n\tds_read_b64 %1, %6 offset:%8\n\tds_read_b64 %2, %6 offset:%9\n\tds_read_b64 %3, %6 offset:%10\n\tds_read_b64 %4, %6 offset:%11" \
                             : "=&v"(fl), "=&v"(in[0]), "=&v"(in[1]), "=&v"(in[2]), "=&v"(in[3]) \
                             : "v"(efi), "v"(exi), "i"(r * 4), "i"(r * 4096), "i"(r * 4096 + 512), "i"(r * 4096 + 1024), "i"(r * 4096 + 1536) : "memory")
            EXRD();
            if (r == 0) {
#pragma unroll
                for (int n = 0; n < 4; ++n) {
                    zz[1][n] = (f32x2){fmaf(lo16(gw[1][0]), ac[1][n][0], zz[1][n][0]), fmaf(lo16(gw[1][1]), ac[1][n][1], zz[1][n][1])};
                    asm volatile("" : "+v"(zz[1][n]));
                }
            }
            LWAIT(in);
            if (__builtin_expect(__builtin_amdgcn_readfirstlane(fl) == 0u, 0)) {
                unsigned spins = 0;
                do {
                    EXRD(); LWAIT(in);
                    if (++spins > SPIN_MAX * 16u) break;
                } while (__builtin_amdgcn_readfirstlane(fl) == 0u);
            }
            f32x4 o[2];
#pragma unroll
            for (int j = 0; j < 2; ++j)
#pragma unroll
                for (int e = 0; e < 4; ++e) o[j][e] = fmaf(lo16(gw[r][j]), in[e][j], zz[r][e][j]);
#pragma unroll
            for (int j = 0; j < 2; ++j)
                __builtin_amdgcn_raw_buffer_store_b128(__builtin_bit_cast(u32x4, o[j]), ors, ov + (unsigned)(((tr[r] + j) * DM + d_base) * 4), 0, WT);
        }
        STAMPT(dbg, 7, 0);
        STAMPT(dbg, 16, 384);
        if (tb == 15 && nb == 0 && wid == 0 && lane == 0) __hip_atomic_store((gu32*)(ws + WS_EPOCH), tag, RLX_AGENT);
    }
}

extern "C" void kernel_launch(void* const* d_in, const int* in_sizes, int n_in,
                              void* d_out, int out_size, void* d_ws, size_t ws_size,
                              hipStream_t stream) {
    const u16* x = (const u16*)d_in[0];
    const u16* lre = (const u16*)d_in[1];
    const u16* lim = (const u16*)d_in[2];
    const u16* Bre = (const u16*)d_in[3];
    const u16* Bim = (const u16*)d_in[4];
    const u16* Cre = (const u16*)d_in[5];
    const u16* Cim = (const u16*)d_in[6];
    const u16* logdt = (const u16*)d_in[7];
    const u16* dskip = (const u16*)d_in[8];
    const u16* kin = (const u16*)d_in[10];
    const u16* vin = (const u16*)d_in[11];
    const u16* shiftk = (const u16*)d_in[12];
    const u16* ssmk = (const u16*)d_in[13];
    const u16* ssmD = (const u16*)d_in[14];
    const u16* gatew = (const u16*)d_in[15];
    float* out = (float*)d_out;

    char* ws = (char*)d_ws;

    hipFuncSetAttribute((const void*)k1_kernel, hipFuncAttributeMaxDynamicSharedMemorySize, K1_LDS);

    k1_kernel<<<256, 512, K1_LDS, stream>>>(x, Bre, Bim, kin, vin, shiftk, gatew, ws, lre, lim, logdt, ssmk, ssmD, Cre, Cim, dskip, out);
}
#if defined(__HIP_DEVICE_COMPILE__)
#pragma clang attribute pop
#endif
```

```cpp
#if defined(__HIP_DEVICE_COMPILE__)
#pragma clang attribute push(__attribute__((target("no-packed-fp32-ops"))), apply_to = function)
#endif
#include <hip/hip_runtime.h>
#include <hip/hip_bf16.h>
#include <stdint.h>

#define TIDX ((int)__builtin_amdgcn_workitem_id_x())
#define BIDX ((int)__builtin_amdgcn_workgroup_id_x())
typedef unsigned short u16;
typedef __attribute__((ext_vector_type(8))) short bf16x8;
typedef __attribute__((ext_vector_type(4))) float f32x4;
typedef __attribute__((ext_vector_type(2))) float f32x2;
typedef __attribute__((ext_vector_type(2))) __bf16 bf16x2;
typedef __attribute__((ext_vector_type(4))) unsigned u32x4;
typedef __attribute__((ext_vector_type(2))) unsigned u32x2;
typedef __attribute__((address_space(1))) unsigned long long gu64;
typedef __attribute__((address_space(1))) unsigned gu32;
typedef __attribute__((address_space(3))) void lds_void;
typedef __attribute__((address_space(3))) unsigned lds_u32;
typedef __attribute__((ext_vector_type(4))) short s16x4;
typedef __attribute__((address_space(3))) s16x4 lds_s16x4;
#define TRD(a) __builtin_amdgcn_ds_read_tr16_b64_v4i16((lds_s16x4*)(size_t)(a))

#define DBGON 0
#define STAMPT(buf, k, t) do { } while (0)
#define CODE_LINES 274
#define SEQ 2048
#define DM 1024
#define SPIN_MAX (1u << 18)
#define PRIO_G 2
#define PSLEEP 4
#define PRIO_L 3
#define WT 16

__device__ __forceinline__ float bf2f(u16 v) { return __uint_as_float(((unsigned)v) << 16); }
__device__ __forceinline__ u16 f2bf(float f) { __bf16 b = (__bf16)f; return __builtin_bit_cast(u16, b); }
__device__ __forceinline__ float rbf(float f) { return bf2f(f2bf(f)); }
__device__ __forceinline__ unsigned pkbf(float lo, float hi) { return __builtin_bit_cast(unsigned, __builtin_convertvector((f32x2){lo, hi}, bf16x2)); }
__device__ __forceinline__ float lo16(unsigned u) { return __uint_as_float(u << 16); }
__device__ __forceinline__ float hi16(unsigned u) { return __uint_as_float(u & 0xffff0000u); }

struct cf { float r, i; };
__device__ __forceinline__ cf cmul(cf a, cf b) { return {a.r * b.r - a.i * b.i, a.r * b.i + a.i * b.r}; }
__device__ __forceinline__ cf cfma(cf a, cf b, cf c) {
    return {fmaf(a.r, b.r, fmaf(-a.i, b.i, c.r)), fmaf(a.r, b.i, fmaf(a.i, b.r, c.i))};
}
__device__ __forceinline__ f32x4 splat4(float v) { return (f32x4){v, v, v, v}; }
__device__ __forceinline__ f32x4 fma4(f32x4 a, f32x4 b, f32x4 c) { return __builtin_elementwise_fma(a, b, c); }
__device__ __forceinline__ f32x4 mul4(f32x4 a, f32x4 b) { return a * b; }

__device__ __forceinline__ float red32(float a, float b) {
    const auto r = __builtin_amdgcn_permlane32_swap(__float_as_uint(a), __float_as_uint(b), false, false);
    return __uint_as_float(r[0]) + __uint_as_float(r[1]);
}
__device__ __forceinline__ float red16(float x) {
    const auto r = __builtin_amdgcn_permlane16_swap(__float_as_uint(x), __float_as_uint(x), false, false);
    return __uint_as_float(r[0]) + __uint_as_float(r[1]);
}
__device__ __forceinline__ int fresh_lane() {
    int l;
    asm volatile("v_mbcnt_lo_u32_b32 %0, -1, 0\n\tv_mbcnt_hi_u32_b32 %0, -1, %0" : "=v"(l));
    return l;
}
#define BAR() __builtin_amdgcn_s_barrier()
#define WAIT_VM(n) asm volatile("s_waitcnt vmcnt(" #n ")" ::: "memory")
#define WAIT_LGKM0() __builtin_amdgcn_s_waitcnt(0xC07F)
#define SCHED() __builtin_amdgcn_sched_barrier(0)
#define MF(c, a, b) asm volatile("v_mfma_f32_16x16x32_bf16 %0, %1, %2, %0" : "+v"(c) : "v"(a), "v"(b))
#define PAD8(c0, c1, c2, c3) asm volatile("s_nop 7" : "+v"(c0), "+v"(c1), "+v"(c2), "+v"(c3))
#define PIN4(c0, c1, c2, c3) asm volatile("" : "+v"(c0), "+v"(c1), "+v"(c2), "+v"(c3))
#define GLDS_S(sbase, voff, sl, IMM) \
    asm volatile("s_add_u32 m0, %2, %3\n\ts_nop 0\n\tglobal_load_lds_dwordx4 %0, %1" \
                 :: "v"(voff), "s"(sbase), "s"(sl), "i"(IMM) : "memory", "scc")
#define GLDS_K(sbase, voff, sl, IMM) \
    asm volatile("s_add_u32 m0, %2, %3\n\ts_nop 0\n\tglobal_load_lds_dwordx4 %0, %1 nt" \
                 :: "v"(voff), "s"(sbase), "s"(sl), "i"(IMM) : "memory", "scc")
#define RLX_AGENT __ATOMIC_RELAXED, __HIP_MEMORY_SCOPE_AGENT

#define UNITS_A 0x0756075607560560ull
#define UNITS_B 0x1432143214321432ull
#define RA(t) ((int)((UNITS_A >> (4 * (t))) & 15) * 16384)
#define RB(t) ((int)((UNITS_B >> (4 * (t))) & 15) * 16384)
#define LA_OFF 0
#define GA_OFF 32768
#define CV_COPY 4160
#define CV_W (4 * CV_COPY)
#define KC_OFF 65536
#define ST_OFF 114688
#define ST_V 8832
#define ST_SZ (ST_V + 8 * 1056)
#define U_OFF (KC_OFF + 16 * CV_COPY)
#define HT_OFF (U_OFF + 256 + 16384)
#define GW_OFF (HT_OFF + 8192)
#define TAG_OFF (GW_OFF + 2048)
#define UFLAG_OFF (TAG_OFF + 16)
#define G1D_OFF (UFLAG_OFF + 16)
#define MISC_OFF (G1D_OFF + 16)
#define YFLAG_OFF (MISC_OFF + 16)
#define RF_OFF (YFLAG_OFF + 16)
#define EF_OFF (RF_OFF + 64)
#define COK_OFF (EF_OFF + 64)
#define NFLAGW 52
#define DUMMY_OFF (COK_OFF + 16)
#define K1_LDS (DUMMY_OFF + 256)
#define WS_GRAN 0
#define WS_KVFLAG (256u << 10)
#define WS_EPOCH (WS_KVFLAG + 65536)
#define WS_CFLAG (WS_EPOCH + 4096)
#define WS_HFLAG (384u << 10)
#define WS_GATE (512u << 10)

#define RD128(a, off) (*(const __attribute__((address_space(3))) bf16x8*)(size_t)(unsigned)((a) + (off)))
#define RDA(dst, A, F) do { const s16x4 lo_ = TRD((A) + 512 * (F) + 8); SCHED(); const s16x4 hi_ = TRD((A) + 512 * (F)); SCHED(); \
                            dst = __builtin_shufflevector(lo_, hi_, 0, 1, 2, 3, 4, 5, 6, 7); } while (0)
#define TOUCH(sbase, voff) asm volatile("s_mov_b32 m0, %2\n\ts_nop 0\n\tglobal_load_lds_dword %0, %1" :: "v"(voff), "s"(sbase), "i"(DUMMY_OFF) : "memory")
#define FLAGD(off) do { unsigned long long pc_; asm volatile("s_getpc_b64 %0\n\ts_mov_b32 m0, %2\n\ts_mov_b64 exec, 1\n\tglobal_load_lds_dword %1, %0\n\ts_mov_b64 exec, -1" \
                                                             : "=&s"(pc_) : "v"(0u), "s"(__builtin_amdgcn_readfirstlane(off)) : "memory"); } while (0)
#define GLDS_SC1(sbase, voff, sl, IMM) \
    asm volatile("s_add_u32 m0, %2, %3\n\ts_nop 0\n\tglobal_load_lds_dwordx4 %0, %1 sc1" \
                 :: "v"(voff), "s"(sbase), "s"(sl), "i"(IMM) : "memory", "scc")
__device__ __forceinline__ unsigned mv(unsigned v) { unsigned r; asm("v_mov_b32 %0, %1" : "=v"(r) : "v"(v)); return r; }
__device__ __forceinline__ unsigned al(unsigned hi, unsigned lo) { unsigned r; asm volatile("v_alignbit_b32 %0, %1, %2, 16" : "=v"(r) : "v"(hi), "v"(lo)); return r; }
__device__ __forceinline__ u32x2 pred2(const u32x4& a, unsigned f0, unsigned f1) {
    return (u32x2){(unsigned)__builtin_amdgcn_update_dpp((int)f0, (int)a.z, 0x138, 0xf, 0xf, false), (unsigned)__builtin_amdgcn_update_dpp((int)f1, (int)a.w, 0x138, 0xf, 0xf, false)};
}
#define RL63(v) ((unsigned)__builtin_amdgcn_readlane((int)(v), 63))
__device__ __forceinline__ void wait_lds4(char* smem, int off) {
    const unsigned a = (unsigned)(size_t)(lds_void*)smem + off;
    unsigned spins = 0;
    for (;;) {
        u32x4 f;
        asm volatile("ds_read_b128 %0, %1\n\ts_waitcnt lgkmcnt(0)" : "=&v"(f) : "v"(a) : "memory");
        if (__builtin_amdgcn_readfirstlane((f.x != 0u) & (f.y != 0u) & (f.z != 0u) & (f.w != 0u))) break;
        if (++spins > SPIN_MAX * 16u) break;
        __builtin_amdgcn_s_sleep(1);
    }
}
__device__ __forceinline__ unsigned lds_word(char* smem, int off) {
    const unsigned a = (unsigned)(size_t)(lds_void*)smem + off;
    unsigned f;
    asm volatile("ds_read_b32 %0, %1\n\ts_waitcnt lgkmcnt(0)" : "=&v"(f) : "v"(a) : "memory");
    return __builtin_amdgcn_readfirstlane(f);
}
__device__ __forceinline__ void wait_lds1(char* smem, int off) {
    unsigned spins = 0;
    while (lds_word(smem, off) == 0u) {
        if (++spins > SPIN_MAX * 16u) break;
        __builtin_amdgcn_s_sleep(1);
    }
}
#define SETW(off, i, v) __hip_atomic_store((lds_u32*)(smem + (off)) + (i), (v), __ATOMIC_RELAXED, __HIP_MEMORY_SCOPE_WORKGROUP)

#define PRIO_CV 1
#define PRIO_KV 3
#define CS0 1
#define CS1 3
#define CS2 5
#define XE 4
#define YPOLL 32
#define ZF 59
template <int I> struct IC { static constexpr int value = I; };
template <int N, int I = 0, class F> __device__ __forceinline__ void unroll(F& f) { if constexpr (I < N) { f(IC<I>{}); unroll<N, I + 1>(f); } }
#define MF0(c, a, b) asm volatile("v_mfma_f32_16x16x32_bf16 %0, %1, %2, 0" : "=&v"(c) : "v"(a), "v"(b))
#define DEPTH 6
#define PRIO2_G 0
#define PRIO2_L 1
__device__ __forceinline__ cf pick4(int q, cf a1, cf a2, cf a3) {
    return {q == 0 ? 1.f : q == 1 ? a1.r : q == 2 ? a2.r : a3.r, q == 0 ? 0.f : q == 1 ? a1.i : q == 2 ? a2.i : a3.i};
}
#define PINC(c) asm volatile("" : "+v"((c).r), "+v"((c).i))

extern "C" __global__ __launch_bounds__(512, 2) void k1_kernel(
        const u16* __restrict__ x, const u16* __restrict__ Bre, const u16* __restrict__ Bim,
        const u16* __restrict__ kin, const u16* __restrict__ vin, const u16* __restrict__ shiftk, const u16* __restrict__ gatew, char* ws,
        const u16* __restrict__ lre, const u16* __restrict__ lim, const u16* __restrict__ logdt,
        const u16* __restrict__ ssmk, const u16* __restrict__ ssmD,
        const u16* __restrict__ Cre, const u16* __restrict__ Cim, const u16* __restrict__ dskip, float* __restrict__ out) {
    extern __shared__ __attribute__((aligned(16))) char smem[];
#define h ((u16*)(ws + (1u << 20)))
#define kvT ((u16*)(ws + (9u << 20)))
#define y2T ((u16*)(ws + (13u << 20)))
#define dbg ((unsigned long long*)(ws + (64u << 20)))
    const int tid = TIDX;
    if (tid < 40 && tid + ((BIDX >> 3) & 7) * 40 < CODE_LINES) {
        const unsigned vo = (unsigned)(tid + ((BIDX >> 3) & 7) * 40) * 128u;
        asm volatile("s_getpc_b64 s[98:99]\n\ts_add_u32 s98, s98, k1_kernel@rel32@lo+4\n\ts_addc_u32 s99, s99, k1_kernel@rel32@hi+12\n\t"
                     "s_mov_b32 m0, %1\n\ts_nop 0\n\tglobal_load_lds_dword %0, s[98:99]"
                     :: "v"(vo), "i"(DUMMY_OFF) : "s98", "s99", "scc", "memory");
    }
    const int lane = tid & 63;
    int wid = __builtin_amdgcn_readfirstlane(tid >> 6);
    const int b = BIDX;
    const int xcd = b & 7, idx = b >> 3;
    const int tb = ((xcd >> 1) ^ (xcd >> 2)) * 4 + (idx & 3), nb = (xcd & 1) * 8 + (idx >> 2);
    const int cb = xcd * 32 + idx;
    const int t_base = tb * 128, p_base = nb * 64, d_base = nb * 64;
    const int l15 = lane & 15, q4 = lane >> 4;
    unsigned stv = 0, sth = 0;
    STAMPT(dbg, 0, 0);
    const unsigned lbase = (unsigned)(size_t)(lds_void*)smem;
    const int rot1 = (xcd >> 1) * 4 + (xcd & 1) * 8 + (idx >> 2);
    unsigned voA[4] = {0u, 0u, 0u, 0u}, voB[4] = {0u, 0u, 0u, 0u}, lw0 = 0;
    if (wid == 4 || wid == 5) {
        __builtin_amdgcn_s_setprio(PRIO_L);
        const int lh = wid - 4;
        const unsigned ch = (unsigned)(((lane & 7) ^ (lane >> 4)) * 16);
        voA[0] = (unsigned)(t_base + lh * 16 + (lane >> 5) * 32 + ((lane >> 3) & 3)) * 2048u + ch;
        voA[1] = (voA[0] ^ 64u) + (131072u - 1024u);
        voA[2] = voA[0] + 6144u; voA[3] = voA[1] + 6144u;
        voB[0] = (unsigned)(p_base + lh * 32 + (lane >> 3)) * 2048u + ch;
        voB[1] = (voB[0] ^ 64u) + (16384u - 1024u);
        voB[2] = voB[0] + 30720u; voB[3] = voB[1] + 30720u;
        PIN4(voA[0], voA[1], voA[2], voA[3]); PIN4(voB[0], voB[1], voB[2], voB[3]);
#define GLDS4(sbase, v0, v1, v2, v3, sl, IMM) \
            asm volatile("s_add_u32 m0, %5, %6\n\ts_nop 0\n\tglobal_load_lds_dwordx4 %0, %4\n\tglobal_load_lds_dwordx4 %1, %4 offset:1024\n\t" \
                         "global_load_lds_dwordx4 %2, %4 offset:2048\n\tglobal_load_lds_dwordx4 %3, %4 offset:3072" \
                         :: "v"(v0), "v"(v1), "v"(v2), "v"(v3), "s"(sbase), "s"(sl), "i"(IMM) : "memory", "scc")
#define STAGE_A(kt, sl) do { const char* sa_ = (const char*)x + (((kt) + rot1) & 15) * 128; \
            GLDS4(sa_, voA[0], voA[1], voA[2], voA[3], sl, 0); GLDS4(sa_ + 16384, voA[0], voA[1], voA[2], voA[3], sl, 4096); } while (0)
#define STAGE_B(kt, sl) do { const int kr_ = (((kt) + rot1) & 15) * 128; const char* sb_ = (const char*)Bre + kr_; const char* sc_ = (const char*)Bim + kr_; \
            GLDS4(sb_, voB[0], voB[1], voB[2], voB[3], sl, 0); GLDS4(sc_, voB[0], voB[1], voB[2], voB[3], sl, 4096); } while (0)
        lw0 = lbase + lh * 8192;
        STAGE_A(0, lw0 + RA(0)); STAGE_B(0, lw0 + RB(0)); STAGE_A(1, lw0 + RA(1)); STAGE_B(1, lw0 + RB(1));
    }
    const int sblk = b >> 3, dblk = b & 7;
    u32x2 tw[4];
    unsigned epoch;
    asm volatile("" : "=v"(tw[0]), "=v"(tw[1]), "=v"(tw[2]), "=v"(tw[3]), "=v"(epoch));
    if (wid >= 6) {
        __builtin_amdgcn_s_setprio(PRIO_L);
        const int vw = wid - 6;
        const int dch = dblk * 128 + vw * 64 + l15;
        const unsigned lk = lbase + ST_OFF + vw * ST_SZ;
        const unsigned vk = (unsigned)((lane >> 3) * DM * 2 + (((lane & 7) ^ ((lane >> 4) & 1)) * 16));
        const char* kb = (const char*)kin + (size_t)(sblk * 64 - 4) * (DM * 2) + dblk * 256 + vw * 128;
#define KLD(n) GLDS_K(kb + (n) * 8 * DM * 2, vk, lk, (n) * 1024 + ((n) >> 1) * 32)
#define VLD(n) GLDS_K(vb + (((n) >> 1) * 16 + ((n) & 1) * 2) * DM * 2, vv_, lk, ST_V + (n) * 1056)
        if (vw == 1) {
            const unsigned vg = (unsigned)((lane * 16 + rot1 * 128) & 2047), lg = lbase + GW_OFF;
            GLDS_S(gatew, vg, lg, 0); GLDS_S(gatew, vg ^ 1024u, lg, 1024);
        }
        asm volatile("global_load_dwordx2 %0, %5, %7\n\tglobal_load_dwordx2 %1, %5, %7 offset:128\n\tglobal_load_dwordx2 %2, %5, %7 offset:256\n\t"
                     "global_load_dwordx2 %3, %5, %7 offset:384\n\tglobal_load_dword %4, %6, %8 sc1"
                     : "+v"(tw[0]), "+v"(tw[1]), "+v"(tw[2]), "+v"(tw[3]), "+v"(epoch) : "v"((unsigned)(dch * 8)), "v"((unsigned)WS_EPOCH), "s"(shiftk), "s"(ws) : "memory");
        if (sblk == 0 && lane < 32) *(u32x4*)(smem + ST_OFF + vw * ST_SZ + lane * 16) = (u32x4){0u, 0u, 0u, 0u};
        if (sblk != 0 || lane >= 32) KLD(0);
        KLD(1); KLD(2); KLD(3); KLD(4); KLD(5); KLD(6); KLD(7);
        if (lane < 32) KLD(8);
        __builtin_amdgcn_s_setprio(0);
    }
    asm volatile("" : "+s"(wid));

#define WSL(w) ((((w) >> 2) ^ (nb >> 3)) * 4 + ((w) & 3))
#define COFF(w, ti) ((2 * WSL(w) + ((ti) >> 1)) * 128 + ((ti) & 1) * 4096)
    bf16x8 cf_[8][4];
    bf16x8 af[DEPTH];
    int cra, crb;
#define CT_SETUP() const unsigned vB0 = (unsigned)((d_base + (lane >> 3) * 4) * 2048 + (((lane & 7) ^ (lane >> 4)) * 16)), vB1 = (vB0 ^ 64u) + 65536u - 1024u
#define CTILE(w, ti, dst) do { const int co_ = COFF(w, ti); const unsigned ds_ = (dst); \
                               GLDS4((const char*)Cre + co_, vB0, vB1, vB0, vB1, ds_, 0); GLDS4((const char*)Cim + co_, vB0, vB1, vB0, vB1, ds_, 4096); } while (0)
#define CF(ti, n) cf_[4 * ((ti) >> 1) + ((n) >> 1)][2 * ((ti) & 1) + ((n) & 1)]
#define CRD(ti, n) CF(ti, n) = RD128((((n) & 4) ? crb : cra) ^ (((n) & 2) * 16), ((n) & 1) * 2048)
#define CREAD(ti) do { _Pragma("unroll") for (int n_ = 0; n_ < 8; ++n_) CRD(ti, n_); } while (0)
#define CPIN(ti) do { PIN4(CF(ti, 0), CF(ti, 1), CF(ti, 2), CF(ti, 3)); PIN4(CF(ti, 4), CF(ti, 5), CF(ti, 6), CF(ti, 7)); } while (0)
#define CRA(base) ((base) + (q4 >> 1) * 4096 + l15 * 128 + (((q4 & 1) ^ ((l15 >> 1) & 7)) * 16))
    const int rot2 = (idx & 16) >> 2;
#define JB(u) (((((u) ^ (rot2 >> 2)) & 1) << 2) | ((wid - ((u) >> 1) + rot2) & 3))
    const __amdgpu_buffer_rsrc_t hs = __builtin_amdgcn_make_buffer_rsrc((char*)h + (size_t)tb * 524288 + WSL(wid) * 8192, 0, 524288, 0x00020000);
#define LOADF(f) do { af[(f) % DEPTH] = __builtin_bit_cast(bf16x8, __builtin_amdgcn_raw_buffer_load_b128(hs, vh + ((f) & 3) * 1024, JB((f) >> 3) * 65536 + ((f) & 4) * 1024, 16)); SCHED(); } while (0)
#define HPOLL_SETUP() gu32* hfp = (gu32*)(ws + WS_HFLAG) + (tb * 16 + 2 * WSL(wid) + (lane & 1)) * 32
#define HWAIT() do { unsigned spins = 0; \
            while (!__all(__hip_atomic_load(hfp, RLX_AGENT) == tag)) { if (++spins > SPIN_MAX) break; __builtin_amdgcn_s_sleep(PSLEEP); } asm volatile("" ::: "memory"); } while (0)

    const __amdgpu_buffer_rsrc_t gs = __builtin_amdgcn_make_buffer_rsrc(ws + WS_GATE, 0, 32768, 0x00020000);
#define GV(q4) ((unsigned)(((q4) >> 1) * 128 + ((q4) & 1) * 32))
#define GSO(r) (tb * 2048 + JB(6 + (r)) * 256 + (wid >> 2) * 16)
#define TR(r) (t_base + JB(6 + (r)) * 4 + (wid >> 2) * 2)
    u32x2 xx[2][2], dk2;
    u32x4 gv[2];
#define XLOAD() do { \
        const __amdgpu_buffer_rsrc_t xs = __builtin_amdgcn_make_buffer_rsrc((void*)x, 0, SEQ * DM * 2, 0x00020000); \
        const __amdgpu_buffer_rsrc_t ks = __builtin_amdgcn_make_buffer_rsrc((void*)dskip, 0, DM * 2, 0x00020000); \
        dk2 = __builtin_amdgcn_raw_buffer_load_b64(ks, (unsigned)(l15 * 8), d_base * 2, 0); \
        _Pragma("unroll") for (int q = 0; q < 4; ++q) \
            xx[q >> 1][q & 1] = __builtin_amdgcn_raw_buffer_load_b64(xs, (unsigned)(q4 * 32 * DM * 2 + l15 * 8 + (q & 1) * DM * 2), (TR(q >> 1) * DM + d_base) * 2, 0); \
        _Pragma("unroll") for (int r = 0; r < 2; ++r) gv[r] = __builtin_amdgcn_raw_buffer_load_b128(gs, GV(q4), GSO(r), 16); \
        SCHED(); } while (0)
#define XPIN() do { PIN4(xx[0][0], xx[0][1], xx[1][0], xx[1][1]); asm volatile("" : "+v"(dk2), "+v"(gv[0]), "+v"(gv[1])); } while (0)

    if (wid < 4) {
        typedef __attribute__((ext_vector_type(8))) unsigned u32x8;
        typedef const __attribute__((address_space(4))) u32x8 cu8;
        const int po = (p_base + wid * 16) * 2;
        u32x8 s0 = *(cu8*)((size_t)logdt + po), s1 = *(cu8*)((size_t)lre + po), s2 = *(cu8*)((size_t)lim + po);
        SCHED();
        const int pq = p_base + wid * 16 + l15;
        unsigned goff[4], gmask[4];
#pragma unroll
        for (int i = 0; i < 4; ++i) {
            goff[i] = (unsigned)(((tb - 1 - q4 - 4 * i) * 1024 + pq) * 16);
            gmask[i] = (tb - 1 - q4 - 4 * i >= 0) ? ~0u : 0u;
            asm volatile("" : "+v"(goff[i]), "+v"(gmask[i]));
        }
        unsigned gpub = q4 == 0 ? (unsigned)((tb * 1024 + pq) * 16) : 0x80000000u;
        unsigned hp = (unsigned)(tb * 524288 + (nb * 4 + wid) * 1024 + lane * 16);
        unsigned wa = (unsigned)(HT_OFF + wid * 2048 + (q4 * 16 + (l15 ^ ((q4 >> 1) * 4))) * 8);
        unsigned ra0 = (unsigned)(HT_OFF + wid * 2048 + ((q4 >> 1) * 64 + (l15 & 3) * 16 + (((q4 & 1) * 8 + (l15 >> 2)) ^ ((l15 & 2) * 2))) * 8), ra1 = ra0 ^ 32u;
        cra = CRA(GA_OFF + wid * 8192); crb = cra ^ 64;
        unsigned vB0 = (unsigned)((d_base + (lane >> 3) * 4) * 2048 + (((lane & 7) ^ (lane >> 4)) * 16)), vB1 = (vB0 ^ 64u) + 65536u - 1024u;
        PIN4(gpub, vB0, hp, wa); PIN4(ra0, ra1, cra, crb); asm volatile("" : "+v"(vB1));
        f32x4 acc[8][2];
#pragma unroll
        for (int m = 0; m < 8; m += 2) {
#pragma unroll
            for (int c = 0; c < 4; ++c) acc[m + (c >> 1)][c & 1] = (f32x4){0.f, 0.f, 0.f, 0.f};
            PIN4(acc[m][0], acc[m][1], acc[m + 1][0], acc[m + 1][1]);
        }
        SCHED();
        cf L1, Lq3, cc, L32, cLq, Lp, L512;
        {
            unsigned w0 = 0, w1 = 0, w2 = 0;
#pragma unroll
            for (int i = 0; i < 8; ++i) {
                asm("v_writelane_b32 %0, %3, %6\n\tv_writelane_b32 %1, %4, %6\n\tv_writelane_b32 %2, %5, %6" : "+v"(w0), "+v"(w1), "+v"(w2) : "s"(s0[i]), "s"(s1[i]), "s"(s2[i]), "i"(2 * i));
            }
            asm("s_nop 1" : "+v"(w0), "+v"(w1), "+v"(w2));
            auto spread = [&](unsigned w) {
                const unsigned e = (unsigned)__builtin_amdgcn_update_dpp(0, (int)w, 0xa0, 0xf, 0xf, false);
                const unsigned a = __builtin_amdgcn_permlane16_swap(e, e, false, false)[0];
                const unsigned c = __builtin_amdgcn_permlane32_swap(a, a, false, false)[0];
                return (l15 & 1) ? hi16(c) : lo16(c);
            };
            const float ldt = spread(w0), re = spread(w1), im = spread(w2);
            if (wid == 3 && lane < NFLAGW) *(unsigned*)(smem + UFLAG_OFF + lane * 4) = 0u;
            const float dt = rbf(expf(ldt));
            const float ea = expf(dt * re);
            float sn, cs;
            sincosf(dt * im, &sn, &cs);
            L1 = {ea * cs, ea * sn};
            const float nr = L1.r - 1.0f, ni = L1.i, den = re * re + im * im;
            cc = {2.f * ((nr * re + ni * im) / den), 2.f * ((ni * re - nr * im) / den)};
            const cf T2 = cmul(L1, L1), T4 = cmul(T2, T2), L8 = cmul(T4, T4), L16 = cmul(L8, L8);
            L32 = cmul(L16, L16);
            const cf L64 = cmul(L32, L32), L96 = cmul(L64, L32), L128 = cmul(L64, L64), L256 = cmul(L128, L128), L384 = cmul(L256, L128);
            L512 = cmul(L256, L256);
            Lp = pick4(q4, L128, L256, L384);
            Lq3 = pick4(3 - q4, L32, L64, L96);
            cLq = cmul(cc, pick4(q4, L32, L64, L96));
            PINC(L1); PINC(Lq3); PINC(cc); PINC(L32); PINC(cLq); PINC(Lp); PINC(L512);
        }
        __builtin_amdgcn_s_setprio(PRIO_G);
        unsigned tagv;
        cf S;
        STAMPT(dbg, 22, 0);
        const int swz = (l15 >> 1) & 7;
        {
            const int a0 = RA(0) + l15 * 128 + ((q4 ^ swz) * 16);
            const int b0 = RB(0) + ((wid >> 1) * 64 + (wid & 1) * 16 + l15) * 128 + ((q4 ^ swz) * 16);
            WAIT_LGKM0();
            BAR();
            asm volatile("" ::: "memory");
            STAMPT(dbg, 19, 64);
            bf16x8 FA[8][2], FB[2][2][2];
            int pa[2] = {a0, a0 ^ 64}, pb[2] = {b0, b0 ^ 64};
            PIN4(pa[0], pa[1], pb[0], pb[1]);
#define LGW(n) __builtin_amdgcn_s_waitcnt(0xC07F | ((n) << 8))
#define RA_(j) FA[(j) >> 1][(j) & 1] = RD128(pa[(j) & 1], ((j) >> 1) * 2048)
#define RB_(bu, j) FB[bu][(j) >> 1][((j) ^ ((j) >> 1)) & 1] = RD128(pb[((j) ^ ((j) >> 1)) & 1], ((j) >> 1) * 4096)
#define RN_(bu, i) do { if ((i) == 0) RB_(bu, 0); else if ((i) < 3) RA_((i) - 1); else if ((i) < 6) RB_(bu, (i) - 2); else RA_((i) - 4); } while (0)
#define G1_H(H, bu, RD, FILL) do { \
            _Pragma("unroll") for (int i = 0; i < 16; ++i) { \
                const int m = (H) * 4 + (i >> 2), n = ((i >> 1) ^ m) & 1, ks = (i ^ (i >> 1)) & 1; \
                if ((H) == 0) { if (i == 0) LGW(6); if (i == 4) LGW(8); if (i == 8) LGW(10); if (i == 12) LGW(8); } \
                SCHED(); \
                MF(acc[m][n], FA[m][ks], FB[bu][n][ks]); SCHED(); \
                if ((H) == 0 && i < 8) RA_(8 + i); \
                if ((H) == 1 && (RD) && i < 12) RN_((bu) ^ 1, i); \
                FILL((H) * 16 + i); \
                SCHED(); \
            } } while (0)
#define NOF(g) do { } while (0)
#pragma unroll
            for (int j = 0; j < 12; ++j) { RN_(0, j); SCHED(); }
            unsigned long long ua_ = UNITS_A;
            int sb = RB(0), sa = RA(0), nx, na;
#define ADV(g) do { if ((g) == 8) { ua_ >>= 4; na = (int)((unsigned)ua_ & 15u) << 14; asm volatile("" : "+s"(na), "+s"(ua_)); } \
                    if ((g) == 9) { const int o_ = sa; sa = na; na -= o_; asm volatile("" : "+s"(na), "+s"(sa)); } \
                    if ((g) == 10) { pa[0] += na; pa[1] += na; asm volatile("" : "+v"(pa[0]), "+v"(pa[1])); } \
                    if ((g) == 11) { nx = (sb == 65536) ? -49152 : 16384; sb += nx; asm volatile("" : "+s"(nx), "+s"(sb)); } \
                    if ((g) == 13) { pb[0] += nx; pb[1] += nx; asm volatile("" : "+v"(pb[0]), "+v"(pb[1])); } } while (0)
#define G1_T(bu) do { \
                G1_H(0, bu, true, ADV); \
                WAIT_LGKM0(); \
                asm volatile("" ::: "memory"); \
                BAR();                     \
                asm volatile("" ::: "memory"); \
                G1_H(1, bu, true, NOF); } while (0)
#pragma unroll 1
            for (int t2 = 0; t2 < 7; ++t2) { G1_T(0); G1_T(1); }
            G1_T(0);
#define AGG(g) do { if ((g) == 8) { asm volatile("" ::: "memory"); SETW(G1D_OFF, wid, 1u); }        \
                    if ((g) == 9) tagv = *(volatile const lds_u32*)(size_t)(unsigned)TAG_OFF; \
                    if ((g) >= 7) { const int t = (g) - 6; \
                        if (t == 1) { asm volatile("s_nop 1" : "+v"(acc[0][0]), "+v"(acc[0][1])); S = {acc[0][0][0], acc[0][1][0]}; } \
                        if ((t & 3) == 0) asm volatile("" : "+v"(acc[t >> 2][0]), "+v"(acc[t >> 2][1])); \
                        S = cfma(L1, S, cf{acc[t >> 2][0][t & 3], acc[t >> 2][1][t & 3]}); PINC(S); } } while (0)
            G1_H(0, 1, false, AGG);
            G1_H(1, 1, false, AGG);
        }
        STAMPT(dbg, 1, 0);
        const unsigned tag = __builtin_amdgcn_readfirstlane(tagv);
        const __amdgpu_buffer_rsrc_t grs = __builtin_amdgcn_make_buffer_rsrc(ws, 0, 15 * 16384, 0x00020000);
        {
#pragma unroll
            for (int t = 26; t < 32; ++t) {
                if (t == 28) { PINC(S); asm volatile("" : "+v"(acc[7][0]), "+v"(acc[7][1])); }
                S = cfma(L1, S, cf{acc[t >> 2][0][t & 3], acc[t >> 2][1][t & 3]});
            }
            const cf W = cmul(S, Lq3);
            const float e2 = red16(red32(W.r, W.i));
            const auto eo = __builtin_amdgcn_permlane32_swap(__float_as_uint(e2), __float_as_uint(e2), false, false);
            __builtin_amdgcn_raw_buffer_store_b128((u32x4){eo[0], tag, eo[1], tag}, grs, gpub, 0, 16);
        }
        SCHED();
        STAMPT(dbg, 2, 0);
        const unsigned cg = lbase + GA_OFF + wid * 8192;
        wait_lds1(smem, COK_OFF + wid * 4);
        CREAD(0);
        WAIT_LGKM0();
        CPIN(0);
        CTILE(wid, 1, cg);
        u32x4 gq[4];
#pragma unroll
        for (int m = 0; m < 8; ++m) {
            const f32x4 R = acc[m][0], I = acc[m][1];
            acc[m][0] = fma4(splat4(cc.r), R, mul4(splat4(-cc.i), I));
            acc[m][1] = fma4(splat4(-cc.r), I, mul4(splat4(-cc.i), R));
            asm volatile("" : "+v"(acc[m][0]), "+v"(acc[m][1]));
            if (m == 3) {
                SCHED();
#pragma unroll
                for (int i = 0; i < 4; ++i) asm volatile("buffer_load_dwordx4 %0, %1, %2, 0 offen sc1" : "=&v"(gq[i]) : "v"(goff[i]), "s"(grs) : "memory");
                SCHED();
            }
        }
        cf Ec;
        {
            const auto sr = __builtin_amdgcn_permlane16_swap(__float_as_uint(S.r), __float_as_uint(S.r), false, false);
            const auto si = __builtin_amdgcn_permlane16_swap(__float_as_uint(S.i), __float_as_uint(S.i), false, false);
            const cf Se = {__uint_as_float(sr[0]), __uint_as_float(si[0])}, So = {__uint_as_float(sr[1]), __uint_as_float(si[1])};
            const cf A = cfma(L32, Se, So);
            const auto ar = __builtin_amdgcn_permlane32_swap(__float_as_uint(A.r), __float_as_uint(A.r), false, false);
            const auto ai = __builtin_amdgcn_permlane32_swap(__float_as_uint(A.i), __float_as_uint(A.i), false, false);
            const cf A01 = {(q4 & 2) ? __uint_as_float(ar[0]) : 0.f, (q4 & 2) ? __uint_as_float(ai[0]) : 0.f};
            const cf kq = {(q4 & 1) ? L32.r : 1.f, (q4 & 1) ? L32.i : 0.f};
            const cf E = cfma(kq, A01, cf{(q4 & 1) ? Se.r : 0.f, (q4 & 1) ? Se.i : 0.f});
            Ec = cmul(cc, E);
            PINC(Ec);
        }
        WAIT_VM(4);
        CREAD(1);
        WAIT_LGKM0();
        CPIN(1);
        CTILE(wid, 2, cg);
        asm volatile("s_waitcnt vmcnt(8)" : "+v"(gq[0]), "+v"(gq[1]), "+v"(gq[2]), "+v"(gq[3]) :: "memory");
        STAMPT(dbg, 27, 0);
        cf carry;
        {
            unsigned spins = 0;
            for (;;) {
                unsigned bad = 0;
#pragma unroll
                for (int i = 0; i < 4; ++i) bad |= ((gq[i].y ^ tag) | (gq[i].w ^ tag)) & gmask[i];
                if (__all(bad == 0u)) break;
                if (++spins > SPIN_MAX) break;
#pragma unroll
                for (int i = 0; i < 4; ++i) asm volatile("buffer_load_dwordx4 %0, %1, %2, 0 offen sc1" : "+v"(gq[i]) : "v"(goff[i]), "s"(grs) : "memory");
                asm volatile("s_waitcnt vmcnt(0)" : "+v"(gq[0]), "+v"(gq[1]), "+v"(gq[2]), "+v"(gq[3]) :: "memory");
            }
            cf E = {__uint_as_float(gq[3].x), __uint_as_float(gq[3].z)};
#pragma unroll
            for (int i = 2; i >= 0; --i) E = cfma(L512, E, cf{__uint_as_float(gq[i].x), __uint_as_float(gq[i].z)});
            E = cmul(E, Lp);
            const float e2 = red16(red32(E.r, E.i));
            const auto eo = __builtin_amdgcn_permlane32_swap(__float_as_uint(e2), __float_as_uint(e2), false, false);
            carry = {__uint_as_float(eo[0]), __uint_as_float(eo[1])};
        }
        STAMPT(dbg, 3, 0);
        {
            const __amdgpu_buffer_rsrc_t hrs = __builtin_amdgcn_make_buffer_rsrc(h, 0, SEQ * 2048 * 2, 0x00020000);
            const cf e = cfma(cLq, carry, Ec);
            float hr = e.r, hn = -e.i;
            s16x4 fr[2][4];
#define HSTORE(m) do { _Pragma("unroll") for (int c = 0; c < 2; ++c) \
                __builtin_amdgcn_raw_buffer_store_b128(__builtin_bit_cast(u32x4, __builtin_shufflevector(fr[(m) & 1][2 * c], fr[(m) & 1][2 * c + 1], 0, 1, 2, 3, 4, 5, 6, 7)), hrs, hp, (2 * (m) + c) * 65536, WT); } while (0)
#pragma unroll
            for (int m = 0; m < 4; ++m) {
#pragma unroll
                for (int j = 0; j < 8; ++j) {
                    const float nr = fmaf(L1.r, hr, fmaf(L1.i, hn, acc[2 * m + (j >> 2)][0][j & 3]));
                    const float nn = fmaf(L1.r, hn, fmaf(-L1.i, hr, acc[2 * m + (j >> 2)][1][j & 3]));
                    hr = nr; hn = nn;
                    acc[2 * m + (j >> 2)][0][j & 3] = nr; acc[2 * m + (j >> 2)][1][j & 3] = nn;
                }
                u32x2 pk[4];
#pragma unroll
                for (int c = 0; c < 4; ++c) { const f32x4 v = acc[2 * m + (c >> 1)][c & 1]; pk[c] = (u32x2){pkbf(v[0], v[1]), pkbf(v[2], v[3])}; }
                PIN4(pk[0], pk[1], pk[2], pk[3]);
                SCHED();
#pragma unroll
                for (int c = 0; c < 4; ++c) { *(volatile __attribute__((address_space(3))) u32x2*)(size_t)(wa + c * 512) = pk[c]; SCHED(); }
                asm volatile("" ::: "memory");
#pragma unroll
                for (int c = 0; c < 2; ++c) { fr[m & 1][2 * c] = TRD(ra0 + c * 1024); SCHED(); fr[m & 1][2 * c + 1] = TRD(ra1 + c * 1024); SCHED(); }
                asm volatile("" ::: "memory");
                if (m > 0) HSTORE(m - 1);
                SCHED();
            }
            HSTORE(3);
        }
        STAMPT(dbg, 4, 0);
        {
            WAIT_VM(8);
            CREAD(2);
            WAIT_VM(0);
            {
                unsigned n;
                asm volatile("s_mov_b64 exec, 1\n\tds_add_rtn_u32 %0, %1, %2\n\ts_mov_b64 exec, -1\n\ts_waitcnt lgkmcnt(0)" : "=&v"(n) : "v"(MISC_OFF), "v"(1u) : "memory");
                if (__builtin_amdgcn_readfirstlane(n) == 3u) {
                    if (lane == 0) __hip_atomic_store((gu32*)(ws + WS_HFLAG) + (tb * 16 + nb) * 32, tag, RLX_AGENT);
                    if (DBGON && lane == 0) dbg[BIDX * 64 + 20] = __builtin_amdgcn_s_memrealtime();
                }
            }
            STAMPT(dbg, 12, 0);
            __builtin_amdgcn_s_setprio(0);
            WAIT_LGKM0();
            CPIN(2);
            CTILE(wid, 3, cg);
            XLOAD();
            HPOLL_SETUP();
            HWAIT();
            STAMPT(dbg, 8, 0);
            const unsigned vh = (unsigned)(lane * 16);
#pragma unroll
            for (int f = 0; f < DEPTH; ++f) LOADF(f);
            __builtin_amdgcn_s_setprio(PRIO2_G);
        }
    } else {
        const int lw = wid - 4;
        const int d = cb * 4 + lw;
        const char* Krow = (const char*)(ssmk + (size_t)d * SEQ);
        cra = CRA(LA_OFF + lw * 8192); crb = cra ^ 64;
        const unsigned cst0 = lbase + LA_OFF + lw * 8192;
        CT_SETUP();
        asm volatile("" : "+v"(cra), "+v"(crb));
        u32x4 KA[4];
        unsigned draw;
        unsigned tag;
        float gacc = 0.f;
#define KLOAD() asm volatile("global_load_dwordx4 %0, %5, %7 nt\n\tglobal_load_dwordx4 %1, %5, %7 offset:1024 nt\n\tglobal_load_dwordx4 %2, %5, %7 offset:2048 nt\n\t" \
                             "global_load_dwordx4 %3, %5, %7 offset:3072 nt\n\tglobal_load_ushort %4, %6, %8" \
                             : "=&v"(KA[0]), "=&v"(KA[1]), "=&v"(KA[2]), "=&v"(KA[3]), "=&v"(draw) : "v"((unsigned)(lane * 16)), "v"((unsigned)(d * 2)), "s"(Krow), "s"(ssmD) : "memory")
#define KWAIT(n) asm volatile("s_waitcnt vmcnt(%5)" : "+v"(KA[0]), "+v"(KA[1]), "+v"(KA[2]), "+v"(KA[3]), "+v"(draw) : "i"(n) : "memory")
        if (wid < 6) {
            STAGE_A(2, lw0 + RA(2)); STAGE_B(2, lw0 + RB(2));
            WAIT_VM(32);
            STAMPT(dbg, 17, 320);
            BAR();
            STAGE_B(3, lw0 + RB(3));
            unsigned long long ua_ = UNITS_A >> 12;
            unsigned sla = lw0 + RA(3), slb = lw0 + RB(4);
            int ka = ((3 + rot1) & 15) * 128, kb_ = ka;
            const char* sa = (const char*)x + ka;
            asm volatile("" : "+s"(sa), "+s"(sla), "+s"(ka), "+s"(ua_));
#define NEXT_A() do { ka = (ka + 128) & 2047; sa = (const char*)x + ka; ua_ >>= 4; sla = lw0 + (((unsigned)ua_ & 15u) << 14); asm volatile("" : "+s"(sa), "+s"(sla), "+s"(ka), "+s"(ua_)); } while (0)
#define LSTEP(n) do { \
                WAIT_VM(n); \
                BAR(); \
                GLDS4(sa, voA[0], voA[1], voA[2], voA[3], sla, 0); \
                GLDS4(sa + 16384, voA[0], voA[1], voA[2], voA[3], sla, 4096); \
                kb_ = (kb_ + 128) & 2047; \
                GLDS4((const char*)Bre + kb_, voB[0], voB[1], voB[2], voB[3], slb, 0); \
                NEXT_A(); \
                const unsigned slb0 = slb; \
                slb = (slb == lw0 + 65536) ? lw0 + 16384 : slb + 16384; \
                asm volatile("" : "+s"(slb), "+s"(kb_), "+s"(t)); \
                GLDS4((const char*)Bim + kb_, voB[0], voB[1], voB[2], voB[3], slb0, 4096); } while (0)
#pragma unroll 1
            for (int t = 1; t < 5; ++t) LSTEP(24);
            GLDS4(sa, voA[0], voA[1], voA[2], voA[3], sla, 0); GLDS4(sa + 16384, voA[0], voA[1], voA[2], voA[3], sla, 4096);
            NEXT_A();
#pragma unroll 1
            for (int t = 5; t < 13; ++t) LSTEP(32);
            KLOAD();
            WAIT_VM(37);
            BAR();
            __builtin_amdgcn_s_setprio(0);
            tag = lds_word(smem, TAG_OFF);
            CTILE(lw, 0, cst0 + GA_OFF);
            FLAGD(COK_OFF + lw * 4);
            WAIT_VM(30);
            BAR();
            CTILE(lw + 2, 0, cst0 + GA_OFF + 16384);
            FLAGD(COK_OFF + lw * 4 + 8);
            WAIT_VM(23);
            BAR();
        } else {
            const int vw = wid - 6;
            const int dch = dblk * 128 + vw * 64 + l15;
            const unsigned lk = lbase + ST_OFF + vw * ST_SZ;
            const unsigned vv_ = (unsigned)(((lane >> 4) * 4 + ((lane >> 3) & 1)) * DM * 2 + (lane & 7) * 16);
            const char* vb = (const char*)vin + (size_t)(sblk * 64) * (DM * 2) + dblk * 256 + vw * 128;
            const int grow = nb * 8 + vw * 4 + q4;
            unsigned gx0 = (unsigned)(grow * 128 + (((l15 >> 1) ^ ((grow >> 1) & 7)) * 16) + (l15 & 1) * 8), gx1 = gx0 + RA(2), gwa = (unsigned)(GW_OFF + l15 * 8);
            asm volatile("" : "+v"(gx0), "+v"(gx1), "+v"(gwa));
            u32x2 gxv, gwv;
            typedef const volatile __attribute__((address_space(3))) u32x2* l2p;
#define VBAR(t) do { SCHED(); asm volatile("" ::: "memory"); BAR(); asm volatile("" ::: "memory"); \
                     gxv = *(l2p)(size_t)(RA(t) == 0 ? gx0 : gx1 + (RA(t) - RA(2))); gwv = *(l2p)(size_t)(gwa + (t) * 128); SCHED(); } while (0)
#define GATE() do { SCHED(); gacc = fmaf(lo16(gxv.x), lo16(gwv.x), gacc); gacc = fmaf(hi16(gxv.x), hi16(gwv.x), gacc); \
                    gacc = fmaf(lo16(gxv.y), lo16(gwv.y), gacc); gacc = fmaf(hi16(gxv.y), hi16(gwv.y), gacc); asm volatile("" : "+v"(gacc)); SCHED(); } while (0)
            unsigned ka[4][2], sel[4];
            const int R0 = 4 + (l15 >> 2) * 16 + (l15 & 3) - (q4 >> 1);
            unsigned kr0 = (unsigned)(2 * R0), kr1 = (unsigned)(ST_OFF + vw * ST_SZ + R0 * 128 + (((q4 ^ (R0 >> 1)) & 1) * 16));
            asm volatile("" : "+v"(kr0), "+v"(kr1));
#pragma unroll
            for (int i = 0; i < 4; ++i)
#pragma unroll
                for (int tp = 0; tp < 2; ++tp) {
                    ka[i][tp] = (kr1 ^ (tp * 16)) + ((kr0 + 8 * i - 4 * tp) & 0xe0) + (4 * i - 2 * tp) * 128;
                    asm volatile("" : "+v"(ka[i][tp]));
                }
#pragma unroll
            for (int r = 0; r < 4; ++r) {
                const unsigned pick = (q4 & 2) ? 0x0302u : 0x0100u;
                sel[r] = ((l15 >> 1) == (q4 & 1) * 4 + r) ? ((l15 & 1) ? (pick << 16) | 0x0c0cu : 0x0c0c0000u | pick) : 0x0c0c0c0cu;
            }
            unsigned va_ = (unsigned)(ST_OFF + vw * ST_SZ + ST_V + (q4 * 2 + (l15 >> 3)) * 1056 + ((l15 >> 2) & 1) * 128 + (l15 & 3) * 8);
            unsigned so = (unsigned)(((dch >> 2) * 32 + sblk) * 512 + q4 * 64 + (dch & 3) * 16);
            PIN4(sel[0], sel[1], sel[2], sel[3]); asm volatile("" : "+v"(so), "+v"(va_));
            asm volatile("s_waitcnt vmcnt(9)" : "+v"(tw[0]), "+v"(tw[1]), "+v"(tw[2]), "+v"(tw[3]), "+v"(epoch) :: "memory");
            STAMPT(dbg, 18, 384);
            VBAR(0);
            bf16x8 wf[4][2];
#pragma unroll
            for (int ps = 0; ps < 4; ++ps)
#pragma unroll
                for (int tp = 0; tp < 2; ++tp) {
                    u32x4 w;
#pragma unroll
                    for (int r = 0; r < 4; ++r) w[r] = __builtin_amdgcn_perm(0u, tw[ps][tp], sel[r]);
                    wf[ps][tp] = __builtin_bit_cast(bf16x8, w);
                    asm volatile("" : "+v"(wf[ps][tp]));
                }
            SCHED();
            VLD(0); VLD(1); VLD(2); VLD(3); VLD(4); VLD(5); VLD(6); VLD(7);
            GATE(); VBAR(1);
            f32x4 ks[4][4];
            auto sconv = [&](int i0) __attribute__((always_inline)) {
                bf16x8 kf[2][4][2];
#pragma unroll
                for (int q = 0; q < 16; ++q) { kf[q >> 3][q & 3][(q >> 2) & 1] = RD128(ka[i0 + (q >> 3)][(q >> 2) & 1], (q & 3) * 32); SCHED(); }
                GATE();
                __builtin_amdgcn_s_setprio(PRIO_KV);
                WAIT_LGKM0();
                SCHED();
#pragma unroll
                for (int i = 0; i < 2; ++i) {
#pragma unroll
                    for (int ps = 0; ps < 4; ++ps) MF0(ks[ps][i0 + i], kf[i][ps][0], wf[ps][0]);
#pragma unroll
                    for (int ps = 0; ps < 4; ++ps) MF(ks[ps][i0 + i], kf[i][ps][1], wf[ps][1]);
                }
                __builtin_amdgcn_s_setprio(0);
                SCHED();
            };
            WAIT_VM(8);
            STAMPT(dbg, 21, 384);
            sconv(0); VBAR(2);
            sconv(2); VBAR(3);
            WAIT_VM(0);
            u32x2 vq[4][4];
#pragma unroll
            for (int ps = 0; ps < 4; ++ps)
#pragma unroll
                for (int j = 0; j < 4; ++j) vq[ps][j] = __builtin_bit_cast(u32x2, TRD(va_ + j * 256 + ps * 32));
            const __amdgpu_buffer_rsrc_t rs = __builtin_amdgcn_make_buffer_rsrc(kvT, 0, DM * SEQ * 2, 0x00020000);
            u32x4 o[8];
            auto kvout = [&](int n) __attribute__((always_inline)) {
                const int hh = n >> 2, ps = n & 3;
                asm volatile("" : "+v"(ks[ps][2 * hh]), "+v"(ks[ps][2 * hh + 1]));
#pragma unroll
                for (int e = 0; e < 4; ++e) {
                    const unsigned vv = vq[ps][2 * hh + (e >> 1)][e & 1];
                    o[n][e] = pkbf(ks[ps][2 * hh + (e >> 1)][2 * (e & 1)] * lo16(vv), ks[ps][2 * hh + (e >> 1)][2 * (e & 1) + 1] * hi16(vv));
                }
                asm volatile("" : "+v"(o[n]));
                SCHED();
            };
            GATE(); kvout(0); kvout(1);
            WAIT_LGKM0();
            VBAR(4);
            kvout(2); kvout(3); GATE(); VBAR(5);
            kvout(4); kvout(5); GATE(); VBAR(6);
            kvout(6); kvout(7); GATE(); VBAR(7);
#pragma unroll
            for (int n = 0; n < 8; ++n) __builtin_amdgcn_raw_buffer_store_b128(o[(n >> 1) + (n & 1) * 4], rs, so + (unsigned)((n & 1) * 256), (n >> 1) * 65536, WT);
            SCHED();
            asm volatile("s_nop 1" ::: "memory");
            GATE(); VBAR(8);
            if (vw == 1 && lane == 0) *(unsigned*)(smem + TAG_OFF) = (epoch + 1u) ? epoch + 1u : 1u;
            GATE();
            WAIT_LGKM0();
            WAIT_VM(0);
            VBAR(9);
            tag = __builtin_amdgcn_readfirstlane(*(const unsigned*)(smem + TAG_OFF));
            if (vw == 0 && lane == 0) __hip_atomic_store((gu32*)(ws + WS_KVFLAG) + (dblk * 32 + sblk) * 32, tag, RLX_AGENT);
            if (vw == 1 && lane < 4) *(u32x4*)(smem + U_OFF + 192 + lane * 16) = (u32x4){0u, 0u, 0u, 0u};
            gu32* kfp = (gu32*)(ws + WS_KVFLAG) + ((cb >> 5) * 32 + vw * 16 + l15) * 32;
            const char* ub = (const char*)kvT + (size_t)__builtin_amdgcn_readfirstlane(cb * 16384 + vw * 8192);
            const unsigned vu = (unsigned)((lane >> 5) * 512 + ((lane >> 2) & 1) * 256 + ((lane >> 3) & 3) * 64 + ((lane - (lane >> 4)) & 3) * 16);
            const unsigned lu = lbase + U_OFF + 256 + vw * 8192;
#define U_DMA1(o) asm volatile("s_add_u32 m0, %2, %3\n\ts_nop 0\n\tglobal_load_lds_dwordx4 %0, %1 sc1\n\tglobal_load_lds_dwordx4 %0, %1 offset:1024 sc1\n\t" \
                             "global_load_lds_dwordx4 %0, %1 offset:2048 sc1\n\tglobal_load_lds_dwordx4 %0, %1 offset:3072 sc1" \
                             :: "v"(vu), "s"(ub + (o)), "s"(lu), "i"(o) : "memory", "scc")
#define U_DMA() do { U_DMA1(0); U_DMA1(4096); FLAGD(UFLAG_OFF + lw * 4); } while (0)
            GATE(); VBAR(10);
            TOUCH((const char*)(vw ? Cim : Cre) + (size_t)b * 8192, (unsigned)(lane * 128));
            GATE(); VBAR(11);
            GATE(); VBAR(12);
            GATE(); VBAR(13);
            unsigned pv;
            asm volatile("global_load_dword %0, %1, off sc1" : "=&v"(pv) : "v"(kfp) : "memory");
            KLOAD();
            GATE(); VBAR(14);
            GATE(); VBAR(15);
            asm volatile("s_waitcnt vmcnt(5)" : "+v"(pv) :: "memory");
            const bool done = __all(pv == tag);
            if (done) U_DMA();
            if (DBGON && TIDX == 384) dbg[BIDX * 64 + 13] = (done ? 100ull : 0ull) | (1ull << 62);
            GATE();
            if (lane == 0) SETW(UFLAG_OFF, vw, 1u);
            WAIT_VM(9);
            if (!done) {
                unsigned spins = 0;
                for (;;) {
                    const unsigned v = __hip_atomic_load(kfp, RLX_AGENT);
                    if (__all(v == tag)) break;
                    if (++spins > SPIN_MAX) break;
                    __builtin_amdgcn_s_sleep(1);
                }
                U_DMA();
            }
        }
        KWAIT(18);
        STAMPT(dbg, 14, 320);
        __builtin_amdgcn_s_sleep(6);
        {
            const unsigned cp = (unsigned)(KC_OFF + lw * CV_W + 64 + lane * 16);
            if (lane == 0) KA[0].x = (KA[0].x & 0xffff0000u) | f2bf(lo16(KA[0].x) + bf2f((u16)draw));
#define WR128(v, off) asm volatile("ds_write_b128 %0, %1 offset:%2" :: "v"(cp), "v"(v), "i"(off) : "memory")
#pragma unroll
            for (int q = 3; q >= 0; --q) WR128(KA[q], q * 1024);
#pragma unroll
            for (int q = 3; q >= 0; --q) {
                const u32x2 p = q ? pred2(KA[q], RL63(KA[q ? q - 1 : 0].z), RL63(KA[q ? q - 1 : 0].w)) : pred2(KA[0], 0u, 0u);
                const u32x4 a = KA[q];
                const u32x4 s1 = {al(a.x, p.y), al(a.y, a.x), al(a.z, a.y), al(a.w, a.z)};
                WR128(s1, q * 1024 + CV_COPY);
                const u32x4 s2 = {p.y, mv(a.x), mv(a.y), mv(a.z)};
                WR128(s2, q * 1024 + 2 * CV_COPY);
                const u32x4 s3 = {al(p.y, p.x), mv(s1.x), mv(s1.y), mv(s1.z)};
                WR128(s3, q * 1024 + 3 * CV_COPY);
            }
            if (lane < 16) *(u32x4*)(smem + KC_OFF + lw * CV_W + (lane >> 2) * CV_COPY + (lane & 3) * 16) = (u32x4){0u, 0u, 0u, 0u};
            SCHED();
        }
        STAMPT(dbg, 26, 320);
        wait_lds4(smem, G1D_OFF);
        wait_lds4(smem, UFLAG_OFF);
        STAMPT(dbg, 9, 256);
        __builtin_amdgcn_s_setprio(PRIO_CV);
        STAMPT(dbg, 10, 384);
        f32x4 cv[8];
#pragma unroll
        for (int g = 0; g < 8; ++g) cv[g] = (f32x4){0.f, 0.f, 0.f, 0.f};
        PIN4(cv[0], cv[1], cv[2], cv[3]); PIN4(cv[4], cv[5], cv[6], cv[7]);
        const int uz = U_OFF + 192;
        const int ua0 = U_OFF + 256 + (2 * l15 + q4 - 2) * 64, rv0 = (2 * l15 + q4 - 2) * 4 + lw * 16;
        const unsigned ka0 = (unsigned)((l15 >> 2) * CV_COPY - 16 * q4 + 8 * (l15 & 3) - 8);
        int ua, rv, pu, pz;
        unsigned adr;
#define UNEXT1() do { pu = ua | (rv & 0x30); asm volatile("" : "+v"(pu)); } while (0)
#define UNEXT2() do { pz = (ua > uz ? ua : uz) | (rv & 0x30); asm volatile("" : "+v"(pz)); } while (0)
#define UNEXT3() do { ua -= 256; rv -= 16; asm volatile("" : "+v"(ua), "+v"(rv)); } while (0)
#define ANEXT() do { adr += 64; asm volatile("" : "+v"(adr)); } while (0)
#define RDU(dst, c) do { dst = (c) ? RD128(pu, (c) * 2048) : RD128(pz, 0); SCHED(); } while (0)
        HPOLL_SETUP();
        unsigned hpv = 0;
        {
            bf16x8 A[8], UB[8];
            ua = ua0; rv = rv0; adr = lbase + KC_OFF + lw * CV_W + ka0 + 96;
            asm volatile("" : "+v"(adr));
            UNEXT1(); UNEXT2(); UNEXT3();
#pragma unroll
            for (int i = 0; i < 8; ++i) { RDA(A[7 - i], adr, 7 - i); RDU(UB[i], i); }
            unsigned gb = lw >= 2 ? (unsigned)((tb * 16 + nb) * 128 + (lw - 2) * 32) : 0x80000000u;
            asm volatile("" : "+s"(gb));
            const unsigned gso = (l15 == 0) ? gb + (unsigned)(q4 * 8) : 0x80000000u;
            float gt = 0.f;
            unsigned vxo = 0, vdo = 0, vgo = 0;
            auto gate_out = [&](int i) __attribute__((always_inline)) {
                if (i == 0) gacc += __uint_as_float(__builtin_amdgcn_update_dpp(0u, __float_as_uint(gacc), 0xb1, 0xf, 0xf, false));
                if (i == 1) gacc += __uint_as_float(__builtin_amdgcn_update_dpp(0u, __float_as_uint(gacc), 0x4e, 0xf, 0xf, false));
                if (i == 2) gacc += __uint_as_float(__builtin_amdgcn_update_dpp(0u, __float_as_uint(gacc), 0x141, 0xf, 0xf, false));
                if (i == 3) gacc += __uint_as_float(__builtin_amdgcn_update_dpp(0u, __float_as_uint(gacc), 0x140, 0xf, 0xf, false));
                if (i == 4) gacc = rbf(gacc);
                if (i == 5) gacc = __builtin_amdgcn_exp2f(gacc * -1.44269504088896f);
                if (i == 6) gacc = __builtin_amdgcn_rcpf(1.0f + gacc);
                if (i == 7) gacc = rbf(gacc);
                if (i == 8) gt = __uint_as_float(pkbf(gacc, 1.0f - gacc));
                if (i == 9) __builtin_amdgcn_raw_buffer_store_b64((u32x2){__float_as_uint(gt), tag}, gs, gso, 0, WT);
                asm volatile("" : "+v"(gacc), "+v"(gt));
            };
            auto step = [&](auto E) __attribute__((always_inline)) {
                constexpr int e = decltype(E)::value;
                constexpr int ti = (e == CS0) ? 0 : (e == CS1) ? 1 : (e == CS2) ? 2 : -1, tj = ti < 0 ? 0 : ti;
                constexpr unsigned char GA[36] = {0x66, 0x77, 0x76, 0x44, 0x55, 0x54, 0x65, 0x64, 0x75, 0x74, 0x22, 0x33, 0x32, 0x43, 0x42, 0x53, 0x52, 0x63, 0x62, 0x73, 0x72,
                                                  0x00, 0x11, 0x10, 0x21, 0x20, 0x31, 0x30, 0x41, 0x40, 0x51, 0x50, 0x61, 0x60, 0x71, 0x70};
                const char* sb_ = (const char*)Cre + COFF(wid, tj + 1);
                const char* sc_ = (const char*)Cim + COFF(wid, tj + 1);
#pragma unroll
                for (int q = 0; q < 36; ++q) {
                    const int g = GA[q] >> 4, a = GA[q] & 15;
                    MF(cv[g], A[a], UB[g - a]); SCHED();
                    if (e < 7) {
                        if (a == 1) RDU(UB[g - 1], g - 1);
                        if (q == 35) RDU(UB[7], 7);
                        if (g == 7) RDA(A[a], adr + 64 * (e + 1), a);
                        if (q == 11) UNEXT1();
                        if (q == 13) UNEXT2();
                        if (q == 15) UNEXT3();
                    }
                    if (ti >= 0) {
                        const int n = (q >= 3 && q <= 7) ? q - 3 : q == 10 ? 5 : q == 12 ? 6 : q == 14 ? 7 : -1;
                        if (q == 3) { if (ti == 0) WAIT_VM(1); else if (e == XE + 1) WAIT_VM(7); else WAIT_VM(0); }
                        if (n >= 0) CRD(tj, n & 7);
                        if (q == 18) CPIN(tj);
                        if (q == 18 || q == 23) asm volatile("s_add_u32 m0, %3, %4\n\ts_nop 0\n\tglobal_load_lds_dwordx4 %0, %2\n\tglobal_load_lds_dwordx4 %1, %2 offset:1024"
                                                             :: "v"(vB0), "v"(vB1), "s"(q == 18 ? sb_ : sc_), "s"(cst0), "i"(q == 18 ? 0 : 4096) : "memory", "scc");
                        if (q == 21 || q == 25) asm volatile("global_load_lds_dwordx4 %0, %2 offset:2048\n\tglobal_load_lds_dwordx4 %1, %2 offset:3072"
                                                             :: "v"(vB0), "v"(vB1), "s"(q == 21 ? sb_ : sc_) : "memory");
                    }
                    if (e == 0 && q >= 3 && q <= 7) gate_out(q - 3);
                    if (e == 0 && q >= 10 && q <= 18 && !(q & 1)) gate_out(5 + (q - 10) / 2);
                    if (e == 0 && q == 4) CTILE(wid, 0, cst0);
                    if (e == XE && q >= 23 && q <= 35 && (q & 1)) {
                        const int i = (q - 23) / 2;
                        const __amdgpu_buffer_rsrc_t xs = __builtin_amdgcn_make_buffer_rsrc((void*)x, 0, SEQ * DM * 2, 0x00020000);
                        const __amdgpu_buffer_rsrc_t ks = __builtin_amdgcn_make_buffer_rsrc((void*)dskip, 0, DM * 2, 0x00020000);
                        if (i < 4) asm volatile("buffer_load_dwordx2 %0, %1, %2, %3 offen offset:%4" : "=&v"(xx[(i >> 1) & 1][i & 1]) : "v"(vxo), "s"(xs), "s"((TR((i >> 1) & 1) * DM + d_base) * 2), "i"((i & 1) * 2048) : "memory");
                        if (i == 4) asm volatile("buffer_load_dwordx2 %0, %1, %2, %3 offen" : "=&v"(dk2) : "v"(vdo), "s"(ks), "s"(d_base * 2) : "memory");
                        if (i >= 5) asm volatile("buffer_load_dwordx4 %0, %1, %2, %3 offen sc1" : "=&v"(gv[(i - 5) & 1]) : "v"(vgo), "s"(gs), "s"(GSO((i - 5) & 1)) : "memory");
                    }
                    if (e == XE && q == 16) { vxo = (unsigned)(q4 * 32 * DM * 2 + l15 * 8); vdo = (unsigned)(l15 * 8); asm volatile("" : "+v"(vxo), "+v"(vdo)); }
                    if (e == XE && q == 17) { vgo = GV(q4); asm volatile("" : "+v"(vgo)); }
                    if (e == 6 && q == 30) hpv = __hip_atomic_load(hfp, RLX_AGENT);
                    SCHED();
                }
            };
            unroll<8>(step);
            WAIT_VM(0);
            XPIN();
        }
        PAD8(cv[0], cv[1], cv[2], cv[3]); PIN4(cv[4], cv[5], cv[6], cv[7]);
        STAMPT(dbg, 11, 384);
        const unsigned vh = (unsigned)((lane ^ 2) * 16);
        if (!__all(hpv == tag)) HWAIT();
        asm volatile("" ::: "memory");
#pragma unroll
        for (int f = 0; f < DEPTH; ++f) LOADF(f);
        STAMPT(dbg, 25, 384);
        {
            const __amdgpu_buffer_rsrc_t yrs = __builtin_amdgcn_make_buffer_rsrc(y2T, 0, DM * SEQ * 2, 0x00020000);
            const unsigned yo = (unsigned)(d * 4096 + (q4 & 1) * 512 + (l15 >> 3) * 256 + (q4 >> 1) * 128 + (l15 & 7) * 16);
#pragma unroll
            for (int g = 0; g < 8; g += 2) {
                const auto r0 = __builtin_amdgcn_permlane16_swap(pkbf(cv[g][0], cv[g][1]), pkbf(cv[g + 1][0], cv[g + 1][1]), false, false);
                const auto r1 = __builtin_amdgcn_permlane16_swap(pkbf(cv[g][2], cv[g][3]), pkbf(cv[g + 1][2], cv[g + 1][3]), false, false);
                __builtin_amdgcn_raw_buffer_store_b128((u32x4){r0[0], r1[0], r0[1], r1[1]}, yrs, yo + (unsigned)(g * 512), 0, WT);
            }
        }
        asm volatile("" ::: "memory");
        STAMPT(dbg, 15, 384);
        __builtin_amdgcn_s_setprio(PRIO2_L);
    }
    {
        const int lane = fresh_lane(), l15 = lane & 15, q4 = lane >> 4;
        const int ring = wid >> 2;
        const unsigned va = (unsigned)((lane ^ (ring * 2)) * 16), vh = va;
        const unsigned tag = lds_word(smem, TAG_OFF);
        const int cb0 = ring ? KC_OFF : GA_OFF, cst = ring ? CV_W : 8192;
        f32x4 ac[2][4];
        unsigned ca[2], fa[2], fl = 0, fv = 0;
#define CHAIN(u) do { const int c_ = (wid - ((u) >> 1)) & 3; ca[(u) & 1] = va + (unsigned)(cb0 + c_ * cst + ((u) & 1) * 4096); \
                      fa[(u) & 1] = (unsigned)(RF_OFF + ring * 32 + ((u) & 1) * 16 + c_ * 4); asm volatile("" : "+v"(ca[(u) & 1]), "+v"(fa[(u) & 1])); } while (0)
#define RDIN(dst, a, f_) asm volatile("ds_read_b32 %0, %5\n\tds_read_b128 %1, %6\n\tds_read_b128 %2, %6 offset:1024\n\tds_read_b128 %3, %6 offset:2048\n\tds_read_b128 %4, %6 offset:3072" \
                     : "=&v"(fl), "=&v"(dst[0]), "=&v"(dst[1]), "=&v"(dst[2]), "=&v"(dst[3]) : "v"(f_), "v"(a) : "memory")
#define LWAIT(dst) asm volatile("s_waitcnt lgkmcnt(0)" : "+v"(fl), "+v"(dst[0]), "+v"(dst[1]), "+v"(dst[2]), "+v"(dst[3]) :: "memory")
        CHAIN(0); CHAIN(1);
        WAIT_LGKM0();
        CPIN(0); CPIN(1); CPIN(2);
        CREAD(3);
        int tr[2] = {TR(0), TR(1)};
        unsigned gw[2][2];
        {
            unsigned spins = 0;
            while (__builtin_expect(!__all((gv[0].y == tag) & (gv[0].w == tag) & (gv[1].y == tag) & (gv[1].w == tag)), 0)) {
                if (++spins > SPIN_MAX) break;
#pragma unroll
                for (int r = 0; r < 2; ++r) asm volatile("s_nop 4\n\tbuffer_load_dwordx4 %0, %1, %2, %3 offen sc1\n\ts_waitcnt vmcnt(0)" : "=&v"(gv[r]) : "v"(GV(q4)), "s"(gs), "s"(__builtin_amdgcn_readfirstlane(GSO(r))) : "memory");
            }
            gw[0][0] = gv[0].x; gw[0][1] = gv[0].z; gw[1][0] = gv[1].x; gw[1][1] = gv[1].z;
            PIN4(gw[0][0], gw[0][1], gw[1][0], gw[1][1]);
        }
        unsigned cpv = 0, yy[2][4];
        f32x2 zz[2][4];
        unsigned exo = 0, exi = 0, efo = 0, efi = 0;
        auto send = [&](int r) __attribute__((always_inline)) {
            const unsigned one = 1u;
#pragma unroll
            for (int n = 0; n < 4; ++n) { const f32x2 v = {ac[r][n][2], ac[r][n][3]}; asm volatile("ds_write_b64 %0, %1 offset:%2" :: "v"(exo), "v"(v), "i"(r * 4096 + n * 512) : "memory"); }
            asm volatile("ds_write_b32 %0, %1 offset:%2" :: "v"(efo), "v"(one), "i"(r * 4) : "memory");
        };
        STAMPT(dbg, 5, 0);
        auto body = [&](auto F) __attribute__((always_inline)) {
            constexpr int f = decltype(F)::value;
            constexpr int u = f >> 3, k = f & 7;
            f32x4* an = ac[u & 1];
            f32x4* ao = ac[(u + 1) & 1];
#pragma unroll
            for (int n = 0; n < 4; ++n) {
                if (k == 0 && u < 2) MF0(an[n], af[f % DEPTH], cf_[k][n]); else MF(an[n], af[f % DEPTH], cf_[k][n]);
                SCHED();
                const int g = k * 4 + n;
                if (u >= 1 && u < 7) {
                    if (g == 1) { fv = ((u - 1) >> 1) + 1; asm volatile("" : "+v"(fv)); }
                    if (g == 4 || g == 6 || g == 8 || g == 10) asm volatile("ds_write_b128 %0, %1 offset:%2" :: "v"(ca[(u + 1) & 1]), "v"(ao[(g - 4) / 2]), "i"((g - 4) * 512) : "memory");
                    if (g == 12) asm volatile("ds_write_b32 %0, %1" :: "v"(fa[(u + 1) & 1]), "v"(fv) : "memory");
                    if (g == 18) CHAIN(u + 1);
                    if (g == 20) asm volatile("ds_read_b32 %0, %1" : "=&v"(fl) : "v"(fa[(u + 1) & 1]) : "memory");
                    if (g > 20 && g <= 24) asm volatile("ds_read_b128 %0, %1 offset:%2" : "=&v"(ao[g - 21]) : "v"(ca[(u + 1) & 1]), "i"((g - 21) * 1024) : "memory");
                    if (g == 30) {
                        LWAIT(ao);
                        if (__builtin_expect(__builtin_amdgcn_readfirstlane(fl) != (unsigned)((u + 1) >> 1), 0)) {
                            unsigned spins = 0;
                            do {
                                __builtin_amdgcn_s_sleep(1);
                                RDIN(ao, ca[(u + 1) & 1], fa[(u + 1) & 1]); LWAIT(ao);
                                if (++spins > SPIN_MAX * 16u) break;
                            } while (__builtin_amdgcn_readfirstlane(fl) != (unsigned)((u + 1) >> 1));
                        }
                    }
                }
                if (u == 7 && g == 6) send(0);
                if (f >= ZF && f < ZF + 4) {
                    constexpr int r = ((f - ZF) >> 1) & 1, j = (f - ZF) & 1;
                    const unsigned xw = xx[r][j][n >> 1], dw = dk2[n >> 1];
                    const float xf = (n & 1) ? hi16(xw) : lo16(xw), df = (n & 1) ? hi16(dw) : lo16(dw), yf = j ? hi16(yy[r][n]) : lo16(yy[r][n]);
                    zz[r][n][j] = xf * fmaf(hi16(gw[r][j]), yf, lo16(gw[r][j]) * df);
                    asm volatile("" : "+v"(zz[r][n]));
                }
                if (f == ZF + 4) {
                    zz[0][n] = (f32x2){fmaf(lo16(gw[0][0]), ac[0][n][0], zz[0][n][0]), fmaf(lo16(gw[0][1]), ac[0][n][1], zz[0][n][1])};
                    asm volatile("" : "+v"(zz[0][n]));
                }
                SCHED();
            }
            if (f + DEPTH < 64) LOADF(f + DEPTH);
            if (f == DEPTH + 1 && ring) {
                WAIT_VM(6);
                if (lane == 0) __hip_atomic_store((gu32*)(ws + WS_CFLAG) + cb * 32 + (wid & 3), tag, RLX_AGENT);
                SCHED();
            }
            if (f == YPOLL && ring) { const int l15 = fresh_lane() & 15; cpv = __hip_atomic_load((gu32*)(ws + WS_CFLAG) + (nb * 16 + (wid & 3) * 4 + (l15 >> 2)) * 32 + (l15 & 3), RLX_AGENT); SCHED(); }
            if (f == YPOLL + DEPTH && ring) {
                const int ln = fresh_lane(), l15 = ln & 15, q4 = ln >> 4;
                if (__builtin_expect(!__all(cpv == tag), 0)) {
                    unsigned spins = 0;
                    while (!__all(__hip_atomic_load((gu32*)(ws + WS_CFLAG) + (nb * 16 + (wid & 3) * 4 + (l15 >> 2)) * 32 + (l15 & 3), RLX_AGENT) == tag)) {
                        if (++spins > SPIN_MAX) break;
                        __builtin_amdgcn_s_sleep(1);
                    }
                }
                const unsigned lx = lbase + LA_OFF + (wid & 3) * 8192;
#pragma unroll
                for (int i = 0; i < 4; ++i) {
                    const unsigned vo = (unsigned)((d_base + (wid & 3) * 16 + i * 4 + q4) * 4096 + tb * 256 + ((l15 ^ ((wid & 3) * 4 + i)) * 16));
                    GLDS_SC1(y2T, vo, lx, i * 1024);
                }
                SCHED();
            }
            if (f == YPOLL + 2 * DEPTH + 1 && ring) {
                WAIT_VM(6);
                SETW(YFLAG_OFF, wid & 3, 1u);
                SCHED();
            }
            if (f == 55) {
                const int ln = fresh_lane(), c_ = (wid - 3) & 3;
                const int eg = GA_OFF + c_ * 8192, el = KC_OFF + c_ * CV_W;
                exo = (unsigned)((ring ? el : eg) + ln * 8);
                exi = (unsigned)((ring ? eg : el) + ln * 8);
                efo = (unsigned)(EF_OFF + wid * 8); efi = (unsigned)(EF_OFF + (wid ^ 4) * 8);
                PIN4(exo, exi, efo, efi);
                SCHED();
            }
            if (f == ZF - 2) {
                const int ln = fresh_lane(), l15 = ln & 15, q4 = ln >> 4;
                wait_lds4(smem, YFLAG_OFF);
#pragma unroll
                for (int r = 0; r < 2; ++r) {
                    const int jb = JB(6 + r);
                    const int ya = LA_OFF + (l15 >> 2) * 8192 + (l15 & 3) * 1024 + (((((jb >> 1) & 1) * 8 + (jb >> 2) + q4 * 2) ^ l15) * 16) + (jb & 1) * 8 + ring * 4;
#pragma unroll
                    for (int e = 0; e < 4; ++e) yy[r][e] = *(const unsigned*)(smem + ya + e * 256);
                }
                SCHED();
            }
        };
        unroll<64>(body);
        PAD8(ac[1][0], ac[1][1], ac[1][2], ac[1][3]);
        send(1);
        STAMPT(dbg, 6, 0);
        const __amdgpu_buffer_rsrc_t ors = __builtin_amdgcn_make_buffer_rsrc(out, 0, SEQ * DM * 4, 0x00020000);
        const unsigned ov = (unsigned)((lane >> 4) * (32 * DM * 4) + (lane & 15) * 16);
#pragma unroll
        for (int r = 0; r < 2; ++r) {
            f32x2 in[4];
#define EXRD() asm volatile("ds_read_b32 %0, %5 offset:%7\n\tds_read_b64 %1, %6 offset:%8\n\tds_read_b64 %2, %6 offset:%9\n\tds_read_b64 %3, %6 offset:%10\n\tds_read_b64 %4, %6 offset:%11" \
                             : "=&v"(fl), "=&v"(in[0]), "=&v"(in[1]), "=&v"(in[2]), "=&v"(in[3]) \
                             : "v"(efi), "v"(exi), "i"(r * 4), "i"(r * 4096), "i"(r * 4096 + 512), "i"(r * 4096 + 1024), "i"(r * 4096 + 1536) : "memory")
            EXRD();
            if (r == 0) {
#pragma unroll
                for (int n = 0; n < 4; ++n) {
                    zz[1][n] = (f32x2){fmaf(lo16(gw[1][0]), ac[1][n][0], zz[1][n][0]), fmaf(lo16(gw[1][1]), ac[1][n][1], zz[1][n][1])};
                    asm volatile("" : "+v"(zz[1][n]));
                }
            }
            LWAIT(in);
            if (__builtin_expect(__builtin_amdgcn_readfirstlane(fl) == 0u, 0)) {
                unsigned spins = 0;
                do {
                    EXRD(); LWAIT(in);
                    if (++spins > SPIN_MAX * 16u) break;
                } while (__builtin_amdgcn_readfirstlane(fl) == 0u);
            }
            f32x4 o[2];
#pragma unroll
            for (int j = 0; j < 2; ++j)
#pragma unroll
                for (int e = 0; e < 4; ++e) o[j][e] = fmaf(lo16(gw[r][j]), in[e][j], zz[r][e][j]);
#pragma unroll
            for (int j = 0; j < 2; ++j)
                __builtin_amdgcn_raw_buffer_store_b128(__builtin_bit_cast(u32x4, o[j]), ors, ov + (unsigned)(((tr[r] + j) * DM + d_base) * 4), 0, WT);
        }
        STAMPT(dbg, 7, 0);
        STAMPT(dbg, 16, 384);
        if (tb == 15 && nb == 0 && wid == 0 && lane == 0) __hip_atomic_store((gu32*)(ws + WS_EPOCH), tag, RLX_AGENT);
    }
}

extern "C" void kernel_launch(void* const* d_in, const int* in_sizes, int n_in,
                              void* d_out, int out_size, void* d_ws, size_t ws_size,
                              hipStream_t stream) {
    const u16* x = (const u16*)d_in[0];
    const u16* lre = (const u16*)d_in[1];
    const u16* lim = (const u16*)d_in[2];
    const u16* Bre = (const u16*)d_in[3];
    const u16* Bim = (const u16*)d_in[4];
    const u16* Cre = (const u16*)d_in[5];
    const u16* Cim = (const u16*)d_in[6];
    const u16* logdt = (const u16*)d_in[7];
    const u16* dskip = (const u16*)d_in[8];
    const u16* kin = (const u16*)d_in[10];
    const u16* vin = (const u16*)d_in[11];
    const u16* shiftk = (const u16*)d_in[12];
    const u16* ssmk = (const u16*)d_in[13];
    const u16* ssmD = (const u16*)d_in[14];
    const u16* gatew = (const u16*)d_in[15];
    float* out = (float*)d_out;

    char* ws = (char*)d_ws;

    hipFuncSetAttribute((const void*)k1_kernel, hipFuncAttributeMaxDynamicSharedMemorySize, K1_LDS);

    k1_kernel<<<256, 512, K1_LDS, stream>>>(x, Bre, Bim, kin, vin, shiftk, gatew, ws, lre, lim, logdt, ssmk, ssmD, Cre, Cim, dskip, out);
}
#if defined(__HIP_DEVICE_COMPILE__)
#pragma clang attribute pop
#endif
```

```cpp
#if defined(__HIP_DEVICE_COMPILE__)
#pragma clang attribute push(__attribute__((target("no-packed-fp32-ops"))), apply_to = function)
#endif
#include <hip/hip_runtime.h>
#include <hip/hip_bf16.h>
#include <stdint.h>

#define TIDX ((int)__builtin_amdgcn_workitem_id_x())
#define BIDX ((int)__builtin_amdgcn_workgroup_id_x())
typedef unsigned short u16;
typedef __attribute__((ext_vector_type(8))) short bf16x8;
typedef __attribute__((ext_vector_type(4))) float f32x4;
typedef __attribute__((ext_vector_type(2))) float f32x2;
typedef __attribute__((ext_vector_type(2))) __bf16 bf16x2;
typedef __attribute__((ext_vector_type(4))) unsigned u32x4;
typedef __attribute__((ext_vector_type(2))) unsigned u32x2;
typedef __attribute__((address_space(1))) unsigned long long gu64;
typedef __attribute__((address_space(1))) unsigned gu32;
typedef __attribute__((address_space(3))) void lds_void;
typedef __attribute__((address_space(3))) unsigned lds_u32;
typedef __attribute__((ext_vector_type(4))) short s16x4;
typedef __attribute__((address_space(3))) s16x4 lds_s16x4;
#define TRD(a) __builtin_amdgcn_ds_read_tr16_b64_v4i16((lds_s16x4*)(size_t)(a))

#define DBGON 0
#define STAMPT(buf, k, t) do { } while (0)
#define CODE_LINES 258
#define SEQ 2048
#define DM 1024
#define SPIN_MAX (1u << 18)
#define PRIO_G 2
#define PSLEEP 4
#define PRIO_L 3
#define WT 16

__device__ __forceinline__ float bf2f(u16 v) { return __uint_as_float(((unsigned)v) << 16); }
__device__ __forceinline__ u16 f2bf(float f) { __bf16 b = (__bf16)f; return __builtin_bit_cast(u16, b); }
__device__ __forceinline__ float rbf(float f) { return bf2f(f2bf(f)); }
__device__ __forceinline__ unsigned pkbf(float lo, float hi) { return __builtin_bit_cast(unsigned, __builtin_convertvector((f32x2){lo, hi}, bf16x2)); }
__device__ __forceinline__ float lo16(unsigned u) { return __uint_as_float(u << 16); }
__device__ __forceinline__ float hi16(unsigned u) { return __uint_as_float(u & 0xffff0000u); }

struct cf { float r, i; };
__device__ __forceinline__ cf cmul(cf a, cf b) { return {a.r * b.r - a.i * b.i, a.r * b.i + a.i * b.r}; }
__device__ __forceinline__ cf cfma(cf a, cf b, cf c) {
    return {fmaf(a.r, b.r, fmaf(-a.i, b.i, c.r)), fmaf(a.r, b.i, fmaf(a.i, b.r, c.i))};
}
__device__ __forceinline__ f32x4 splat4(float v) { return (f32x4){v, v, v, v}; }
__device__ __forceinline__ f32x4 fma4(f32x4 a, f32x4 b, f32x4 c) { return __builtin_elementwise_fma(a, b, c); }
__device__ __forceinline__ f32x4 mul4(f32x4 a, f32x4 b) { return a * b; }

__device__ __forceinline__ float red32(float a, float b) {
    const auto r = __builtin_amdgcn_permlane32_swap(__float_as_uint(a), __float_as_uint(b), false, false);
    return __uint_as_float(r[0]) + __uint_as_float(r[1]);
}
__device__ __forceinline__ float red16(float x) {
    const auto r = __builtin_amdgcn_permlane16_swap(__float_as_uint(x), __float_as_uint(x), false, false);
    return __uint_as_float(r[0]) + __uint_as_float(r[1]);
}
__device__ __forceinline__ int fresh_lane() {
    int l;
    asm volatile("v_mbcnt_lo_u32_b32 %0, -1, 0\n\tv_mbcnt_hi_u32_b32 %0, -1, %0" : "=v"(l));
    return l;
}
#define BAR() __builtin_amdgcn_s_barrier()
#define WAIT_VM(n) asm volatile("s_waitcnt vmcnt(" #n ")" ::: "memory")
#define WAIT_LGKM0() __builtin_amdgcn_s_waitcnt(0xC07F)
#define SCHED() __builtin_amdgcn_sched_barrier(0)
#define MF(c, a, b) asm volatile("v_mfma_f32_16x16x32_bf16 %0, %1, %2, %0" : "+v"(c) : "v"(a), "v"(b))
#define PAD8(c0, c1, c2, c3) asm volatile("s_nop 7" : "+v"(c0), "+v"(c1), "+v"(c2), "+v"(c3))
#define PIN4(c0, c1, c2, c3) asm volatile("" : "+v"(c0), "+v"(c1), "+v"(c2), "+v"(c3))
#define GLDS_S(sbase, voff, sl, IMM) \
    asm volatile("s_add_u32 m0, %2, %3\n\ts_nop 0\n\tglobal_load_lds_dwordx4 %0, %1" \
                 :: "v"(voff), "s"(sbase), "s"(sl), "i"(IMM) : "memory", "scc")
#define GLDS_K(sbase, voff, sl, IMM) \
    asm volatile("s_add_u32 m0, %2, %3\n\ts_nop 0\n\tglobal_load_lds_dwordx4 %0, %1 nt" \
                 :: "v"(voff), "s"(sbase), "s"(sl), "i"(IMM) : "memory", "scc")
#define RLX_AGENT __ATOMIC_RELAXED, __HIP_MEMORY_SCOPE_AGENT

#define UNITS_A 0x0756075607560560ull
#define UNITS_B 0x1432143214321432ull
#define RA(t) ((int)((UNITS_A >> (4 * (t))) & 15) * 16384)
#define RB(t) ((int)((UNITS_B >> (4 * (t))) & 15) * 16384)
#define LA_OFF 0
#define GA_OFF 32768
#define CV_COPY 4160
#define CV_W (4 * CV_COPY)
#define KC_OFF 65536
#define ST_OFF 114688
#define ST_V 8832
#define ST_SZ (ST_V + 8 * 1056)
#define U_OFF (KC_OFF + 16 * CV_COPY)
#define HT_OFF (U_OFF + 256 + 16384)
#define GW_OFF (HT_OFF + 8192)
#define TAG_OFF (GW_OFF + 2048)
#define UFLAG_OFF (TAG_OFF + 16)
#define G1D_OFF (UFLAG_OFF + 16)
#define MISC_OFF (G1D_OFF + 16)
#define YFLAG_OFF (MISC_OFF + 16)
#define RF_OFF (YFLAG_OFF + 16)
#define EF_OFF (RF_OFF + 64)
#define COK_OFF (EF_OFF + 64)
#define NFLAGW 52
#define DUMMY_OFF (COK_OFF + 16)
#define K1_LDS (DUMMY_OFF + 256)
#define WS_GRAN 0
#define WS_KVFLAG (256u << 10)
#define WS_EPOCH (WS_KVFLAG + 65536)
#define WS_CFLAG (WS_EPOCH + 4096)
#define WS_HFLAG (384u << 10)
#define WS_GATE (512u << 10)

#define CODE_TOUCH(vl) asm volatile("s_getpc_b64 s[98:99]\n\ts_add_u32 s98, s98, k1_kernel@rel32@lo+4\n\ts_addc_u32 s99, s99, k1_kernel@rel32@hi+12\n\t" \
                                    "s_mov_b32 m0, %1\n\ts_nop 0\n\tglobal_load_lds_dword %0, s[98:99]" :: "v"((unsigned)(vl) * 128u), "i"(DUMMY_OFF) : "s98", "s99", "scc", "memory")
#define RD128(a, off) (*(const __attribute__((address_space(3))) bf16x8*)(size_t)(unsigned)((a) + (off)))
#define RDA(dst, A, F) do { const s16x4 lo_ = TRD((A) + 512 * (F) + 8); SCHED(); const s16x4 hi_ = TRD((A) + 512 * (F)); SCHED(); \
                            dst = __builtin_shufflevector(lo_, hi_, 0, 1, 2, 3, 4, 5, 6, 7); } while (0)
#define TOUCH(sbase, voff) asm volatile("s_mov_b32 m0, %2\n\ts_nop 0\n\tglobal_load_lds_dword %0, %1" :: "v"(voff), "s"(sbase), "i"(DUMMY_OFF) : "memory")
#define FLAGD(off) do { unsigned long long pc_; asm volatile("s_getpc_b64 %0\n\ts_mov_b32 m0, %2\n\ts_mov_b64 exec, 1\n\tglobal_load_lds_dword %1, %0\n\ts_mov_b64 exec, -1" \
                                                             : "=&s"(pc_) : "v"(0u), "s"(__builtin_amdgcn_readfirstlane(off)) : "memory"); } while (0)
#define GLDS_SC1(sbase, voff, sl, IMM) \
    asm volatile("s_add_u32 m0, %2, %3\n\ts_nop 0\n\tglobal_load_lds_dwordx4 %0, %1 sc1" \
                 :: "v"(voff), "s"(sbase), "s"(sl), "i"(IMM) : "memory", "scc")
__device__ __forceinline__ unsigned mv(unsigned v) { unsigned r; asm("v_mov_b32 %0, %1" : "=v"(r) : "v"(v)); return r; }
__device__ __forceinline__ unsigned al(unsigned hi, unsigned lo) { unsigned r; asm volatile("v_alignbit_b32 %0, %1, %2, 16" : "=v"(r) : "v"(hi), "v"(lo)); return r; }
__device__ __forceinline__ u32x2 pred2(const u32x4& a, unsigned f0, unsigned f1) {
    return (u32x2){(unsigned)__builtin_amdgcn_update_dpp((int)f0, (int)a.z, 0x138, 0xf, 0xf, false), (unsigned)__builtin_amdgcn_update_dpp((int)f1, (int)a.w, 0x138, 0xf, 0xf, false)};
}
#define RL63(v) ((unsigned)__builtin_amdgcn_readlane((int)(v), 63))
__device__ __forceinline__ void wait_lds4(char* smem, int off) {
    const unsigned a = (unsigned)(size_t)(lds_void*)smem + off;
    unsigned spins = 0;
    for (;;) {
        u32x4 f;
        asm volatile("ds_read_b128 %0, %1\n\ts_waitcnt lgkmcnt(0)" : "=&v"(f) : "v"(a) : "memory");
        if (__builtin_amdgcn_readfirstlane((f.x != 0u) & (f.y != 0u) & (f.z != 0u) & (f.w != 0u))) break;
        if (++spins > SPIN_MAX * 16u) break;
        __builtin_amdgcn_s_sleep(1);
    }
}
__device__ __forceinline__ unsigned lds_word(char* smem, int off) {
    const unsigned a = (unsigned)(size_t)(lds_void*)smem + off;
    unsigned f;
    asm volatile("ds_read_b32 %0, %1\n\ts_waitcnt lgkmcnt(0)" : "=&v"(f) : "v"(a) : "memory");
    return __builtin_amdgcn_readfirstlane(f);
}
__device__ __forceinline__ void wait_lds1(char* smem, int off) {
    unsigned spins = 0;
    while (lds_word(smem, off) == 0u) {
        if (++spins > SPIN_MAX * 16u) break;
        __builtin_amdgcn_s_sleep(1);
    }
}
#define SETW(off, i, v) __hip_atomic_store((lds_u32*)(smem + (off)) + (i), (v), __ATOMIC_RELAXED, __HIP_MEMORY_SCOPE_WORKGROUP)

#define PRIO_CV 1
#define PRIO_KV 3
#define CS0 1
#define CS1 3
#define CS2 5
#define XE 4
#define YPOLL 32
#define ZF 59
template <int I> struct IC { static constexpr int value = I; };
template <int N, int I = 0, class F> __device__ __forceinline__ void unroll(F& f) { if constexpr (I < N) { f(IC<I>{}); unroll<N, I + 1>(f); } }
#define MF0(c, a, b) asm volatile("v_mfma_f32_16x16x32_bf16 %0, %1, %2, 0" : "=&v"(c) : "v"(a), "v"(b))
#define DEPTH 6
#define PRIO2_G 0
#define PRIO2_L 1
__device__ __forceinline__ cf pick4(int q, cf a1, cf a2, cf a3) {
    return {q == 0 ? 1.f : q == 1 ? a1.r : q == 2 ? a2.r : a3.r, q == 0 ? 0.f : q == 1 ? a1.i : q == 2 ? a2.i : a3.i};
}
#define PINC(c) asm volatile("" : "+v"((c).r), "+v"((c).i))

extern "C" __global__ __launch_bounds__(512, 2) void k1_kernel(
        const u16* __restrict__ x, const u16* __restrict__ Bre, const u16* __restrict__ Bim,
        const u16* __restrict__ kin, const u16* __restrict__ vin, const u16* __restrict__ shiftk, const u16* __restrict__ gatew, char* ws,
        const u16* __restrict__ lre, const u16* __restrict__ lim, const u16* __restrict__ logdt,
        const u16* __restrict__ ssmk, const u16* __restrict__ ssmD,
        const u16* __restrict__ Cre, const u16* __restrict__ Cim, const u16* __restrict__ dskip, float* __restrict__ out) {
    extern __shared__ __attribute__((aligned(16))) char smem[];
#define h ((u16*)(ws + (1u << 20)))
#define kvT ((u16*)(ws + (9u << 20)))
#define y2T ((u16*)(ws + (13u << 20)))
#define dbg ((unsigned long long*)(ws + (64u << 20)))
    const int tid = TIDX;
    if (tid < 40 && tid + ((BIDX >> 3) & 7) * 40 < CODE_LINES) CODE_TOUCH(tid + ((BIDX >> 3) & 7) * 40);
    const int lane = tid & 63;
    int wid = __builtin_amdgcn_readfirstlane(tid >> 6);
    const int b = BIDX;
    const int xcd = b & 7, idx = b >> 3;
    const int tb = ((xcd >> 1) ^ (xcd >> 2)) * 4 + (idx & 3), nb = (xcd & 1) * 8 + (idx >> 2);
    const int cb = xcd * 32 + idx;
    const int t_base = tb * 128, p_base = nb * 64, d_base = nb * 64;
    const int l15 = lane & 15, q4 = lane >> 4;
    unsigned stv = 0, sth = 0;
    STAMPT(dbg, 0, 0);
    const unsigned lbase = (unsigned)(size_t)(lds_void*)smem;
    const int rot1 = (xcd >> 1) * 4 + (xcd & 1) * 8 + (idx >> 2) + (idx & 1);
    unsigned voA[4] = {0u, 0u, 0u, 0u}, voB[4] = {0u, 0u, 0u, 0u}, lw0 = 0;
    if (wid == 4 || wid == 5) {
        __builtin_amdgcn_s_setprio(PRIO_L);
        const int lh = wid - 4;
        const unsigned ch = (unsigned)(((lane & 7) ^ (lane >> 4)) * 16);
        voA[0] = (unsigned)(t_base + lh * 16 + (lane >> 5) * 32 + ((lane >> 3) & 3)) * 2048u + ch;
        voA[1] = (voA[0] ^ 64u) + (131072u - 1024u);
        voA[2] = voA[0] + 6144u; voA[3] = voA[1] + 6144u;
        voB[0] = (unsigned)(p_base + lh * 32 + (lane >> 3)) * 2048u + ch;
        voB[1] = (voB[0] ^ 64u) + (16384u - 1024u);
        voB[2] = voB[0] + 30720u; voB[3] = voB[1] + 30720u;
        PIN4(voA[0], voA[1], voA[2], voA[3]); PIN4(voB[0], voB[1], voB[2], voB[3]);
#define GLDS4(sbase, v0, v1, v2, v3, sl, IMM) \
            asm volatile("s_add_u32 m0, %5, %6\n\ts_nop 0\n\tglobal_load_lds_dwordx4 %0, %4\n\tglobal_load_lds_dwordx4 %1, %4 offset:1024\n\t" \
                         "global_load_lds_dwordx4 %2, %4 offset:2048\n\tglobal_load_lds_dwordx4 %3, %4 offset:3072" \
                         :: "v"(v0), "v"(v1), "v"(v2), "v"(v3), "s"(sbase), "s"(sl), "i"(IMM) : "memory", "scc")
#define STAGE_A(kt, sl) do { const char* sa_ = (const char*)x + (((kt) + rot1) & 15) * 128; \
            GLDS4(sa_, voA[0], voA[1], voA[2], voA[3], sl, 0); GLDS4(sa_ + 16384, voA[0], voA[1], voA[2], voA[3], sl, 4096); } while (0)
#define STAGE_B(kt, sl) do { const int kr_ = (((kt) + rot1) & 15) * 128; const char* sb_ = (const char*)Bre + kr_; const char* sc_ = (const char*)Bim + kr_; \
            GLDS4(sb_, voB[0], voB[1], voB[2], voB[3], sl, 0); GLDS4(sc_, voB[0], voB[1], voB[2], voB[3], sl, 4096); } while (0)
        lw0 = lbase + lh * 8192;
        STAGE_A(0, lw0 + RA(0)); STAGE_B(0, lw0 + RB(0)); STAGE_A(1, lw0 + RA(1)); STAGE_B(1, lw0 + RB(1));
    }
    const int sblk = b >> 3, dblk = b & 7;
    u32x2 tw[4];
    unsigned epoch;
    asm volatile("" : "=v"(tw[0]), "=v"(tw[1]), "=v"(tw[2]), "=v"(tw[3]), "=v"(epoch));
    if (wid >= 6) {
        __builtin_amdgcn_s_setprio(PRIO_L);
        const int vw = wid - 6;
        const int dch = dblk * 128 + vw * 64 + l15;
        const unsigned lk = lbase + ST_OFF + vw * ST_SZ;
        const unsigned vk = (unsigned)((lane >> 3) * DM * 2 + (((lane & 7) ^ ((lane >> 4) & 1)) * 16));
        const char* kb = (const char*)kin + (size_t)(sblk * 64 - 4) * (DM * 2) + dblk * 256 + vw * 128;
#define KLD(n) GLDS_K(kb + (n) * 8 * DM * 2, vk, lk, (n) * 1024 + ((n) >> 1) * 32)
#define VLD(n) GLDS_K(vb + (((n) >> 1) * 16 + ((n) & 1) * 2) * DM * 2, vv_, lk, ST_V + (n) * 1056)
        if (vw == 1) {
            const unsigned vg = (unsigned)((lane * 16 + rot1 * 128) & 2047), lg = lbase + GW_OFF;
            GLDS_S(gatew, vg, lg, 0); GLDS_S(gatew, vg ^ 1024u, lg, 1024);
        }
        asm volatile("global_load_dwordx2 %0, %5, %7\n\tglobal_load_dwordx2 %1, %5, %7 offset:128\n\tglobal_load_dwordx2 %2, %5, %7 offset:256\n\t"
                     "global_load_dwordx2 %3, %5, %7 offset:384\n\tglobal_load_dword %4, %6, %8 sc1"
                     : "+v"(tw[0]), "+v"(tw[1]), "+v"(tw[2]), "+v"(tw[3]), "+v"(epoch) : "v"((unsigned)(dch * 8)), "v"((unsigned)WS_EPOCH), "s"(shiftk), "s"(ws) : "memory");
        if (sblk == 0 && lane < 32) *(u32x4*)(smem + ST_OFF + vw * ST_SZ + lane * 16) = (u32x4){0u, 0u, 0u, 0u};
        if (sblk != 0 || lane >= 32) KLD(0);
        KLD(1); KLD(2); KLD(3); KLD(4); KLD(5); KLD(6); KLD(7);
        if (lane < 32) KLD(8);
        __builtin_amdgcn_s_setprio(0);
    }
    asm volatile("" : "+s"(wid));

#define WSL(w) ((((w) >> 2) ^ (nb >> 3)) * 4 + ((w) & 3))
#define COFF(w, ti) ((2 * WSL(w) + ((ti) >> 1)) * 128 + ((ti) & 1) * 4096)
    bf16x8 cf_[8][4];
    bf16x8 af[DEPTH];
    int cra, crb;
#define CT_SETUP() const unsigned vB0 = (unsigned)((d_base + (lane >> 3) * 4) * 2048 + (((lane & 7) ^ (lane >> 4)) * 16)), vB1 = (vB0 ^ 64u) + 65536u - 1024u
#define CTILE(w, ti, dst) do { const int co_ = COFF(w, ti); const unsigned ds_ = (dst); \
                               GLDS4((const char*)Cre + co_, vB0, vB1, vB0, vB1, ds_, 0); GLDS4((const char*)Cim + co_, vB0, vB1, vB0, vB1, ds_, 4096); } while (0)
#define CF(ti, n) cf_[4 * ((ti) >> 1) + ((n) >> 1)][2 * ((ti) & 1) + ((n) & 1)]
#define CRD(ti, n) CF(ti, n) = RD128((((n) & 4) ? crb : cra) ^ (((n) & 2) * 16), ((n) & 1) * 2048)
#define CREAD(ti) do { _Pragma("unroll") for (int n_ = 0; n_ < 8; ++n_) CRD(ti, n_); } while (0)
#define CPIN(ti) do { PIN4(CF(ti, 0), CF(ti, 1), CF(ti, 2), CF(ti, 3)); PIN4(CF(ti, 4), CF(ti, 5), CF(ti, 6), CF(ti, 7)); } while (0)
#define CRA(base) ((base) + (q4 >> 1) * 4096 + l15 * 128 + (((q4 & 1) ^ ((l15 >> 1) & 7)) * 16))
    const int rot2 = (idx & 16) >> 2;
#define JB(u) (((((u) ^ (rot2 >> 2)) & 1) << 2) | ((wid - ((u) >> 1) + rot2) & 3))
    const __amdgpu_buffer_rsrc_t hs = __builtin_amdgcn_make_buffer_rsrc((char*)h + (size_t)tb * 524288 + WSL(wid) * 8192, 0, 524288, 0x00020000);
#define LOADF(f) do { af[(f) % DEPTH] = __builtin_bit_cast(bf16x8, __builtin_amdgcn_raw_buffer_load_b128(hs, vh + ((f) & 3) * 1024, JB((f) >> 3) * 65536 + ((f) & 4) * 1024, 16)); SCHED(); } while (0)
#define HPOLL_SETUP() gu32* hfp = (gu32*)(ws + WS_HFLAG) + (tb * 16 + 2 * WSL(wid) + (lane & 1)) * 32
#define HWAIT() do { unsigned spins = 0; \
            while (!__all(__hip_atomic_load(hfp, RLX_AGENT) == tag)) { if (++spins > SPIN_MAX) break; __builtin_amdgcn_s_sleep(PSLEEP); } asm volatile("" ::: "memory"); } while (0)

    const __amdgpu_buffer_rsrc_t gs = __builtin_amdgcn_make_buffer_rsrc(ws + WS_GATE, 0, 32768, 0x00020000);
#define GV(q4) ((unsigned)(((q4) >> 1) * 128 + ((q4) & 1) * 32))
#define GSO(r) (tb * 2048 + JB(6 + (r)) * 256 + (wid >> 2) * 16)
#define TR(r) (t_base + JB(6 + (r)) * 4 + (wid >> 2) * 2)
    u32x2 xx[2][2], dk2;
    u32x4 gv[2];
#define XLOAD() do { \
        const __amdgpu_buffer_rsrc_t xs = __builtin_amdgcn_make_buffer_rsrc((void*)x, 0, SEQ * DM * 2, 0x00020000); \
        const __amdgpu_buffer_rsrc_t ks = __builtin_amdgcn_make_buffer_rsrc((void*)dskip, 0, DM * 2, 0x00020000); \
        dk2 = __builtin_amdgcn_raw_buffer_load_b64(ks, (unsigned)(l15 * 8), d_base * 2, 0); \
        _Pragma("unroll") for (int q = 0; q < 4; ++q) \
            xx[q >> 1][q & 1] = __builtin_amdgcn_raw_buffer_load_b64(xs, (unsigned)(q4 * 32 * DM * 2 + l15 * 8 + (q & 1) * DM * 2), (TR(q >> 1) * DM + d_base) * 2, 0); \
        _Pragma("unroll") for (int r = 0; r < 2; ++r) gv[r] = __builtin_amdgcn_raw_buffer_load_b128(gs, GV(q4), GSO(r), 16); \
        SCHED(); } while (0)
#define XPIN() do { PIN4(xx[0][0], xx[0][1], xx[1][0], xx[1][1]); asm volatile("" : "+v"(dk2), "+v"(gv[0]), "+v"(gv[1])); } while (0)

    if (wid < 4) {
        typedef __attribute__((ext_vector_type(8))) unsigned u32x8;
        typedef const __attribute__((address_space(4))) u32x8 cu8;
        const int po = (p_base + wid * 16) * 2;
        u32x8 s0 = *(cu8*)((size_t)logdt + po), s1 = *(cu8*)((size_t)lre + po), s2 = *(cu8*)((size_t)lim + po);
        SCHED();
        const int pq = p_base + wid * 16 + l15;
        unsigned goff[4], gmask[4];
#pragma unroll
        for (int i = 0; i < 4; ++i) {
            goff[i] = (unsigned)(((tb - 1 - q4 - 4 * i) * 1024 + pq) * 16);
            gmask[i] = (tb - 1 - q4 - 4 * i >= 0) ? ~0u : 0u;
            asm volatile("" : "+v"(goff[i]), "+v"(gmask[i]));
        }
        unsigned gpub = q4 == 0 ? (unsigned)((tb * 1024 + pq) * 16) : 0x80000000u;
        unsigned hp = (unsigned)(tb * 524288 + (nb * 4 + wid) * 1024 + lane * 16);
        unsigned wa = (unsigned)(HT_OFF + wid * 2048 + (q4 * 16 + (l15 ^ ((q4 >> 1) * 4))) * 8);
        unsigned ra0 = (unsigned)(HT_OFF + wid * 2048 + ((q4 >> 1) * 64 + (l15 & 3) * 16 + (((q4 & 1) * 8 + (l15 >> 2)) ^ ((l15 & 2) * 2))) * 8), ra1 = ra0 ^ 32u;
        cra = CRA(GA_OFF + wid * 8192); crb = cra ^ 64;
        unsigned vB0 = (unsigned)((d_base + (lane >> 3) * 4) * 2048 + (((lane & 7) ^ (lane >> 4)) * 16)), vB1 = (vB0 ^ 64u) + 65536u - 1024u;
        PIN4(gpub, vB0, hp, wa); PIN4(ra0, ra1, cra, crb); asm volatile("" : "+v"(vB1));
        f32x4 acc[8][2];
#pragma unroll
        for (int m = 0; m < 8; m += 2) {
#pragma unroll
            for (int c = 0; c < 4; ++c) acc[m + (c >> 1)][c & 1] = (f32x4){0.f, 0.f, 0.f, 0.f};
            PIN4(acc[m][0], acc[m][1], acc[m + 1][0], acc[m + 1][1]);
        }
        SCHED();
        cf L1, Lq3, cc, L32, cLq, Lp, L512;
        {
            unsigned w0 = 0, w1 = 0, w2 = 0;
#pragma unroll
            for (int i = 0; i < 8; ++i) {
                asm("v_writelane_b32 %0, %3, %6\n\tv_writelane_b32 %1, %4, %6\n\tv_writelane_b32 %2, %5, %6" : "+v"(w0), "+v"(w1), "+v"(w2) : "s"(s0[i]), "s"(s1[i]), "s"(s2[i]), "i"(2 * i));
            }
            asm("s_nop 1" : "+v"(w0), "+v"(w1), "+v"(w2));
            auto spread = [&](unsigned w) {
                const unsigned e = (unsigned)__builtin_amdgcn_update_dpp(0, (int)w, 0xa0, 0xf, 0xf, false);
                const unsigned a = __builtin_amdgcn_permlane16_swap(e, e, false, false)[0];
                const unsigned c = __builtin_amdgcn_permlane32_swap(a, a, false, false)[0];
                return (l15 & 1) ? hi16(c) : lo16(c);
            };
            const float ldt = spread(w0), re = spread(w1), im = spread(w2);
            if (wid == 3 && lane < NFLAGW) *(unsigned*)(smem + UFLAG_OFF + lane * 4) = 0u;
            const float dt = rbf(expf(ldt));
            const float ea = expf(dt * re);
            float sn, cs;
            sincosf(dt * im, &sn, &cs);
            L1 = {ea * cs, ea * sn};
            const float nr = L1.r - 1.0f, ni = L1.i, den = re * re + im * im;
            cc = {2.f * ((nr * re + ni * im) / den), 2.f * ((ni * re - nr * im) / den)};
            const cf T2 = cmul(L1, L1), T4 = cmul(T2, T2), L8 = cmul(T4, T4), L16 = cmul(L8, L8);
            L32 = cmul(L16, L16);
            const cf L64 = cmul(L32, L32), L96 = cmul(L64, L32), L128 = cmul(L64, L64), L256 = cmul(L128, L128), L384 = cmul(L256, L128);
            L512 = cmul(L256, L256);
            Lp = pick4(q4, L128, L256, L384);
            Lq3 = pick4(3 - q4, L32, L64, L96);
            cLq = cmul(cc, pick4(q4, L32, L64, L96));
            PINC(L1); PINC(Lq3); PINC(cc); PINC(L32); PINC(cLq); PINC(Lp); PINC(L512);
        }
        __builtin_amdgcn_s_setprio(PRIO_G);
        unsigned tagv;
        cf S;
        STAMPT(dbg, 22, 0);
        const int swz = (l15 >> 1) & 7;
        {
            const int a0 = RA(0) + l15 * 128 + ((q4 ^ swz) * 16);
            const int b0 = RB(0) + ((wid >> 1) * 64 + (wid & 1) * 16 + l15) * 128 + ((q4 ^ swz) * 16);
            WAIT_LGKM0();
            BAR();
            asm volatile("" ::: "memory");
            STAMPT(dbg, 19, 64);
            bf16x8 FA[8][2], FB[2][2][2];
            int pa[2] = {a0, a0 ^ 64}, pb[2] = {b0, b0 ^ 64};
            PIN4(pa[0], pa[1], pb[0], pb[1]);
#define LGW(n) __builtin_amdgcn_s_waitcnt(0xC07F | ((n) << 8))
#define RA_(j) FA[(j) >> 1][(j) & 1] = RD128(pa[(j) & 1], ((j) >> 1) * 2048)
#define RB_(bu, j) FB[bu][(j) >> 1][((j) ^ ((j) >> 1)) & 1] = RD128(pb[((j) ^ ((j) >> 1)) & 1], ((j) >> 1) * 4096)
#define RN_(bu, i) do { if ((i) == 0) RB_(bu, 0); else if ((i) < 3) RA_((i) - 1); else if ((i) < 6) RB_(bu, (i) - 2); else RA_((i) - 4); } while (0)
#define G1_H(H, bu, RD, FILL) do { \
            _Pragma("unroll") for (int i = 0; i < 16; ++i) { \
                const int m = (H) * 4 + (i >> 2), n = ((i >> 1) ^ m) & 1, ks = (i ^ (i >> 1)) & 1; \
                if ((H) == 0) { if (i == 0) LGW(6); if (i == 4) LGW(8); if (i == 8) LGW(10); if (i == 12) LGW(8); } \
                SCHED(); \
                MF(acc[m][n], FA[m][ks], FB[bu][n][ks]); SCHED(); \
                if ((H) == 0 && i < 8) RA_(8 + i); \
                if ((H) == 1 && (RD) && i < 12) RN_((bu) ^ 1, i); \
                FILL((H) * 16 + i); \
                SCHED(); \
            } } while (0)
#define NOF(g) do { } while (0)
#pragma unroll
            for (int j = 0; j < 12; ++j) { RN_(0, j); SCHED(); }
            unsigned long long ua_ = UNITS_A;
            int sb = RB(0), sa = RA(0), nx, na;
#define ADV(g) do { if ((g) == 8) { ua_ >>= 4; na = (int)((unsigned)ua_ & 15u) << 14; asm volatile("" : "+s"(na), "+s"(ua_)); } \
                    if ((g) == 9) { const int o_ = sa; sa = na; na -= o_; asm volatile("" : "+s"(na), "+s"(sa)); } \
                    if ((g) == 10) { pa[0] += na; pa[1] += na; asm volatile("" : "+v"(pa[0]), "+v"(pa[1])); } \
                    if ((g) == 11) { nx = (sb == 65536) ? -49152 : 16384; sb += nx; asm volatile("" : "+s"(nx), "+s"(sb)); } \
                    if ((g) == 13) { pb[0] += nx; pb[1] += nx; asm volatile("" : "+v"(pb[0]), "+v"(pb[1])); } } while (0)
#define G1_T(bu) do { \
                G1_H(0, bu, true, ADV); \
                WAIT_LGKM0(); \
                asm volatile("" ::: "memory"); \
                BAR();                     \
                asm volatile("" ::: "memory"); \
                G1_H(1, bu, true, NOF); } while (0)
#pragma unroll 1
            for (int t2 = 0; t2 < 7; ++t2) { G1_T(0); G1_T(1); }
            G1_T(0);
#define AGG(g) do { if ((g) == 8) { asm volatile("" ::: "memory"); SETW(G1D_OFF, wid, 1u); }        \
                    if ((g) == 9) tagv = *(volatile const lds_u32*)(size_t)(unsigned)TAG_OFF; \
                    if ((g) >= 7) { const int t = (g) - 6; \
                        if (t == 1) { asm volatile("s_nop 1" : "+v"(acc[0][0]), "+v"(acc[0][1])); S = {acc[0][0][0], acc[0][1][0]}; } \
                        if ((t & 3) == 0) asm volatile("" : "+v"(acc[t >> 2][0]), "+v"(acc[t >> 2][1])); \
                        S = cfma(L1, S, cf{acc[t >> 2][0][t & 3], acc[t >> 2][1][t & 3]}); PINC(S); } } while (0)
            G1_H(0, 1, false, AGG);
            G1_H(1, 1, false, AGG);
        }
        STAMPT(dbg, 1, 0);
        const unsigned tag = __builtin_amdgcn_readfirstlane(tagv);
        const __amdgpu_buffer_rsrc_t grs = __builtin_amdgcn_make_buffer_rsrc(ws, 0, 15 * 16384, 0x00020000);
        {
#pragma unroll
            for (int t = 26; t < 32; ++t) {
                if (t == 28) { PINC(S); asm volatile("" : "+v"(acc[7][0]), "+v"(acc[7][1])); }
                S = cfma(L1, S, cf{acc[t >> 2][0][t & 3], acc[t >> 2][1][t & 3]});
            }
            const cf W = cmul(S, Lq3);
            const float e2 = red16(red32(W.r, W.i));
            const auto eo = __builtin_amdgcn_permlane32_swap(__float_as_uint(e2), __float_as_uint(e2), false, false);
            __builtin_amdgcn_raw_buffer_store_b128((u32x4){eo[0], tag, eo[1], tag}, grs, gpub, 0, 16);
        }
        SCHED();
        STAMPT(dbg, 2, 0);
        const unsigned cg = lbase + GA_OFF + wid * 8192;
        wait_lds1(smem, COK_OFF + wid * 4);
        CREAD(0);
        WAIT_LGKM0();
        CPIN(0);
        CTILE(wid, 1, cg);
        u32x4 gq[4];
#pragma unroll
        for (int m = 0; m < 8; ++m) {
            const f32x4 R = acc[m][0], I = acc[m][1];
            acc[m][0] = fma4(splat4(cc.r), R, mul4(splat4(-cc.i), I));
            acc[m][1] = fma4(splat4(-cc.r), I, mul4(splat4(-cc.i), R));
            asm volatile("" : "+v"(acc[m][0]), "+v"(acc[m][1]));
            if (m == 3) {
                SCHED();
#pragma unroll
                for (int i = 0; i < 4; ++i) asm volatile("buffer_load_dwordx4 %0, %1, %2, 0 offen sc1" : "=&v"(gq[i]) : "v"(goff[i]), "s"(grs) : "memory");
                SCHED();
            }
        }
        cf Ec;
        {
            const auto sr = __builtin_amdgcn_permlane16_swap(__float_as_uint(S.r), __float_as_uint(S.r), false, false);
            const auto si = __builtin_amdgcn_permlane16_swap(__float_as_uint(S.i), __float_as_uint(S.i), false, false);
            const cf Se = {__uint_as_float(sr[0]), __uint_as_float(si[0])}, So = {__uint_as_float(sr[1]), __uint_as_float(si[1])};
            const cf A = cfma(L32, Se, So);
            const auto ar = __builtin_amdgcn_permlane32_swap(__float_as_uint(A.r), __float_as_uint(A.r), false, false);
            const auto ai = __builtin_amdgcn_permlane32_swap(__float_as_uint(A.i), __float_as_uint(A.i), false, false);
            const cf A01 = {(q4 & 2) ? __uint_as_float(ar[0]) : 0.f, (q4 & 2) ? __uint_as_float(ai[0]) : 0.f};
            const cf kq = {(q4 & 1) ? L32.r : 1.f, (q4 & 1) ? L32.i : 0.f};
            const cf E = cfma(kq, A01, cf{(q4 & 1) ? Se.r : 0.f, (q4 & 1) ? Se.i : 0.f});
            Ec = cmul(cc, E);
            PINC(Ec);
        }
        WAIT_VM(4);
        CREAD(1);
        WAIT_LGKM0();
        CPIN(1);
        CTILE(wid, 2, cg);
        asm volatile("s_waitcnt vmcnt(8)" : "+v"(gq[0]), "+v"(gq[1]), "+v"(gq[2]), "+v"(gq[3]) :: "memory");
        STAMPT(dbg, 27, 0);
        cf carry;
        {
            unsigned spins = 0;
            for (;;) {
                unsigned bad = 0;
#pragma unroll
                for (int i = 0; i < 4; ++i) bad |= ((gq[i].y ^ tag) | (gq[i].w ^ tag)) & gmask[i];
                if (__all(bad == 0u)) break;
                if (++spins > SPIN_MAX) break;
#pragma unroll
                for (int i = 0; i < 4; ++i) asm volatile("buffer_load_dwordx4 %0, %1, %2, 0 offen sc1" : "+v"(gq[i]) : "v"(goff[i]), "s"(grs) : "memory");
                asm volatile("s_waitcnt vmcnt(0)" : "+v"(gq[0]), "+v"(gq[1]), "+v"(gq[2]), "+v"(gq[3]) :: "memory");
            }
            cf E = {__uint_as_float(gq[3].x), __uint_as_float(gq[3].z)};
#pragma unroll
            for (int i = 2; i >= 0; --i) E = cfma(L512, E, cf{__uint_as_float(gq[i].x), __uint_as_float(gq[i].z)});
            E = cmul(E, Lp);
            const float e2 = red16(red32(E.r, E.i));
            const auto eo = __builtin_amdgcn_permlane32_swap(__float_as_uint(e2), __float_as_uint(e2), false, false);
            carry = {__uint_as_float(eo[0]), __uint_as_float(eo[1])};
        }
        STAMPT(dbg, 3, 0);
        {
            const __amdgpu_buffer_rsrc_t hrs = __builtin_amdgcn_make_buffer_rsrc(h, 0, SEQ * 2048 * 2, 0x00020000);
            const cf e = cfma(cLq, carry, Ec);
            float hr = e.r, hn = -e.i;
            s16x4 fr[2][4];
#define HSTORE(m) do { _Pragma("unroll") for (int c = 0; c < 2; ++c) \
                __builtin_amdgcn_raw_buffer_store_b128(__builtin_bit_cast(u32x4, __builtin_shufflevector(fr[(m) & 1][2 * c], fr[(m) & 1][2 * c + 1], 0, 1, 2, 3, 4, 5, 6, 7)), hrs, hp, (2 * (m) + c) * 65536, WT); } while (0)
#pragma unroll
            for (int m = 0; m < 4; ++m) {
#pragma unroll
                for (int j = 0; j < 8; ++j) {
                    const float nr = fmaf(L1.r, hr, fmaf(L1.i, hn, acc[2 * m + (j >> 2)][0][j & 3]));
                    const float nn = fmaf(L1.r, hn, fmaf(-L1.i, hr, acc[2 * m + (j >> 2)][1][j & 3]));
                    hr = nr; hn = nn;
                    acc[2 * m + (j >> 2)][0][j & 3] = nr; acc[2 * m + (j >> 2)][1][j & 3] = nn;
                }
                u32x2 pk[4];
#pragma unroll
                for (int c = 0; c < 4; ++c) { const f32x4 v = acc[2 * m + (c >> 1)][c & 1]; pk[c] = (u32x2){pkbf(v[0], v[1]), pkbf(v[2], v[3])}; }
                PIN4(pk[0], pk[1], pk[2], pk[3]);
                SCHED();
#pragma unroll
                for (int c = 0; c < 4; ++c) { *(volatile __attribute__((address_space(3))) u32x2*)(size_t)(wa + c * 512) = pk[c]; SCHED(); }
                asm volatile("" ::: "memory");
#pragma unroll
                for (int c = 0; c < 2; ++c) { fr[m & 1][2 * c] = TRD(ra0 + c * 1024); SCHED(); fr[m & 1][2 * c + 1] = TRD(ra1 + c * 1024); SCHED(); }
                asm volatile("" ::: "memory");
                if (m > 0) HSTORE(m - 1);
                SCHED();
            }
            HSTORE(3);
        }
        STAMPT(dbg, 4, 0);
        {
            WAIT_VM(8);
            CREAD(2);
            WAIT_VM(0);
            {
                unsigned n;
                asm volatile("s_mov_b64 exec, 1\n\tds_add_rtn_u32 %0, %1, %2\n\ts_mov_b64 exec, -1\n\ts_waitcnt lgkmcnt(0)" : "=&v"(n) : "v"(MISC_OFF), "v"(1u) : "memory");
                if (__builtin_amdgcn_readfirstlane(n) == 3u) {
                    if (lane == 0) __hip_atomic_store((gu32*)(ws + WS_HFLAG) + (tb * 16 + nb) * 32, tag, RLX_AGENT);
                    if (DBGON && lane == 0) dbg[BIDX * 64 + 20] = __builtin_amdgcn_s_memrealtime();
                }
            }
            STAMPT(dbg, 12, 0);
            if (wid == 0 && lane < 7) CODE_TOUCH(CODE_LINES - 1 - lane - (idx & 7) * 7);
            __builtin_amdgcn_s_setprio(0);
            WAIT_LGKM0();
            CPIN(2);
            CTILE(wid, 3, cg);
            XLOAD();
            HPOLL_SETUP();
            HWAIT();
            STAMPT(dbg, 8, 0);
            const unsigned vh = (unsigned)(lane * 16);
#pragma unroll
            for (int f = 0; f < DEPTH; ++f) LOADF(f);
            __builtin_amdgcn_s_setprio(PRIO2_G);
        }
    } else {
        const int lw = wid - 4;
        const int d = cb * 4 + lw;
        const char* Krow = (const char*)(ssmk + (size_t)d * SEQ);
        cra = CRA(LA_OFF + lw * 8192); crb = cra ^ 64;
        const unsigned cst0 = lbase + LA_OFF + lw * 8192;
        CT_SETUP();
        asm volatile("" : "+v"(cra), "+v"(crb));
        u32x4 KA[4];
        unsigned draw;
        unsigned tag;
        float gacc = 0.f;
#define KLOAD() asm volatile("global_load_dwordx4 %0, %5, %7 nt\n\tglobal_load_dwordx4 %1, %5, %7 offset:1024 nt\n\tglobal_load_dwordx4 %2, %5, %7 offset:2048 nt\n\t" \
                             "global_load_dwordx4 %3, %5, %7 offset:3072 nt\n\tglobal_load_ushort %4, %6, %8" \
                             : "=&v"(KA[0]), "=&v"(KA[1]), "=&v"(KA[2]), "=&v"(KA[3]), "=&v"(draw) : "v"((unsigned)(lane * 16)), "v"((unsigned)(d * 2)), "s"(Krow), "s"(ssmD) : "memory")
#define KWAIT(n) asm volatile("s_waitcnt vmcnt(%5)" : "+v"(KA[0]), "+v"(KA[1]), "+v"(KA[2]), "+v"(KA[3]), "+v"(draw) : "i"(n) : "memory")
        if (wid < 6) {
            STAGE_A(2, lw0 + RA(2)); STAGE_B(2, lw0 + RB(2));
            WAIT_VM(32);
            STAMPT(dbg, 17, 320);
            BAR();
            STAGE_B(3, lw0 + RB(3));
            unsigned long long ua_ = UNITS_A >> 12;
            unsigned sla = lw0 + RA(3), slb = lw0 + RB(4);
            int ka = ((3 + rot1) & 15) * 128, kb_ = ka;
            const char* sa = (const char*)x + ka;
            asm volatile("" : "+s"(sa), "+s"(sla), "+s"(ka), "+s"(ua_));
#define NEXT_A() do { ka = (ka + 128) & 2047; sa = (const char*)x + ka; ua_ >>= 4; sla = lw0 + (((unsigned)ua_ & 15u) << 14); asm volatile("" : "+s"(sa), "+s"(sla), "+s"(ka), "+s"(ua_)); } while (0)
#define LSTEP(n) do { \
                WAIT_VM(n); \
                BAR(); \
                GLDS4(sa, voA[0], voA[1], voA[2], voA[3], sla, 0); \
                GLDS4(sa + 16384, voA[0], voA[1], voA[2], voA[3], sla, 4096); \
                kb_ = (kb_ + 128) & 2047; \
                GLDS4((const char*)Bre + kb_, voB[0], voB[1], voB[2], voB[3], slb, 0); \
                NEXT_A(); \
                const unsigned slb0 = slb; \
                slb = (slb == lw0 + 65536) ? lw0 + 16384 : slb + 16384; \
                asm volatile("" : "+s"(slb), "+s"(kb_), "+s"(t)); \
                GLDS4((const char*)Bim + kb_, voB[0], voB[1], voB[2], voB[3], slb0, 4096); } while (0)
#pragma unroll 1
            for (int t = 1; t < 5; ++t) LSTEP(24);
            GLDS4(sa, voA[0], voA[1], voA[2], voA[3], sla, 0); GLDS4(sa + 16384, voA[0], voA[1], voA[2], voA[3], sla, 4096);
            NEXT_A();
#pragma unroll 1
            for (int t = 5; t < 13; ++t) LSTEP(32);
            KLOAD();
            WAIT_VM(37);
            BAR();
            __builtin_amdgcn_s_setprio(0);
            tag = lds_word(smem, TAG_OFF);
            CTILE(lw, 0, cst0 + GA_OFF);
            FLAGD(COK_OFF + lw * 4);
            WAIT_VM(30);
            BAR();
            CTILE(lw + 2, 0, cst0 + GA_OFF + 16384);
            FLAGD(COK_OFF + lw * 4 + 8);
            WAIT_VM(23);
            BAR();
        } else {
            const int vw = wid - 6;
            const int dch = dblk * 128 + vw * 64 + l15;
            const unsigned lk = lbase + ST_OFF + vw * ST_SZ;
            const unsigned vv_ = (unsigned)(((lane >> 4) * 4 + ((lane >> 3) & 1)) * DM * 2 + (lane & 7) * 16);
            const char* vb = (const char*)vin + (size_t)(sblk * 64) * (DM * 2) + dblk * 256 + vw * 128;
            const int grow = nb * 8 + vw * 4 + q4;
            unsigned gx0 = (unsigned)(grow * 128 + (((l15 >> 1) ^ ((grow >> 1) & 7)) * 16) + (l15 & 1) * 8), gx1 = gx0 + RA(2), gwa = (unsigned)(GW_OFF + l15 * 8);
            asm volatile("" : "+v"(gx0), "+v"(gx1), "+v"(gwa));
            u32x2 gxv, gwv;
            typedef const volatile __attribute__((address_space(3))) u32x2* l2p;
#define VBAR(t) do { SCHED(); asm volatile("" ::: "memory"); BAR(); asm volatile("" ::: "memory"); \
                     gxv = *(l2p)(size_t)(RA(t) == 0 ? gx0 : gx1 + (RA(t) - RA(2))); gwv = *(l2p)(size_t)(gwa + (t) * 128); SCHED(); } while (0)
#define GATE() do { SCHED(); gacc = fmaf(lo16(gxv.x), lo16(gwv.x), gacc); gacc = fmaf(hi16(gxv.x), hi16(gwv.x), gacc); \
                    gacc = fmaf(lo16(gxv.y), lo16(gwv.y), gacc); gacc = fmaf(hi16(gxv.y), hi16(gwv.y), gacc); asm volatile("" : "+v"(gacc)); SCHED(); } while (0)
            unsigned ka[4][2], sel[4];
            const int R0 = 4 + (l15 >> 2) * 16 + (l15 & 3) - (q4 >> 1);
            unsigned kr0 = (unsigned)(2 * R0), kr1 = (unsigned)(ST_OFF + vw * ST_SZ + R0 * 128 + (((q4 ^ (R0 >> 1)) & 1) * 16));
            asm volatile("" : "+v"(kr0), "+v"(kr1));
#pragma unroll
            for (int i = 0; i < 4; ++i)
#pragma unroll
                for (int tp = 0; tp < 2; ++tp) {
                    ka[i][tp] = (kr1 ^ (tp * 16)) + ((kr0 + 8 * i - 4 * tp) & 0xe0) + (4 * i - 2 * tp) * 128;
                    asm volatile("" : "+v"(ka[i][tp]));
                }
#pragma unroll
            for (int r = 0; r < 4; ++r) {
                const unsigned pick = (q4 & 2) ? 0x0302u : 0x0100u;
                sel[r] = ((l15 >> 1) == (q4 & 1) * 4 + r) ? ((l15 & 1) ? (pick << 16) | 0x0c0cu : 0x0c0c0000u | pick) : 0x0c0c0c0cu;
            }
            unsigned va_ = (unsigned)(ST_OFF + vw * ST_SZ + ST_V + (q4 * 2 + (l15 >> 3)) * 1056 + ((l15 >> 2) & 1) * 128 + (l15 & 3) * 8);
            unsigned so = (unsigned)(((dch >> 2) * 32 + sblk) * 512 + q4 * 64 + (dch & 3) * 16);
            PIN4(sel[0], sel[1], sel[2], sel[3]); asm volatile("" : "+v"(so), "+v"(va_));
            asm volatile("s_waitcnt vmcnt(9)" : "+v"(tw[0]), "+v"(tw[1]), "+v"(tw[2]), "+v"(tw[3]), "+v"(epoch) :: "memory");
            STAMPT(dbg, 18, 384);
            VBAR(0);
            bf16x8 wf[4][2];
#pragma unroll
            for (int ps = 0; ps < 4; ++ps)
#pragma unroll
                for (int tp = 0; tp < 2; ++tp) {
                    u32x4 w;
#pragma unroll
                    for (int r = 0; r < 4; ++r) w[r] = __builtin_amdgcn_perm(0u, tw[ps][tp], sel[r]);
                    wf[ps][tp] = __builtin_bit_cast(bf16x8, w);
                    asm volatile("" : "+v"(wf[ps][tp]));
                }
            SCHED();
            VLD(0); VLD(1); VLD(2); VLD(3); VLD(4); VLD(5); VLD(6); VLD(7);
            GATE(); VBAR(1);
            f32x4 ks[4][4];
            auto sconv = [&](int i0) __attribute__((always_inline)) {
                bf16x8 kf[2][4][2];
#pragma unroll
                for (int q = 0; q < 16; ++q) { kf[q >> 3][q & 3][(q >> 2) & 1] = RD128(ka[i0 + (q >> 3)][(q >> 2) & 1], (q & 3) * 32); SCHED(); }
                GATE();
                __builtin_amdgcn_s_setprio(PRIO_KV);
                WAIT_LGKM0();
                SCHED();
#pragma unroll
                for (int i = 0; i < 2; ++i) {
#pragma unroll
                    for (int ps = 0; ps < 4; ++ps) MF0(ks[ps][i0 + i], kf[i][ps][0], wf[ps][0]);
#pragma unroll
                    for (int ps = 0; ps < 4; ++ps) MF(ks[ps][i0 + i], kf[i][ps][1], wf[ps][1]);
                }
                __builtin_amdgcn_s_setprio(0);
                SCHED();
            };
            WAIT_VM(8);
            STAMPT(dbg, 21, 384);
            sconv(0); VBAR(2);
            sconv(2); VBAR(3);
            WAIT_VM(0);
            u32x2 vq[4][4];
#pragma unroll
            for (int ps = 0; ps < 4; ++ps)
#pragma unroll
                for (int j = 0; j < 4; ++j) vq[ps][j] = __builtin_bit_cast(u32x2, TRD(va_ + j * 256 + ps * 32));
            const __amdgpu_buffer_rsrc_t rs = __builtin_amdgcn_make_buffer_rsrc(kvT, 0, DM * SEQ * 2, 0x00020000);
            u32x4 o[8];
            auto kvout = [&](int n) __attribute__((always_inline)) {
                const int hh = n >> 2, ps = n & 3;
                asm volatile("" : "+v"(ks[ps][2 * hh]), "+v"(ks[ps][2 * hh + 1]));
#pragma unroll
                for (int e = 0; e < 4; ++e) {
                    const unsigned vv = vq[ps][2 * hh + (e >> 1)][e & 1];
                    o[n][e] = pkbf(ks[ps][2 * hh + (e >> 1)][2 * (e & 1)] * lo16(vv), ks[ps][2 * hh + (e >> 1)][2 * (e & 1) + 1] * hi16(vv));
                }
                asm volatile("" : "+v"(o[n]));
                SCHED();
            };
            GATE(); kvout(0); kvout(1);
            WAIT_LGKM0();
            VBAR(4);
            kvout(2); kvout(3); GATE(); VBAR(5);
            kvout(4); kvout(5); GATE(); VBAR(6);
            kvout(6); kvout(7); GATE(); VBAR(7);
#pragma unroll
            for (int n = 0; n < 8; ++n) __builtin_amdgcn_raw_buffer_store_b128(o[(n >> 1) + (n & 1) * 4], rs, so + (unsigned)((n & 1) * 256), (n >> 1) * 65536, WT);
            SCHED();
            asm volatile("s_nop 1" ::: "memory");
            GATE(); VBAR(8);
            if (vw == 1 && lane == 0) *(unsigned*)(smem + TAG_OFF) = (epoch + 1u) ? epoch + 1u : 1u;
            GATE();
            WAIT_LGKM0();
            WAIT_VM(0);
            VBAR(9);
            tag = __builtin_amdgcn_readfirstlane(*(const unsigned*)(smem + TAG_OFF));
            if (vw == 0 && lane == 0) __hip_atomic_store((gu32*)(ws + WS_KVFLAG) + (dblk * 32 + sblk) * 32, tag, RLX_AGENT);
            if (vw == 1 && lane < 4) *(u32x4*)(smem + U_OFF + 192 + lane * 16) = (u32x4){0u, 0u, 0u, 0u};
            gu32* kfp = (gu32*)(ws + WS_KVFLAG) + ((cb >> 5) * 32 + vw * 16 + l15) * 32;
            const char* ub = (const char*)kvT + (size_t)__builtin_amdgcn_readfirstlane(cb * 16384 + vw * 8192);
            const unsigned vu = (unsigned)((lane >> 5) * 512 + ((lane >> 2) & 1) * 256 + ((lane >> 3) & 3) * 64 + ((lane - (lane >> 4)) & 3) * 16);
            const unsigned lu = lbase + U_OFF + 256 + vw * 8192;
#define U_DMA1(o) asm volatile("s_add_u32 m0, %2, %3\n\ts_nop 0\n\tglobal_load_lds_dwordx4 %0, %1 sc1\n\tglobal_load_lds_dwordx4 %0, %1 offset:1024 sc1\n\t" \
                             "global_load_lds_dwordx4 %0, %1 offset:2048 sc1\n\tglobal_load_lds_dwordx4 %0, %1 offset:3072 sc1" \
                             :: "v"(vu), "s"(ub + (o)), "s"(lu), "i"(o) : "memory", "scc")
#define U_DMA() do { U_DMA1(0); U_DMA1(4096); FLAGD(UFLAG_OFF + lw * 4); } while (0)
            GATE(); VBAR(10);
            TOUCH((const char*)(vw ? Cim : Cre) + (size_t)b * 8192, (unsigned)(lane * 128));
            {
                const int cl = 64 + lane + ((idx & 15) * 2 + vw) * 7;
                if (lane < 7 && cl < CODE_LINES) CODE_TOUCH(cl);
            }
            GATE(); VBAR(11);
            GATE(); VBAR(12);
            GATE(); VBAR(13);
            unsigned pv;
            asm volatile("global_load_dword %0, %1, off sc1" : "=&v"(pv) : "v"(kfp) : "memory");
            KLOAD();
            GATE(); VBAR(14);
            GATE(); VBAR(15);
            asm volatile("s_waitcnt vmcnt(5)" : "+v"(pv) :: "memory");
            const bool done = __all(pv == tag);
            if (done) U_DMA();
            if (DBGON && TIDX == 384) dbg[BIDX * 64 + 13] = (done ? 100ull : 0ull) | (1ull << 62);
            GATE();
            if (lane == 0) SETW(UFLAG_OFF, vw, 1u);
            WAIT_VM(9);
            if (!done) {
                unsigned spins = 0;
                for (;;) {
                    const unsigned v = __hip_atomic_load(kfp, RLX_AGENT);
                    if (__all(v == tag)) break;
                    if (++spins > SPIN_MAX) break;
                    __builtin_amdgcn_s_sleep(1);
                }
                U_DMA();
            }
        }
        KWAIT(18);
        STAMPT(dbg, 14, 320);
        __builtin_amdgcn_s_sleep(6);
        {
            const unsigned cp = (unsigned)(KC_OFF + lw * CV_W + 64 + lane * 16);
            if (lane == 0) KA[0].x = (KA[0].x & 0xffff0000u) | f2bf(lo16(KA[0].x) + bf2f((u16)draw));
#define WR128(v, off) asm volatile("ds_write_b128 %0, %1 offset:%2" :: "v"(cp), "v"(v), "i"(off) : "memory")
#pragma unroll
            for (int q = 3; q >= 0; --q) WR128(KA[q], q * 1024);
#pragma unroll
            for (int q = 3; q >= 0; --q) {
                const u32x2 p = q ? pred2(KA[q], RL63(KA[q ? q - 1 : 0].z), RL63(KA[q ? q - 1 : 0].w)) : pred2(KA[0], 0u, 0u);
                const u32x4 a = KA[q];
                const u32x4 s1 = {al(a.x, p.y), al(a.y, a.x), al(a.z, a.y), al(a.w, a.z)};
                WR128(s1, q * 1024 + CV_COPY);
                const u32x4 s2 = {p.y, mv(a.x), mv(a.y), mv(a.z)};
                WR128(s2, q * 1024 + 2 * CV_COPY);
                const u32x4 s3 = {al(p.y, p.x), mv(s1.x), mv(s1.y), mv(s1.z)};
                WR128(s3, q * 1024 + 3 * CV_COPY);
            }
            if (lane < 16) *(u32x4*)(smem + KC_OFF + lw * CV_W + (lane >> 2) * CV_COPY + (lane & 3) * 16) = (u32x4){0u, 0u, 0u, 0u};
            SCHED();
        }
        STAMPT(dbg, 26, 320);
        wait_lds4(smem, G1D_OFF);
        wait_lds4(smem, UFLAG_OFF);
        STAMPT(dbg, 9, 256);
        __builtin_amdgcn_s_setprio(PRIO_CV);
        STAMPT(dbg, 10, 384);
        f32x4 cv[8];
#pragma unroll
        for (int g = 0; g < 8; ++g) cv[g] = (f32x4){0.f, 0.f, 0.f, 0.f};
        PIN4(cv[0], cv[1], cv[2], cv[3]); PIN4(cv[4], cv[5], cv[6], cv[7]);
        const int uz = U_OFF + 192;
        const int ua0 = U_OFF + 256 + (2 * l15 + q4 - 2) * 64, rv0 = (2 * l15 + q4 - 2) * 4 + lw * 16;
        const unsigned ka0 = (unsigned)((l15 >> 2) * CV_COPY - 16 * q4 + 8 * (l15 & 3) - 8);
        int ua, rv, pu, pz;
        unsigned adr;
#define UNEXT1() do { pu = ua | (rv & 0x30); asm volatile("" : "+v"(pu)); } while (0)
#define UNEXT2() do { pz = (ua > uz ? ua : uz) | (rv & 0x30); asm volatile("" : "+v"(pz)); } while (0)
#define UNEXT3() do { ua -= 256; rv -= 16; asm volatile("" : "+v"(ua), "+v"(rv)); } while (0)
#define ANEXT() do { adr += 64; asm volatile("" : "+v"(adr)); } while (0)
#define RDU(dst, c) do { dst = (c) ? RD128(pu, (c) * 2048) : RD128(pz, 0); SCHED(); } while (0)
        HPOLL_SETUP();
        unsigned hpv = 0;
        {
            bf16x8 A[8], UB[8];
            ua = ua0; rv = rv0; adr = lbase + KC_OFF + lw * CV_W + ka0 + 96;
            asm volatile("" : "+v"(adr));
            UNEXT1(); UNEXT2(); UNEXT3();
#pragma unroll
            for (int i = 0; i < 8; ++i) { RDA(A[7 - i], adr, 7 - i); RDU(UB[i], i); }
            unsigned gb = lw >= 2 ? (unsigned)((tb * 16 + nb) * 128 + (lw - 2) * 32) : 0x80000000u;
            asm volatile("" : "+s"(gb));
            const unsigned gso = (l15 == 0) ? gb + (unsigned)(q4 * 8) : 0x80000000u;
            float gt = 0.f;
            unsigned vxo = 0, vdo = 0, vgo = 0;
            auto gate_out = [&](int i) __attribute__((always_inline)) {
                if (i == 0) gacc += __uint_as_float(__builtin_amdgcn_update_dpp(0u, __float_as_uint(gacc), 0xb1, 0xf, 0xf, false));
                if (i == 1) gacc += __uint_as_float(__builtin_amdgcn_update_dpp(0u, __float_as_uint(gacc), 0x4e, 0xf, 0xf, false));
                if (i == 2) gacc += __uint_as_float(__builtin_amdgcn_update_dpp(0u, __float_as_uint(gacc), 0x141, 0xf, 0xf, false));
                if (i == 3) gacc += __uint_as_float(__builtin_amdgcn_update_dpp(0u, __float_as_uint(gacc), 0x140, 0xf, 0xf, false));
                if (i == 4) gacc = rbf(gacc);
                if (i == 5) gacc = __builtin_amdgcn_exp2f(gacc * -1.44269504088896f);
                if (i == 6) gacc = __builtin_amdgcn_rcpf(1.0f + gacc);
                if (i == 7) gacc = rbf(gacc);
                if (i == 8) gt = __uint_as_float(pkbf(gacc, 1.0f - gacc));
                if (i == 9) __builtin_amdgcn_raw_buffer_store_b64((u32x2){__float_as_uint(gt), tag}, gs, gso, 0, WT);
                asm volatile("" : "+v"(gacc), "+v"(gt));
            };
            auto step = [&](auto E) __attribute__((always_inline)) {
                constexpr int e = decltype(E)::value;
                constexpr int ti = (e == CS0) ? 0 : (e == CS1) ? 1 : (e == CS2) ? 2 : -1, tj = ti < 0 ? 0 : ti;
                constexpr unsigned char GA[36] = {0x66, 0x77, 0x76, 0x44, 0x55, 0x54, 0x65, 0x64, 0x75, 0x74, 0x22, 0x33, 0x32, 0x43, 0x42, 0x53, 0x52, 0x63, 0x62, 0x73, 0x72,
                                                  0x00, 0x11, 0x10, 0x21, 0x20, 0x31, 0x30, 0x41, 0x40, 0x51, 0x50, 0x61, 0x60, 0x71, 0x70};
                const char* sb_ = (const char*)Cre + COFF(wid, tj + 1);
                const char* sc_ = (const char*)Cim + COFF(wid, tj + 1);
#pragma unroll
                for (int q = 0; q < 36; ++q) {
                    const int g = GA[q] >> 4, a = GA[q] & 15;
                    MF(cv[g], A[a], UB[g - a]); SCHED();
                    if (e < 7) {
                        if (a == 1) RDU(UB[g - 1], g - 1);
                        if (q == 35) RDU(UB[7], 7);
                        if (g == 7) RDA(A[a], adr + 64 * (e + 1), a);
                        if (q == 11) UNEXT1();
                        if (q == 13) UNEXT2();
                        if (q == 15) UNEXT3();
                    }
                    if (ti >= 0) {
                        const int n = (q >= 3 && q <= 7) ? q - 3 : q == 10 ? 5 : q == 12 ? 6 : q == 14 ? 7 : -1;
                        if (q == 3) { if (ti == 0) WAIT_VM(1); else if (e == XE + 1) WAIT_VM(7); else WAIT_VM(0); }
                        if (n >= 0) CRD(tj, n & 7);
                        if (q == 18) CPIN(tj);
                        if (q == 18 || q == 23) asm volatile("s_add_u32 m0, %3, %4\n\ts_nop 0\n\tglobal_load_lds_dwordx4 %0, %2\n\tglobal_load_lds_dwordx4 %1, %2 offset:1024"
                                                             :: "v"(vB0), "v"(vB1), "s"(q == 18 ? sb_ : sc_), "s"(cst0), "i"(q == 18 ? 0 : 4096) : "memory", "scc");
                        if (q == 21 || q == 25) asm volatile("global_load_lds_dwordx4 %0, %2 offset:2048\n\tglobal_load_lds_dwordx4 %1, %2 offset:3072"
                                                             :: "v"(vB0), "v"(vB1), "s"(q == 21 ? sb_ : sc_) : "memory");
                    }
                    if (e == 0 && q >= 3 && q <= 7) gate_out(q - 3);
                    if (e == 0 && q >= 10 && q <= 18 && !(q & 1)) gate_out(5 + (q - 10) / 2);
                    if (e == 0 && q == 4) CTILE(wid, 0, cst0);
                    if (e == XE && q >= 23 && q <= 35 && (q & 1)) {
                        const int i = (q - 23) / 2;
                        const __amdgpu_buffer_rsrc_t xs = __builtin_amdgcn_make_buffer_rsrc((void*)x, 0, SEQ * DM * 2, 0x00020000);
                        const __amdgpu_buffer_rsrc_t ks = __builtin_amdgcn_make_buffer_rsrc((void*)dskip, 0, DM * 2, 0x00020000);
                        if (i < 4) asm volatile("buffer_load_dwordx2 %0, %1, %2, %3 offen offset:%4" : "=&v"(xx[(i >> 1) & 1][i & 1]) : "v"(vxo), "s"(xs), "s"((TR((i >> 1) & 1) * DM + d_base) * 2), "i"((i & 1) * 2048) : "memory");
                        if (i == 4) asm volatile("buffer_load_dwordx2 %0, %1, %2, %3 offen" : "=&v"(dk2) : "v"(vdo), "s"(ks), "s"(d_base * 2) : "memory");
                        if (i >= 5) asm volatile("buffer_load_dwordx4 %0, %1, %2, %3 offen sc1" : "=&v"(gv[(i - 5) & 1]) : "v"(vgo), "s"(gs), "s"(GSO((i - 5) & 1)) : "memory");
                    }
                    if (e == XE && q == 16) { vxo = (unsigned)(q4 * 32 * DM * 2 + l15 * 8); vdo = (unsigned)(l15 * 8); asm volatile("" : "+v"(vxo), "+v"(vdo)); }
                    if (e == XE && q == 17) { vgo = GV(q4); asm volatile("" : "+v"(vgo)); }
                    if (e == 6 && q == 30) hpv = __hip_atomic_load(hfp, RLX_AGENT);
                    SCHED();
                }
            };
            unroll<8>(step);
            WAIT_VM(0);
            XPIN();
        }
        PAD8(cv[0], cv[1], cv[2], cv[3]); PIN4(cv[4], cv[5], cv[6], cv[7]);
        STAMPT(dbg, 11, 384);
        const unsigned vh = (unsigned)((lane ^ 2) * 16);
        if (!__all(hpv == tag)) HWAIT();
        asm volatile("" ::: "memory");
#pragma unroll
        for (int f = 0; f < DEPTH; ++f) LOADF(f);
        STAMPT(dbg, 25, 384);
        {
            const __amdgpu_buffer_rsrc_t yrs = __builtin_amdgcn_make_buffer_rsrc(y2T, 0, DM * SEQ * 2, 0x00020000);
            const unsigned yo = (unsigned)(d * 4096 + (q4 & 1) * 512 + (l15 >> 3) * 256 + (q4 >> 1) * 128 + (l15 & 7) * 16);
#pragma unroll
            for (int g = 0; g < 8; g += 2) {
                const auto r0 = __builtin_amdgcn_permlane16_swap(pkbf(cv[g][0], cv[g][1]), pkbf(cv[g + 1][0], cv[g + 1][1]), false, false);
                const auto r1 = __builtin_amdgcn_permlane16_swap(pkbf(cv[g][2], cv[g][3]), pkbf(cv[g + 1][2], cv[g + 1][3]), false, false);
                __builtin_amdgcn_raw_buffer_store_b128((u32x4){r0[0], r1[0], r0[1], r1[1]}, yrs, yo + (unsigned)(g * 512), 0, WT);
            }
        }
        asm volatile("" ::: "memory");
        STAMPT(dbg, 15, 384);
        __builtin_amdgcn_s_setprio(PRIO2_L);
    }
    {
        const int lane = fresh_lane(), l15 = lane & 15, q4 = lane >> 4;
        const int ring = wid >> 2;
        const unsigned va = (unsigned)((lane ^ (ring * 2)) * 16), vh = va;
        const unsigned tag = lds_word(smem, TAG_OFF);
        const int cb0 = ring ? KC_OFF : GA_OFF, cst = ring ? CV_W : 8192;
        f32x4 ac[2][4];
        unsigned ca[2], fa[2], fl = 0, fv = 0;
#define CHAIN(u) do { const int c_ = (wid - ((u) >> 1)) & 3; ca[(u) & 1] = va + (unsigned)(cb0 + c_ * cst + ((u) & 1) * 4096); \
                      fa[(u) & 1] = (unsigned)(RF_OFF + ring * 32 + ((u) & 1) * 16 + c_ * 4); asm volatile("" : "+v"(ca[(u) & 1]), "+v"(fa[(u) & 1])); } while (0)
#define RDIN(dst, a, f_) asm volatile("ds_read_b32 %0, %5\n\tds_read_b128 %1, %6\n\tds_read_b128 %2, %6 offset:1024\n\tds_read_b128 %3, %6 offset:2048\n\tds_read_b128 %4, %6 offset:3072" \
                     : "=&v"(fl), "=&v"(dst[0]), "=&v"(dst[1]), "=&v"(dst[2]), "=&v"(dst[3]) : "v"(f_), "v"(a) : "memory")
#define LWAIT(dst) asm volatile("s_waitcnt lgkmcnt(0)" : "+v"(fl), "+v"(dst[0]), "+v"(dst[1]), "+v"(dst[2]), "+v"(dst[3]) :: "memory")
        CHAIN(0); CHAIN(1);
        WAIT_LGKM0();
        CPIN(0); CPIN(1); CPIN(2);
        CREAD(3);
        int tr[2] = {TR(0), TR(1)};
        unsigned gw[2][2];
        {
            unsigned spins = 0;
            while (__builtin_expect(!__all((gv[0].y == tag) & (gv[0].w == tag) & (gv[1].y == tag) & (gv[1].w == tag)), 0)) {
                if (++spins > SPIN_MAX) break;
#pragma unroll
                for (int r = 0; r < 2; ++r) asm volatile("s_nop 4\n\tbuffer_load_dwordx4 %0, %1, %2, %3 offen sc1\n\ts_waitcnt vmcnt(0)" : "=&v"(gv[r]) : "v"(GV(q4)), "s"(gs), "s"(__builtin_amdgcn_readfirstlane(GSO(r))) : "memory");
            }
            gw[0][0] = gv[0].x; gw[0][1] = gv[0].z; gw[1][0] = gv[1].x; gw[1][1] = gv[1].z;
            PIN4(gw[0][0], gw[0][1], gw[1][0], gw[1][1]);
        }
        unsigned cpv = 0, yy[2][4];
        f32x2 zz[2][4];
        unsigned exo = 0, exi = 0, efo = 0, efi = 0;
        auto send = [&](int r) __attribute__((always_inline)) {
            const unsigned one = 1u;
#pragma unroll
            for (int n = 0; n < 4; ++n) { const f32x2 v = {ac[r][n][2], ac[r][n][3]}; asm volatile("ds_write_b64 %0, %1 offset:%2" :: "v"(exo), "v"(v), "i"(r * 4096 + n * 512) : "memory"); }
            asm volatile("ds_write_b32 %0, %1 offset:%2" :: "v"(efo), "v"(one), "i"(r * 4) : "memory");
        };
        STAMPT(dbg, 5, 0);
        auto body = [&](auto F) __attribute__((always_inline)) {
            constexpr int f = decltype(F)::value;
            constexpr int u = f >> 3, k = f & 7;
            f32x4* an = ac[u & 1];
            f32x4* ao = ac[(u + 1) & 1];
#pragma unroll
            for (int n = 0; n < 4; ++n) {
                if (k == 0 && u < 2) MF0(an[n], af[f % DEPTH], cf_[k][n]); else MF(an[n], af[f % DEPTH], cf_[k][n]);
                SCHED();
                const int g = k * 4 + n;
                if (u >= 1 && u < 7) {
                    if (g == 1) { fv = ((u - 1) >> 1) + 1; asm volatile("" : "+v"(fv)); }
                    if (g == 4 || g == 6 || g == 8 || g == 10) asm volatile("ds_write_b128 %0, %1 offset:%2" :: "v"(ca[(u + 1) & 1]), "v"(ao[(g - 4) / 2]), "i"((g - 4) * 512) : "memory");
                    if (g == 12) asm volatile("ds_write_b32 %0, %1" :: "v"(fa[(u + 1) & 1]), "v"(fv) : "memory");
                    if (g == 18) CHAIN(u + 1);
                    if (g == 20) asm volatile("ds_read_b32 %0, %1" : "=&v"(fl) : "v"(fa[(u + 1) & 1]) : "memory");
                    if (g > 20 && g <= 24) asm volatile("ds_read_b128 %0, %1 offset:%2" : "=&v"(ao[g - 21]) : "v"(ca[(u + 1) & 1]), "i"((g - 21) * 1024) : "memory");
                    if (g == 30) {
                        LWAIT(ao);
                        if (__builtin_expect(__builtin_amdgcn_readfirstlane(fl) != (unsigned)((u + 1) >> 1), 0)) {
                            unsigned spins = 0;
#pragma unroll 1
                            do {
                                __builtin_amdgcn_s_sleep(1);
                                RDIN(ao, ca[(u + 1) & 1], fa[(u + 1) & 1]); LWAIT(ao);
                                if (++spins > SPIN_MAX * 16u) break;
                            } while (__builtin_amdgcn_readfirstlane(fl) != (unsigned)((u + 1) >> 1));
                        }
                    }
                }
                if (u == 7 && g == 6) send(0);
                if (f >= ZF && f < ZF + 4) {
                    constexpr int r = ((f - ZF) >> 1) & 1, j = (f - ZF) & 1;
                    const unsigned xw = xx[r][j][n >> 1], dw = dk2[n >> 1];
                    const float xf = (n & 1) ? hi16(xw) : lo16(xw), df = (n & 1) ? hi16(dw) : lo16(dw), yf = j ? hi16(yy[r][n]) : lo16(yy[r][n]);
                    zz[r][n][j] = xf * fmaf(hi16(gw[r][j]), yf, lo16(gw[r][j]) * df);
                    asm volatile("" : "+v"(zz[r][n]));
                }
                if (f == ZF + 4) {
                    zz[0][n] = (f32x2){fmaf(lo16(gw[0][0]), ac[0][n][0], zz[0][n][0]), fmaf(lo16(gw[0][1]), ac[0][n][1], zz[0][n][1])};
                    asm volatile("" : "+v"(zz[0][n]));
                }
                SCHED();
            }
            if (f + DEPTH < 64) LOADF(f + DEPTH);
            if (f == DEPTH + 1 && ring) {
                WAIT_VM(6);
                if (lane == 0) __hip_atomic_store((gu32*)(ws + WS_CFLAG) + cb * 32 + (wid & 3), tag, RLX_AGENT);
                SCHED();
            }
            if (f == YPOLL && ring) { const int l15 = fresh_lane() & 15; cpv = __hip_atomic_load((gu32*)(ws + WS_CFLAG) + (nb * 16 + (wid & 3) * 4 + (l15 >> 2)) * 32 + (l15 & 3), RLX_AGENT); SCHED(); }
            if (f == YPOLL + DEPTH && ring) {
                const int ln = fresh_lane(), l15 = ln & 15, q4 = ln >> 4;
                if (__builtin_expect(!__all(cpv == tag), 0)) {
                    unsigned spins = 0;
                    while (!__all(__hip_atomic_load((gu32*)(ws + WS_CFLAG) + (nb * 16 + (wid & 3) * 4 + (l15 >> 2)) * 32 + (l15 & 3), RLX_AGENT) == tag)) {
                        if (++spins > SPIN_MAX) break;
                        __builtin_amdgcn_s_sleep(1);
                    }
                }
                const unsigned lx = lbase + LA_OFF + (wid & 3) * 8192;
#pragma unroll
                for (int i = 0; i < 4; ++i) {
                    const unsigned vo = (unsigned)((d_base + (wid & 3) * 16 + i * 4 + q4) * 4096 + tb * 256 + ((l15 ^ ((wid & 3) * 4 + i)) * 16));
                    GLDS_SC1(y2T, vo, lx, i * 1024);
                }
                SCHED();
            }
            if (f == YPOLL + 2 * DEPTH + 1 && ring) {
                WAIT_VM(6);
                SETW(YFLAG_OFF, wid & 3, 1u);
                SCHED();
            }
            if (f == 55) {
                const int ln = fresh_lane(), c_ = (wid - 3) & 3;
                const int eg = GA_OFF + c_ * 8192, el = KC_OFF + c_ * CV_W;
                exo = (unsigned)((ring ? el : eg) + ln * 8);
                exi = (unsigned)((ring ? eg : el) + ln * 8);
                efo = (unsigned)(EF_OFF + wid * 8); efi = (unsigned)(EF_OFF + (wid ^ 4) * 8);
                PIN4(exo, exi, efo, efi);
                SCHED();
            }
            if (f == ZF - 2) {
                const int ln = fresh_lane(), l15 = ln & 15, q4 = ln >> 4;
                wait_lds4(smem, YFLAG_OFF);
#pragma unroll
                for (int r = 0; r < 2; ++r) {
                    const int jb = JB(6 + r);
                    const int ya = LA_OFF + (l15 >> 2) * 8192 + (l15 & 3) * 1024 + (((((jb >> 1) & 1) * 8 + (jb >> 2) + q4 * 2) ^ l15) * 16) + (jb & 1) * 8 + ring * 4;
#pragma unroll
                    for (int e = 0; e < 4; ++e) yy[r][e] = *(const unsigned*)(smem + ya + e * 256);
                }
                SCHED();
            }
        };
        unroll<64>(body);
        PAD8(ac[1][0], ac[1][1], ac[1][2], ac[1][3]);
        send(1);
        STAMPT(dbg, 6, 0);
        const __amdgpu_buffer_rsrc_t ors = __builtin_amdgcn_make_buffer_rsrc(out, 0, SEQ * DM * 4, 0x00020000);
        const unsigned ov = (unsigned)((lane >> 4) * (32 * DM * 4) + (lane & 15) * 16);
#pragma unroll
        for (int r = 0; r < 2; ++r) {
            f32x2 in[4];
#define EXRD() asm volatile("ds_read_b32 %0, %5 offset:%7\n\tds_read_b64 %1, %6 offset:%8\n\tds_read_b64 %2, %6 offset:%9\n\tds_read_b64 %3, %6 offset:%10\n\tds_read_b64 %4, %6 offset:%11" \
                             : "=&v"(fl), "=&v"(in[0]), "=&v"(in[1]), "=&v"(in[2]), "=&v"(in[3]) \
                             : "v"(efi), "v"(exi), "i"(r * 4), "i"(r * 4096), "i"(r * 4096 + 512), "i"(r * 4096 + 1024), "i"(r * 4096 + 1536) : "memory")
            EXRD();
            if (r == 0) {
#pragma unroll
                for (int n = 0; n < 4; ++n) {
                    zz[1][n] = (f32x2){fmaf(lo16(gw[1][0]), ac[1][n][0], zz[1][n][0]), fmaf(lo16(gw[1][1]), ac[1][n][1], zz[1][n][1])};
                    asm volatile("" : "+v"(zz[1][n]));
                }
            }
            LWAIT(in);
            if (__builtin_expect(__builtin_amdgcn_readfirstlane(fl) == 0u, 0)) {
                unsigned spins = 0;
#pragma unroll 1
                do {
                    EXRD(); LWAIT(in);
                    if (++spins > SPIN_MAX * 16u) break;
                } while (__builtin_amdgcn_readfirstlane(fl) == 0u);
            }
            f32x4 o[2];
#pragma unroll
            for (int j = 0; j < 2; ++j)
#pragma unroll
                for (int e = 0; e < 4; ++e) o[j][e] = fmaf(lo16(gw[r][j]), in[e][j], zz[r][e][j]);
#pragma unroll
            for (int j = 0; j < 2; ++j)
                __builtin_amdgcn_raw_buffer_store_b128(__builtin_bit_cast(u32x4, o[j]), ors, ov + (unsigned)(((tr[r] + j) * DM + d_base) * 4), 0, WT);
        }
        STAMPT(dbg, 7, 0);
        STAMPT(dbg, 16, 384);
        if (tb == 15 && nb == 0 && wid == 0 && lane == 0) __hip_atomic_store((gu32*)(ws + WS_EPOCH), tag, RLX_AGENT);
    }
}

extern "C" void kernel_launch(void* const* d_in, const int* in_sizes, int n_in,
                              void* d_out, int out_size, void* d_ws, size_t ws_size,
                              hipStream_t stream) {
    const u16* x = (const u16*)d_in[0];
    const u16* lre = (const u16*)d_in[1];
    const u16* lim = (const u16*)d_in[2];
    const u16* Bre = (const u16*)d_in[3];
    const u16* Bim = (const u16*)d_in[4];
    const u16* Cre = (const u16*)d_in[5];
    const u16* Cim = (const u16*)d_in[6];
    const u16* logdt = (const u16*)d_in[7];
    const u16* dskip = (const u16*)d_in[8];
    const u16* kin = (const u16*)d_in[10];
    const u16* vin = (const u16*)d_in[11];
    const u16* shiftk = (const u16*)d_in[12];
    const u16* ssmk = (const u16*)d_in[13];
    const u16* ssmD = (const u16*)d_in[14];
    const u16* gatew = (const u16*)d_in[15];
    float* out = (float*)d_out;

    char* ws = (char*)d_ws;

    hipFuncSetAttribute((const void*)k1_kernel, hipFuncAttributeMaxDynamicSharedMemorySize, K1_LDS);

    k1_kernel<<<256, 512, K1_LDS, stream>>>(x, Bre, Bim, kin, vin, shiftk, gatew, ws, lre, lim, logdt, ssmk, ssmD, Cre, Cim, dskip, out);
}
#if defined(__HIP_DEVICE_COMPILE__)
#pragma clang attribute pop
#endif
```

```cpp
#if defined(__HIP_DEVICE_COMPILE__)
#pragma clang attribute push(__attribute__((target("no-packed-fp32-ops"))), apply_to = function)
#endif
#include <hip/hip_runtime.h>
#include <hip/hip_bf16.h>
#include <stdint.h>

#define TIDX ((int)__builtin_amdgcn_workitem_id_x())
#define BIDX ((int)__builtin_amdgcn_workgroup_id_x())
typedef unsigned short u16;
typedef __attribute__((ext_vector_type(8))) short bf16x8;
typedef __attribute__((ext_vector_type(4))) float f32x4;
typedef __attribute__((ext_vector_type(2))) float f32x2;
typedef __attribute__((ext_vector_type(2))) __bf16 bf16x2;
typedef __attribute__((ext_vector_type(4))) unsigned u32x4;
typedef __attribute__((ext_vector_type(2))) unsigned u32x2;
typedef __attribute__((address_space(1))) unsigned long long gu64;
typedef __attribute__((address_space(1))) unsigned gu32;
typedef __attribute__((address_space(3))) void lds_void;
typedef __attribute__((address_space(3))) unsigned lds_u32;
typedef __attribute__((ext_vector_type(4))) short s16x4;
typedef __attribute__((address_space(3))) s16x4 lds_s16x4;
#define TRD(a) __builtin_amdgcn_ds_read_tr16_b64_v4i16((lds_s16x4*)(size_t)(a))

#define DBGON 0
#define STAMPT(buf, k, t) do { } while (0)
#define CODE_LINES 258
#define SEQ 2048
#define DM 1024
#define SPIN_MAX (1u << 18)
#define PRIO_G 2
#define PSLEEP 4
#define PRIO_L 3
#define WT 16

__device__ __forceinline__ float bf2f(u16 v) { return __uint_as_float(((unsigned)v) << 16); }
__device__ __forceinline__ u16 f2bf(float f) { __bf16 b = (__bf16)f; return __builtin_bit_cast(u16, b); }
__device__ __forceinline__ float rbf(float f) { return bf2f(f2bf(f)); }
__device__ __forceinline__ unsigned pkbf(float lo, float hi) { return __builtin_bit_cast(unsigned, __builtin_convertvector((f32x2){lo, hi}, bf16x2)); }
__device__ __forceinline__ float lo16(unsigned u) { return __uint_as_float(u << 16); }
__device__ __forceinline__ float hi16(unsigned u) { return __uint_as_float(u & 0xffff0000u); }

struct cf { float r, i; };
__device__ __forceinline__ cf cmul(cf a, cf b) { return {a.r * b.r - a.i * b.i, a.r * b.i + a.i * b.r}; }
__device__ __forceinline__ cf cfma(cf a, cf b, cf c) {
    return {fmaf(a.r, b.r, fmaf(-a.i, b.i, c.r)), fmaf(a.r, b.i, fmaf(a.i, b.r, c.i))};
}
__device__ __forceinline__ f32x4 splat4(float v) { return (f32x4){v, v, v, v}; }
__device__ __forceinline__ f32x4 fma4(f32x4 a, f32x4 b, f32x4 c) { return __builtin_elementwise_fma(a, b, c); }
__device__ __forceinline__ f32x4 mul4(f32x4 a, f32x4 b) { return a * b; }

__device__ __forceinline__ float red32(float a, float b) {
    const auto r = __builtin_amdgcn_permlane32_swap(__float_as_uint(a), __float_as_uint(b), false, false);
    return __uint_as_float(r[0]) + __uint_as_float(r[1]);
}
__device__ __forceinline__ float red16(float x) {
    const auto r = __builtin_amdgcn_permlane16_swap(__float_as_uint(x), __float_as_uint(x), false, false);
    return __uint_as_float(r[0]) + __uint_as_float(r[1]);
}
__device__ __forceinline__ int fresh_lane() {
    int l;
    asm volatile("v_mbcnt_lo_u32_b32 %0, -1, 0\n\tv_mbcnt_hi_u32_b32 %0, -1, %0" : "=v"(l));
    return l;
}
#define BAR() __builtin_amdgcn_s_barrier()
#define WAIT_VM(n) asm volatile("s_waitcnt vmcnt(" #n ")" ::: "memory")
#define WAIT_LGKM0() __builtin_amdgcn_s_waitcnt(0xC07F)
#define SCHED() __builtin_amdgcn_sched_barrier(0)
#define MF(c, a, b) asm volatile("v_mfma_f32_16x16x32_bf16 %0, %1, %2, %0" : "+v"(c) : "v"(a), "v"(b))
#define PAD8(c0, c1, c2, c3) asm volatile("s_nop 7" : "+v"(c0), "+v"(c1), "+v"(c2), "+v"(c3))
#define PIN4(c0, c1, c2, c3) asm volatile("" : "+v"(c0), "+v"(c1), "+v"(c2), "+v"(c3))
#define GLDS_S(sbase, voff, sl, IMM) \
    asm volatile("s_add_u32 m0, %2, %3\n\ts_nop 0\n\tglobal_load_lds_dwordx4 %0, %1" \
                 :: "v"(voff), "s"(sbase), "s"(sl), "i"(IMM) : "memory", "scc")
#define GLDS_K(sbase, voff, sl, IMM) \
    asm volatile("s_add_u32 m0, %2, %3\n\ts_nop 0\n\tglobal_load_lds_dwordx4 %0, %1 nt" \
                 :: "v"(voff), "s"(sbase), "s"(sl), "i"(IMM) : "memory", "scc")
#define RLX_AGENT __ATOMIC_RELAXED, __HIP_MEMORY_SCOPE_AGENT

#define UNITS_A 0x0756075607560560ull
#define UNITS_B 0x1432143214321432ull
#define RA(t) ((int)((UNITS_A >> (4 * (t))) & 15) * 16384)
#define RB(t) ((int)((UNITS_B >> (4 * (t))) & 15) * 16384)
#define LA_OFF 0
#define GA_OFF 32768
#define CV_COPY 4160
#define CV_W (4 * CV_COPY)
#define KC_OFF 65536
#define ST_OFF 114688
#define ST_V 8832
#define ST_SZ (ST_V + 8 * 1056)
#define U_OFF (KC_OFF + 16 * CV_COPY)
#define HT_OFF (U_OFF + 256 + 16384)
#define GW_OFF (HT_OFF + 8192)
#define TAG_OFF (GW_OFF + 2048)
#define UFLAG_OFF (TAG_OFF + 16)
#define G1D_OFF (UFLAG_OFF + 16)
#define MISC_OFF (G1D_OFF + 16)
#define YFLAG_OFF (MISC_OFF + 16)
#define RF_OFF (YFLAG_OFF + 16)
#define EF_OFF (RF_OFF + 64)
#define COK_OFF (EF_OFF + 64)
#define NFLAGW 52
#define DUMMY_OFF (COK_OFF + 16)
#define K1_LDS (DUMMY_OFF + 256)
#define WS_GRAN 0
#define WS_KVFLAG (256u << 10)
#define WS_EPOCH (WS_KVFLAG + 65536)
#define WS_CFLAG (WS_EPOCH + 4096)
#define WS_HFLAG (384u << 10)
#define WS_GATE (512u << 10)

#define CODE_TOUCH(vl) asm volatile("s_getpc_b64 s[98:99]\n\ts_add_u32 s98, s98, k1_kernel@rel32@lo+4\n\ts_addc_u32 s99, s99, k1_kernel@rel32@hi+12\n\t" \
                                    "s_mov_b32 m0, %1\n\ts_nop 0\n\tglobal_load_lds_dword %0, s[98:99]" :: "v"((unsigned)(vl) * 128u), "i"(DUMMY_OFF) : "s98", "s99", "scc", "memory")
#define RD128(a, off) (*(const __attribute__((address_space(3))) bf16x8*)(size_t)(unsigned)((a) + (off)))
#define RDA(dst, A, F) do { const s16x4 lo_ = TRD((A) + 512 * (F) + 8); SCHED(); const s16x4 hi_ = TRD((A) + 512 * (F)); SCHED(); \
                            dst = __builtin_shufflevector(lo_, hi_, 0, 1, 2, 3, 4, 5, 6, 7); } while (0)
#define TOUCH(sbase, voff) asm volatile("s_mov_b32 m0, %2\n\ts_nop 0\n\tglobal_load_lds_dword %0, %1" :: "v"(voff), "s"(sbase), "i"(DUMMY_OFF) : "memory")
#define FLAGD(off) do { unsigned long long pc_; asm volatile("s_getpc_b64 %0\n\ts_mov_b32 m0, %2\n\ts_mov_b64 exec, 1\n\tglobal_load_lds_dword %1, %0\n\ts_mov_b64 exec, -1" \
                                                             : "=&s"(pc_) : "v"(0u), "s"(__builtin_amdgcn_readfirstlane(off)) : "memory"); } while (0)
#define GLDS_SC1(sbase, voff, sl, IMM) \
    asm volatile("s_add_u32 m0, %2, %3\n\ts_nop 0\n\tglobal_load_lds_dwordx4 %0, %1 sc1" \
                 :: "v"(voff), "s"(sbase), "s"(sl), "i"(IMM) : "memory", "scc")
__device__ __forceinline__ unsigned mv(unsigned v) { unsigned r; asm("v_mov_b32 %0, %1" : "=v"(r) : "v"(v)); return r; }
__device__ __forceinline__ unsigned al(unsigned hi, unsigned lo) { unsigned r; asm volatile("v_alignbit_b32 %0, %1, %2, 16" : "=v"(r) : "v"(hi), "v"(lo)); return r; }
__device__ __forceinline__ u32x2 pred2(const u32x4& a, unsigned f0, unsigned f1) {
    return (u32x2){(unsigned)__builtin_amdgcn_update_dpp((int)f0, (int)a.z, 0x138, 0xf, 0xf, false), (unsigned)__builtin_amdgcn_update_dpp((int)f1, (int)a.w, 0x138, 0xf, 0xf, false)};
}
#define RL63(v) ((unsigned)__builtin_amdgcn_readlane((int)(v), 63))
__device__ __forceinline__ void wait_lds4(char* smem, int off) {
    const unsigned a = (unsigned)(size_t)(lds_void*)smem + off;
    unsigned spins = 0;
    for (;;) {
        u32x4 f;
        asm volatile("ds_read_b128 %0, %1\n\ts_waitcnt lgkmcnt(0)" : "=&v"(f) : "v"(a) : "memory");
        if (__builtin_amdgcn_readfirstlane((f.x != 0u) & (f.y != 0u) & (f.z != 0u) & (f.w != 0u))) break;
        if (++spins > SPIN_MAX * 16u) break;
        __builtin_amdgcn_s_sleep(1);
    }
}
__device__ __forceinline__ unsigned lds_word(char* smem, int off) {
    const unsigned a = (unsigned)(size_t)(lds_void*)smem + off;
    unsigned f;
    asm volatile("ds_read_b32 %0, %1\n\ts_waitcnt lgkmcnt(0)" : "=&v"(f) : "v"(a) : "memory");
    return __builtin_amdgcn_readfirstlane(f);
}
__device__ __forceinline__ void wait_lds1(char* smem, int off) {
    unsigned spins = 0;
    while (lds_word(smem, off) == 0u) {
        if (++spins > SPIN_MAX * 16u) break;
        __builtin_amdgcn_s_sleep(1);
    }
}
#define SETW(off, i, v) __hip_atomic_store((lds_u32*)(smem + (off)) + (i), (v), __ATOMIC_RELAXED, __HIP_MEMORY_SCOPE_WORKGROUP)

#define PRIO_CV 1
#define PRIO_KV 3
#define CS0 1
#define CS1 3
#define CS2 5
#define XE 4
#define YPOLL 32
#define ZF 59
template <int I> struct IC { static constexpr int value = I; };
template <int N, int I = 0, class F> __device__ __forceinline__ void unroll(F& f) { if constexpr (I < N) { f(IC<I>{}); unroll<N, I + 1>(f); } }
#define MF0(c, a, b) asm volatile("v_mfma_f32_16x16x32_bf16 %0, %1, %2, 0" : "=&v"(c) : "v"(a), "v"(b))
#define DEPTH 6
#define PRIO2_G 0
#define PRIO2_L 1
__device__ __forceinline__ cf pick4(int q, cf a1, cf a2, cf a3) {
    return {q == 0 ? 1.f : q == 1 ? a1.r : q == 2 ? a2.r : a3.r, q == 0 ? 0.f : q == 1 ? a1.i : q == 2 ? a2.i : a3.i};
}
#define PINC(c) asm volatile("" : "+v"((c).r), "+v"((c).i))

extern "C" __global__ __launch_bounds__(512, 2) void k1_kernel(
        const u16* __restrict__ x, const u16* __restrict__ Bre, const u16* __restrict__ Bim,
        const u16* __restrict__ kin, const u16* __restrict__ vin, const u16* __restrict__ shiftk, const u16* __restrict__ gatew, char* ws,
        const u16* __restrict__ lre, const u16* __restrict__ lim, const u16* __restrict__ logdt,
        const u16* __restrict__ ssmk, const u16* __restrict__ ssmD,
        const u16* __restrict__ Cre, const u16* __restrict__ Cim, const u16* __restrict__ dskip, float* __restrict__ out) {
    extern __shared__ __attribute__((aligned(16))) char smem[];
#define h ((u16*)(ws + (1u << 20)))
#define kvT ((u16*)(ws + (9u << 20)))
#define y2T ((u16*)(ws + (13u << 20)))
#define dbg ((unsigned long long*)(ws + (64u << 20)))
    const int tid = TIDX;
    if (tid < 40 && tid + ((BIDX >> 3) & 7) * 40 < CODE_LINES) CODE_TOUCH(tid + ((BIDX >> 3) & 7) * 40);
    const int lane = tid & 63;
    int wid = __builtin_amdgcn_readfirstlane(tid >> 6);
    const int b = BIDX;
    const int xcd = b & 7, idx = b >> 3;
    const int tb = ((xcd >> 1) ^ (xcd >> 2)) * 4 + (idx & 3), nb = (xcd & 1) * 8 + (idx >> 2);
    const int cb = xcd * 32 + idx;
    const int t_base = tb * 128, p_base = nb * 64, d_base = nb * 64;
    const int l15 = lane & 15, q4 = lane >> 4;
    unsigned stv = 0, sth = 0;
    STAMPT(dbg, 0, 0);
    const unsigned lbase = (unsigned)(size_t)(lds_void*)smem;
    const int rot1 = (xcd >> 1) * 4 + (xcd & 1) * 8 + (idx >> 2) + (idx & 1);
    unsigned voA[4] = {0u, 0u, 0u, 0u}, voB[4] = {0u, 0u, 0u, 0u}, lw0 = 0;
    if (wid == 4 || wid == 5) {
        __builtin_amdgcn_s_setprio(PRIO_L);
        const int lh = wid - 4;
        const unsigned ch = (unsigned)(((lane & 7) ^ (lane >> 4)) * 16);
        voA[0] = (unsigned)(t_base + lh * 16 + (lane >> 5) * 32 + ((lane >> 3) & 3)) * 2048u + ch;
        voA[1] = (voA[0] ^ 64u) + (131072u - 1024u);
        voA[2] = voA[0] + 6144u; voA[3] = voA[1] + 6144u;
        voB[0] = (unsigned)(p_base + lh * 32 + (lane >> 3)) * 2048u + ch;
        voB[1] = (voB[0] ^ 64u) + (16384u - 1024u);
        voB[2] = voB[0] + 30720u; voB[3] = voB[1] + 30720u;
        PIN4(voA[0], voA[1], voA[2], voA[3]); PIN4(voB[0], voB[1], voB[2], voB[3]);
#define GLDS4(sbase, v0, v1, v2, v3, sl, IMM) \
            asm volatile("s_add_u32 m0, %5, %6\n\ts_nop 0\n\tglobal_load_lds_dwordx4 %0, %4\n\tglobal_load_lds_dwordx4 %1, %4 offset:1024\n\t" \
                         "global_load_lds_dwordx4 %2, %4 offset:2048\n\tglobal_load_lds_dwordx4 %3, %4 offset:3072" \
                         :: "v"(v0), "v"(v1), "v"(v2), "v"(v3), "s"(sbase), "s"(sl), "i"(IMM) : "memory", "scc")
#define STAGE_A(kt, sl) do { const char* sa_ = (const char*)x + (((kt) + rot1) & 15) * 128; \
            GLDS4(sa_, voA[0], voA[1], voA[2], voA[3], sl, 0); GLDS4(sa_ + 16384, voA[0], voA[1], voA[2], voA[3], sl, 4096); } while (0)
#define STAGE_B(kt, sl) do { const int kr_ = (((kt) + rot1) & 15) * 128; const char* sb_ = (const char*)Bre + kr_; const char* sc_ = (const char*)Bim + kr_; \
            GLDS4(sb_, voB[0], voB[1], voB[2], voB[3], sl, 0); GLDS4(sc_, voB[0], voB[1], voB[2], voB[3], sl, 4096); } while (0)
        lw0 = lbase + lh * 8192;
        STAGE_A(0, lw0 + RA(0)); STAGE_B(0, lw0 + RB(0)); STAGE_A(1, lw0 + RA(1)); STAGE_B(1, lw0 + RB(1));
    }
    const int sblk = b >> 3, dblk = b & 7;
    u32x2 tw[4];
    unsigned epoch;
    asm volatile("" : "=v"(tw[0]), "=v"(tw[1]), "=v"(tw[2]), "=v"(tw[3]), "=v"(epoch));
    if (wid >= 6) {
        __builtin_amdgcn_s_setprio(PRIO_L);
        const int vw = wid - 6;
        const int dch = dblk * 128 + vw * 64 + l15;
        const unsigned lk = lbase + ST_OFF + vw * ST_SZ;
        const unsigned vk = (unsigned)((lane >> 3) * DM * 2 + (((lane & 7) ^ ((lane >> 4) & 1)) * 16));
        const char* kb = (const char*)kin + (size_t)(sblk * 64 - 4) * (DM * 2) + dblk * 256 + vw * 128;
#define KLD(n) GLDS_K(kb + (n) * 8 * DM * 2, vk, lk, (n) * 1024 + ((n) >> 1) * 32)
#define VLD(n) GLDS_K(vb + (((n) >> 1) * 16 + ((n) & 1) * 2) * DM * 2, vv_, lk, ST_V + (n) * 1056)
        if (vw == 1) {
            const unsigned vg = (unsigned)((lane * 16 + rot1 * 128) & 2047), lg = lbase + GW_OFF;
            GLDS_S(gatew, vg, lg, 0); GLDS_S(gatew, vg ^ 1024u, lg, 1024);
        }
        asm volatile("global_load_dwordx2 %0, %5, %7\n\tglobal_load_dwordx2 %1, %5, %7 offset:128\n\tglobal_load_dwordx2 %2, %5, %7 offset:256\n\t"
                     "global_load_dwordx2 %3, %5, %7 offset:384\n\tglobal_load_dword %4, %6, %8 sc1"
                     : "+v"(tw[0]), "+v"(tw[1]), "+v"(tw[2]), "+v"(tw[3]), "+v"(epoch) : "v"((unsigned)(dch * 8)), "v"((unsigned)WS_EPOCH), "s"(shiftk), "s"(ws) : "memory");
        if (sblk == 0 && lane < 32) *(u32x4*)(smem + ST_OFF + vw * ST_SZ + lane * 16) = (u32x4){0u, 0u, 0u, 0u};
        if (sblk != 0 || lane >= 32) KLD(0);
        KLD(1); KLD(2); KLD(3); KLD(4); KLD(5); KLD(6); KLD(7);
        if (lane < 32) KLD(8);
        __builtin_amdgcn_s_setprio(0);
    }
    asm volatile("" : "+s"(wid));

#define WSL(w) ((((w) >> 2) ^ (nb >> 3)) * 4 + ((w) & 3))
#define COFF(w, ti) ((2 * WSL(w) + ((ti) >> 1)) * 128 + ((ti) & 1) * 4096)
    bf16x8 cf_[8][4];
    bf16x8 af[DEPTH];
    int cra, crb;
#define CT_SETUP() const unsigned vB0 = (unsigned)((d_base + (lane >> 3) * 4) * 2048 + (((lane & 7) ^ (lane >> 4)) * 16)), vB1 = (vB0 ^ 64u) + 65536u - 1024u
#define CTILE(w, ti, dst) do { const int co_ = COFF(w, ti); const unsigned ds_ = (dst); \
                               GLDS4((const char*)Cre + co_, vB0, vB1, vB0, vB1, ds_, 0); GLDS4((const char*)Cim + co_, vB0, vB1, vB0, vB1, ds_, 4096); } while (0)
#define CF(ti, n) cf_[4 * ((ti) >> 1) + ((n) >> 1)][2 * ((ti) & 1) + ((n) & 1)]
#define CRD(ti, n) CF(ti, n) = RD128((((n) & 4) ? crb : cra) ^ (((n) & 2) * 16), ((n) & 1) * 2048)
#define CREAD(ti) do { _Pragma("unroll") for (int n_ = 0; n_ < 8; ++n_) CRD(ti, n_); } while (0)
#define CPIN(ti) do { PIN4(CF(ti, 0), CF(ti, 1), CF(ti, 2), CF(ti, 3)); PIN4(CF(ti, 4), CF(ti, 5), CF(ti, 6), CF(ti, 7)); } while (0)
#define CRA(base) ((base) + (q4 >> 1) * 4096 + l15 * 128 + (((q4 & 1) ^ ((l15 >> 1) & 7)) * 16))
    const int rot2 = (idx & 16) >> 2;
#define JB(u) (((((u) ^ (rot2 >> 2)) & 1) << 2) | ((wid - ((u) >> 1) + rot2) & 3))
    const __amdgpu_buffer_rsrc_t hs = __builtin_amdgcn_make_buffer_rsrc((char*)h + (size_t)tb * 524288 + WSL(wid) * 8192, 0, 524288, 0x00020000);
#define LOADF(f) do { af[(f) % DEPTH] = __builtin_bit_cast(bf16x8, __builtin_amdgcn_raw_buffer_load_b128(hs, vh + ((f) & 3) * 1024, JB((f) >> 3) * 65536 + ((f) & 4) * 1024, 16)); SCHED(); } while (0)
#define HPOLL_SETUP() gu32* hfp = (gu32*)(ws + WS_HFLAG) + (tb * 16 + 2 * WSL(wid) + (lane & 1)) * 32
#define HWAIT() do { unsigned spins = 0; \
            while (!__all(__hip_atomic_load(hfp, RLX_AGENT) == tag)) { if (++spins > SPIN_MAX) break; __builtin_amdgcn_s_sleep(PSLEEP); } asm volatile("" ::: "memory"); } while (0)

    const __amdgpu_buffer_rsrc_t gs = __builtin_amdgcn_make_buffer_rsrc(ws + WS_GATE, 0, 32768, 0x00020000);
#define GV(q4) ((unsigned)(((q4) >> 1) * 128 + ((q4) & 1) * 32))
#define GSO(r) (tb * 2048 + JB(6 + (r)) * 256 + (wid >> 2) * 16)
#define TR(r) (t_base + JB(6 + (r)) * 4 + (wid >> 2) * 2)
    u32x2 xx[2][2], dk2;
    u32x4 gv[2];
#define XLOAD() do { \
        const __amdgpu_buffer_rsrc_t xs = __builtin_amdgcn_make_buffer_rsrc((void*)x, 0, SEQ * DM * 2, 0x00020000); \
        const __amdgpu_buffer_rsrc_t ks = __builtin_amdgcn_make_buffer_rsrc((void*)dskip, 0, DM * 2, 0x00020000); \
        dk2 = __builtin_amdgcn_raw_buffer_load_b64(ks, (unsigned)(l15 * 8), d_base * 2, 0); \
        _Pragma("unroll") for (int q = 0; q < 4; ++q) \
            xx[q >> 1][q & 1] = __builtin_amdgcn_raw_buffer_load_b64(xs, (unsigned)(q4 * 32 * DM * 2 + l15 * 8 + (q & 1) * DM * 2), (TR(q >> 1) * DM + d_base) * 2, 0); \
        _Pragma("unroll") for (int r = 0; r < 2; ++r) gv[r] = __builtin_amdgcn_raw_buffer_load_b128(gs, GV(q4), GSO(r), 16); \
        SCHED(); } while (0)
#define XPIN() do { PIN4(xx[0][0], xx[0][1], xx[1][0], xx[1][1]); asm volatile("" : "+v"(dk2), "+v"(gv[0]), "+v"(gv[1])); } while (0)

    if (wid < 4) {
        typedef __attribute__((ext_vector_type(8))) unsigned u32x8;
        typedef const __attribute__((address_space(4))) u32x8 cu8;
        const int po = (p_base + wid * 16) * 2;
        u32x8 s0 = *(cu8*)((size_t)logdt + po), s1 = *(cu8*)((size_t)lre + po), s2 = *(cu8*)((size_t)lim + po);
        SCHED();
        const int pq = p_base + wid * 16 + l15;
        unsigned goff[4], gmask[4];
#pragma unroll
        for (int i = 0; i < 4; ++i) {
            goff[i] = (unsigned)(((tb - 1 - q4 - 4 * i) * 1024 + pq) * 16);
            gmask[i] = (tb - 1 - q4 - 4 * i >= 0) ? ~0u : 0u;
            asm volatile("" : "+v"(goff[i]), "+v"(gmask[i]));
        }
        unsigned gpub = q4 == 0 ? (unsigned)((tb * 1024 + pq) * 16) : 0x80000000u;
        unsigned hp = (unsigned)(tb * 524288 + (nb * 4 + wid) * 1024 + lane * 16);
        unsigned wa = (unsigned)(HT_OFF + wid * 2048 + (q4 * 16 + (l15 ^ ((q4 >> 1) * 4))) * 8);
        unsigned ra0 = (unsigned)(HT_OFF + wid * 2048 + ((q4 >> 1) * 64 + (l15 & 3) * 16 + (((q4 & 1) * 8 + (l15 >> 2)) ^ ((l15 & 2) * 2))) * 8), ra1 = ra0 ^ 32u;
        cra = CRA(GA_OFF + wid * 8192); crb = cra ^ 64;
        unsigned vB0 = (unsigned)((d_base + (lane >> 3) * 4) * 2048 + (((lane & 7) ^ (lane >> 4)) * 16)), vB1 = (vB0 ^ 64u) + 65536u - 1024u;
        PIN4(gpub, vB0, hp, wa); PIN4(ra0, ra1, cra, crb); asm volatile("" : "+v"(vB1));
        f32x4 acc[8][2];
#pragma unroll
        for (int m = 0; m < 8; m += 2) {
#pragma unroll
            for (int c = 0; c < 4; ++c) acc[m + (c >> 1)][c & 1] = (f32x4){0.f, 0.f, 0.f, 0.f};
            PIN4(acc[m][0], acc[m][1], acc[m + 1][0], acc[m + 1][1]);
        }
        SCHED();
        cf L1, Lq3, cc, L32, cLq, Lp, L512;
        {
            unsigned w0 = 0, w1 = 0, w2 = 0;
#pragma unroll
            for (int i = 0; i < 8; ++i) {
                asm("v_writelane_b32 %0, %3, %6\n\tv_writelane_b32 %1, %4, %6\n\tv_writelane_b32 %2, %5, %6" : "+v"(w0), "+v"(w1), "+v"(w2) : "s"(s0[i]), "s"(s1[i]), "s"(s2[i]), "i"(2 * i));
            }
            asm("s_nop 1" : "+v"(w0), "+v"(w1), "+v"(w2));
            auto spread = [&](unsigned w) {
                const unsigned e = (unsigned)__builtin_amdgcn_update_dpp(0, (int)w, 0xa0, 0xf, 0xf, false);
                const unsigned a = __builtin_amdgcn_permlane16_swap(e, e, false, false)[0];
                const unsigned c = __builtin_amdgcn_permlane32_swap(a, a, false, false)[0];
                return (l15 & 1) ? hi16(c) : lo16(c);
            };
            const float ldt = spread(w0), re = spread(w1), im = spread(w2);
            if (wid == 3 && lane < NFLAGW) *(unsigned*)(smem + UFLAG_OFF + lane * 4) = 0u;
            const float dt = rbf(expf(ldt));
            const float ea = expf(dt * re);
            float sn, cs;
            sincosf(dt * im, &sn, &cs);
            L1 = {ea * cs, ea * sn};
            const float nr = L1.r - 1.0f, ni = L1.i, den = re * re + im * im;
            cc = {2.f * ((nr * re + ni * im) / den), 2.f * ((ni * re - nr * im) / den)};
            const cf T2 = cmul(L1, L1), T4 = cmul(T2, T2), L8 = cmul(T4, T4), L16 = cmul(L8, L8);
            L32 = cmul(L16, L16);
            const cf L64 = cmul(L32, L32), L96 = cmul(L64, L32), L128 = cmul(L64, L64), L256 = cmul(L128, L128), L384 = cmul(L256, L128);
            L512 = cmul(L256, L256);
            Lp = pick4(q4, L128, L256, L384);
            Lq3 = pick4(3 - q4, L32, L64, L96);
            cLq = cmul(cc, pick4(q4, L32, L64, L96));
            PINC(L1); PINC(Lq3); PINC(cc); PINC(L32); PINC(cLq); PINC(Lp); PINC(L512);
        }
        __builtin_amdgcn_s_setprio(PRIO_G);
        unsigned tagv;
        cf S;
        STAMPT(dbg, 22, 0);
        const int swz = (l15 >> 1) & 7;
        {
            const int a0 = RA(0) + l15 * 128 + ((q4 ^ swz) * 16);
            const int b0 = RB(0) + ((wid >> 1) * 64 + (wid & 1) * 16 + l15) * 128 + ((q4 ^ swz) * 16);
            WAIT_LGKM0();
            BAR();
            asm volatile("" ::: "memory");
            STAMPT(dbg, 19, 64);
            bf16x8 FA[8][2], FB[2][2][2];
            int pa[2] = {a0, a0 ^ 64}, pb[2] = {b0, b0 ^ 64};
            PIN4(pa[0], pa[1], pb[0], pb[1]);
#define LGW(n) __builtin_amdgcn_s_waitcnt(0xC07F | ((n) << 8))
#define RA_(j) FA[(j) >> 1][(j) & 1] = RD128(pa[(j) & 1], ((j) >> 1) * 2048)
#define RB_(bu, j) FB[bu][(j) >> 1][((j) ^ ((j) >> 1)) & 1] = RD128(pb[((j) ^ ((j) >> 1)) & 1], ((j) >> 1) * 4096)
#define RN_(bu, i) do { if ((i) == 0) RB_(bu, 0); else if ((i) < 3) RA_((i) - 1); else if ((i) < 6) RB_(bu, (i) - 2); else RA_((i) - 4); } while (0)
#define G1_H(H, bu, RD, FILL) do { \
            _Pragma("unroll") for (int i = 0; i < 16; ++i) { \
                const int m = (H) * 4 + (i >> 2), n = ((i >> 1) ^ m) & 1, ks = (i ^ (i >> 1)) & 1; \
                if ((H) == 0) { if (i == 0) LGW(6); if (i == 4) LGW(8); if (i == 8) LGW(10); if (i == 12) LGW(8); } \
                SCHED(); \
                MF(acc[m][n], FA[m][ks], FB[bu][n][ks]); SCHED(); \
                if ((H) == 0 && i < 8) RA_(8 + i); \
                if ((H) == 1 && (RD) && i < 12) RN_((bu) ^ 1, i); \
                FILL((H) * 16 + i); \
                SCHED(); \
            } } while (0)
#define NOF(g) do { } while (0)
#pragma unroll
            for (int j = 0; j < 12; ++j) { RN_(0, j); SCHED(); }
            unsigned long long ua_ = UNITS_A;
            int sb = RB(0), sa = RA(0), nx, na;
#define ADV(g) do { if ((g) == 8) { ua_ >>= 4; na = (int)((unsigned)ua_ & 15u) << 14; asm volatile("" : "+s"(na), "+s"(ua_)); } \
                    if ((g) == 9) { const int o_ = sa; sa = na; na -= o_; asm volatile("" : "+s"(na), "+s"(sa)); } \
                    if ((g) == 10) { pa[0] += na; pa[1] += na; asm volatile("" : "+v"(pa[0]), "+v"(pa[1])); } \
                    if ((g) == 11) { nx = (sb == 65536) ? -49152 : 16384; sb += nx; asm volatile("" : "+s"(nx), "+s"(sb)); } \
                    if ((g) == 13) { pb[0] += nx; pb[1] += nx; asm volatile("" : "+v"(pb[0]), "+v"(pb[1])); } } while (0)
#define G1_T(bu) do { \
                G1_H(0, bu, true, ADV); \
                WAIT_LGKM0(); \
                asm volatile("" ::: "memory"); \
                BAR();                     \
                asm volatile("" ::: "memory"); \
                G1_H(1, bu, true, NOF); } while (0)
#pragma unroll 1
            for (int t2 = 0; t2 < 7; ++t2) { G1_T(0); G1_T(1); }
            G1_T(0);
#define AGG(g) do { if ((g) == 8) { asm volatile("" ::: "memory"); SETW(G1D_OFF, wid, 1u); }        \
                    if ((g) == 9) tagv = *(volatile const lds_u32*)(size_t)(unsigned)TAG_OFF; \
                    if ((g) >= 7) { const int t = (g) - 6; \
                        if (t == 1) { asm volatile("s_nop 1" : "+v"(acc[0][0]), "+v"(acc[0][1])); S = {acc[0][0][0], acc[0][1][0]}; } \
                        if ((t & 3) == 0) asm volatile("" : "+v"(acc[t >> 2][0]), "+v"(acc[t >> 2][1])); \
                        S = cfma(L1, S, cf{acc[t >> 2][0][t & 3], acc[t >> 2][1][t & 3]}); PINC(S); } } while (0)
            G1_H(0, 1, false, AGG);
            G1_H(1, 1, false, AGG);
        }
        STAMPT(dbg, 1, 0);
        const unsigned tag = __builtin_amdgcn_readfirstlane(tagv);
        const __amdgpu_buffer_rsrc_t grs = __builtin_amdgcn_make_buffer_rsrc(ws, 0, 15 * 16384, 0x00020000);
        {
#pragma unroll
            for (int t = 26; t < 32; ++t) {
                if (t == 28) { PINC(S); asm volatile("" : "+v"(acc[7][0]), "+v"(acc[7][1])); }
                S = cfma(L1, S, cf{acc[t >> 2][0][t & 3], acc[t >> 2][1][t & 3]});
            }
            const cf W = cmul(S, Lq3);
            const float e2 = red16(red32(W.r, W.i));
            const auto eo = __builtin_amdgcn_permlane32_swap(__float_as_uint(e2), __float_as_uint(e2), false, false);
            __builtin_amdgcn_raw_buffer_store_b128((u32x4){eo[0], tag, eo[1], tag}, grs, gpub, 0, 16);
        }
        SCHED();
        STAMPT(dbg, 2, 0);
        const unsigned cg = lbase + GA_OFF + wid * 8192;
        wait_lds1(smem, COK_OFF + wid * 4);
        CREAD(0);
        WAIT_LGKM0();
        CPIN(0);
        CTILE(wid, 1, cg);
        u32x4 gq[4];
#pragma unroll
        for (int m = 0; m < 8; ++m) {
            const f32x4 R = acc[m][0], I = acc[m][1];
            acc[m][0] = fma4(splat4(cc.r), R, mul4(splat4(-cc.i), I));
            acc[m][1] = fma4(splat4(-cc.r), I, mul4(splat4(-cc.i), R));
            asm volatile("" : "+v"(acc[m][0]), "+v"(acc[m][1]));
            if (m == 3) {
                SCHED();
#pragma unroll
                for (int i = 0; i < 4; ++i) asm volatile("buffer_load_dwordx4 %0, %1, %2, 0 offen sc1" : "=&v"(gq[i]) : "v"(goff[i]), "s"(grs) : "memory");
                SCHED();
            }
        }
        cf Ec;
        {
            const auto sr = __builtin_amdgcn_permlane16_swap(__float_as_uint(S.r), __float_as_uint(S.r), false, false);
            const auto si = __builtin_amdgcn_permlane16_swap(__float_as_uint(S.i), __float_as_uint(S.i), false, false);
            const cf Se = {__uint_as_float(sr[0]), __uint_as_float(si[0])}, So = {__uint_as_float(sr[1]), __uint_as_float(si[1])};
            const cf A = cfma(L32, Se, So);
            const auto ar = __builtin_amdgcn_permlane32_swap(__float_as_uint(A.r), __float_as_uint(A.r), false, false);
            const auto ai = __builtin_amdgcn_permlane32_swap(__float_as_uint(A.i), __float_as_uint(A.i), false, false);
            const cf A01 = {(q4 & 2) ? __uint_as_float(ar[0]) : 0.f, (q4 & 2) ? __uint_as_float(ai[0]) : 0.f};
            const cf kq = {(q4 & 1) ? L32.r : 1.f, (q4 & 1) ? L32.i : 0.f};
            const cf E = cfma(kq, A01, cf{(q4 & 1) ? Se.r : 0.f, (q4 & 1) ? Se.i : 0.f});
            Ec = cmul(cc, E);
            PINC(Ec);
        }
        WAIT_VM(4);
        CREAD(1);
        WAIT_LGKM0();
        CPIN(1);
        CTILE(wid, 2, cg);
        asm volatile("s_waitcnt vmcnt(8)" : "+v"(gq[0]), "+v"(gq[1]), "+v"(gq[2]), "+v"(gq[3]) :: "memory");
        STAMPT(dbg, 27, 0);
        cf carry;
        {
            unsigned spins = 0;
            for (;;) {
                unsigned bad = 0;
#pragma unroll
                for (int i = 0; i < 4; ++i) bad |= ((gq[i].y ^ tag) | (gq[i].w ^ tag)) & gmask[i];
                if (__all(bad == 0u)) break;
                if (++spins > SPIN_MAX) break;
#pragma unroll
                for (int i = 0; i < 4; ++i) asm volatile("buffer_load_dwordx4 %0, %1, %2, 0 offen sc1" : "+v"(gq[i]) : "v"(goff[i]), "s"(grs) : "memory");
                asm volatile("s_waitcnt vmcnt(0)" : "+v"(gq[0]), "+v"(gq[1]), "+v"(gq[2]), "+v"(gq[3]) :: "memory");
            }
            cf E = {__uint_as_float(gq[3].x), __uint_as_float(gq[3].z)};
#pragma unroll
            for (int i = 2; i >= 0; --i) E = cfma(L512, E, cf{__uint_as_float(gq[i].x), __uint_as_float(gq[i].z)});
            E = cmul(E, Lp);
            const float e2 = red16(red32(E.r, E.i));
            const auto eo = __builtin_amdgcn_permlane32_swap(__float_as_uint(e2), __float_as_uint(e2), false, false);
            carry = {__uint_as_float(eo[0]), __uint_as_float(eo[1])};
        }
        STAMPT(dbg, 3, 0);
        {
            const __amdgpu_buffer_rsrc_t hrs = __builtin_amdgcn_make_buffer_rsrc(h, 0, SEQ * 2048 * 2, 0x00020000);
            const cf e = cfma(cLq, carry, Ec);
            float hr = e.r, hn = -e.i;
            s16x4 fr[2][4];
#define HSTORE(m) do { _Pragma("unroll") for (int c = 0; c < 2; ++c) \
                __builtin_amdgcn_raw_buffer_store_b128(__builtin_bit_cast(u32x4, __builtin_shufflevector(fr[(m) & 1][2 * c], fr[(m) & 1][2 * c + 1], 0, 1, 2, 3, 4, 5, 6, 7)), hrs, hp, (2 * (m) + c) * 65536, WT); } while (0)
#pragma unroll
            for (int m = 0; m < 4; ++m) {
#pragma unroll
                for (int j = 0; j < 8; ++j) {
                    const float nr = fmaf(L1.r, hr, fmaf(L1.i, hn, acc[2 * m + (j >> 2)][0][j & 3]));
                    const float nn = fmaf(L1.r, hn, fmaf(-L1.i, hr, acc[2 * m + (j >> 2)][1][j & 3]));
                    hr = nr; hn = nn;
                    acc[2 * m + (j >> 2)][0][j & 3] = nr; acc[2 * m + (j >> 2)][1][j & 3] = nn;
                }
                u32x2 pk[4];
#pragma unroll
                for (int c = 0; c < 4; ++c) { const f32x4 v = acc[2 * m + (c >> 1)][c & 1]; pk[c] = (u32x2){pkbf(v[0], v[1]), pkbf(v[2], v[3])}; }
                PIN4(pk[0], pk[1], pk[2], pk[3]);
                SCHED();
#pragma unroll
                for (int c = 0; c < 4; ++c) { *(volatile __attribute__((address_space(3))) u32x2*)(size_t)(wa + c * 512) = pk[c]; SCHED(); }
                asm volatile("" ::: "memory");
#pragma unroll
                for (int c = 0; c < 2; ++c) { fr[m & 1][2 * c] = TRD(ra0 + c * 1024); SCHED(); fr[m & 1][2 * c + 1] = TRD(ra1 + c * 1024); SCHED(); }
                asm volatile("" ::: "memory");
                if (m > 0) HSTORE(m - 1);
                SCHED();
            }
            HSTORE(3);
        }
        STAMPT(dbg, 4, 0);
        {
            WAIT_VM(8);
            CREAD(2);
            WAIT_VM(0);
            {
                unsigned n;
                asm volatile("s_mov_b64 exec, 1\n\tds_add_rtn_u32 %0, %1, %2\n\ts_mov_b64 exec, -1\n\ts_waitcnt lgkmcnt(0)" : "=&v"(n) : "v"(MISC_OFF), "v"(1u) : "memory");
                if (__builtin_amdgcn_readfirstlane(n) == 3u) {
                    if (lane == 0) __hip_atomic_store((gu32*)(ws + WS_HFLAG) + (tb * 16 + nb) * 32, tag, RLX_AGENT);
                    if (DBGON && lane == 0) dbg[BIDX * 64 + 20] = __builtin_amdgcn_s_memrealtime();
                }
            }
            STAMPT(dbg, 12, 0);
            if (wid == 0 && lane < 7) CODE_TOUCH(CODE_LINES - 1 - lane - (idx & 7) * 7);
            __builtin_amdgcn_s_setprio(0);
            WAIT_LGKM0();
            CPIN(2);
            CTILE(wid, 3, cg);
            XLOAD();
            HPOLL_SETUP();
            HWAIT();
            STAMPT(dbg, 8, 0);
            const unsigned vh = (unsigned)(lane * 16);
#pragma unroll
            for (int f = 0; f < DEPTH; ++f) LOADF(f);
            __builtin_amdgcn_s_setprio(PRIO2_G);
        }
    } else {
        const int lw = wid - 4;
        const int d = cb * 4 + lw;
        const char* Krow = (const char*)(ssmk + (size_t)d * SEQ);
        cra = CRA(LA_OFF + lw * 8192); crb = cra ^ 64;
        const unsigned cst0 = lbase + LA_OFF + lw * 8192;
        CT_SETUP();
        asm volatile("" : "+v"(cra), "+v"(crb));
        u32x4 KA[4];
        unsigned draw;
        unsigned tag;
        float gacc = 0.f;
#define KLOAD() asm volatile("global_load_dwordx4 %0, %5, %7 nt\n\tglobal_load_dwordx4 %1, %5, %7 offset:1024 nt\n\tglobal_load_dwordx4 %2, %5, %7 offset:2048 nt\n\t" \
                             "global_load_dwordx4 %3, %5, %7 offset:3072 nt\n\tglobal_load_ushort %4, %6, %8" \
                             : "=&v"(KA[0]), "=&v"(KA[1]), "=&v"(KA[2]), "=&v"(KA[3]), "=&v"(draw) : "v"((unsigned)(lane * 16)), "v"((unsigned)(d * 2)), "s"(Krow), "s"(ssmD) : "memory")
#define KWAIT(n) asm volatile("s_waitcnt vmcnt(%5)" : "+v"(KA[0]), "+v"(KA[1]), "+v"(KA[2]), "+v"(KA[3]), "+v"(draw) : "i"(n) : "memory")
        if (wid < 6) {
            STAGE_A(2, lw0 + RA(2)); STAGE_B(2, lw0 + RB(2));
            WAIT_VM(32);
            STAMPT(dbg, 17, 320);
            BAR();
            STAGE_B(3, lw0 + RB(3));
            unsigned long long ua_ = UNITS_A >> 12;
            unsigned sla = lw0 + RA(3), slb = lw0 + RB(4);
            int ka = ((3 + rot1) & 15) * 128, kb_ = ka;
            const char* sa = (const char*)x + ka;
            asm volatile("" : "+s"(sa), "+s"(sla), "+s"(ka), "+s"(ua_));
#define NEXT_A() do { ka = (ka + 128) & 2047; sa = (const char*)x + ka; ua_ >>= 4; sla = lw0 + (((unsigned)ua_ & 15u) << 14); asm volatile("" : "+s"(sa), "+s"(sla), "+s"(ka), "+s"(ua_)); } while (0)
#define LSTEP(n) do { \
                WAIT_VM(n); \
                BAR(); \
                GLDS4(sa, voA[0], voA[1], voA[2], voA[3], sla, 0); \
                GLDS4(sa + 16384, voA[0], voA[1], voA[2], voA[3], sla, 4096); \
                kb_ = (kb_ + 128) & 2047; \
                GLDS4((const char*)Bre + kb_, voB[0], voB[1], voB[2], voB[3], slb, 0); \
                NEXT_A(); \
                const unsigned slb0 = slb; \
                slb = (slb == lw0 + 65536) ? lw0 + 16384 : slb + 16384; \
                asm volatile("" : "+s"(slb), "+s"(kb_), "+s"(t)); \
                GLDS4((const char*)Bim + kb_, voB[0], voB[1], voB[2], voB[3], slb0, 4096); } while (0)
#pragma unroll 1
            for (int t = 1; t < 5; ++t) LSTEP(24);
            GLDS4(sa, voA[0], voA[1], voA[2], voA[3], sla, 0); GLDS4(sa + 16384, voA[0], voA[1], voA[2], voA[3], sla, 4096);
            NEXT_A();
#pragma unroll 1
            for (int t = 5; t < 13; ++t) LSTEP(32);
            KLOAD();
            WAIT_VM(37);
            BAR();
            __builtin_amdgcn_s_setprio(0);
            tag = lds_word(smem, TAG_OFF);
            CTILE(lw, 0, cst0 + GA_OFF);
            FLAGD(COK_OFF + lw * 4);
            WAIT_VM(30);
            BAR();
            CTILE(lw + 2, 0, cst0 + GA_OFF + 16384);
            FLAGD(COK_OFF + lw * 4 + 8);
            WAIT_VM(23);
            BAR();
        } else {
            const int vw = wid - 6;
            const int dch = dblk * 128 + vw * 64 + l15;
            const unsigned lk = lbase + ST_OFF + vw * ST_SZ;
            const unsigned vv_ = (unsigned)(((lane >> 4) * 4 + ((lane >> 3) & 1)) * DM * 2 + (lane & 7) * 16);
            const char* vb = (const char*)vin + (size_t)(sblk * 64) * (DM * 2) + dblk * 256 + vw * 128;
            const int grow = nb * 8 + vw * 4 + q4;
            unsigned gx0 = (unsigned)(grow * 128 + (((l15 >> 1) ^ ((grow >> 1) & 7)) * 16) + (l15 & 1) * 8), gx1 = gx0 + RA(2), gwa = (unsigned)(GW_OFF + l15 * 8);
            asm volatile("" : "+v"(gx0), "+v"(gx1), "+v"(gwa));
            u32x2 gxv, gwv;
            typedef const volatile __attribute__((address_space(3))) u32x2* l2p;
#define VBAR(t) do { SCHED(); asm volatile("" ::: "memory"); BAR(); asm volatile("" ::: "memory"); \
                     gxv = *(l2p)(size_t)(RA(t) == 0 ? gx0 : gx1 + (RA(t) - RA(2))); gwv = *(l2p)(size_t)(gwa + (t) * 128); SCHED(); } while (0)
#define GATE() do { SCHED(); gacc = fmaf(lo16(gxv.x), lo16(gwv.x), gacc); gacc = fmaf(hi16(gxv.x), hi16(gwv.x), gacc); \
                    gacc = fmaf(lo16(gxv.y), lo16(gwv.y), gacc); gacc = fmaf(hi16(gxv.y), hi16(gwv.y), gacc); asm volatile("" : "+v"(gacc)); SCHED(); } while (0)
            unsigned ka[4][2], sel[4];
            const int R0 = 4 + (l15 >> 2) * 16 + (l15 & 3) - (q4 >> 1);
            unsigned kr0 = (unsigned)(2 * R0), kr1 = (unsigned)(ST_OFF + vw * ST_SZ + R0 * 128 + (((q4 ^ (R0 >> 1)) & 1) * 16));
            asm volatile("" : "+v"(kr0), "+v"(kr1));
#pragma unroll
            for (int i = 0; i < 4; ++i)
#pragma unroll
                for (int tp = 0; tp < 2; ++tp) {
                    ka[i][tp] = (kr1 ^ (tp * 16)) + ((kr0 + 8 * i - 4 * tp) & 0xe0) + (4 * i - 2 * tp) * 128;
                    asm volatile("" : "+v"(ka[i][tp]));
                }
#pragma unroll
            for (int r = 0; r < 4; ++r) {
                const unsigned pick = (q4 & 2) ? 0x0302u : 0x0100u;
                sel[r] = ((l15 >> 1) == (q4 & 1) * 4 + r) ? ((l15 & 1) ? (pick << 16) | 0x0c0cu : 0x0c0c0000u | pick) : 0x0c0c0c0cu;
            }
            unsigned va_ = (unsigned)(ST_OFF + vw * ST_SZ + ST_V + (q4 * 2 + (l15 >> 3)) * 1056 + ((l15 >> 2) & 1) * 128 + (l15 & 3) * 8);
            unsigned so = (unsigned)(((dch >> 2) * 32 + sblk) * 512 + q4 * 64 + (dch & 3) * 16);
            PIN4(sel[0], sel[1], sel[2], sel[3]); asm volatile("" : "+v"(so), "+v"(va_));
            asm volatile("s_waitcnt vmcnt(9)" : "+v"(tw[0]), "+v"(tw[1]), "+v"(tw[2]), "+v"(tw[3]), "+v"(epoch) :: "memory");
            STAMPT(dbg, 18, 384);
            VBAR(0);
            bf16x8 wf[4][2];
#pragma unroll
            for (int ps = 0; ps < 4; ++ps)
#pragma unroll
                for (int tp = 0; tp < 2; ++tp) {
                    u32x4 w;
#pragma unroll
                    for (int r = 0; r < 4; ++r) w[r] = __builtin_amdgcn_perm(0u, tw[ps][tp], sel[r]);
                    wf[ps][tp] = __builtin_bit_cast(bf16x8, w);
                    asm volatile("" : "+v"(wf[ps][tp]));
                }
            SCHED();
            VLD(0); VLD(1); VLD(2); VLD(3); VLD(4); VLD(5); VLD(6); VLD(7);
            GATE(); VBAR(1);
            f32x4 ks[4][4];
            auto sconv = [&](int i0) __attribute__((always_inline)) {
                bf16x8 kf[2][4][2];
#pragma unroll
                for (int q = 0; q < 16; ++q) { kf[q >> 3][q & 3][(q >> 2) & 1] = RD128(ka[i0 + (q >> 3)][(q >> 2) & 1], (q & 3) * 32); SCHED(); }
                GATE();
                __builtin_amdgcn_s_setprio(PRIO_KV);
                WAIT_LGKM0();
                SCHED();
#pragma unroll
                for (int i = 0; i < 2; ++i) {
#pragma unroll
                    for (int ps = 0; ps < 4; ++ps) MF0(ks[ps][i0 + i], kf[i][ps][0], wf[ps][0]);
#pragma unroll
                    for (int ps = 0; ps < 4; ++ps) MF(ks[ps][i0 + i], kf[i][ps][1], wf[ps][1]);
                }
                __builtin_amdgcn_s_setprio(0);
                SCHED();
            };
            WAIT_VM(8);
            STAMPT(dbg, 21, 384);
            sconv(0); VBAR(2);
            sconv(2); VBAR(3);
            WAIT_VM(0);
            u32x2 vq[4][4];
#pragma unroll
            for (int ps = 0; ps < 4; ++ps)
#pragma unroll
                for (int j = 0; j < 4; ++j) vq[ps][j] = __builtin_bit_cast(u32x2, TRD(va_ + j * 256 + ps * 32));
            const __amdgpu_buffer_rsrc_t rs = __builtin_amdgcn_make_buffer_rsrc(kvT, 0, DM * SEQ * 2, 0x00020000);
            u32x4 o[8];
            auto kvout = [&](int n) __attribute__((always_inline)) {
                const int hh = n >> 2, ps = n & 3;
                asm volatile("" : "+v"(ks[ps][2 * hh]), "+v"(ks[ps][2 * hh + 1]));
#pragma unroll
                for (int e = 0; e < 4; ++e) {
                    const unsigned vv = vq[ps][2 * hh + (e >> 1)][e & 1];
                    o[n][e] = pkbf(ks[ps][2 * hh + (e >> 1)][2 * (e & 1)] * lo16(vv), ks[ps][2 * hh + (e >> 1)][2 * (e & 1) + 1] * hi16(vv));
                }
                asm volatile("" : "+v"(o[n]));
                SCHED();
            };
            GATE(); kvout(0); kvout(1);
            WAIT_LGKM0();
            VBAR(4);
            kvout(2); kvout(3); GATE(); VBAR(5);
            kvout(4); kvout(5); GATE(); VBAR(6);
            kvout(6); kvout(7); GATE(); VBAR(7);
#pragma unroll
            for (int n = 0; n < 8; ++n) __builtin_amdgcn_raw_buffer_store_b128(o[(n >> 1) + (n & 1) * 4], rs, so + (unsigned)((n & 1) * 256), (n >> 1) * 65536, WT);
            SCHED();
            asm volatile("s_nop 1" ::: "memory");
            GATE(); VBAR(8);
            if (vw == 1 && lane == 0) *(unsigned*)(smem + TAG_OFF) = (epoch + 1u) ? epoch + 1u : 1u;
            GATE();
            WAIT_LGKM0();
            WAIT_VM(0);
            VBAR(9);
            tag = __builtin_amdgcn_readfirstlane(*(const unsigned*)(smem + TAG_OFF));
            if (vw == 0 && lane == 0) __hip_atomic_store((gu32*)(ws + WS_KVFLAG) + (dblk * 32 + sblk) * 32, tag, RLX_AGENT);
            if (vw == 1 && lane < 4) *(u32x4*)(smem + U_OFF + 192 + lane * 16) = (u32x4){0u, 0u, 0u, 0u};
            gu32* kfp = (gu32*)(ws + WS_KVFLAG) + ((cb >> 5) * 32 + vw * 16 + l15) * 32;
            const char* ub = (const char*)kvT + (size_t)__builtin_amdgcn_readfirstlane(cb * 16384 + vw * 8192);
            const unsigned vu = (unsigned)((lane >> 5) * 512 + ((lane >> 2) & 1) * 256 + ((lane >> 3) & 3) * 64 + ((lane - (lane >> 4)) & 3) * 16);
            const unsigned lu = lbase + U_OFF + 256 + vw * 8192;
#define U_DMA1(o) asm volatile("s_add_u32 m0, %2, %3\n\ts_nop 0\n\tglobal_load_lds_dwordx4 %0, %1 sc1\n\tglobal_load_lds_dwordx4 %0, %1 offset:1024 sc1\n\t" \
                             "global_load_lds_dwordx4 %0, %1 offset:2048 sc1\n\tglobal_load_lds_dwordx4 %0, %1 offset:3072 sc1" \
                             :: "v"(vu), "s"(ub + (o)), "s"(lu), "i"(o) : "memory", "scc")
#define U_DMA() do { U_DMA1(0); U_DMA1(4096); FLAGD(UFLAG_OFF + lw * 4); } while (0)
            GATE(); VBAR(10);
            TOUCH((const char*)(vw ? Cim : Cre) + (size_t)b * 8192, (unsigned)(lane * 128));
            if (vw == 0) TOUCH(dskip, (unsigned)(d_base * 2)); else TOUCH(ssmD, (unsigned)(cb * 8));
            if (vw == 0 && lane < 11) {
                const unsigned fo = lane < 8 ? (unsigned)((tb * 1024 + p_base) * 16 + lane * 128) : lane == 8 ? (unsigned)(WS_HFLAG + (tb * 16 + nb) * 128)
                                  : lane == 9 ? (unsigned)(WS_CFLAG + cb * 128) : (unsigned)(WS_GATE + (tb * 16 + nb) * 128);
                TOUCH(ws, fo);
            }
            {
                const int cl = 64 + lane + ((idx & 15) * 2 + vw) * 7;
                if (lane < 7 && cl < CODE_LINES) CODE_TOUCH(cl);
            }
            GATE(); VBAR(11);
            GATE(); VBAR(12);
            GATE(); VBAR(13);
            unsigned pv;
            asm volatile("global_load_dword %0, %1, off sc1" : "=&v"(pv) : "v"(kfp) : "memory");
            KLOAD();
            GATE(); VBAR(14);
            GATE(); VBAR(15);
            asm volatile("s_waitcnt vmcnt(5)" : "+v"(pv) :: "memory");
            const bool done = __all(pv == tag);
            if (done) U_DMA();
            if (DBGON && TIDX == 384) dbg[BIDX * 64 + 13] = (done ? 100ull : 0ull) | (1ull << 62);
            GATE();
            if (lane == 0) SETW(UFLAG_OFF, vw, 1u);
            WAIT_VM(9);
            if (!done) {
                unsigned spins = 0;
                for (;;) {
                    const unsigned v = __hip_atomic_load(kfp, RLX_AGENT);
                    if (__all(v == tag)) break;
                    if (++spins > SPIN_MAX) break;
                    __builtin_amdgcn_s_sleep(1);
                }
                U_DMA();
            }
        }
        KWAIT(18);
        STAMPT(dbg, 14, 320);
        __builtin_amdgcn_s_sleep(6);
        {
            const unsigned cp = (unsigned)(KC_OFF + lw * CV_W + 64 + lane * 16);
            if (lane == 0) KA[0].x = (KA[0].x & 0xffff0000u) | f2bf(lo16(KA[0].x) + bf2f((u16)draw));
#define WR128(v, off) asm volatile("ds_write_b128 %0, %1 offset:%2" :: "v"(cp), "v"(v), "i"(off) : "memory")
#pragma unroll
            for (int q = 3; q >= 0; --q) WR128(KA[q], q * 1024);
#pragma unroll
            for (int q = 3; q >= 0; --q) {
                const u32x2 p = q ? pred2(KA[q], RL63(KA[q ? q - 1 : 0].z), RL63(KA[q ? q - 1 : 0].w)) : pred2(KA[0], 0u, 0u);
                const u32x4 a = KA[q];
                const u32x4 s1 = {al(a.x, p.y), al(a.y, a.x), al(a.z, a.y), al(a.w, a.z)};
                WR128(s1, q * 1024 + CV_COPY);
                const u32x4 s2 = {p.y, mv(a.x), mv(a.y), mv(a.z)};
                WR128(s2, q * 1024 + 2 * CV_COPY);
                const u32x4 s3 = {al(p.y, p.x), mv(s1.x), mv(s1.y), mv(s1.z)};
                WR128(s3, q * 1024 + 3 * CV_COPY);
            }
            if (lane < 16) *(u32x4*)(smem + KC_OFF + lw * CV_W + (lane >> 2) * CV_COPY + (lane & 3) * 16) = (u32x4){0u, 0u, 0u, 0u};
            SCHED();
        }
        STAMPT(dbg, 26, 320);
        wait_lds4(smem, G1D_OFF);
        wait_lds4(smem, UFLAG_OFF);
        STAMPT(dbg, 9, 256);
        __builtin_amdgcn_s_setprio(PRIO_CV);
        STAMPT(dbg, 10, 384);
        f32x4 cv[8];
#pragma unroll
        for (int g = 0; g < 8; ++g) cv[g] = (f32x4){0.f, 0.f, 0.f, 0.f};
        PIN4(cv[0], cv[1], cv[2], cv[3]); PIN4(cv[4], cv[5], cv[6], cv[7]);
        const int uz = U_OFF + 192;
        const int ua0 = U_OFF + 256 + (2 * l15 + q4 - 2) * 64, rv0 = (2 * l15 + q4 - 2) * 4 + lw * 16;
        const unsigned ka0 = (unsigned)((l15 >> 2) * CV_COPY - 16 * q4 + 8 * (l15 & 3) - 8);
        int ua, rv, pu, pz;
        unsigned adr;
#define UNEXT1() do { pu = ua | (rv & 0x30); asm volatile("" : "+v"(pu)); } while (0)
#define UNEXT2() do { pz = (ua > uz ? ua : uz) | (rv & 0x30); asm volatile("" : "+v"(pz)); } while (0)
#define UNEXT3() do { ua -= 256; rv -= 16; asm volatile("" : "+v"(ua), "+v"(rv)); } while (0)
#define ANEXT() do { adr += 64; asm volatile("" : "+v"(adr)); } while (0)
#define RDU(dst, c) do { dst = (c) ? RD128(pu, (c) * 2048) : RD128(pz, 0); SCHED(); } while (0)
        HPOLL_SETUP();
        unsigned hpv = 0;
        {
            bf16x8 A[8], UB[8];
            ua = ua0; rv = rv0; adr = lbase + KC_OFF + lw * CV_W + ka0 + 96;
            asm volatile("" : "+v"(adr));
            UNEXT1(); UNEXT2(); UNEXT3();
#pragma unroll
            for (int i = 0; i < 8; ++i) { RDA(A[7 - i], adr, 7 - i); RDU(UB[i], i); }
            unsigned gb = lw >= 2 ? (unsigned)((tb * 16 + nb) * 128 + (lw - 2) * 32) : 0x80000000u;
            asm volatile("" : "+s"(gb));
            const unsigned gso = (l15 == 0) ? gb + (unsigned)(q4 * 8) : 0x80000000u;
            float gt = 0.f;
            unsigned vxo = 0, vdo = 0, vgo = 0;
            auto gate_out = [&](int i) __attribute__((always_inline)) {
                if (i == 0) gacc += __uint_as_float(__builtin_amdgcn_update_dpp(0u, __float_as_uint(gacc), 0xb1, 0xf, 0xf, false));
                if (i == 1) gacc += __uint_as_float(__builtin_amdgcn_update_dpp(0u, __float_as_uint(gacc), 0x4e, 0xf, 0xf, false));
                if (i == 2) gacc += __uint_as_float(__builtin_amdgcn_update_dpp(0u, __float_as_uint(gacc), 0x141, 0xf, 0xf, false));
                if (i == 3) gacc += __uint_as_float(__builtin_amdgcn_update_dpp(0u, __float_as_uint(gacc), 0x140, 0xf, 0xf, false));
                if (i == 4) gacc = rbf(gacc);
                if (i == 5) gacc = __builtin_amdgcn_exp2f(gacc * -1.44269504088896f);
                if (i == 6) gacc = __builtin_amdgcn_rcpf(1.0f + gacc);
                if (i == 7) gacc = rbf(gacc);
                if (i == 8) gt = __uint_as_float(pkbf(gacc, 1.0f - gacc));
                if (i == 9) __builtin_amdgcn_raw_buffer_store_b64((u32x2){__float_as_uint(gt), tag}, gs, gso, 0, WT);
                asm volatile("" : "+v"(gacc), "+v"(gt));
            };
            auto step = [&](auto E) __attribute__((always_inline)) {
                constexpr int e = decltype(E)::value;
                constexpr int ti = (e == CS0) ? 0 : (e == CS1) ? 1 : (e == CS2) ? 2 : -1, tj = ti < 0 ? 0 : ti;
                constexpr unsigned char GA[36] = {0x66, 0x77, 0x76, 0x44, 0x55, 0x54, 0x65, 0x64, 0x75, 0x74, 0x22, 0x33, 0x32, 0x43, 0x42, 0x53, 0x52, 0x63, 0x62, 0x73, 0x72,
                                                  0x00, 0x11, 0x10, 0x21, 0x20, 0x31, 0x30, 0x41, 0x40, 0x51, 0x50, 0x61, 0x60, 0x71, 0x70};
                const char* sb_ = (const char*)Cre + COFF(wid, tj + 1);
                const char* sc_ = (const char*)Cim + COFF(wid, tj + 1);
#pragma unroll
                for (int q = 0; q < 36; ++q) {
                    const int g = GA[q] >> 4, a = GA[q] & 15;
                    MF(cv[g], A[a], UB[g - a]); SCHED();
                    if (e < 7) {
                        if (a == 1) RDU(UB[g - 1], g - 1);
                        if (q == 35) RDU(UB[7], 7);
                        if (g == 7) RDA(A[a], adr + 64 * (e + 1), a);
                        if (q == 11) UNEXT1();
                        if (q == 13) UNEXT2();
                        if (q == 15) UNEXT3();
                    }
                    if (ti >= 0) {
                        const int n = (q >= 3 && q <= 7) ? q - 3 : q == 10 ? 5 : q == 12 ? 6 : q == 14 ? 7 : -1;
                        if (q == 3) { if (ti == 0) WAIT_VM(1); else if (e == XE + 1) WAIT_VM(7); else WAIT_VM(0); }
                        if (n >= 0) CRD(tj, n & 7);
                        if (q == 18) CPIN(tj);
                        if (q == 18 || q == 23) asm volatile("s_add_u32 m0, %3, %4\n\ts_nop 0\n\tglobal_load_lds_dwordx4 %0, %2\n\tglobal_load_lds_dwordx4 %1, %2 offset:1024"
                                                             :: "v"(vB0), "v"(vB1), "s"(q == 18 ? sb_ : sc_), "s"(cst0), "i"(q == 18 ? 0 : 4096) : "memory", "scc");
                        if (q == 21 || q == 25) asm volatile("global_load_lds_dwordx4 %0, %2 offset:2048\n\tglobal_load_lds_dwordx4 %1, %2 offset:3072"
                                                             :: "v"(vB0), "v"(vB1), "s"(q == 21 ? sb_ : sc_) : "memory");
                    }
                    if (e == 0 && q >= 3 && q <= 7) gate_out(q - 3);
                    if (e == 0 && q >= 10 && q <= 18 && !(q & 1)) gate_out(5 + (q - 10) / 2);
                    if (e == 0 && q == 4) CTILE(wid, 0, cst0);
                    if (e == XE && q >= 23 && q <= 35 && (q & 1)) {
                        const int i = (q - 23) / 2;
                        const __amdgpu_buffer_rsrc_t xs = __builtin_amdgcn_make_buffer_rsrc((void*)x, 0, SEQ * DM * 2, 0x00020000);
                        const __amdgpu_buffer_rsrc_t ks = __builtin_amdgcn_make_buffer_rsrc((void*)dskip, 0, DM * 2, 0x00020000);
                        if (i < 4) asm volatile("buffer_load_dwordx2 %0, %1, %2, %3 offen offset:%4" : "=&v"(xx[(i >> 1) & 1][i & 1]) : "v"(vxo), "s"(xs), "s"((TR((i >> 1) & 1) * DM + d_base) * 2), "i"((i & 1) * 2048) : "memory");
                        if (i == 4) asm volatile("buffer_load_dwordx2 %0, %1, %2, %3 offen" : "=&v"(dk2) : "v"(vdo), "s"(ks), "s"(d_base * 2) : "memory");
                        if (i >= 5) asm volatile("buffer_load_dwordx4 %0, %1, %2, %3 offen sc1" : "=&v"(gv[(i - 5) & 1]) : "v"(vgo), "s"(gs), "s"(GSO((i - 5) & 1)) : "memory");
                    }
                    if (e == XE && q == 16) { vxo = (unsigned)(q4 * 32 * DM * 2 + l15 * 8); vdo = (unsigned)(l15 * 8); asm volatile("" : "+v"(vxo), "+v"(vdo)); }
                    if (e == XE && q == 17) { vgo = GV(q4); asm volatile("" : "+v"(vgo)); }
                    if (e == 6 && q == 30) hpv = __hip_atomic_load(hfp, RLX_AGENT);
                    SCHED();
                }
            };
            unroll<8>(step);
            WAIT_VM(0);
            XPIN();
        }
        PAD8(cv[0], cv[1], cv[2], cv[3]); PIN4(cv[4], cv[5], cv[6], cv[7]);
        STAMPT(dbg, 11, 384);
        const unsigned vh = (unsigned)((lane ^ 2) * 16);
        if (!__all(hpv == tag)) HWAIT();
        asm volatile("" ::: "memory");
#pragma unroll
        for (int f = 0; f < DEPTH; ++f) LOADF(f);
        STAMPT(dbg, 25, 384);
        {
            const __amdgpu_buffer_rsrc_t yrs = __builtin_amdgcn_make_buffer_rsrc(y2T, 0, DM * SEQ * 2, 0x00020000);
            const unsigned yo = (unsigned)(d * 4096 + (q4 & 1) * 512 + (l15 >> 3) * 256 + (q4 >> 1) * 128 + (l15 & 7) * 16);
#pragma unroll
            for (int g = 0; g < 8; g += 2) {
                const auto r0 = __builtin_amdgcn_permlane16_swap(pkbf(cv[g][0], cv[g][1]), pkbf(cv[g + 1][0], cv[g + 1][1]), false, false);
                const auto r1 = __builtin_amdgcn_permlane16_swap(pkbf(cv[g][2], cv[g][3]), pkbf(cv[g + 1][2], cv[g + 1][3]), false, false);
                __builtin_amdgcn_raw_buffer_store_b128((u32x4){r0[0], r1[0], r0[1], r1[1]}, yrs, yo + (unsigned)(g * 512), 0, WT);
            }
        }
        asm volatile("" ::: "memory");
        STAMPT(dbg, 15, 384);
        __builtin_amdgcn_s_setprio(PRIO2_L);
    }
    {
        const int lane = fresh_lane(), l15 = lane & 15, q4 = lane >> 4;
        const int ring = wid >> 2;
        const unsigned va = (unsigned)((lane ^ (ring * 2)) * 16), vh = va;
        const unsigned tag = lds_word(smem, TAG_OFF);
        const int cb0 = ring ? KC_OFF : GA_OFF, cst = ring ? CV_W : 8192;
        f32x4 ac[2][4];
        unsigned ca[2], fa[2], fl = 0, fv = 0;
#define CHAIN(u) do { const int c_ = (wid - ((u) >> 1)) & 3; ca[(u) & 1] = va + (unsigned)(cb0 + c_ * cst + ((u) & 1) * 4096); \
                      fa[(u) & 1] = (unsigned)(RF_OFF + ring * 32 + ((u) & 1) * 16 + c_ * 4); asm volatile("" : "+v"(ca[(u) & 1]), "+v"(fa[(u) & 1])); } while (0)
#define RDIN(dst, a, f_) asm volatile("ds_read_b32 %0, %5\n\tds_read_b128 %1, %6\n\tds_read_b128 %2, %6 offset:1024\n\tds_read_b128 %3, %6 offset:2048\n\tds_read_b128 %4, %6 offset:3072" \
                     : "=&v"(fl), "=&v"(dst[0]), "=&v"(dst[1]), "=&v"(dst[2]), "=&v"(dst[3]) : "v"(f_), "v"(a) : "memory")
#define LWAIT(dst) asm volatile("s_waitcnt lgkmcnt(0)" : "+v"(fl), "+v"(dst[0]), "+v"(dst[1]), "+v"(dst[2]), "+v"(dst[3]) :: "memory")
        CHAIN(0); CHAIN(1);
        WAIT_LGKM0();
        CPIN(0); CPIN(1); CPIN(2);
        CREAD(3);
        int tr[2] = {TR(0), TR(1)};
        unsigned gw[2][2];
        {
            unsigned spins = 0;
            while (__builtin_expect(!__all((gv[0].y == tag) & (gv[0].w == tag) & (gv[1].y == tag) & (gv[1].w == tag)), 0)) {
                if (++spins > SPIN_MAX) break;
#pragma unroll
                for (int r = 0; r < 2; ++r) asm volatile("s_nop 4\n\tbuffer_load_dwordx4 %0, %1, %2, %3 offen sc1\n\ts_waitcnt vmcnt(0)" : "=&v"(gv[r]) : "v"(GV(q4)), "s"(gs), "s"(__builtin_amdgcn_readfirstlane(GSO(r))) : "memory");
            }
            gw[0][0] = gv[0].x; gw[0][1] = gv[0].z; gw[1][0] = gv[1].x; gw[1][1] = gv[1].z;
            PIN4(gw[0][0], gw[0][1], gw[1][0], gw[1][1]);
        }
        unsigned cpv = 0, yy[2][4];
        f32x2 zz[2][4];
        unsigned exo = 0, exi = 0, efo = 0, efi = 0;
        auto send = [&](int r) __attribute__((always_inline)) {
            const unsigned one = 1u;
#pragma unroll
            for (int n = 0; n < 4; ++n) { const f32x2 v = {ac[r][n][2], ac[r][n][3]}; asm volatile("ds_write_b64 %0, %1 offset:%2" :: "v"(exo), "v"(v), "i"(r * 4096 + n * 512) : "memory"); }
            asm volatile("ds_write_b32 %0, %1 offset:%2" :: "v"(efo), "v"(one), "i"(r * 4) : "memory");
        };
        STAMPT(dbg, 5, 0);
        auto body = [&](auto F) __attribute__((always_inline)) {
            constexpr int f = decltype(F)::value;
            constexpr int u = f >> 3, k = f & 7;
            f32x4* an = ac[u & 1];
            f32x4* ao = ac[(u + 1) & 1];
#pragma unroll
            for (int n = 0; n < 4; ++n) {
                if (k == 0 && u < 2) MF0(an[n], af[f % DEPTH], cf_[k][n]); else MF(an[n], af[f % DEPTH], cf_[k][n]);
                SCHED();
                const int g = k * 4 + n;
                if (u >= 1 && u < 7) {
                    if (g == 1) { fv = ((u - 1) >> 1) + 1; asm volatile("" : "+v"(fv)); }
                    if (g == 4 || g == 6 || g == 8 || g == 10) asm volatile("ds_write_b128 %0, %1 offset:%2" :: "v"(ca[(u + 1) & 1]), "v"(ao[(g - 4) / 2]), "i"((g - 4) * 512) : "memory");
                    if (g == 12) asm volatile("ds_write_b32 %0, %1" :: "v"(fa[(u + 1) & 1]), "v"(fv) : "memory");
                    if (g == 18) CHAIN(u + 1);
                    if (g == 20) asm volatile("ds_read_b32 %0, %1" : "=&v"(fl) : "v"(fa[(u + 1) & 1]) : "memory");
                    if (g > 20 && g <= 24) asm volatile("ds_read_b128 %0, %1 offset:%2" : "=&v"(ao[g - 21]) : "v"(ca[(u + 1) & 1]), "i"((g - 21) * 1024) : "memory");
                    if (g == 30) {
                        LWAIT(ao);
                        if (__builtin_expect(__builtin_amdgcn_readfirstlane(fl) != (unsigned)((u + 1) >> 1), 0)) {
                            unsigned spins = 0;
#pragma unroll 1
                            do {
                                __builtin_amdgcn_s_sleep(1);
                                RDIN(ao, ca[(u + 1) & 1], fa[(u + 1) & 1]); LWAIT(ao);
                                if (++spins > SPIN_MAX * 16u) break;
                            } while (__builtin_amdgcn_readfirstlane(fl) != (unsigned)((u + 1) >> 1));
                        }
                    }
                }
                if (u == 7 && g == 6) send(0);
                if (f >= ZF && f < ZF + 4) {
                    constexpr int r = ((f - ZF) >> 1) & 1, j = (f - ZF) & 1;
                    const unsigned xw = xx[r][j][n >> 1], dw = dk2[n >> 1];
                    const float xf = (n & 1) ? hi16(xw) : lo16(xw), df = (n & 1) ? hi16(dw) : lo16(dw), yf = j ? hi16(yy[r][n]) : lo16(yy[r][n]);
                    zz[r][n][j] = xf * fmaf(hi16(gw[r][j]), yf, lo16(gw[r][j]) * df);
                    asm volatile("" : "+v"(zz[r][n]));
                }
                if (f == ZF + 4) {
                    zz[0][n] = (f32x2){fmaf(lo16(gw[0][0]), ac[0][n][0], zz[0][n][0]), fmaf(lo16(gw[0][1]), ac[0][n][1], zz[0][n][1])};
                    asm volatile("" : "+v"(zz[0][n]));
                }
                SCHED();
            }
            if (f + DEPTH < 64) LOADF(f + DEPTH);
            if (f == DEPTH + 1 && ring) {
                WAIT_VM(6);
                if (lane == 0) __hip_atomic_store((gu32*)(ws + WS_CFLAG) + cb * 32 + (wid & 3), tag, RLX_AGENT);
                SCHED();
            }
            if (f == YPOLL && ring) { const int l15 = fresh_lane() & 15; cpv = __hip_atomic_load((gu32*)(ws + WS_CFLAG) + (nb * 16 + (wid & 3) * 4 + (l15 >> 2)) * 32 + (l15 & 3), RLX_AGENT); SCHED(); }
            if (f == YPOLL + DEPTH && ring) {
                const int ln = fresh_lane(), l15 = ln & 15, q4 = ln >> 4;
                if (__builtin_expect(!__all(cpv == tag), 0)) {
                    unsigned spins = 0;
                    while (!__all(__hip_atomic_load((gu32*)(ws + WS_CFLAG) + (nb * 16 + (wid & 3) * 4 + (l15 >> 2)) * 32 + (l15 & 3), RLX_AGENT) == tag)) {
                        if (++spins > SPIN_MAX) break;
                        __builtin_amdgcn_s_sleep(1);
                    }
                }
                const unsigned lx = lbase + LA_OFF + (wid & 3) * 8192;
#pragma unroll
                for (int i = 0; i < 4; ++i) {
                    const unsigned vo = (unsigned)((d_base + (wid & 3) * 16 + i * 4 + q4) * 4096 + tb * 256 + ((l15 ^ ((wid & 3) * 4 + i)) * 16));
                    GLDS_SC1(y2T, vo, lx, i * 1024);
                }
                SCHED();
            }
            if (f == YPOLL + 2 * DEPTH + 1 && ring) {
                WAIT_VM(6);
                SETW(YFLAG_OFF, wid & 3, 1u);
                SCHED();
            }
            if (f == 55) {
                const int ln = fresh_lane(), c_ = (wid - 3) & 3;
                const int eg = GA_OFF + c_ * 8192, el = KC_OFF + c_ * CV_W;
                exo = (unsigned)((ring ? el : eg) + ln * 8);
                exi = (unsigned)((ring ? eg : el) + ln * 8);
                efo = (unsigned)(EF_OFF + wid * 8); efi = (unsigned)(EF_OFF + (wid ^ 4) * 8);
                PIN4(exo, exi, efo, efi);
                SCHED();
            }
            if (f == ZF - 2) {
                const int ln = fresh_lane(), l15 = ln & 15, q4 = ln >> 4;
                wait_lds4(smem, YFLAG_OFF);
#pragma unroll
                for (int r = 0; r < 2; ++r) {
                    const int jb = JB(6 + r);
                    const int ya = LA_OFF + (l15 >> 2) * 8192 + (l15 & 3) * 1024 + (((((jb >> 1) & 1) * 8 + (jb >> 2) + q4 * 2) ^ l15) * 16) + (jb & 1) * 8 + ring * 4;
#pragma unroll
                    for (int e = 0; e < 4; ++e) yy[r][e] = *(const unsigned*)(smem + ya + e * 256);
                }
                SCHED();
            }
        };
        unroll<64>(body);
        PAD8(ac[1][0], ac[1][1], ac[1][2], ac[1][3]);
        send(1);
        STAMPT(dbg, 6, 0);
        const __amdgpu_buffer_rsrc_t ors = __builtin_amdgcn_make_buffer_rsrc(out, 0, SEQ * DM * 4, 0x00020000);
        const unsigned ov = (unsigned)((lane >> 4) * (32 * DM * 4) + (lane & 15) * 16);
#pragma unroll
        for (int r = 0; r < 2; ++r) {
            f32x2 in[4];
#define EXRD() asm volatile("ds_read_b32 %0, %5 offset:%7\n\tds_read_b64 %1, %6 offset:%8\n\tds_read_b64 %2, %6 offset:%9\n\tds_read_b64 %3, %6 offset:%10\n\tds_read_b64 %4, %6 offset:%11" \
                             : "=&v"(fl), "=&v"(in[0]), "=&v"(in[1]), "=&v"(in[2]), "=&v"(in[3]) \
                             : "v"(efi), "v"(exi), "i"(r * 4), "i"(r * 4096), "i"(r * 4096 + 512), "i"(r * 4096 + 1024), "i"(r * 4096 + 1536) : "memory")
            EXRD();
            if (r == 0) {
#pragma unroll
                for (int n = 0; n < 4; ++n) {
                    zz[1][n] = (f32x2){fmaf(lo16(gw[1][0]), ac[1][n][0], zz[1][n][0]), fmaf(lo16(gw[1][1]), ac[1][n][1], zz[1][n][1])};
                    asm volatile("" : "+v"(zz[1][n]));
                }
            }
            LWAIT(in);
            if (__builtin_expect(__builtin_amdgcn_readfirstlane(fl) == 0u, 0)) {
                unsigned spins = 0;
#pragma unroll 1
                do {
                    EXRD(); LWAIT(in);
                    if (++spins > SPIN_MAX * 16u) break;
                } while (__builtin_amdgcn_readfirstlane(fl) == 0u);
            }
            f32x4 o[2];
#pragma unroll
            for (int j = 0; j < 2; ++j)
#pragma unroll
                for (int e = 0; e < 4; ++e) o[j][e] = fmaf(lo16(gw[r][j]), in[e][j], zz[r][e][j]);
#pragma unroll
            for (int j = 0; j < 2; ++j)
                __builtin_amdgcn_raw_buffer_store_b128(__builtin_bit_cast(u32x4, o[j]), ors, ov + (unsigned)(((tr[r] + j) * DM + d_base) * 4), 0, WT);
        }
        STAMPT(dbg, 7, 0);
        STAMPT(dbg, 16, 384);
        if (tb == 15 && nb == 0 && wid == 0 && lane == 0) __hip_atomic_store((gu32*)(ws + WS_EPOCH), tag, RLX_AGENT);
    }
}

extern "C" void kernel_launch(void* const* d_in, const int* in_sizes, int n_in,
                              void* d_out, int out_size, void* d_ws, size_t ws_size,
                              hipStream_t stream) {
    const u16* x = (const u16*)d_in[0];
    const u16* lre = (const u16*)d_in[1];
    const u16* lim = (const u16*)d_in[2];
    const u16* Bre = (const u16*)d_in[3];
    const u16* Bim = (const u16*)d_in[4];
    const u16* Cre = (const u16*)d_in[5];
    const u16* Cim = (const u16*)d_in[6];
    const u16* logdt = (const u16*)d_in[7];
    const u16* dskip = (const u16*)d_in[8];
    const u16* kin = (const u16*)d_in[10];
    const u16* vin = (const u16*)d_in[11];
    const u16* shiftk = (const u16*)d_in[12];
    const u16* ssmk = (const u16*)d_in[13];
    const u16* ssmD = (const u16*)d_in[14];
    const u16* gatew = (const u16*)d_in[15];
    float* out = (float*)d_out;

    char* ws = (char*)d_ws;

    hipFuncSetAttribute((const void*)k1_kernel, hipFuncAttributeMaxDynamicSharedMemorySize, K1_LDS);

    k1_kernel<<<256, 512, K1_LDS, stream>>>(x, Bre, Bim, kin, vin, shiftk, gatew, ws, lre, lim, logdt, ssmk, ssmD, Cre, Cim, dskip, out);
}
#if defined(__HIP_DEVICE_COMPILE__)
#pragma clang attribute pop
#endif
```

```cpp
#if defined(__HIP_DEVICE_COMPILE__)
#pragma clang attribute push(__attribute__((target("no-packed-fp32-ops"))), apply_to = function)
#endif
#include <hip/hip_runtime.h>
#include <hip/hip_bf16.h>
#include <stdint.h>

#define TIDX ((int)__builtin_amdgcn_workitem_id_x())
#define BIDX ((int)__builtin_amdgcn_workgroup_id_x())
typedef unsigned short u16;
typedef __attribute__((ext_vector_type(8))) short bf16x8;
typedef __attribute__((ext_vector_type(4))) float f32x4;
typedef __attribute__((ext_vector_type(2))) float f32x2;
typedef __attribute__((ext_vector_type(2))) __bf16 bf16x2;
typedef __attribute__((ext_vector_type(4))) unsigned u32x4;
typedef __attribute__((ext_vector_type(2))) unsigned u32x2;
typedef __attribute__((address_space(1))) unsigned long long gu64;
typedef __attribute__((address_space(1))) unsigned gu32;
typedef __attribute__((address_space(3))) void lds_void;
typedef __attribute__((address_space(3))) unsigned lds_u32;
typedef __attribute__((ext_vector_type(4))) short s16x4;
typedef __attribute__((address_space(3))) s16x4 lds_s16x4;
#define TRD(a) __builtin_amdgcn_ds_read_tr16_b64_v4i16((lds_s16x4*)(size_t)(a))

#define DBGON 0
#define STAMPT(buf, k, t) do { } while (0)
#define CODE_LINES 258
#define SEQ 2048
#define DM 1024
#define SPIN_MAX (1u << 18)
#define PRIO_G 2
#define PSLEEP 4
#define PRIO_L 3
#define WT 16

__device__ __forceinline__ float bf2f(u16 v) { return __uint_as_float(((unsigned)v) << 16); }
__device__ __forceinline__ u16 f2bf(float f) { __bf16 b = (__bf16)f; return __builtin_bit_cast(u16, b); }
__device__ __forceinline__ float rbf(float f) { return bf2f(f2bf(f)); }
__device__ __forceinline__ unsigned pkbf(float lo, float hi) { return __builtin_bit_cast(unsigned, __builtin_convertvector((f32x2){lo, hi}, bf16x2)); }
__device__ __forceinline__ float lo16(unsigned u) { return __uint_as_float(u << 16); }
__device__ __forceinline__ float hi16(unsigned u) { return __uint_as_float(u & 0xffff0000u); }

struct cf { float r, i; };
__device__ __forceinline__ cf cmul(cf a, cf b) { return {a.r * b.r - a.i * b.i, a.r * b.i + a.i * b.r}; }
__device__ __forceinline__ cf cfma(cf a, cf b, cf c) {
    return {fmaf(a.r, b.r, fmaf(-a.i, b.i, c.r)), fmaf(a.r, b.i, fmaf(a.i, b.r, c.i))};
}
__device__ __forceinline__ f32x4 splat4(float v) { return (f32x4){v, v, v, v}; }
__device__ __forceinline__ f32x4 fma4(f32x4 a, f32x4 b, f32x4 c) { return __builtin_elementwise_fma(a, b, c); }
__device__ __forceinline__ f32x4 mul4(f32x4 a, f32x4 b) { return a * b; }

__device__ __forceinline__ float red32(float a, float b) {
    const auto r = __builtin_amdgcn_permlane32_swap(__float_as_uint(a), __float_as_uint(b), false, false);
    return __uint_as_float(r[0]) + __uint_as_float(r[1]);
}
__device__ __forceinline__ float red16(float x) {
    const auto r = __builtin_amdgcn_permlane16_swap(__float_as_uint(x), __float_as_uint(x), false, false);
    return __uint_as_float(r[0]) + __uint_as_float(r[1]);
}
__device__ __forceinline__ int fresh_lane() {
    int l;
    asm volatile("v_mbcnt_lo_u32_b32 %0, -1, 0\n\tv_mbcnt_hi_u32_b32 %0, -1, %0" : "=v"(l));
    return l;
}
#define BAR() __builtin_amdgcn_s_barrier()
#define WAIT_VM(n) asm volatile("s_waitcnt vmcnt(" #n ")" ::: "memory")
#define WAIT_LGKM0() __builtin_amdgcn_s_waitcnt(0xC07F)
#define SCHED() __builtin_amdgcn_sched_barrier(0)
#define MF(c, a, b) asm volatile("v_mfma_f32_16x16x32_bf16 %0, %1, %2, %0" : "+v"(c) : "v"(a), "v"(b))
#define PAD8(c0, c1, c2, c3) asm volatile("s_nop 7" : "+v"(c0), "+v"(c1), "+v"(c2), "+v"(c3))
#define PIN4(c0, c1, c2, c3) asm volatile("" : "+v"(c0), "+v"(c1), "+v"(c2), "+v"(c3))
#define GLDS_S(sbase, voff, sl, IMM) \
    asm volatile("s_add_u32 m0, %2, %3\n\ts_nop 0\n\tglobal_load_lds_dwordx4 %0, %1" \
                 :: "v"(voff), "s"(sbase), "s"(sl), "i"(IMM) : "memory", "scc")
#define GLDS_K(sbase, voff, sl, IMM) \
    asm volatile("s_add_u32 m0, %2, %3\n\ts_nop 0\n\tglobal_load_lds_dwordx4 %0, %1 nt" \
                 :: "v"(voff), "s"(sbase), "s"(sl), "i"(IMM) : "memory", "scc")
#define RLX_AGENT __ATOMIC_RELAXED, __HIP_MEMORY_SCOPE_AGENT

#define UNITS_A 0x0756075607560560ull
#define UNITS_B 0x1432143214321432ull
#define RA(t) ((int)((UNITS_A >> (4 * (t))) & 15) * 16384)
#define RB(t) ((int)((UNITS_B >> (4 * (t))) & 15) * 16384)
#define LA_OFF 0
#define GA_OFF 32768
#define CV_COPY 4160
#define CV_W (4 * CV_COPY)
#define KC_OFF 65536
#define ST_OFF 114688
#define ST_V 8832
#define ST_SZ (ST_V + 8 * 1056)
#define U_OFF (KC_OFF + 16 * CV_COPY)
#define HT_OFF (U_OFF + 256 + 16384)
#define GW_OFF (HT_OFF + 8192)
#define TAG_OFF (GW_OFF + 2048)
#define UFLAG_OFF (TAG_OFF + 16)
#define G1D_OFF (UFLAG_OFF + 16)
#define MISC_OFF (G1D_OFF + 16)
#define YFLAG_OFF (MISC_OFF + 16)
#define RF_OFF (YFLAG_OFF + 16)
#define EF_OFF (RF_OFF + 64)
#define COK_OFF (EF_OFF + 64)
#define NFLAGW 52
#define DUMMY_OFF (COK_OFF + 16)
#define K1_LDS (DUMMY_OFF + 256)
#define WS_GRAN 0
#define WS_KVFLAG (256u << 10)
#define WS_EPOCH (WS_KVFLAG + 65536)
#define WS_CFLAG (WS_EPOCH + 4096)
#define WS_HFLAG (384u << 10)
#define WS_GATE (512u << 10)

#define CODE_TOUCH(vl) asm volatile("s_getpc_b64 s[98:99]\n\ts_add_u32 s98, s98, k1_kernel@rel32@lo+4\n\ts_addc_u32 s99, s99, k1_kernel@rel32@hi+12\n\t" \
                                    "s_mov_b32 m0, %1\n\ts_nop 0\n\tglobal_load_lds_dword %0, s[98:99]" :: "v"((unsigned)(vl) * 128u), "i"(DUMMY_OFF) : "s98", "s99", "scc", "memory")
#define RD128(a, off) (*(const __attribute__((address_space(3))) bf16x8*)(size_t)(unsigned)((a) + (off)))
#define RDA(dst, A, F) do { const s16x4 lo_ = TRD((A) + 512 * (F) + 8); SCHED(); const s16x4 hi_ = TRD((A) + 512 * (F)); SCHED(); \
                            dst = __builtin_shufflevector(lo_, hi_, 0, 1, 2, 3, 4, 5, 6, 7); } while (0)
#define TOUCH(sbase, voff) asm volatile("s_mov_b32 m0, %2\n\ts_nop 0\n\tglobal_load_lds_dword %0, %1" :: "v"(voff), "s"(sbase), "i"(DUMMY_OFF) : "memory")
#define FLAGD(off) do { unsigned long long pc_; asm volatile("s_getpc_b64 %0\n\ts_mov_b32 m0, %2\n\ts_mov_b64 exec, 1\n\tglobal_load_lds_dword %1, %0\n\ts_mov_b64 exec, -1" \
                                                             : "=&s"(pc_) : "v"(0u), "s"(__builtin_amdgcn_readfirstlane(off)) : "memory"); } while (0)
#define GLDS_SC1(sbase, voff, sl, IMM) \
    asm volatile("s_add_u32 m0, %2, %3\n\ts_nop 0\n\tglobal_load_lds_dwordx4 %0, %1 sc1" \
                 :: "v"(voff), "s"(sbase), "s"(sl), "i"(IMM) : "memory", "scc")
__device__ __forceinline__ unsigned mv(unsigned v) { unsigned r; asm("v_mov_b32 %0, %1" : "=v"(r) : "v"(v)); return r; }
__device__ __forceinline__ unsigned al(unsigned hi, unsigned lo) { unsigned r; asm volatile("v_alignbit_b32 %0, %1, %2, 16" : "=v"(r) : "v"(hi), "v"(lo)); return r; }
__device__ __forceinline__ u32x2 pred2(const u32x4& a, unsigned f0, unsigned f1) {
    return (u32x2){(unsigned)__builtin_amdgcn_update_dpp((int)f0, (int)a.z, 0x138, 0xf, 0xf, false), (unsigned)__builtin_amdgcn_update_dpp((int)f1, (int)a.w, 0x138, 0xf, 0xf, false)};
}
#define RL63(v) ((unsigned)__builtin_amdgcn_readlane((int)(v), 63))
__device__ __forceinline__ void wait_lds4(char* smem, int off) {
    const unsigned a = (unsigned)(size_t)(lds_void*)smem + off;
    unsigned spins = 0;
    for (;;) {
        u32x4 f;
        asm volatile("ds_read_b128 %0, %1\n\ts_waitcnt lgkmcnt(0)" : "=&v"(f) : "v"(a) : "memory");
        if (__builtin_amdgcn_readfirstlane((f.x != 0u) & (f.y != 0u) & (f.z != 0u) & (f.w != 0u))) break;
        if (++spins > SPIN_MAX * 16u) break;
        __builtin_amdgcn_s_sleep(1);
    }
}
__device__ __forceinline__ unsigned lds_word(char* smem, int off) {
    const unsigned a = (unsigned)(size_t)(lds_void*)smem + off;
    unsigned f;
    asm volatile("ds_read_b32 %0, %1\n\ts_waitcnt lgkmcnt(0)" : "=&v"(f) : "v"(a) : "memory");
    return __builtin_amdgcn_readfirstlane(f);
}
__device__ __forceinline__ void wait_lds1(char* smem, int off) {
    unsigned spins = 0;
    while (lds_word(smem, off) == 0u) {
        if (++spins > SPIN_MAX * 16u) break;
        __builtin_amdgcn_s_sleep(1);
    }
}
#define SETW(off, i, v) __hip_atomic_store((lds_u32*)(smem + (off)) + (i), (v), __ATOMIC_RELAXED, __HIP_MEMORY_SCOPE_WORKGROUP)

#define PRIO_CV 1
#define PRIO_KV 3
#define CS0 1
#define CS1 3
#define CS2 5
#define XE 4
#define YPOLL 32
#define ZF 59
template <int I> struct IC { static constexpr int value = I; };
template <int N, int I = 0, class F> __device__ __forceinline__ void unroll(F& f) { if constexpr (I < N) { f(IC<I>{}); unroll<N, I + 1>(f); } }
#define MF0(c, a, b) asm volatile("v_mfma_f32_16x16x32_bf16 %0, %1, %2, 0" : "=&v"(c) : "v"(a), "v"(b))
#define DEPTH 6
#define PRIO2_G 0
#define PRIO2_L 1
__device__ __forceinline__ cf pick4(int q, cf a1, cf a2, cf a3) {
    return {q == 0 ? 1.f : q == 1 ? a1.r : q == 2 ? a2.r : a3.r, q == 0 ? 0.f : q == 1 ? a1.i : q == 2 ? a2.i : a3.i};
}
#define PINC(c) asm volatile("" : "+v"((c).r), "+v"((c).i))

extern "C" __global__ __launch_bounds__(512, 2) void k1_kernel(
        const u16* __restrict__ x, const u16* __restrict__ Bre, const u16* __restrict__ Bim,
        const u16* __restrict__ kin, const u16* __restrict__ vin, const u16* __restrict__ shiftk, const u16* __restrict__ gatew, char* ws,
        const u16* __restrict__ lre, const u16* __restrict__ lim, const u16* __restrict__ logdt,
        const u16* __restrict__ ssmk, const u16* __restrict__ ssmD,
        const u16* __restrict__ Cre, const u16* __restrict__ Cim, const u16* __restrict__ dskip, float* __restrict__ out) {
    extern __shared__ __attribute__((aligned(16))) char smem[];
#define h ((u16*)(ws + (1u << 20)))
#define kvT ((u16*)(ws + (9u << 20)))
#define y2T ((u16*)(ws + (13u << 20)))
#define dbg ((unsigned long long*)(ws + (64u << 20)))
    const int tid = TIDX;
    if (tid < 40 && tid + ((BIDX >> 3) & 7) * 40 < CODE_LINES) CODE_TOUCH(tid + ((BIDX >> 3) & 7) * 40);
    const int lane = tid & 63;
    int wid = __builtin_amdgcn_readfirstlane(tid >> 6);
    const int b = BIDX;
    const int xcd = b & 7, idx = b >> 3;
    const int tb = ((xcd >> 1) ^ (xcd >> 2)) * 4 + (idx & 3), nb = (xcd & 1) * 8 + (idx >> 2);
    const int cb = xcd * 32 + idx;
    const int t_base = tb * 128, p_base = nb * 64, d_base = nb * 64;
    const int l15 = lane & 15, q4 = lane >> 4;
    unsigned stv = 0, sth = 0;
    STAMPT(dbg, 0, 0);
    const unsigned lbase = (unsigned)(size_t)(lds_void*)smem;
    const int rot1 = (xcd >> 1) * 4 + (xcd & 1) * 8 + (idx >> 2) + (idx & 1);
    unsigned voA[4] = {0u, 0u, 0u, 0u}, voB[4] = {0u, 0u, 0u, 0u}, lw0 = 0;
    if (wid == 4 || wid == 5) {
        __builtin_amdgcn_s_setprio(PRIO_L);
        const int lh = wid - 4;
        const unsigned ch = (unsigned)(((lane & 7) ^ (lane >> 4)) * 16);
        voA[0] = (unsigned)(t_base + lh * 16 + (lane >> 5) * 32 + ((lane >> 3) & 3)) * 2048u + ch;
        voA[1] = (voA[0] ^ 64u) + (131072u - 1024u);
        voA[2] = voA[0] + 6144u; voA[3] = voA[1] + 6144u;
        voB[0] = (unsigned)(p_base + lh * 32 + (lane >> 3)) * 2048u + ch;
        voB[1] = (voB[0] ^ 64u) + (16384u - 1024u);
        voB[2] = voB[0] + 30720u; voB[3] = voB[1] + 30720u;
        PIN4(voA[0], voA[1], voA[2], voA[3]); PIN4(voB[0], voB[1], voB[2], voB[3]);
#define GLDS4(sbase, v0, v1, v2, v3, sl, IMM) \
            asm volatile("s_add_u32 m0, %5, %6\n\ts_nop 0\n\tglobal_load_lds_dwordx4 %0, %4\n\tglobal_load_lds_dwordx4 %1, %4 offset:1024\n\t" \
                         "global_load_lds_dwordx4 %2, %4 offset:2048\n\tglobal_load_lds_dwordx4 %3, %4 offset:3072" \
                         :: "v"(v0), "v"(v1), "v"(v2), "v"(v3), "s"(sbase), "s"(sl), "i"(IMM) : "memory", "scc")
#define STAGE_A(kt, sl) do { const char* sa_ = (const char*)x + (((kt) + rot1) & 15) * 128; \
            GLDS4(sa_, voA[0], voA[1], voA[2], voA[3], sl, 0); GLDS4(sa_ + 16384, voA[0], voA[1], voA[2], voA[3], sl, 4096); } while (0)
#define STAGE_B(kt, sl) do { const int kr_ = (((kt) + rot1) & 15) * 128; const char* sb_ = (const char*)Bre + kr_; const char* sc_ = (const char*)Bim + kr_; \
            GLDS4(sb_, voB[0], voB[1], voB[2], voB[3], sl, 0); GLDS4(sc_, voB[0], voB[1], voB[2], voB[3], sl, 4096); } while (0)
        lw0 = lbase + lh * 8192;
        STAGE_A(0, lw0 + RA(0)); STAGE_B(0, lw0 + RB(0)); STAGE_A(1, lw0 + RA(1)); STAGE_B(1, lw0 + RB(1));
    }
    const int sblk = b >> 3, dblk = b & 7;
    u32x2 tw[4];
    unsigned epoch;
    asm volatile("" : "=v"(tw[0]), "=v"(tw[1]), "=v"(tw[2]), "=v"(tw[3]), "=v"(epoch));
    if (wid >= 6) {
        __builtin_amdgcn_s_setprio(PRIO_L);
        const int vw = wid - 6;
        const int dch = dblk * 128 + vw * 64 + l15;
        const unsigned lk = lbase + ST_OFF + vw * ST_SZ;
        const unsigned vk = (unsigned)((lane >> 3) * DM * 2 + (((lane & 7) ^ ((lane >> 4) & 1)) * 16));
        const char* kb = (const char*)kin + (size_t)(sblk * 64 - 4) * (DM * 2) + dblk * 256 + vw * 128;
#define KLD(n) GLDS_K(kb + (n) * 8 * DM * 2, vk, lk, (n) * 1024 + ((n) >> 1) * 32)
#define VLD(n) GLDS_K(vb + (((n) >> 1) * 16 + ((n) & 1) * 2) * DM * 2, vv_, lk, ST_V + (n) * 1056)
        if (vw == 1) {
            const unsigned vg = (unsigned)((lane * 16 + rot1 * 128) & 2047), lg = lbase + GW_OFF;
            GLDS_S(gatew, vg, lg, 0); GLDS_S(gatew, vg ^ 1024u, lg, 1024);
        }
        asm volatile("global_load_dwordx2 %0, %5, %7\n\tglobal_load_dwordx2 %1, %5, %7 offset:128\n\tglobal_load_dwordx2 %2, %5, %7 offset:256\n\t"
                     "global_load_dwordx2 %3, %5, %7 offset:384\n\tglobal_load_dword %4, %6, %8 sc1"
                     : "+v"(tw[0]), "+v"(tw[1]), "+v"(tw[2]), "+v"(tw[3]), "+v"(epoch) : "v"((unsigned)(dch * 8)), "v"((unsigned)WS_EPOCH), "s"(shiftk), "s"(ws) : "memory");
        if (sblk == 0 && lane < 32) *(u32x4*)(smem + ST_OFF + vw * ST_SZ + lane * 16) = (u32x4){0u, 0u, 0u, 0u};
        if (sblk != 0 || lane >= 32) KLD(0);
        KLD(1); KLD(2); KLD(3); KLD(4); KLD(5); KLD(6); KLD(7);
        if (lane < 32) KLD(8);
        __builtin_amdgcn_s_setprio(0);
    }
    asm volatile("" : "+s"(wid));

#define WSL(w) ((((w) >> 2) ^ (nb >> 3)) * 4 + ((w) & 3))
#define COFF(w, ti) ((2 * WSL(w) + ((ti) >> 1)) * 128 + ((ti) & 1) * 4096)
    bf16x8 cf_[8][4];
    bf16x8 af[DEPTH];
    int cra, crb;
#define CT_SETUP() const unsigned vB0 = (unsigned)((d_base + (lane >> 3) * 4) * 2048 + (((lane & 7) ^ (lane >> 4)) * 16)), vB1 = (vB0 ^ 64u) + 65536u - 1024u
#define CTILE(w, ti, dst) do { const int co_ = COFF(w, ti); const unsigned ds_ = (dst); \
                               GLDS4((const char*)Cre + co_, vB0, vB1, vB0, vB1, ds_, 0); GLDS4((const char*)Cim + co_, vB0, vB1, vB0, vB1, ds_, 4096); } while (0)
#define CF(ti, n) cf_[4 * ((ti) >> 1) + ((n) >> 1)][2 * ((ti) & 1) + ((n) & 1)]
#define CRD(ti, n) CF(ti, n) = RD128((((n) & 4) ? crb : cra) ^ (((n) & 2) * 16), ((n) & 1) * 2048)
#define CREAD(ti) do { _Pragma("unroll") for (int n_ = 0; n_ < 8; ++n_) CRD(ti, n_); } while (0)
#define CPIN(ti) do { PIN4(CF(ti, 0), CF(ti, 1), CF(ti, 2), CF(ti, 3)); PIN4(CF(ti, 4), CF(ti, 5), CF(ti, 6), CF(ti, 7)); } while (0)
#define CRA(base) ((base) + (q4 >> 1) * 4096 + l15 * 128 + (((q4 & 1) ^ ((l15 >> 1) & 7)) * 16))
    const int rot2 = (idx & 16) >> 2;
#define JB(u) (((((u) ^ (rot2 >> 2)) & 1) << 2) | ((wid - ((u) >> 1) + rot2) & 3))
    const __amdgpu_buffer_rsrc_t hs = __builtin_amdgcn_make_buffer_rsrc((char*)h + (size_t)tb * 524288 + WSL(wid) * 8192, 0, 524288, 0x00020000);
#define LOADF(f) do { af[(f) % DEPTH] = __builtin_bit_cast(bf16x8, __builtin_amdgcn_raw_buffer_load_b128(hs, vh + ((f) & 3) * 1024, JB((f) >> 3) * 65536 + ((f) & 4) * 1024, 16)); SCHED(); } while (0)
#define HPOLL_SETUP() gu32* hfp = (gu32*)(ws + WS_HFLAG) + (tb * 16 + 2 * WSL(wid) + (lane & 1)) * 32
#define HWAIT() do { unsigned spins = 0; \
            while (!__all(__hip_atomic_load(hfp, RLX_AGENT) == tag)) { if (++spins > SPIN_MAX) break; __builtin_amdgcn_s_sleep(PSLEEP); } asm volatile("" ::: "memory"); } while (0)

    const __amdgpu_buffer_rsrc_t gs = __builtin_amdgcn_make_buffer_rsrc(ws + WS_GATE, 0, 32768, 0x00020000);
#define GV(q4) ((unsigned)(((q4) >> 1) * 128 + ((q4) & 1) * 32))
#define GSO(r) (tb * 2048 + JB(6 + (r)) * 256 + (wid >> 2) * 16)
#define TR(r) (t_base + JB(6 + (r)) * 4 + (wid >> 2) * 2)
    u32x2 xx[2][2], dk2;
    u32x4 gv[2];
#define XLOAD() do { \
        const __amdgpu_buffer_rsrc_t xs = __builtin_amdgcn_make_buffer_rsrc((void*)x, 0, SEQ * DM * 2, 0x00020000); \
        const __amdgpu_buffer_rsrc_t ks = __builtin_amdgcn_make_buffer_rsrc((void*)dskip, 0, DM * 2, 0x00020000); \
        dk2 = __builtin_amdgcn_raw_buffer_load_b64(ks, (unsigned)(l15 * 8), d_base * 2, 0); \
        _Pragma("unroll") for (int q = 0; q < 4; ++q) \
            xx[q >> 1][q & 1] = __builtin_amdgcn_raw_buffer_load_b64(xs, (unsigned)(q4 * 32 * DM * 2 + l15 * 8 + (q & 1) * DM * 2), (TR(q >> 1) * DM + d_base) * 2, 0); \
        _Pragma("unroll") for (int r = 0; r < 2; ++r) gv[r] = __builtin_amdgcn_raw_buffer_load_b128(gs, GV(q4), GSO(r), 16); \
        SCHED(); } while (0)
#define XPIN() do { PIN4(xx[0][0], xx[0][1], xx[1][0], xx[1][1]); asm volatile("" : "+v"(dk2), "+v"(gv[0]), "+v"(gv[1])); } while (0)

    if (wid < 4) {
        typedef __attribute__((ext_vector_type(8))) unsigned u32x8;
        typedef const __attribute__((address_space(4))) u32x8 cu8;
        const int po = (p_base + wid * 16) * 2;
        u32x8 s0 = *(cu8*)((size_t)logdt + po), s1 = *(cu8*)((size_t)lre + po), s2 = *(cu8*)((size_t)lim + po);
        SCHED();
        const int pq = p_base + wid * 16 + l15;
        unsigned goff[4], gmask[4];
#pragma unroll
        for (int i = 0; i < 4; ++i) {
            goff[i] = (unsigned)(((tb - 1 - q4 - 4 * i) * 1024 + pq) * 16);
            gmask[i] = (tb - 1 - q4 - 4 * i >= 0) ? ~0u : 0u;
            asm volatile("" : "+v"(goff[i]), "+v"(gmask[i]));
        }
        unsigned gpub = q4 == 0 ? (unsigned)((tb * 1024 + pq) * 16) : 0x80000000u;
        unsigned hp = (unsigned)(tb * 524288 + (nb * 4 + wid) * 1024 + lane * 16);
        unsigned wa = (unsigned)(HT_OFF + wid * 2048 + (q4 * 16 + (l15 ^ ((q4 >> 1) * 4))) * 8);
        unsigned ra0 = (unsigned)(HT_OFF + wid * 2048 + ((q4 >> 1) * 64 + (l15 & 3) * 16 + (((q4 & 1) * 8 + (l15 >> 2)) ^ ((l15 & 2) * 2))) * 8), ra1 = ra0 ^ 32u;
        cra = CRA(GA_OFF + wid * 8192); crb = cra ^ 64;
        unsigned vB0 = (unsigned)((d_base + (lane >> 3) * 4) * 2048 + (((lane & 7) ^ (lane >> 4)) * 16)), vB1 = (vB0 ^ 64u) + 65536u - 1024u;
        PIN4(gpub, vB0, hp, wa); PIN4(ra0, ra1, cra, crb); asm volatile("" : "+v"(vB1));
        f32x4 acc[8][2];
#pragma unroll
        for (int m = 0; m < 8; m += 2) {
#pragma unroll
            for (int c = 0; c < 4; ++c) acc[m + (c >> 1)][c & 1] = (f32x4){0.f, 0.f, 0.f, 0.f};
            PIN4(acc[m][0], acc[m][1], acc[m + 1][0], acc[m + 1][1]);
        }
        SCHED();
        cf L1, Lq3, cc, L32, cLq, Lp, L512;
        {
            unsigned w0 = 0, w1 = 0, w2 = 0;
#pragma unroll
            for (int i = 0; i < 8; ++i) {
                asm("v_writelane_b32 %0, %3, %6\n\tv_writelane_b32 %1, %4, %6\n\tv_writelane_b32 %2, %5, %6" : "+v"(w0), "+v"(w1), "+v"(w2) : "s"(s0[i]), "s"(s1[i]), "s"(s2[i]), "i"(2 * i));
            }
            asm("s_nop 1" : "+v"(w0), "+v"(w1), "+v"(w2));
            auto spread = [&](unsigned w) {
                const unsigned e = (unsigned)__builtin_amdgcn_update_dpp(0, (int)w, 0xa0, 0xf, 0xf, false);
                const unsigned a = __builtin_amdgcn_permlane16_swap(e, e, false, false)[0];
                const unsigned c = __builtin_amdgcn_permlane32_swap(a, a, false, false)[0];
                return (l15 & 1) ? hi16(c) : lo16(c);
            };
            const float ldt = spread(w0), re = spread(w1), im = spread(w2);
            if (wid == 3 && lane < NFLAGW) *(unsigned*)(smem + UFLAG_OFF + lane * 4) = 0u;
            const float dt = rbf(expf(ldt));
            const float ea = expf(dt * re);
            float sn, cs;
            sincosf(dt * im, &sn, &cs);
            L1 = {ea * cs, ea * sn};
            const float nr = L1.r - 1.0f, ni = L1.i, den = re * re + im * im;
            cc = {2.f * ((nr * re + ni * im) / den), 2.f * ((ni * re - nr * im) / den)};
            const cf T2 = cmul(L1, L1), T4 = cmul(T2, T2), L8 = cmul(T4, T4), L16 = cmul(L8, L8);
            L32 = cmul(L16, L16);
            const cf L64 = cmul(L32, L32), L96 = cmul(L64, L32), L128 = cmul(L64, L64), L256 = cmul(L128, L128), L384 = cmul(L256, L128);
            L512 = cmul(L256, L256);
            Lp = pick4(q4, L128, L256, L384);
            Lq3 = pick4(3 - q4, L32, L64, L96);
            cLq = cmul(cc, pick4(q4, L32, L64, L96));
            PINC(L1); PINC(Lq3); PINC(cc); PINC(L32); PINC(cLq); PINC(Lp); PINC(L512);
        }
        __builtin_amdgcn_s_setprio(PRIO_G);
        unsigned tagv;
        cf S;
        STAMPT(dbg, 22, 0);
        const int swz = (l15 >> 1) & 7;
        {
            const int a0 = RA(0) + l15 * 128 + ((q4 ^ swz) * 16);
            const int b0 = RB(0) + ((wid >> 1) * 64 + (wid & 1) * 16 + l15) * 128 + ((q4 ^ swz) * 16);
            WAIT_LGKM0();
            BAR();
            asm volatile("" ::: "memory");
            STAMPT(dbg, 19, 64);
            bf16x8 FA[8][2], FB[2][2][2];
            int pa[2] = {a0, a0 ^ 64}, pb[2] = {b0, b0 ^ 64};
            PIN4(pa[0], pa[1], pb[0], pb[1]);
#define LGW(n) __builtin_amdgcn_s_waitcnt(0xC07F | ((n) << 8))
#define RA_(j) FA[(j) >> 1][(j) & 1] = RD128(pa[(j) & 1], ((j) >> 1) * 2048)
#define RB_(bu, j) FB[bu][(j) >> 1][((j) ^ ((j) >> 1)) & 1] = RD128(pb[((j) ^ ((j) >> 1)) & 1], ((j) >> 1) * 4096)
#define RN_(bu, i) do { if ((i) == 0) RB_(bu, 0); else if ((i) < 3) RA_((i) - 1); else if ((i) < 6) RB_(bu, (i) - 2); else RA_((i) - 4); } while (0)
#define G1_H(H, bu, RD, FILL) do { \
            _Pragma("unroll") for (int i = 0; i < 16; ++i) { \
                const int m = (H) * 4 + (i >> 2), n = ((i >> 1) ^ m) & 1, ks = (i ^ (i >> 1)) & 1; \
                if ((H) == 0) { if (i == 0) LGW(6); if (i == 4) LGW(8); if (i == 8) LGW(10); if (i == 12) LGW(8); } \
                SCHED(); \
                MF(acc[m][n], FA[m][ks], FB[bu][n][ks]); SCHED(); \
                if ((H) == 0 && i < 8) RA_(8 + i); \
                if ((H) == 1 && (RD) && i < 12) RN_((bu) ^ 1, i); \
                FILL((H) * 16 + i); \
                SCHED(); \
            } } while (0)
#define NOF(g) do { } while (0)
#pragma unroll
            for (int j = 0; j < 12; ++j) { RN_(0, j); SCHED(); }
            unsigned long long ua_ = UNITS_A;
            int sb = RB(0), sa = RA(0), nx, na;
#define ADV(g) do { if ((g) == 8) { ua_ >>= 4; na = (int)((unsigned)ua_ & 15u) << 14; asm volatile("" : "+s"(na), "+s"(ua_)); } \
                    if ((g) == 9) { const int o_ = sa; sa = na; na -= o_; asm volatile("" : "+s"(na), "+s"(sa)); } \
                    if ((g) == 10) { pa[0] += na; pa[1] += na; asm volatile("" : "+v"(pa[0]), "+v"(pa[1])); } \
                    if ((g) == 11) { nx = (sb == 65536) ? -49152 : 16384; sb += nx; asm volatile("" : "+s"(nx), "+s"(sb)); } \
                    if ((g) == 13) { pb[0] += nx; pb[1] += nx; asm volatile("" : "+v"(pb[0]), "+v"(pb[1])); } } while (0)
#define G1_T(bu) do { \
                G1_H(0, bu, true, ADV); \
                WAIT_LGKM0(); \
                asm volatile("" ::: "memory"); \
                BAR();                     \
                asm volatile("" ::: "memory"); \
                G1_H(1, bu, true, NOF); } while (0)
#pragma unroll 1
            for (int t2 = 0; t2 < 7; ++t2) { G1_T(0); G1_T(1); }
            G1_T(0);
#define AGG(g) do { if ((g) == 8) { asm volatile("" ::: "memory"); SETW(G1D_OFF, wid, 1u); }        \
                    if ((g) == 9) tagv = *(volatile const lds_u32*)(size_t)(unsigned)TAG_OFF; \
                    if ((g) >= 7) { const int t = (g) - 6; \
                        if (t == 1) { asm volatile("s_nop 1" : "+v"(acc[0][0]), "+v"(acc[0][1])); S = {acc[0][0][0], acc[0][1][0]}; } \
                        if ((t & 3) == 0) asm volatile("" : "+v"(acc[t >> 2][0]), "+v"(acc[t >> 2][1])); \
                        S = cfma(L1, S, cf{acc[t >> 2][0][t & 3], acc[t >> 2][1][t & 3]}); PINC(S); } } while (0)
            G1_H(0, 1, false, AGG);
            G1_H(1, 1, false, AGG);
        }
        STAMPT(dbg, 1, 0);
        const unsigned tag = __builtin_amdgcn_readfirstlane(tagv);
        const __amdgpu_buffer_rsrc_t grs = __builtin_amdgcn_make_buffer_rsrc(ws, 0, 15 * 16384, 0x00020000);
        {
#pragma unroll
            for (int t = 26; t < 32; ++t) {
                if (t == 28) { PINC(S); asm volatile("" : "+v"(acc[7][0]), "+v"(acc[7][1])); }
                S = cfma(L1, S, cf{acc[t >> 2][0][t & 3], acc[t >> 2][1][t & 3]});
            }
            const cf W = cmul(S, Lq3);
            const float e2 = red16(red32(W.r, W.i));
            const auto eo = __builtin_amdgcn_permlane32_swap(__float_as_uint(e2), __float_as_uint(e2), false, false);
            __builtin_amdgcn_raw_buffer_store_b128((u32x4){eo[0], tag, eo[1], tag}, grs, gpub, 0, 16);
        }
        SCHED();
        STAMPT(dbg, 2, 0);
        const unsigned cg = lbase + GA_OFF + wid * 8192;
        wait_lds1(smem, COK_OFF + wid * 4);
        CREAD(0);
        WAIT_LGKM0();
        CPIN(0);
        CTILE(wid, 1, cg);
        u32x4 gq[4];
#pragma unroll
        for (int m = 0; m < 8; ++m) {
            const f32x4 R = acc[m][0], I = acc[m][1];
            acc[m][0] = fma4(splat4(cc.r), R, mul4(splat4(-cc.i), I));
            acc[m][1] = fma4(splat4(-cc.r), I, mul4(splat4(-cc.i), R));
            asm volatile("" : "+v"(acc[m][0]), "+v"(acc[m][1]));
            if (m == 3) {
                SCHED();
#pragma unroll
                for (int i = 0; i < 4; ++i) asm volatile("buffer_load_dwordx4 %0, %1, %2, 0 offen sc1" : "=&v"(gq[i]) : "v"(goff[i]), "s"(grs) : "memory");
                SCHED();
            }
        }
        cf Ec;
        {
            const auto sr = __builtin_amdgcn_permlane16_swap(__float_as_uint(S.r), __float_as_uint(S.r), false, false);
            const auto si = __builtin_amdgcn_permlane16_swap(__float_as_uint(S.i), __float_as_uint(S.i), false, false);
            const cf Se = {__uint_as_float(sr[0]), __uint_as_float(si[0])}, So = {__uint_as_float(sr[1]), __uint_as_float(si[1])};
            const cf A = cfma(L32, Se, So);
            const auto ar = __builtin_amdgcn_permlane32_swap(__float_as_uint(A.r), __float_as_uint(A.r), false, false);
            const auto ai = __builtin_amdgcn_permlane32_swap(__float_as_uint(A.i), __float_as_uint(A.i), false, false);
            const cf A01 = {(q4 & 2) ? __uint_as_float(ar[0]) : 0.f, (q4 & 2) ? __uint_as_float(ai[0]) : 0.f};
            const cf kq = {(q4 & 1) ? L32.r : 1.f, (q4 & 1) ? L32.i : 0.f};
            const cf E = cfma(kq, A01, cf{(q4 & 1) ? Se.r : 0.f, (q4 & 1) ? Se.i : 0.f});
            Ec = cmul(cc, E);
            PINC(Ec);
        }
        WAIT_VM(4);
        CREAD(1);
        WAIT_LGKM0();
        CPIN(1);
        CTILE(wid, 2, cg);
        asm volatile("s_waitcnt vmcnt(8)" : "+v"(gq[0]), "+v"(gq[1]), "+v"(gq[2]), "+v"(gq[3]) :: "memory");
        STAMPT(dbg, 27, 0);
        cf carry;
        {
            unsigned spins = 0;
            for (;;) {
                unsigned bad = 0;
#pragma unroll
                for (int i = 0; i < 4; ++i) bad |= ((gq[i].y ^ tag) | (gq[i].w ^ tag)) & gmask[i];
                if (__all(bad == 0u)) break;
                if (++spins > SPIN_MAX) break;
#pragma unroll
                for (int i = 0; i < 4; ++i) asm volatile("buffer_load_dwordx4 %0, %1, %2, 0 offen sc1" : "+v"(gq[i]) : "v"(goff[i]), "s"(grs) : "memory");
                asm volatile("s_waitcnt vmcnt(0)" : "+v"(gq[0]), "+v"(gq[1]), "+v"(gq[2]), "+v"(gq[3]) :: "memory");
            }
            cf E = {__uint_as_float(gq[3].x), __uint_as_float(gq[3].z)};
#pragma unroll
            for (int i = 2; i >= 0; --i) E = cfma(L512, E, cf{__uint_as_float(gq[i].x), __uint_as_float(gq[i].z)});
            E = cmul(E, Lp);
            const float e2 = red16(red32(E.r, E.i));
            const auto eo = __builtin_amdgcn_permlane32_swap(__float_as_uint(e2), __float_as_uint(e2), false, false);
            carry = {__uint_as_float(eo[0]), __uint_as_float(eo[1])};
        }
        STAMPT(dbg, 3, 0);
        {
            const __amdgpu_buffer_rsrc_t hrs = __builtin_amdgcn_make_buffer_rsrc(h, 0, SEQ * 2048 * 2, 0x00020000);
            const cf e = cfma(cLq, carry, Ec);
            float hr = e.r, hn = -e.i;
            s16x4 fr[2][4];
#define HSTORE(m) do { _Pragma("unroll") for (int c = 0; c < 2; ++c) \
                __builtin_amdgcn_raw_buffer_store_b128(__builtin_bit_cast(u32x4, __builtin_shufflevector(fr[(m) & 1][2 * c], fr[(m) & 1][2 * c + 1], 0, 1, 2, 3, 4, 5, 6, 7)), hrs, hp, (2 * (m) + c) * 65536, WT); } while (0)
#pragma unroll
            for (int m = 0; m < 4; ++m) {
#pragma unroll
                for (int j = 0; j < 8; ++j) {
                    const float nr = fmaf(L1.r, hr, fmaf(L1.i, hn, acc[2 * m + (j >> 2)][0][j & 3]));
                    const float nn = fmaf(L1.r, hn, fmaf(-L1.i, hr, acc[2 * m + (j >> 2)][1][j & 3]));
                    hr = nr; hn = nn;
                    acc[2 * m + (j >> 2)][0][j & 3] = nr; acc[2 * m + (j >> 2)][1][j & 3] = nn;
                }
                u32x2 pk[4];
#pragma unroll
                for (int c = 0; c < 4; ++c) { const f32x4 v = acc[2 * m + (c >> 1)][c & 1]; pk[c] = (u32x2){pkbf(v[0], v[1]), pkbf(v[2], v[3])}; }
                PIN4(pk[0], pk[1], pk[2], pk[3]);
                SCHED();
#pragma unroll
                for (int c = 0; c < 4; ++c) { *(volatile __attribute__((address_space(3))) u32x2*)(size_t)(wa + c * 512) = pk[c]; SCHED(); }
                asm volatile("" ::: "memory");
#pragma unroll
                for (int c = 0; c < 2; ++c) { fr[m & 1][2 * c] = TRD(ra0 + c * 1024); SCHED(); fr[m & 1][2 * c + 1] = TRD(ra1 + c * 1024); SCHED(); }
                asm volatile("" ::: "memory");
                if (m > 0) HSTORE(m - 1);
                SCHED();
            }
            HSTORE(3);
        }
        STAMPT(dbg, 4, 0);
        {
            WAIT_VM(8);
            CREAD(2);
            WAIT_VM(0);
            {
                unsigned n;
                asm volatile("s_mov_b64 exec, 1\n\tds_add_rtn_u32 %0, %1, %2\n\ts_mov_b64 exec, -1\n\ts_waitcnt lgkmcnt(0)" : "=&v"(n) : "v"(MISC_OFF), "v"(1u) : "memory");
                if (__builtin_amdgcn_readfirstlane(n) == 3u) {
                    if (lane == 0) __hip_atomic_store((gu32*)(ws + WS_HFLAG) + (tb * 16 + nb) * 32, tag, RLX_AGENT);
                    if (DBGON && lane == 0) dbg[BIDX * 64 + 20] = __builtin_amdgcn_s_memrealtime();
                }
            }
            STAMPT(dbg, 12, 0);
            if (wid == 0 && lane < 7) CODE_TOUCH(CODE_LINES - 1 - lane - (idx & 7) * 7);
            __builtin_amdgcn_s_setprio(0);
            WAIT_LGKM0();
            CPIN(2);
            CTILE(wid, 3, cg);
            XLOAD();
            HPOLL_SETUP();
            HWAIT();
            STAMPT(dbg, 8, 0);
            const unsigned vh = (unsigned)(lane * 16);
#pragma unroll
            for (int f = 0; f < DEPTH; ++f) LOADF(f);
            __builtin_amdgcn_s_setprio(PRIO2_G);
        }
    } else {
        const int lw = wid - 4;
        const int d = cb * 4 + lw;
        const char* Krow = (const char*)(ssmk + (size_t)d * SEQ);
        cra = CRA(LA_OFF + lw * 8192); crb = cra ^ 64;
        const unsigned cst0 = lbase + LA_OFF + lw * 8192;
        CT_SETUP();
        asm volatile("" : "+v"(cra), "+v"(crb));
        u32x4 KA[4];
        unsigned draw;
        unsigned tag;
        float gacc = 0.f;
#define KLOAD() asm volatile("global_load_dwordx4 %0, %5, %7 nt\n\tglobal_load_dwordx4 %1, %5, %7 offset:1024 nt\n\tglobal_load_dwordx4 %2, %5, %7 offset:2048 nt\n\t" \
                             "global_load_dwordx4 %3, %5, %7 offset:3072 nt\n\tglobal_load_ushort %4, %6, %8" \
                             : "=&v"(KA[0]), "=&v"(KA[1]), "=&v"(KA[2]), "=&v"(KA[3]), "=&v"(draw) : "v"((unsigned)(lane * 16)), "v"((unsigned)(d * 2)), "s"(Krow), "s"(ssmD) : "memory")
#define KWAIT(n) asm volatile("s_waitcnt vmcnt(%5)" : "+v"(KA[0]), "+v"(KA[1]), "+v"(KA[2]), "+v"(KA[3]), "+v"(draw) : "i"(n) : "memory")
        if (wid < 6) {
            STAGE_A(2, lw0 + RA(2)); STAGE_B(2, lw0 + RB(2));
            WAIT_VM(32);
            STAMPT(dbg, 17, 320);
            BAR();
            STAGE_B(3, lw0 + RB(3));
            unsigned long long ua_ = UNITS_A >> 12;
            unsigned sla = lw0 + RA(3), slb = lw0 + RB(4);
            int ka = ((3 + rot1) & 15) * 128, kb_ = ka;
            const char* sa = (const char*)x + ka;
            asm volatile("" : "+s"(sa), "+s"(sla), "+s"(ka), "+s"(ua_));
#define NEXT_A() do { ka = (ka + 128) & 2047; sa = (const char*)x + ka; ua_ >>= 4; sla = lw0 + (((unsigned)ua_ & 15u) << 14); asm volatile("" : "+s"(sa), "+s"(sla), "+s"(ka), "+s"(ua_)); } while (0)
#define LSTEP(n) do { \
                WAIT_VM(n); \
                BAR(); \
                GLDS4(sa, voA[0], voA[1], voA[2], voA[3], sla, 0); \
                GLDS4(sa + 16384, voA[0], voA[1], voA[2], voA[3], sla, 4096); \
                kb_ = (kb_ + 128) & 2047; \
                GLDS4((const char*)Bre + kb_, voB[0], voB[1], voB[2], voB[3], slb, 0); \
                NEXT_A(); \
                const unsigned slb0 = slb; \
                slb = (slb == lw0 + 65536) ? lw0 + 16384 : slb + 16384; \
                asm volatile("" : "+s"(slb), "+s"(kb_), "+s"(t)); \
                GLDS4((const char*)Bim + kb_, voB[0], voB[1], voB[2], voB[3], slb0, 4096); } while (0)
#pragma unroll 1
            for (int t = 1; t < 5; ++t) LSTEP(24);
            GLDS4(sa, voA[0], voA[1], voA[2], voA[3], sla, 0); GLDS4(sa + 16384, voA[0], voA[1], voA[2], voA[3], sla, 4096);
            NEXT_A();
#pragma unroll 1
            for (int t = 5; t < 13; ++t) LSTEP(32);
            KLOAD();
            WAIT_VM(37);
            BAR();
            __builtin_amdgcn_s_setprio(0);
            tag = lds_word(smem, TAG_OFF);
            CTILE(lw, 0, cst0 + GA_OFF);
            FLAGD(COK_OFF + lw * 4);
            WAIT_VM(30);
            BAR();
            CTILE(lw + 2, 0, cst0 + GA_OFF + 16384);
            FLAGD(COK_OFF + lw * 4 + 8);
            WAIT_VM(23);
            BAR();
        } else {
            const int vw = wid - 6;
            const int dch = dblk * 128 + vw * 64 + l15;
            const unsigned lk = lbase + ST_OFF + vw * ST_SZ;
            const unsigned vv_ = (unsigned)(((lane >> 4) * 4 + ((lane >> 3) & 1)) * DM * 2 + (lane & 7) * 16);
            const char* vb = (const char*)vin + (size_t)(sblk * 64) * (DM * 2) + dblk * 256 + vw * 128;
            const int grow = nb * 8 + vw * 4 + q4;
            unsigned gx0 = (unsigned)(grow * 128 + (((l15 >> 1) ^ ((grow >> 1) & 7)) * 16) + (l15 & 1) * 8), gx1 = gx0 + RA(2), gwa = (unsigned)(GW_OFF + l15 * 8);
            asm volatile("" : "+v"(gx0), "+v"(gx1), "+v"(gwa));
            u32x2 gxv, gwv;
            typedef const volatile __attribute__((address_space(3))) u32x2* l2p;
#define VBAR(t) do { SCHED(); asm volatile("" ::: "memory"); BAR(); asm volatile("" ::: "memory"); \
                     gxv = *(l2p)(size_t)(RA(t) == 0 ? gx0 : gx1 + (RA(t) - RA(2))); gwv = *(l2p)(size_t)(gwa + (t) * 128); SCHED(); } while (0)
#define GATE() do { SCHED(); gacc = fmaf(lo16(gxv.x), lo16(gwv.x), gacc); gacc = fmaf(hi16(gxv.x), hi16(gwv.x), gacc); \
                    gacc = fmaf(lo16(gxv.y), lo16(gwv.y), gacc); gacc = fmaf(hi16(gxv.y), hi16(gwv.y), gacc); asm volatile("" : "+v"(gacc)); SCHED(); } while (0)
            unsigned ka[4][2], sel[4];
            const int R0 = 4 + (l15 >> 2) * 16 + (l15 & 3) - (q4 >> 1);
            unsigned kr0 = (unsigned)(2 * R0), kr1 = (unsigned)(ST_OFF + vw * ST_SZ + R0 * 128 + (((q4 ^ (R0 >> 1)) & 1) * 16));
            asm volatile("" : "+v"(kr0), "+v"(kr1));
#pragma unroll
            for (int i = 0; i < 4; ++i)
#pragma unroll
                for (int tp = 0; tp < 2; ++tp) {
                    ka[i][tp] = (kr1 ^ (tp * 16)) + ((kr0 + 8 * i - 4 * tp) & 0xe0) + (4 * i - 2 * tp) * 128;
                    asm volatile("" : "+v"(ka[i][tp]));
                }
#pragma unroll
            for (int r = 0; r < 4; ++r) {
                const unsigned pick = (q4 & 2) ? 0x0302u : 0x0100u;
                sel[r] = ((l15 >> 1) == (q4 & 1) * 4 + r) ? ((l15 & 1) ? (pick << 16) | 0x0c0cu : 0x0c0c0000u | pick) : 0x0c0c0c0cu;
            }
            unsigned va_ = (unsigned)(ST_OFF + vw * ST_SZ + ST_V + (q4 * 2 + (l15 >> 3)) * 1056 + ((l15 >> 2) & 1) * 128 + (l15 & 3) * 8);
            unsigned so = (unsigned)(((dch >> 2) * 32 + sblk) * 512 + q4 * 64 + (dch & 3) * 16);
            PIN4(sel[0], sel[1], sel[2], sel[3]); asm volatile("" : "+v"(so), "+v"(va_));
            asm volatile("s_waitcnt vmcnt(9)" : "+v"(tw[0]), "+v"(tw[1]), "+v"(tw[2]), "+v"(tw[3]), "+v"(epoch) :: "memory");
            STAMPT(dbg, 18, 384);
            VBAR(0);
            bf16x8 wf[4][2];
#pragma unroll
            for (int ps = 0; ps < 4; ++ps)
#pragma unroll
                for (int tp = 0; tp < 2; ++tp) {
                    u32x4 w;
#pragma unroll
                    for (int r = 0; r < 4; ++r) w[r] = __builtin_amdgcn_perm(0u, tw[ps][tp], sel[r]);
                    wf[ps][tp] = __builtin_bit_cast(bf16x8, w);
                    asm volatile("" : "+v"(wf[ps][tp]));
                }
            SCHED();
            VLD(0); VLD(1); VLD(2); VLD(3); VLD(4); VLD(5); VLD(6); VLD(7);
            GATE(); VBAR(1);
            f32x4 ks[4][4];
            auto sconv = [&](int i0) __attribute__((always_inline)) {
                bf16x8 kf[2][4][2];
#pragma unroll
                for (int q = 0; q < 16; ++q) { kf[q >> 3][q & 3][(q >> 2) & 1] = RD128(ka[i0 + (q >> 3)][(q >> 2) & 1], (q & 3) * 32); SCHED(); }
                GATE();
                __builtin_amdgcn_s_setprio(PRIO_KV);
                WAIT_LGKM0();
                SCHED();
#pragma unroll
                for (int i = 0; i < 2; ++i) {
#pragma unroll
                    for (int ps = 0; ps < 4; ++ps) MF0(ks[ps][i0 + i], kf[i][ps][0], wf[ps][0]);
#pragma unroll
                    for (int ps = 0; ps < 4; ++ps) MF(ks[ps][i0 + i], kf[i][ps][1], wf[ps][1]);
                }
                __builtin_amdgcn_s_setprio(0);
                SCHED();
            };
            WAIT_VM(8);
            STAMPT(dbg, 21, 384);
            sconv(0); VBAR(2);
            sconv(2); VBAR(3);
            WAIT_VM(0);
            u32x2 vq[4][4];
#pragma unroll
            for (int ps = 0; ps < 4; ++ps)
#pragma unroll
                for (int j = 0; j < 4; ++j) vq[ps][j] = __builtin_bit_cast(u32x2, TRD(va_ + j * 256 + ps * 32));
            const __amdgpu_buffer_rsrc_t rs = __builtin_amdgcn_make_buffer_rsrc(kvT, 0, DM * SEQ * 2, 0x00020000);
            u32x4 o[8];
            auto kvout = [&](int n) __attribute__((always_inline)) {
                const int hh = n >> 2, ps = n & 3;
                asm volatile("" : "+v"(ks[ps][2 * hh]), "+v"(ks[ps][2 * hh + 1]));
#pragma unroll
                for (int e = 0; e < 4; ++e) {
                    const unsigned vv = vq[ps][2 * hh + (e >> 1)][e & 1];
                    o[n][e] = pkbf(ks[ps][2 * hh + (e >> 1)][2 * (e & 1)] * lo16(vv), ks[ps][2 * hh + (e >> 1)][2 * (e & 1) + 1] * hi16(vv));
                }
                asm volatile("" : "+v"(o[n]));
                SCHED();
            };
            GATE(); kvout(0); kvout(1);
            WAIT_LGKM0();
            VBAR(4);
            kvout(2); kvout(3); GATE(); VBAR(5);
            kvout(4); kvout(5); GATE(); VBAR(6);
            kvout(6); kvout(7); GATE(); VBAR(7);
#pragma unroll
            for (int n = 0; n < 8; ++n) __builtin_amdgcn_raw_buffer_store_b128(o[(n >> 1) + (n & 1) * 4], rs, so + (unsigned)((n & 1) * 256), (n >> 1) * 65536, WT);
            SCHED();
            asm volatile("s_nop 1" ::: "memory");
            GATE(); VBAR(8);
            if (vw == 1 && lane == 0) *(unsigned*)(smem + TAG_OFF) = (epoch + 1u) ? epoch + 1u : 1u;
            GATE();
            WAIT_LGKM0();
            WAIT_VM(0);
            VBAR(9);
            tag = __builtin_amdgcn_readfirstlane(*(const unsigned*)(smem + TAG_OFF));
            if (vw == 0 && lane == 0) __hip_atomic_store((gu32*)(ws + WS_KVFLAG) + (dblk * 32 + sblk) * 32, tag, RLX_AGENT);
            if (vw == 1 && lane < 4) *(u32x4*)(smem + U_OFF + 192 + lane * 16) = (u32x4){0u, 0u, 0u, 0u};
            gu32* kfp = (gu32*)(ws + WS_KVFLAG) + ((cb >> 5) * 32 + vw * 16 + l15) * 32;
            const char* ub = (const char*)kvT + (size_t)__builtin_amdgcn_readfirstlane(cb * 16384 + vw * 8192);
            const unsigned vu = (unsigned)((lane >> 5) * 512 + ((lane >> 2) & 1) * 256 + ((lane >> 3) & 3) * 64 + ((lane - (lane >> 4)) & 3) * 16);
            const unsigned lu = lbase + U_OFF + 256 + vw * 8192;
#define U_DMA1(o) asm volatile("s_add_u32 m0, %2, %3\n\ts_nop 0\n\tglobal_load_lds_dwordx4 %0, %1 sc1\n\tglobal_load_lds_dwordx4 %0, %1 offset:1024 sc1\n\t" \
                             "global_load_lds_dwordx4 %0, %1 offset:2048 sc1\n\tglobal_load_lds_dwordx4 %0, %1 offset:3072 sc1" \
                             :: "v"(vu), "s"(ub + (o)), "s"(lu), "i"(o) : "memory", "scc")
#define U_DMA() do { U_DMA1(0); U_DMA1(4096); FLAGD(UFLAG_OFF + lw * 4); } while (0)
            GATE(); VBAR(10);
            TOUCH((const char*)(vw ? Cim : Cre) + (size_t)b * 8192, (unsigned)(lane * 128));
            if (vw == 0) TOUCH(dskip, (unsigned)(d_base * 2)); else TOUCH(ssmD, (unsigned)(cb * 8));
            {
                const int cl = 64 + lane + ((idx & 15) * 2 + vw) * 7;
                if (lane < 7 && cl < CODE_LINES) CODE_TOUCH(cl);
            }
            GATE(); VBAR(11);
            GATE(); VBAR(12);
            GATE(); VBAR(13);
            unsigned pv;
            asm volatile("global_load_dword %0, %1, off sc1" : "=&v"(pv) : "v"(kfp) : "memory");
            KLOAD();
            GATE(); VBAR(14);
            GATE(); VBAR(15);
            asm volatile("s_waitcnt vmcnt(5)" : "+v"(pv) :: "memory");
            const bool done = __all(pv == tag);
            if (done) U_DMA();
            if (DBGON && TIDX == 384) dbg[BIDX * 64 + 13] = (done ? 100ull : 0ull) | (1ull << 62);
            GATE();
            if (lane == 0) SETW(UFLAG_OFF, vw, 1u);
            WAIT_VM(9);
            if (!done) {
                unsigned spins = 0;
                for (;;) {
                    const unsigned v = __hip_atomic_load(kfp, RLX_AGENT);
                    if (__all(v == tag)) break;
                    if (++spins > SPIN_MAX) break;
                    __builtin_amdgcn_s_sleep(1);
                }
                U_DMA();
            }
        }
        KWAIT(18);
        STAMPT(dbg, 14, 320);
        __builtin_amdgcn_s_sleep(6);
        {
            const unsigned cp = (unsigned)(KC_OFF + lw * CV_W + 64 + lane * 16);
            if (lane == 0) KA[0].x = (KA[0].x & 0xffff0000u) | f2bf(lo16(KA[0].x) + bf2f((u16)draw));
#define WR128(v, off) asm volatile("ds_write_b128 %0, %1 offset:%2" :: "v"(cp), "v"(v), "i"(off) : "memory")
#pragma unroll
            for (int q = 3; q >= 0; --q) WR128(KA[q], q * 1024);
#pragma unroll
            for (int q = 3; q >= 0; --q) {
                const u32x2 p = q ? pred2(KA[q], RL63(KA[q ? q - 1 : 0].z), RL63(KA[q ? q - 1 : 0].w)) : pred2(KA[0], 0u, 0u);
                const u32x4 a = KA[q];
                const u32x4 s1 = {al(a.x, p.y), al(a.y, a.x), al(a.z, a.y), al(a.w, a.z)};
                WR128(s1, q * 1024 + CV_COPY);
                const u32x4 s2 = {p.y, mv(a.x), mv(a.y), mv(a.z)};
                WR128(s2, q * 1024 + 2 * CV_COPY);
                const u32x4 s3 = {al(p.y, p.x), mv(s1.x), mv(s1.y), mv(s1.z)};
                WR128(s3, q * 1024 + 3 * CV_COPY);
            }
            if (lane < 16) *(u32x4*)(smem + KC_OFF + lw * CV_W + (lane >> 2) * CV_COPY + (lane & 3) * 16) = (u32x4){0u, 0u, 0u, 0u};
            SCHED();
        }
        STAMPT(dbg, 26, 320);
        wait_lds4(smem, G1D_OFF);
        wait_lds4(smem, UFLAG_OFF);
        STAMPT(dbg, 9, 256);
        __builtin_amdgcn_s_setprio(PRIO_CV);
        STAMPT(dbg, 10, 384);
        f32x4 cv[8];
#pragma unroll
        for (int g = 0; g < 8; ++g) cv[g] = (f32x4){0.f, 0.f, 0.f, 0.f};
        PIN4(cv[0], cv[1], cv[2], cv[3]); PIN4(cv[4], cv[5], cv[6], cv[7]);
        const int uz = U_OFF + 192;
        const int ua0 = U_OFF + 256 + (2 * l15 + q4 - 2) * 64, rv0 = (2 * l15 + q4 - 2) * 4 + lw * 16;
        const unsigned ka0 = (unsigned)((l15 >> 2) * CV_COPY - 16 * q4 + 8 * (l15 & 3) - 8);
        int ua, rv, pu, pz;
        unsigned adr;
#define UNEXT1() do { pu = ua | (rv & 0x30); asm volatile("" : "+v"(pu)); } while (0)
#define UNEXT2() do { pz = (ua > uz ? ua : uz) | (rv & 0x30); asm volatile("" : "+v"(pz)); } while (0)
#define UNEXT3() do { ua -= 256; rv -= 16; asm volatile("" : "+v"(ua), "+v"(rv)); } while (0)
#define ANEXT() do { adr += 64; asm volatile("" : "+v"(adr)); } while (0)
#define RDU(dst, c) do { dst = (c) ? RD128(pu, (c) * 2048) : RD128(pz, 0); SCHED(); } while (0)
        HPOLL_SETUP();
        unsigned hpv = 0;
        {
            bf16x8 A[8], UB[8];
            ua = ua0; rv = rv0; adr = lbase + KC_OFF + lw * CV_W + ka0 + 96;
            asm volatile("" : "+v"(adr));
            UNEXT1(); UNEXT2(); UNEXT3();
#pragma unroll
            for (int i = 0; i < 8; ++i) { RDA(A[7 - i], adr, 7 - i); RDU(UB[i], i); }
            unsigned gb = lw >= 2 ? (unsigned)((tb * 16 + nb) * 128 + (lw - 2) * 32) : 0x80000000u;
            asm volatile("" : "+s"(gb));
            const unsigned gso = (l15 == 0) ? gb + (unsigned)(q4 * 8) : 0x80000000u;
            float gt = 0.f;
            unsigned vxo = 0, vdo = 0, vgo = 0;
            auto gate_out = [&](int i) __attribute__((always_inline)) {
                if (i == 0) gacc += __uint_as_float(__builtin_amdgcn_update_dpp(0u, __float_as_uint(gacc), 0xb1, 0xf, 0xf, false));
                if (i == 1) gacc += __uint_as_float(__builtin_amdgcn_update_dpp(0u, __float_as_uint(gacc), 0x4e, 0xf, 0xf, false));
                if (i == 2) gacc += __uint_as_float(__builtin_amdgcn_update_dpp(0u, __float_as_uint(gacc), 0x141, 0xf, 0xf, false));
                if (i == 3) gacc += __uint_as_float(__builtin_amdgcn_update_dpp(0u, __float_as_uint(gacc), 0x140, 0xf, 0xf, false));
                if (i == 4) gacc = rbf(gacc);
                if (i == 5) gacc = __builtin_amdgcn_exp2f(gacc * -1.44269504088896f);
                if (i == 6) gacc = __builtin_amdgcn_rcpf(1.0f + gacc);
                if (i == 7) gacc = rbf(gacc);
                if (i == 8) gt = __uint_as_float(pkbf(gacc, 1.0f - gacc));
                if (i == 9) __builtin_amdgcn_raw_buffer_store_b64((u32x2){__float_as_uint(gt), tag}, gs, gso, 0, WT);
                asm volatile("" : "+v"(gacc), "+v"(gt));
            };
            auto step = [&](auto E) __attribute__((always_inline)) {
                constexpr int e = decltype(E)::value;
                constexpr int ti = (e == CS0) ? 0 : (e == CS1) ? 1 : (e == CS2) ? 2 : -1, tj = ti < 0 ? 0 : ti;
                constexpr unsigned char GA[36] = {0x66, 0x77, 0x76, 0x44, 0x55, 0x54, 0x65, 0x64, 0x75, 0x74, 0x22, 0x33, 0x32, 0x43, 0x42, 0x53, 0x52, 0x63, 0x62, 0x73, 0x72,
                                                  0x00, 0x11, 0x10, 0x21, 0x20, 0x31, 0x30, 0x41, 0x40, 0x51, 0x50, 0x61, 0x60, 0x71, 0x70};
                const char* sb_ = (const char*)Cre + COFF(wid, tj + 1);
                const char* sc_ = (const char*)Cim + COFF(wid, tj + 1);
#pragma unroll
                for (int q = 0; q < 36; ++q) {
                    const int g = GA[q] >> 4, a = GA[q] & 15;
                    MF(cv[g], A[a], UB[g - a]); SCHED();
                    if (e < 7) {
                        if (a == 1) RDU(UB[g - 1], g - 1);
                        if (q == 35) RDU(UB[7], 7);
                        if (g == 7) RDA(A[a], adr + 64 * (e + 1), a);
                        if (q == 11) UNEXT1();
                        if (q == 13) UNEXT2();
                        if (q == 15) UNEXT3();
                    }
                    if (ti >= 0) {
                        const int n = (q >= 3 && q <= 7) ? q - 3 : q == 10 ? 5 : q == 12 ? 6 : q == 14 ? 7 : -1;
                        if (q == 3) { if (ti == 0) WAIT_VM(1); else if (e == XE + 1) WAIT_VM(7); else WAIT_VM(0); }
                        if (n >= 0) CRD(tj, n & 7);
                        if (q == 18) CPIN(tj);
                        if (q == 18 || q == 23) asm volatile("s_add_u32 m0, %3, %4\n\ts_nop 0\n\tglobal_load_lds_dwordx4 %0, %2\n\tglobal_load_lds_dwordx4 %1, %2 offset:1024"
                                                             :: "v"(vB0), "v"(vB1), "s"(q == 18 ? sb_ : sc_), "s"(cst0), "i"(q == 18 ? 0 : 4096) : "memory", "scc");
                        if (q == 21 || q == 25) asm volatile("global_load_lds_dwordx4 %0, %2 offset:2048\n\tglobal_load_lds_dwordx4 %1, %2 offset:3072"
                                                             :: "v"(vB0), "v"(vB1), "s"(q == 21 ? sb_ : sc_) : "memory");
                    }
                    if (e == 0 && q >= 3 && q <= 7) gate_out(q - 3);
                    if (e == 0 && q >= 10 && q <= 18 && !(q & 1)) gate_out(5 + (q - 10) / 2);
                    if (e == 0 && q == 4) CTILE(wid, 0, cst0);
                    if (e == XE && q >= 23 && q <= 35 && (q & 1)) {
                        const int i = (q - 23) / 2;
                        const __amdgpu_buffer_rsrc_t xs = __builtin_amdgcn_make_buffer_rsrc((void*)x, 0, SEQ * DM * 2, 0x00020000);
                        const __amdgpu_buffer_rsrc_t ks = __builtin_amdgcn_make_buffer_rsrc((void*)dskip, 0, DM * 2, 0x00020000);
                        if (i < 4) asm volatile("buffer_load_dwordx2 %0, %1, %2, %3 offen offset:%4" : "=&v"(xx[(i >> 1) & 1][i & 1]) : "v"(vxo), "s"(xs), "s"((TR((i >> 1) & 1) * DM + d_base) * 2), "i"((i & 1) * 2048) : "memory");
                        if (i == 4) asm volatile("buffer_load_dwordx2 %0, %1, %2, %3 offen" : "=&v"(dk2) : "v"(vdo), "s"(ks), "s"(d_base * 2) : "memory");
                        if (i >= 5) asm volatile("buffer_load_dwordx4 %0, %1, %2, %3 offen sc1" : "=&v"(gv[(i - 5) & 1]) : "v"(vgo), "s"(gs), "s"(GSO((i - 5) & 1)) : "memory");
                    }
                    if (e == XE && q == 16) { vxo = (unsigned)(q4 * 32 * DM * 2 + l15 * 8); vdo = (unsigned)(l15 * 8); asm volatile("" : "+v"(vxo), "+v"(vdo)); }
                    if (e == XE && q == 17) { vgo = GV(q4); asm volatile("" : "+v"(vgo)); }
                    if (e == 6 && q == 30) hpv = __hip_atomic_load(hfp, RLX_AGENT);
                    SCHED();
                }
            };
            unroll<8>(step);
            WAIT_VM(0);
            XPIN();
        }
        PAD8(cv[0], cv[1], cv[2], cv[3]); PIN4(cv[4], cv[5], cv[6], cv[7]);
        STAMPT(dbg, 11, 384);
        const unsigned vh = (unsigned)((lane ^ 2) * 16);
        if (!__all(hpv == tag)) HWAIT();
        asm volatile("" ::: "memory");
#pragma unroll
        for (int f = 0; f < DEPTH; ++f) LOADF(f);
        STAMPT(dbg, 25, 384);
        {
            const __amdgpu_buffer_rsrc_t yrs = __builtin_amdgcn_make_buffer_rsrc(y2T, 0, DM * SEQ * 2, 0x00020000);
            const unsigned yo = (unsigned)(d * 4096 + (q4 & 1) * 512 + (l15 >> 3) * 256 + (q4 >> 1) * 128 + (l15 & 7) * 16);
#pragma unroll
            for (int g = 0; g < 8; g += 2) {
                const auto r0 = __builtin_amdgcn_permlane16_swap(pkbf(cv[g][0], cv[g][1]), pkbf(cv[g + 1][0], cv[g + 1][1]), false, false);
                const auto r1 = __builtin_amdgcn_permlane16_swap(pkbf(cv[g][2], cv[g][3]), pkbf(cv[g + 1][2], cv[g + 1][3]), false, false);
                __builtin_amdgcn_raw_buffer_store_b128((u32x4){r0[0], r1[0], r0[1], r1[1]}, yrs, yo + (unsigned)(g * 512), 0, WT);
            }
        }
        asm volatile("" ::: "memory");
        STAMPT(dbg, 15, 384);
        __builtin_amdgcn_s_setprio(PRIO2_L);
    }
    {
        const int lane = fresh_lane(), l15 = lane & 15, q4 = lane >> 4;
        const int ring = wid >> 2;
        const unsigned va = (unsigned)((lane ^ (ring * 2)) * 16), vh = va;
        const unsigned tag = lds_word(smem, TAG_OFF);
        const int cb0 = ring ? KC_OFF : GA_OFF, cst = ring ? CV_W : 8192;
        f32x4 ac[2][4];
        unsigned ca[2], fa[2], fl = 0, fv = 0;
#define CHAIN(u) do { const int c_ = (wid - ((u) >> 1)) & 3; ca[(u) & 1] = va + (unsigned)(cb0 + c_ * cst + ((u) & 1) * 4096); \
                      fa[(u) & 1] = (unsigned)(RF_OFF + ring * 32 + ((u) & 1) * 16 + c_ * 4); asm volatile("" : "+v"(ca[(u) & 1]), "+v"(fa[(u) & 1])); } while (0)
#define RDIN(dst, a, f_) asm volatile("ds_read_b32 %0, %5\n\tds_read_b128 %1, %6\n\tds_read_b128 %2, %6 offset:1024\n\tds_read_b128 %3, %6 offset:2048\n\tds_read_b128 %4, %6 offset:3072" \
                     : "=&v"(fl), "=&v"(dst[0]), "=&v"(dst[1]), "=&v"(dst[2]), "=&v"(dst[3]) : "v"(f_), "v"(a) : "memory")
#define LWAIT(dst) asm volatile("s_waitcnt lgkmcnt(0)" : "+v"(fl), "+v"(dst[0]), "+v"(dst[1]), "+v"(dst[2]), "+v"(dst[3]) :: "memory")
        CHAIN(0); CHAIN(1);
        WAIT_LGKM0();
        CPIN(0); CPIN(1); CPIN(2);
        CREAD(3);
        int tr[2] = {TR(0), TR(1)};
        unsigned gw[2][2];
        {
            unsigned spins = 0;
            while (__builtin_expect(!__all((gv[0].y == tag) & (gv[0].w == tag) & (gv[1].y == tag) & (gv[1].w == tag)), 0)) {
                if (++spins > SPIN_MAX) break;
#pragma unroll
                for (int r = 0; r < 2; ++r) asm volatile("s_nop 4\n\tbuffer_load_dwordx4 %0, %1, %2, %3 offen sc1\n\ts_waitcnt vmcnt(0)" : "=&v"(gv[r]) : "v"(GV(q4)), "s"(gs), "s"(__builtin_amdgcn_readfirstlane(GSO(r))) : "memory");
            }
            gw[0][0] = gv[0].x; gw[0][1] = gv[0].z; gw[1][0] = gv[1].x; gw[1][1] = gv[1].z;
            PIN4(gw[0][0], gw[0][1], gw[1][0], gw[1][1]);
        }
        unsigned cpv = 0, yy[2][4];
        f32x2 zz[2][4];
        unsigned exo = 0, exi = 0, efo = 0, efi = 0;
        auto send = [&](int r) __attribute__((always_inline)) {
            const unsigned one = 1u;
#pragma unroll
            for (int n = 0; n < 4; ++n) { const f32x2 v = {ac[r][n][2], ac[r][n][3]}; asm volatile("ds_write_b64 %0, %1 offset:%2" :: "v"(exo), "v"(v), "i"(r * 4096 + n * 512) : "memory"); }
            asm volatile("ds_write_b32 %0, %1 offset:%2" :: "v"(efo), "v"(one), "i"(r * 4) : "memory");
        };
        STAMPT(dbg, 5, 0);
        auto body = [&](auto F) __attribute__((always_inline)) {
            constexpr int f = decltype(F)::value;
            constexpr int u = f >> 3, k = f & 7;
            f32x4* an = ac[u & 1];
            f32x4* ao = ac[(u + 1) & 1];
#pragma unroll
            for (int n = 0; n < 4; ++n) {
                if (k == 0 && u < 2) MF0(an[n], af[f % DEPTH], cf_[k][n]); else MF(an[n], af[f % DEPTH], cf_[k][n]);
                SCHED();
                const int g = k * 4 + n;
                if (u >= 1 && u < 7) {
                    if (g == 1) { fv = ((u - 1) >> 1) + 1; asm volatile("" : "+v"(fv)); }
                    if (g == 4 || g == 6 || g == 8 || g == 10) asm volatile("ds_write_b128 %0, %1 offset:%2" :: "v"(ca[(u + 1) & 1]), "v"(ao[(g - 4) / 2]), "i"((g - 4) * 512) : "memory");
                    if (g == 12) asm volatile("ds_write_b32 %0, %1" :: "v"(fa[(u + 1) & 1]), "v"(fv) : "memory");
                    if (g == 18) CHAIN(u + 1);
                    if (g == 20) asm volatile("ds_read_b32 %0, %1" : "=&v"(fl) : "v"(fa[(u + 1) & 1]) : "memory");
                    if (g > 20 && g <= 24) asm volatile("ds_read_b128 %0, %1 offset:%2" : "=&v"(ao[g - 21]) : "v"(ca[(u + 1) & 1]), "i"((g - 21) * 1024) : "memory");
                    if (g == 30) {
                        LWAIT(ao);
                        if (__builtin_expect(__builtin_amdgcn_readfirstlane(fl) != (unsigned)((u + 1) >> 1), 0)) {
                            unsigned spins = 0;
#pragma unroll 1
                            do {
                                __builtin_amdgcn_s_sleep(1);
                                RDIN(ao, ca[(u + 1) & 1], fa[(u + 1) & 1]); LWAIT(ao);
                                if (++spins > SPIN_MAX * 16u) break;
                            } while (__builtin_amdgcn_readfirstlane(fl) != (unsigned)((u + 1) >> 1));
                        }
                    }
                }
                if (u == 7 && g == 6) send(0);
                if (f >= ZF && f < ZF + 4) {
                    constexpr int r = ((f - ZF) >> 1) & 1, j = (f - ZF) & 1;
                    const unsigned xw = xx[r][j][n >> 1], dw = dk2[n >> 1];
                    const float xf = (n & 1) ? hi16(xw) : lo16(xw), df = (n & 1) ? hi16(dw) : lo16(dw), yf = j ? hi16(yy[r][n]) : lo16(yy[r][n]);
                    zz[r][n][j] = xf * fmaf(hi16(gw[r][j]), yf, lo16(gw[r][j]) * df);
                    asm volatile("" : "+v"(zz[r][n]));
                }
                if (f == ZF + 4) {
                    zz[0][n] = (f32x2){fmaf(lo16(gw[0][0]), ac[0][n][0], zz[0][n][0]), fmaf(lo16(gw[0][1]), ac[0][n][1], zz[0][n][1])};
                    asm volatile("" : "+v"(zz[0][n]));
                }
                SCHED();
            }
            if (f + DEPTH < 64) LOADF(f + DEPTH);
            if (f == DEPTH + 1 && ring) {
                WAIT_VM(6);
                if (lane == 0) __hip_atomic_store((gu32*)(ws + WS_CFLAG) + cb * 32 + (wid & 3), tag, RLX_AGENT);
                SCHED();
            }
            if (f == YPOLL && ring) { const int l15 = fresh_lane() & 15; cpv = __hip_atomic_load((gu32*)(ws + WS_CFLAG) + (nb * 16 + (wid & 3) * 4 + (l15 >> 2)) * 32 + (l15 & 3), RLX_AGENT); SCHED(); }
            if (f == YPOLL + DEPTH && ring) {
                const int ln = fresh_lane(), l15 = ln & 15, q4 = ln >> 4;
                if (__builtin_expect(!__all(cpv == tag), 0)) {
                    unsigned spins = 0;
                    while (!__all(__hip_atomic_load((gu32*)(ws + WS_CFLAG) + (nb * 16 + (wid & 3) * 4 + (l15 >> 2)) * 32 + (l15 & 3), RLX_AGENT) == tag)) {
                        if (++spins > SPIN_MAX) break;
                        __builtin_amdgcn_s_sleep(1);
                    }
                }
                const unsigned lx = lbase + LA_OFF + (wid & 3) * 8192;
#pragma unroll
                for (int i = 0; i < 4; ++i) {
                    const unsigned vo = (unsigned)((d_base + (wid & 3) * 16 + i * 4 + q4) * 4096 + tb * 256 + ((l15 ^ ((wid & 3) * 4 + i)) * 16));
                    GLDS_SC1(y2T, vo, lx, i * 1024);
                }
                SCHED();
            }
            if (f == YPOLL + 2 * DEPTH + 1 && ring) {
                WAIT_VM(6);
                SETW(YFLAG_OFF, wid & 3, 1u);
                SCHED();
            }
            if (f == 55) {
                const int ln = fresh_lane(), c_ = (wid - 3) & 3;
                const int eg = GA_OFF + c_ * 8192, el = KC_OFF + c_ * CV_W;
                exo = (unsigned)((ring ? el : eg) + ln * 8);
                exi = (unsigned)((ring ? eg : el) + ln * 8);
                efo = (unsigned)(EF_OFF + wid * 8); efi = (unsigned)(EF_OFF + (wid ^ 4) * 8);
                PIN4(exo, exi, efo, efi);
                SCHED();
            }
            if (f == ZF - 2) {
                const int ln = fresh_lane(), l15 = ln & 15, q4 = ln >> 4;
                wait_lds4(smem, YFLAG_OFF);
#pragma unroll
                for (int r = 0; r < 2; ++r) {
                    const int jb = JB(6 + r);
                    const int ya = LA_OFF + (l15 >> 2) * 8192 + (l15 & 3) * 1024 + (((((jb >> 1) & 1) * 8 + (jb >> 2) + q4 * 2) ^ l15) * 16) + (jb & 1) * 8 + ring * 4;
#pragma unroll
                    for (int e = 0; e < 4; ++e) yy[r][e] = *(const unsigned*)(smem + ya + e * 256);
                }
                SCHED();
            }
        };
        unroll<64>(body);
        PAD8(ac[1][0], ac[1][1], ac[1][2], ac[1][3]);
        send(1);
        STAMPT(dbg, 6, 0);
        const __amdgpu_buffer_rsrc_t ors = __builtin_amdgcn_make_buffer_rsrc(out, 0, SEQ * DM * 4, 0x00020000);
        const unsigned ov = (unsigned)((lane >> 4) * (32 * DM * 4) + (lane & 15) * 16);
#pragma unroll
        for (int r = 0; r < 2; ++r) {
            f32x2 in[4];
#define EXRD() asm volatile("ds_read_b32 %0, %5 offset:%7\n\tds_read_b64 %1, %6 offset:%8\n\tds_read_b64 %2, %6 offset:%9\n\tds_read_b64 %3, %6 offset:%10\n\tds_read_b64 %4, %6 offset:%11" \
                             : "=&v"(fl), "=&v"(in[0]), "=&v"(in[1]), "=&v"(in[2]), "=&v"(in[3]) \
                             : "v"(efi), "v"(exi), "i"(r * 4), "i"(r * 4096), "i"(r * 4096 + 512), "i"(r * 4096 + 1024), "i"(r * 4096 + 1536) : "memory")
            EXRD();
            if (r == 0) {
#pragma unroll
                for (int n = 0; n < 4; ++n) {
                    zz[1][n] = (f32x2){fmaf(lo16(gw[1][0]), ac[1][n][0], zz[1][n][0]), fmaf(lo16(gw[1][1]), ac[1][n][1], zz[1][n][1])};
                    asm volatile("" : "+v"(zz[1][n]));
                }
            }
            LWAIT(in);
            if (__builtin_expect(__builtin_amdgcn_readfirstlane(fl) == 0u, 0)) {
                unsigned spins = 0;
#pragma unroll 1
                do {
                    EXRD(); LWAIT(in);
                    if (++spins > SPIN_MAX * 16u) break;
                } while (__builtin_amdgcn_readfirstlane(fl) == 0u);
            }
            f32x4 o[2];
#pragma unroll
            for (int j = 0; j < 2; ++j)
#pragma unroll
                for (int e = 0; e < 4; ++e) o[j][e] = fmaf(lo16(gw[r][j]), in[e][j], zz[r][e][j]);
#pragma unroll
            for (int j = 0; j < 2; ++j)
                __builtin_amdgcn_raw_buffer_store_b128(__builtin_bit_cast(u32x4, o[j]), ors, ov + (unsigned)(((tr[r] + j) * DM + d_base) * 4), 0, WT);
        }
        STAMPT(dbg, 7, 0);
        STAMPT(dbg, 16, 384);
        if (tb == 15 && nb == 0 && wid == 0 && lane == 0) __hip_atomic_store((gu32*)(ws + WS_EPOCH), tag, RLX_AGENT);
    }
}

extern "C" void kernel_launch(void* const* d_in, const int* in_sizes, int n_in,
                              void* d_out, int out_size, void* d_ws, size_t ws_size,
                              hipStream_t stream) {
    const u16* x = (const u16*)d_in[0];
    const u16* lre = (const u16*)d_in[1];
    const u16* lim = (const u16*)d_in[2];
    const u16* Bre = (const u16*)d_in[3];
    const u16* Bim = (const u16*)d_in[4];
    const u16* Cre = (const u16*)d_in[5];
    const u16* Cim = (const u16*)d_in[6];
    const u16* logdt = (const u16*)d_in[7];
    const u16* dskip = (const u16*)d_in[8];
    const u16* kin = (const u16*)d_in[10];
    const u16* vin = (const u16*)d_in[11];
    const u16* shiftk = (const u16*)d_in[12];
    const u16* ssmk = (const u16*)d_in[13];
    const u16* ssmD = (const u16*)d_in[14];
    const u16* gatew = (const u16*)d_in[15];
    float* out = (float*)d_out;

    char* ws = (char*)d_ws;

    hipFuncSetAttribute((const void*)k1_kernel, hipFuncAttributeMaxDynamicSharedMemorySize, K1_LDS);

    k1_kernel<<<256, 512, K1_LDS, stream>>>(x, Bre, Bim, kin, vin, shiftk, gatew, ws, lre, lim, logdt, ssmk, ssmD, Cre, Cim, dskip, out);
}
#if defined(__HIP_DEVICE_COMPILE__)
#pragma clang attribute pop
#endif
```

```cpp
#define TP 10
#define ES 3
#define WU1 24
#define RP 10
#if defined(__HIP_DEVICE_COMPILE__)
#pragma clang attribute push(__attribute__((target("no-packed-fp32-ops"))), apply_to = function)
#endif
#include <hip/hip_runtime.h>
#include <hip/hip_bf16.h>
#include <stdint.h>

#define TIDX ((int)__builtin_amdgcn_workitem_id_x())
#define BIDX ((int)__builtin_amdgcn_workgroup_id_x())
typedef unsigned short u16;
typedef __attribute__((ext_vector_type(8))) short bf16x8;
typedef __attribute__((ext_vector_type(4))) float f32x4;
typedef __attribute__((ext_vector_type(2))) float f32x2;
typedef __attribute__((ext_vector_type(2))) __bf16 bf16x2;
typedef __attribute__((ext_vector_type(4))) unsigned u32x4;
typedef __attribute__((ext_vector_type(2))) unsigned u32x2;
typedef __attribute__((address_space(1))) unsigned long long gu64;
typedef __attribute__((address_space(1))) unsigned gu32;
typedef __attribute__((address_space(3))) void lds_void;
typedef __attribute__((address_space(3))) unsigned lds_u32;
typedef __attribute__((ext_vector_type(4))) short s16x4;
typedef __attribute__((address_space(3))) s16x4 lds_s16x4;
#define TRD(a) __builtin_amdgcn_ds_read_tr16_b64_v4i16((lds_s16x4*)(size_t)(a))

#define DBGON 0
#define STAMPT(buf, k, t) do { } while (0)
#define CODE_LINES 264
#define SEQ 2048
#define DM 1024
#define SPIN_MAX (1u << 18)
#define PRIO_G 2
#define PSLEEP 4
#define PRIO_L 3
#define WT 16

__device__ __forceinline__ float bf2f(u16 v) { return __uint_as_float(((unsigned)v) << 16); }
__device__ __forceinline__ u16 f2bf(float f) { __bf16 b = (__bf16)f; return __builtin_bit_cast(u16, b); }
__device__ __forceinline__ float rbf(float f) { return bf2f(f2bf(f)); }
__device__ __forceinline__ unsigned pkbf(float lo, float hi) { return __builtin_bit_cast(unsigned, __builtin_convertvector((f32x2){lo, hi}, bf16x2)); }
__device__ __forceinline__ float lo16(unsigned u) { return __uint_as_float(u << 16); }
__device__ __forceinline__ float hi16(unsigned u) { return __uint_as_float(u & 0xffff0000u); }

struct cf { float r, i; };
__device__ __forceinline__ cf cmul(cf a, cf b) { return {a.r * b.r - a.i * b.i, a.r * b.i + a.i * b.r}; }
__device__ __forceinline__ cf cfma(cf a, cf b, cf c) {
    return {fmaf(a.r, b.r, fmaf(-a.i, b.i, c.r)), fmaf(a.r, b.i, fmaf(a.i, b.r, c.i))};
}
__device__ __forceinline__ f32x4 splat4(float v) { return (f32x4){v, v, v, v}; }
__device__ __forceinline__ f32x4 fma4(f32x4 a, f32x4 b, f32x4 c) { return __builtin_elementwise_fma(a, b, c); }
__device__ __forceinline__ f32x4 mul4(f32x4 a, f32x4 b) { return a * b; }

__device__ __forceinline__ float red32(float a, float b) {
    const auto r = __builtin_amdgcn_permlane32_swap(__float_as_uint(a), __float_as_uint(b), false, false);
    return __uint_as_float(r[0]) + __uint_as_float(r[1]);
}
__device__ __forceinline__ float red16(float x) {
    const auto r = __builtin_amdgcn_permlane16_swap(__float_as_uint(x), __float_as_uint(x), false, false);
    return __uint_as_float(r[0]) + __uint_as_float(r[1]);
}
__device__ __forceinline__ int fresh_lane() {
    int l;
    asm volatile("v_mbcnt_lo_u32_b32 %0, -1, 0\n\tv_mbcnt_hi_u32_b32 %0, -1, %0" : "=v"(l));
    return l;
}
#define BAR() __builtin_amdgcn_s_barrier()
#define WAIT_VM(n) asm volatile("s_waitcnt vmcnt(" #n ")" ::: "memory")
#define WAIT_LGKM0() __builtin_amdgcn_s_waitcnt(0xC07F)
#define SCHED() __builtin_amdgcn_sched_barrier(0)
#define MF(c, a, b) asm volatile("v_mfma_f32_16x16x32_bf16 %0, %1, %2, %0" : "+v"(c) : "v"(a), "v"(b))
#define PAD8(c0, c1, c2, c3) asm volatile("s_nop 7" : "+v"(c0), "+v"(c1), "+v"(c2), "+v"(c3))
#define PIN4(c0, c1, c2, c3) asm volatile("" : "+v"(c0), "+v"(c1), "+v"(c2), "+v"(c3))
#define GLDS_S(sbase, voff, sl, IMM) \
    asm volatile("s_add_u32 m0, %2, %3\n\ts_nop 0\n\tglobal_load_lds_dwordx4 %0, %1" \
                 :: "v"(voff), "s"(sbase), "s"(sl), "i"(IMM) : "memory", "scc")
#define GLDS_K(sbase, voff, sl, IMM) \
    asm volatile("s_add_u32 m0, %2, %3\n\ts_nop 0\n\tglobal_load_lds_dwordx4 %0, %1 nt" \
                 :: "v"(voff), "s"(sbase), "s"(sl), "i"(IMM) : "memory", "scc")
#define RLX_AGENT __ATOMIC_RELAXED, __HIP_MEMORY_SCOPE_AGENT

#define UNITS_A 0x0756075607560560ull
#define UNITS_B 0x1432143214321432ull
#define RA(t) ((int)((UNITS_A >> (4 * (t))) & 15) * 16384)
#define RB(t) ((int)((UNITS_B >> (4 * (t))) & 15) * 16384)
#define LA_OFF 0
#define GA_OFF 32768
#define CV_COPY 4160
#define CV_W (4 * CV_COPY)
#define KC_OFF 65536
#define ST_OFF 114688
#define ST_V 8832
#define ST_SZ (ST_V + 8 * 1056)
#define U_OFF (KC_OFF + 16 * CV_COPY)
#define HT_OFF (U_OFF + 256 + 16384)
#define GW_OFF (HT_OFF + 8192)
#define TAG_OFF (GW_OFF + 2048)
#define UFLAG_OFF (TAG_OFF + 16)
#define G1D_OFF (UFLAG_OFF + 16)
#define MISC_OFF (G1D_OFF + 16)
#define YFLAG_OFF (MISC_OFF + 16)
#define RF_OFF (YFLAG_OFF + 16)
#define EF_OFF (RF_OFF + 64)
#define COK_OFF (EF_OFF + 64)
#define NFLAGW 52
#define DUMMY_OFF (COK_OFF + 16)
#define K1_LDS (DUMMY_OFF + 256)
#define WS_GRAN 0
#define WS_KVFLAG (256u << 10)
#define WS_EPOCH (WS_KVFLAG + 65536)
#define WS_CFLAG (WS_EPOCH + 4096)
#define WS_HFLAG (384u << 10)
#define WS_GATE (512u << 10)

#define CODE_TOUCH(vl) asm volatile("s_getpc_b64 s[98:99]\n\ts_add_u32 s98, s98, k1_kernel@rel32@lo+4\n\ts_addc_u32 s99, s99, k1_kernel@rel32@hi+12\n\t" \
                                    "s_mov_b32 m0, %1\n\ts_nop 0\n\tglobal_load_lds_dword %0, s[98:99]" :: "v"((unsigned)(vl) * 128u), "i"(DUMMY_OFF) : "s98", "s99", "scc", "memory")
#define RD128(a, off) (*(const __attribute__((address_space(3))) bf16x8*)(size_t)(unsigned)((a) + (off)))
#define RDA(dst, A, F) do { const s16x4 lo_ = TRD((A) + 512 * (F) + 8); SCHED(); const s16x4 hi_ = TRD((A) + 512 * (F)); SCHED(); \
                            dst = __builtin_shufflevector(lo_, hi_, 0, 1, 2, 3, 4, 5, 6, 7); } while (0)
#define TOUCH(sbase, voff) asm volatile("s_mov_b32 m0, %2\n\ts_nop 0\n\tglobal_load_lds_dword %0, %1" :: "v"(voff), "s"(sbase), "i"(DUMMY_OFF) : "memory")
#define FLAGD(off) do { unsigned long long pc_; asm volatile("s_getpc_b64 %0\n\ts_mov_b32 m0, %2\n\ts_mov_b64 exec, 1\n\tglobal_load_lds_dword %1, %0\n\ts_mov_b64 exec, -1" \
                                                             : "=&s"(pc_) : "v"(0u), "s"(__builtin_amdgcn_readfirstlane(off)) : "memory"); } while (0)
#define GLDS_SC1(sbase, voff, sl, IMM) \
    asm volatile("s_add_u32 m0, %2, %3\n\ts_nop 0\n\tglobal_load_lds_dwordx4 %0, %1 sc1" \
                 :: "v"(voff), "s"(sbase), "s"(sl), "i"(IMM) : "memory", "scc")
__device__ __forceinline__ unsigned mv(unsigned v) { unsigned r; asm("v_mov_b32 %0, %1" : "=v"(r) : "v"(v)); return r; }
__device__ __forceinline__ unsigned al(unsigned hi, unsigned lo) { unsigned r; asm volatile("v_alignbit_b32 %0, %1, %2, 16" : "=v"(r) : "v"(hi), "v"(lo)); return r; }
__device__ __forceinline__ u32x2 pred2(const u32x4& a, unsigned f0, unsigned f1) {
    return (u32x2){(unsigned)__builtin_amdgcn_update_dpp((int)f0, (int)a.z, 0x138, 0xf, 0xf, false), (unsigned)__builtin_amdgcn_update_dpp((int)f1, (int)a.w, 0x138, 0xf, 0xf, false)};
}
#define RL63(v) ((unsigned)__builtin_amdgcn_readlane((int)(v), 63))
__device__ __forceinline__ void wait_lds4(char* smem, int off) {
    const unsigned a = (unsigned)(size_t)(lds_void*)smem + off;
    unsigned spins = 0;
    for (;;) {
        u32x4 f;
        asm volatile("ds_read_b128 %0, %1\n\ts_waitcnt lgkmcnt(0)" : "=&v"(f) : "v"(a) : "memory");
        if (__builtin_amdgcn_readfirstlane((f.x != 0u) & (f.y != 0u) & (f.z != 0u) & (f.w != 0u))) break;
        if (++spins > SPIN_MAX * 16u) break;
        __builtin_amdgcn_s_sleep(1);
    }
}
__device__ __forceinline__ unsigned lds_word(char* smem, int off) {
    const unsigned a = (unsigned)(size_t)(lds_void*)smem + off;
    unsigned f;
    asm volatile("ds_read_b32 %0, %1\n\ts_waitcnt lgkmcnt(0)" : "=&v"(f) : "v"(a) : "memory");
    return __builtin_amdgcn_readfirstlane(f);
}
__device__ __forceinline__ void wait_lds1(char* smem, int off) {
    unsigned spins = 0;
    while (lds_word(smem, off) == 0u) {
        if (++spins > SPIN_MAX * 16u) break;
        __builtin_amdgcn_s_sleep(1);
    }
}
#define SETW(off, i, v) __hip_atomic_store((lds_u32*)(smem + (off)) + (i), (v), __ATOMIC_RELAXED, __HIP_MEMORY_SCOPE_WORKGROUP)

#define PRIO_CV 1
#define PRIO_KV 3
#define CS0 1
#define CS1 3
#define CS2 5
#define XE 4
#define YPOLL 32
#define ZF 59
template <int I> struct IC { static constexpr int value = I; };
template <int N, int I = 0, class F> __device__ __forceinline__ void unroll(F& f) { if constexpr (I < N) { f(IC<I>{}); unroll<N, I + 1>(f); } }
#define MF0(c, a, b) asm volatile("v_mfma_f32_16x16x32_bf16 %0, %1, %2, 0" : "=&v"(c) : "v"(a), "v"(b))
#define DEPTH 6
#define PRIO2_G 0
#define PRIO2_L 1
__device__ __forceinline__ cf pick4(int q, cf a1, cf a2, cf a3) {
    return {q == 0 ? 1.f : q == 1 ? a1.r : q == 2 ? a2.r : a3.r, q == 0 ? 0.f : q == 1 ? a1.i : q == 2 ? a2.i : a3.i};
}
#define PINC(c) asm volatile("" : "+v"((c).r), "+v"((c).i))

extern "C" __global__ __launch_bounds__(512, 2) void k1_kernel(
        const u16* __restrict__ x, const u16* __restrict__ Bre, const u16* __restrict__ Bim,
        const u16* __restrict__ kin, const u16* __restrict__ vin, const u16* __restrict__ shiftk, const u16* __restrict__ gatew, char* ws,
        const u16* __restrict__ lre, const u16* __restrict__ lim, const u16* __restrict__ logdt,
        const u16* __restrict__ ssmk, const u16* __restrict__ ssmD,
        const u16* __restrict__ Cre, const u16* __restrict__ Cim, const u16* __restrict__ dskip, float* __restrict__ out) {
    extern __shared__ __attribute__((aligned(16))) char smem[];
#define h ((u16*)(ws + (1u << 20)))
#define kvT ((u16*)(ws + (9u << 20)))
#define y2T ((u16*)(ws + (13u << 20)))
#define dbg ((unsigned long long*)(ws + (64u << 20)))
    const int tid = TIDX;
    if (tid < 40 && tid + ((BIDX >> 3) & 7) * 40 < CODE_LINES) CODE_TOUCH(tid + ((BIDX >> 3) & 7) * 40);
    const unsigned t_start = (unsigned)__builtin_amdgcn_s_memrealtime();
    const int lane = tid & 63;
    int wid = __builtin_amdgcn_readfirstlane(tid >> 6);
    const int b = BIDX;
    const int xcd = b & 7, idx = b >> 3;
    const int tb = ((xcd >> 1) ^ (xcd >> 2)) * 4 + (idx & 3), nb = (xcd & 1) * 8 + (idx >> 2);
    const int cb = xcd * 32 + idx;
    const int t_base = tb * 128, p_base = nb * 64, d_base = nb * 64;
    const int l15 = lane & 15, q4 = lane >> 4;
    unsigned stv = 0, sth = 0;
    STAMPT(dbg, 0, 0);
    const unsigned lbase = (unsigned)(size_t)(lds_void*)smem;
    const int rot1 = (xcd >> 1) * 4 + (xcd & 1) * 8 + (idx >> 2) + (idx & 1);
    unsigned voA[4] = {0u, 0u, 0u, 0u}, voB[4] = {0u, 0u, 0u, 0u}, lw0 = 0;
    if (wid == 4 || wid == 5) {
        __builtin_amdgcn_s_setprio(PRIO_L);
        const int lh = wid - 4;
        const unsigned ch = (unsigned)(((lane & 7) ^ (lane >> 4)) * 16);
        voA[0] = (unsigned)(t_base + lh * 16 + (lane >> 5) * 32 + ((lane >> 3) & 3)) * 2048u + ch;
        voA[1] = (voA[0] ^ 64u) + (131072u - 1024u);
        voA[2] = voA[0] + 6144u; voA[3] = voA[1] + 6144u;
        voB[0] = (unsigned)(p_base + lh * 32 + (lane >> 3)) * 2048u + ch;
        voB[1] = (voB[0] ^ 64u) + (16384u - 1024u);
        voB[2] = voB[0] + 30720u; voB[3] = voB[1] + 30720u;
        PIN4(voA[0], voA[1], voA[2], voA[3]); PIN4(voB[0], voB[1], voB[2], voB[3]);
#define GLDS4(sbase, v0, v1, v2, v3, sl, IMM) \
            asm volatile("s_add_u32 m0, %5, %6\n\ts_nop 0\n\tglobal_load_lds_dwordx4 %0, %4\n\tglobal_load_lds_dwordx4 %1, %4 offset:1024\n\t" \
                         "global_load_lds_dwordx4 %2, %4 offset:2048\n\tglobal_load_lds_dwordx4 %3, %4 offset:3072" \
                         :: "v"(v0), "v"(v1), "v"(v2), "v"(v3), "s"(sbase), "s"(sl), "i"(IMM) : "memory", "scc")
#define STAGE_A(kt, sl) do { const char* sa_ = (const char*)x + (((kt) + rot1) & 15) * 128; \
            GLDS4(sa_, voA[0], voA[1], voA[2], voA[3], sl, 0); GLDS4(sa_ + 16384, voA[0], voA[1], voA[2], voA[3], sl, 4096); } while (0)
#define STAGE_B(kt, sl) do { const int kr_ = (((kt) + rot1) & 15) * 128; const char* sb_ = (const char*)Bre + kr_; const char* sc_ = (const char*)Bim + kr_; \
            GLDS4(sb_, voB[0], voB[1], voB[2], voB[3], sl, 0); GLDS4(sc_, voB[0], voB[1], voB[2], voB[3], sl, 4096); } while (0)
        lw0 = lbase + lh * 8192;
#define HU 1
#define HW3 0
#define HW4 0
#define HW2 0
#define HV 0
#define HVP 2
#define HW 48
#define HWARM(a, b, HW) do { if (HW) { f32x4 w_; u32x4 p_ = {a[0], a[1], b[2], a[3]}, q_ = {b[0] * 40503u, b[1] * 2654435761u, a[2] * 3266489917u, b[3] * 2246822519u}; \
            p_ = ((p_ ^ q_.yzwx) & 0xbfffbfffu) | 0x3c003c00u; q_ = (q_ & 0xbfffbfffu) | 0x3c003c00u; asm volatile("s_nop 1" : "+v"(p_), "+v"(q_)); MF0(w_, p_, q_); \
            _Pragma("unroll 1") for (int i_ = 0; i_ < HW / (4 * HU); ++i_) { _Pragma("unroll") for (int j_ = 0; j_ < HU; ++j_) { MF(w_, q_, p_); MF(w_, p_, p_); MF(w_, q_, q_); MF(w_, p_, q_); } } asm volatile("s_nop 7" : "+v"(w_)); } } while (0)
        STAGE_A(0, lw0 + RA(0)); STAGE_B(0, lw0 + RB(0));
        HWARM(voA, voB, HW);
        STAGE_A(1, lw0 + RA(1));
        HWARM(voA, voB, HW2);
        STAGE_B(1, lw0 + RB(1));
    }
    const int sblk = b >> 3, dblk = b & 7;
    u32x2 tw[4];
    unsigned epoch;
    asm volatile("" : "=v"(tw[0]), "=v"(tw[1]), "=v"(tw[2]), "=v"(tw[3]), "=v"(epoch));
    if (wid >= 6) {
        __builtin_amdgcn_s_setprio(PRIO_L);
        const int vw = wid - 6;
        const int dch = dblk * 128 + vw * 64 + l15;
        const unsigned lk = lbase + ST_OFF + vw * ST_SZ;
        const unsigned vk = (unsigned)((lane >> 3) * DM * 2 + (((lane & 7) ^ ((lane >> 4) & 1)) * 16));
        const char* kb = (const char*)kin + (size_t)(sblk * 64 - 4) * (DM * 2) + dblk * 256 + vw * 128;
#define KLD(n) GLDS_K(kb + (n) * 8 * DM * 2, vk, lk, (n) * 1024 + ((n) >> 1) * 32)
#define VLD(n) GLDS_K(vb + (((n) >> 1) * 16 + ((n) & 1) * 2) * DM * 2, vv_, lk, ST_V + (n) * 1056)
        if (vw == 1) {
            const unsigned vg = (unsigned)((lane * 16 + rot1 * 128) & 2047), lg = lbase + GW_OFF;
            GLDS_S(gatew, vg, lg, 0); GLDS_S(gatew, vg ^ 1024u, lg, 1024);
        }
        asm volatile("global_load_dwordx2 %0, %5, %7\n\tglobal_load_dwordx2 %1, %5, %7 offset:128\n\tglobal_load_dwordx2 %2, %5, %7 offset:256\n\t"
                     "global_load_dwordx2 %3, %5, %7 offset:384\n\tglobal_load_dword %4, %6, %8 sc1"
                     : "+v"(tw[0]), "+v"(tw[1]), "+v"(tw[2]), "+v"(tw[3]), "+v"(epoch) : "v"((unsigned)(dch * 8)), "v"((unsigned)WS_EPOCH), "s"(shiftk), "s"(ws) : "memory");
        if (sblk == 0 && lane < 32) *(u32x4*)(smem + ST_OFF + vw * ST_SZ + lane * 16) = (u32x4){0u, 0u, 0u, 0u};
        if (sblk != 0 || lane >= 32) KLD(0);
        unsigned hv_[4] = {vk, vk + dch, (unsigned)lane, (unsigned)dch};
        KLD(1); KLD(2);
        if (HVP == 2) HWARM(hv_, hv_, HV);
        KLD(3); KLD(4); KLD(5); KLD(6); KLD(7);
        if (lane < 32) KLD(8);
        if (HVP == 8) HWARM(hv_, hv_, HV);
        __builtin_amdgcn_s_setprio(0);
    }
    asm volatile("" : "+s"(wid));

#define WSL(w) ((((w) >> 2) ^ (nb >> 3)) * 4 + ((w) & 3))
#define COFF(w, ti) ((2 * WSL(w) + ((ti) >> 1)) * 128 + ((ti) & 1) * 4096)
    bf16x8 cf_[8][4];
    bf16x8 af[DEPTH];
    int cra, crb;
#define CT_SETUP() const unsigned vB0 = (unsigned)((d_base + (lane >> 3) * 4) * 2048 + (((lane & 7) ^ (lane >> 4)) * 16)), vB1 = (vB0 ^ 64u) + 65536u - 1024u
#define CTILE(w, ti, dst) do { const int co_ = COFF(w, ti); const unsigned ds_ = (dst); \
                               GLDS4((const char*)Cre + co_, vB0, vB1, vB0, vB1, ds_, 0); GLDS4((const char*)Cim + co_, vB0, vB1, vB0, vB1, ds_, 4096); } while (0)
#define CF(ti, n) cf_[4 * ((ti) >> 1) + ((n) >> 1)][2 * ((ti) & 1) + ((n) & 1)]
#define CRD(ti, n) CF(ti, n) = RD128((((n) & 4) ? crb : cra) ^ (((n) & 2) * 16), ((n) & 1) * 2048)
#define CREAD(ti) do { _Pragma("unroll") for (int n_ = 0; n_ < 8; ++n_) CRD(ti, n_); } while (0)
#define CPIN(ti) do { PIN4(CF(ti, 0), CF(ti, 1), CF(ti, 2), CF(ti, 3)); PIN4(CF(ti, 4), CF(ti, 5), CF(ti, 6), CF(ti, 7)); } while (0)
#define CRA(base) ((base) + (q4 >> 1) * 4096 + l15 * 128 + (((q4 & 1) ^ ((l15 >> 1) & 7)) * 16))
    const int rot2 = (idx & 16) >> 2;
#define JB(u) (((((u) ^ (rot2 >> 2)) & 1) << 2) | ((wid - ((u) >> 1) + rot2) & 3))
    const __amdgpu_buffer_rsrc_t hs = __builtin_amdgcn_make_buffer_rsrc((char*)h + (size_t)tb * 524288 + WSL(wid) * 8192, 0, 524288, 0x00020000);
#define LOADF(f) do { af[(f) % DEPTH] = __builtin_bit_cast(bf16x8, __builtin_amdgcn_raw_buffer_load_b128(hs, vh + ((f) & 3) * 1024, JB((f) >> 3) * 65536 + ((f) & 4) * 1024, 16)); SCHED(); } while (0)
#define HPOLL_SETUP() gu32* hfp = (gu32*)(ws + WS_HFLAG) + (tb * 16 + 2 * WSL(wid) + (lane & 1)) * 32
#define HWAIT() do { unsigned spins = 0; \
            while (!__all(__hip_atomic_load(hfp, RLX_AGENT) == tag)) { if (++spins > SPIN_MAX) break; __builtin_amdgcn_s_sleep(PSLEEP); } asm volatile("" ::: "memory"); } while (0)

    const __amdgpu_buffer_rsrc_t gs = __builtin_amdgcn_make_buffer_rsrc(ws + WS_GATE, 0, 32768, 0x00020000);
#define GV(q4) ((unsigned)(((q4) >> 1) * 128 + ((q4) & 1) * 32))
#define GSO(r) (tb * 2048 + JB(6 + (r)) * 256 + (wid >> 2) * 16)
#define TR(r) (t_base + JB(6 + (r)) * 4 + (wid >> 2) * 2)
    u32x2 xx[2][2], dk2;
    u32x4 gv[2];
#define XLOAD() do { \
        const __amdgpu_buffer_rsrc_t xs = __builtin_amdgcn_make_buffer_rsrc((void*)x, 0, SEQ * DM * 2, 0x00020000); \
        const __amdgpu_buffer_rsrc_t ks = __builtin_amdgcn_make_buffer_rsrc((void*)dskip, 0, DM * 2, 0x00020000); \
        dk2 = __builtin_amdgcn_raw_buffer_load_b64(ks, (unsigned)(l15 * 8), d_base * 2, 0); \
        _Pragma("unroll") for (int q = 0; q < 4; ++q) \
            xx[q >> 1][q & 1] = __builtin_amdgcn_raw_buffer_load_b64(xs, (unsigned)(q4 * 32 * DM * 2 + l15 * 8 + (q & 1) * DM * 2), (TR(q >> 1) * DM + d_base) * 2, 0); \
        _Pragma("unroll") for (int r = 0; r < 2; ++r) gv[r] = __builtin_amdgcn_raw_buffer_load_b128(gs, GV(q4), GSO(r), 16); \
        SCHED(); } while (0)
#define XPIN() do { PIN4(xx[0][0], xx[0][1], xx[1][0], xx[1][1]); asm volatile("" : "+v"(dk2), "+v"(gv[0]), "+v"(gv[1])); } while (0)

    if (wid < 4) {
        typedef __attribute__((ext_vector_type(8))) unsigned u32x8;
        typedef const __attribute__((address_space(4))) u32x8 cu8;
        const int po = (p_base + wid * 16) * 2;
        u32x8 s0 = *(cu8*)((size_t)logdt + po), s1 = *(cu8*)((size_t)lre + po), s2 = *(cu8*)((size_t)lim + po);
        SCHED();
#define WU0 0
#define WUT 0
#define WUB 2
#define WUN 0
        u32x4 ra_ = {(unsigned)tid * 2654435761u, (unsigned)tid * 40503u + 12345u, (unsigned)tid * 2246822519u, (unsigned)tid * 3266489917u};
        ra_ = (ra_ & 0xbfffbfffu) | 0x3c003c00u;
        u32x4 rb_ = ra_.yzwx ^ 0x01230456u, rc_ = ra_.zwxy ^ 0x00770033u;
        f32x4 w0_ = {0.f, 0.f, 0.f, 0.f}, w1_ = w0_;
        if (WU0 + WU1 + WUT) { PIN4(ra_, rb_, rc_, w0_); asm volatile("s_nop 1" : "+v"(w1_)); }
#define WARM(n) do { _Pragma("unroll 1") for (int i_ = 0; i_ < (n) / (4 * HU); ++i_) { _Pragma("unroll") for (int j_ = 0; j_ < HU; ++j_) { MF(w0_, ra_, rb_); MF(w0_, rb_, rc_); MF(w1_, rc_, ra_); MF(w1_, ra_, rc_); } if (WUN) __builtin_amdgcn_s_sleep(WUN); } SCHED(); } while (0)
        if (WU0) WARM(WU0);
        const int pq = p_base + wid * 16 + l15;
        unsigned goff[4], gmask[4];
#pragma unroll
        for (int i = 0; i < 4; ++i) {
            goff[i] = (unsigned)(((tb - 1 - q4 - 4 * i) * 1024 + pq) * 16);
            gmask[i] = (tb - 1 - q4 - 4 * i >= 0) ? ~0u : 0u;
            asm volatile("" : "+v"(goff[i]), "+v"(gmask[i]));
        }
        unsigned gpub = q4 == 0 ? (unsigned)((tb * 1024 + pq) * 16) : 0x80000000u;
        unsigned hp = (unsigned)(tb * 524288 + (nb * 4 + wid) * 1024 + lane * 16);
        unsigned wa = (unsigned)(HT_OFF + wid * 2048 + (q4 * 16 + (l15 ^ ((q4 >> 1) * 4))) * 8);
        unsigned ra0 = (unsigned)(HT_OFF + wid * 2048 + ((q4 >> 1) * 64 + (l15 & 3) * 16 + (((q4 & 1) * 8 + (l15 >> 2)) ^ ((l15 & 2) * 2))) * 8), ra1 = ra0 ^ 32u;
        cra = CRA(GA_OFF + wid * 8192); crb = cra ^ 64;
        unsigned vB0 = (unsigned)((d_base + (lane >> 3) * 4) * 2048 + (((lane & 7) ^ (lane >> 4)) * 16)), vB1 = (vB0 ^ 64u) + 65536u - 1024u;
        PIN4(gpub, vB0, hp, wa); PIN4(ra0, ra1, cra, crb); asm volatile("" : "+v"(vB1));
        f32x4 acc[8][2];
#pragma unroll
        for (int m = 0; m < 8; m += 2) {
#pragma unroll
            for (int c = 0; c < 4; ++c) acc[m + (c >> 1)][c & 1] = (f32x4){0.f, 0.f, 0.f, 0.f};
            PIN4(acc[m][0], acc[m][1], acc[m + 1][0], acc[m + 1][1]);
        }
        SCHED();
        cf L1, Lq3, cc, L32, cLq, Lp, L512;
        {
            unsigned w0 = 0, w1 = 0, w2 = 0;
#pragma unroll
            for (int i = 0; i < 8; ++i) {
                asm("v_writelane_b32 %0, %3, %6\n\tv_writelane_b32 %1, %4, %6\n\tv_writelane_b32 %2, %5, %6" : "+v"(w0), "+v"(w1), "+v"(w2) : "s"(s0[i]), "s"(s1[i]), "s"(s2[i]), "i"(2 * i));
            }
            asm("s_nop 1" : "+v"(w0), "+v"(w1), "+v"(w2));
            auto spread = [&](unsigned w) {
                const unsigned e = (unsigned)__builtin_amdgcn_update_dpp(0, (int)w, 0xa0, 0xf, 0xf, false);
                const unsigned a = __builtin_amdgcn_permlane16_swap(e, e, false, false)[0];
                const unsigned c = __builtin_amdgcn_permlane32_swap(a, a, false, false)[0];
                return (l15 & 1) ? hi16(c) : lo16(c);
            };
            const float ldt = spread(w0), re = spread(w1), im = spread(w2);
            if (wid == 3 && lane < NFLAGW) *(unsigned*)(smem + UFLAG_OFF + lane * 4) = 0u;
            const float dt = rbf(expf(ldt));
            const float ea = expf(dt * re);
            float sn, cs;
            sincosf(dt * im, &sn, &cs);
            L1 = {ea * cs, ea * sn};
            const float nr = L1.r - 1.0f, ni = L1.i, den = re * re + im * im;
            cc = {2.f * ((nr * re + ni * im) / den), 2.f * ((ni * re - nr * im) / den)};
            const cf T2 = cmul(L1, L1), T4 = cmul(T2, T2), L8 = cmul(T4, T4), L16 = cmul(L8, L8);
            L32 = cmul(L16, L16);
            const cf L64 = cmul(L32, L32), L96 = cmul(L64, L32), L128 = cmul(L64, L64), L256 = cmul(L128, L128), L384 = cmul(L256, L128);
            L512 = cmul(L256, L256);
            Lp = pick4(q4, L128, L256, L384);
            Lq3 = pick4(3 - q4, L32, L64, L96);
            cLq = cmul(cc, pick4(q4, L32, L64, L96));
            PINC(L1); PINC(Lq3); PINC(cc); PINC(L32); PINC(cLq); PINC(Lp); PINC(L512);
        }
        __builtin_amdgcn_s_setprio(PRIO_G);
        unsigned tagv;
        cf S;
        STAMPT(dbg, 22, 0);
        const int swz = (l15 >> 1) & 7;
        {
            const int a0 = RA(0) + l15 * 128 + ((q4 ^ swz) * 16);
            const int b0 = RB(0) + ((wid >> 1) * 64 + (wid & 1) * 16 + l15) * 128 + ((q4 ^ swz) * 16);
            WAIT_LGKM0();
            if (WU1) WARM(WU1);
            if (WUT) {
#pragma unroll 1
                for (int i_ = 0; i_ < 64; ++i_) {
                    const unsigned now = (unsigned)__builtin_amdgcn_s_memrealtime();
                    SCHED();
#pragma unroll
                    for (int j_ = 0; j_ < WUB; ++j_) { MF(w0_, ra_, rb_); MF(w0_, rb_, rc_); MF(w1_, rc_, ra_); MF(w1_, ra_, rc_); }
                    SCHED();
                    if (now - t_start >= (unsigned)WUT) break;
                }
            }
            BAR();
            asm volatile("" ::: "memory");
            STAMPT(dbg, 19, 64);
            bf16x8 FA[8][2], FB[2][2][2];
            int pa[2] = {a0, a0 ^ 64}, pb[2] = {b0, b0 ^ 64};
            PIN4(pa[0], pa[1], pb[0], pb[1]);
#define LGW(n) __builtin_amdgcn_s_waitcnt(0xC07F | ((n) << 8))
#define RA_(j) FA[(j) >> 1][(j) & 1] = RD128(pa[(j) & 1], ((j) >> 1) * 2048)
#define RB_(bu, j) FB[bu][(j) >> 1][((j) ^ ((j) >> 1)) & 1] = RD128(pb[((j) ^ ((j) >> 1)) & 1], ((j) >> 1) * 4096)
#define RN_(bu, i) do { if ((i) == 0) RB_(bu, 0); else if ((i) < 3) RA_((i) - 1); else if ((i) < 6) RB_(bu, (i) - 2); else RA_((i) - 4); } while (0)
#define G1_H(H, bu, RD, FILL) do { \
            _Pragma("unroll") for (int i = 0; i < 16; ++i) { \
                const int m = (H) * 4 + (i >> 2), n = ((i >> 1) ^ m) & 1, ks = (i ^ (i >> 1)) & 1; \
                if ((H) == 0) { if (i == 0) LGW(6); if (i == 4) LGW(8); if (i == 8) LGW(10); if (i == 12) LGW(8); } \
                SCHED(); \
                MF(acc[m][n], FA[m][ks], FB[bu][n][ks]); SCHED(); \
                if ((H) == 0 && i < 8) RA_(8 + i); \
                if ((H) == 1 && (RD) && i < 12) RN_((bu) ^ 1, i); \
                FILL((H) * 16 + i); \
                SCHED(); \
            } } while (0)
#define NOF(g) do { } while (0)
#pragma unroll
            for (int j = 0; j < 12; ++j) { RN_(0, j); SCHED(); }
            unsigned long long ua_ = UNITS_A;
            int sb = RB(0), sa = RA(0), nx, na;
#define ADV(g) do { if ((g) == 8) { ua_ >>= 4; na = (int)((unsigned)ua_ & 15u) << 14; asm volatile("" : "+s"(na), "+s"(ua_)); } \
                    if ((g) == 9) { const int o_ = sa; sa = na; na -= o_; asm volatile("" : "+s"(na), "+s"(sa)); } \
                    if ((g) == 10) { pa[0] += na; pa[1] += na; asm volatile("" : "+v"(pa[0]), "+v"(pa[1])); } \
                    if ((g) == 11) { nx = (sb == 65536) ? -49152 : 16384; sb += nx; asm volatile("" : "+s"(nx), "+s"(sb)); } \
                    if ((g) == 13) { pb[0] += nx; pb[1] += nx; asm volatile("" : "+v"(pb[0]), "+v"(pb[1])); } } while (0)
#define G1_T(bu) do { \
                G1_H(0, bu, true, ADV); \
                WAIT_LGKM0(); \
                asm volatile("" ::: "memory"); \
                BAR();                     \
                asm volatile("" ::: "memory"); \
                G1_H(1, bu, true, NOF); } while (0)
#pragma unroll 1
            for (int t2 = 0; t2 < 7; ++t2) { G1_T(0); G1_T(1); }
            G1_T(0);
#define AGG(g) do { if ((g) == 8) { asm volatile("" ::: "memory"); SETW(G1D_OFF, wid, 1u); }        \
                    if ((g) == 9) tagv = *(volatile const lds_u32*)(size_t)(unsigned)TAG_OFF; \
                    if ((g) >= 7) { const int t = (g) - 6; \
                        if (t == 1) { asm volatile("s_nop 1" : "+v"(acc[0][0]), "+v"(acc[0][1])); S = {acc[0][0][0], acc[0][1][0]}; } \
                        if ((t & 3) == 0) asm volatile("" : "+v"(acc[t >> 2][0]), "+v"(acc[t >> 2][1])); \
                        S = cfma(L1, S, cf{acc[t >> 2][0][t & 3], acc[t >> 2][1][t & 3]}); PINC(S); } } while (0)
            G1_H(0, 1, false, AGG);
            G1_H(1, 1, false, AGG);
        }
        STAMPT(dbg, 1, 0);
        const unsigned tag = __builtin_amdgcn_readfirstlane(tagv);
        const __amdgpu_buffer_rsrc_t grs = __builtin_amdgcn_make_buffer_rsrc(ws, 0, 15 * 16384, 0x00020000);
        {
#pragma unroll
            for (int t = 26; t < 32; ++t) {
                if (t == 28) { PINC(S); asm volatile("" : "+v"(acc[7][0]), "+v"(acc[7][1])); }
                S = cfma(L1, S, cf{acc[t >> 2][0][t & 3], acc[t >> 2][1][t & 3]});
            }
            const cf W = cmul(S, Lq3);
            const float e2 = red16(red32(W.r, W.i));
            const auto eo = __builtin_amdgcn_permlane32_swap(__float_as_uint(e2), __float_as_uint(e2), false, false);
            __builtin_amdgcn_raw_buffer_store_b128((u32x4){eo[0], tag, eo[1], tag}, grs, gpub, 0, 16);
        }
        SCHED();
        STAMPT(dbg, 2, 0);
        const unsigned cg = lbase + GA_OFF + wid * 8192;
        wait_lds1(smem, COK_OFF + wid * 4);
        CREAD(0);
        WAIT_LGKM0();
        CPIN(0);
        CTILE(wid, 1, cg);
        u32x4 gq[4];
#pragma unroll
        for (int m = 0; m < 8; ++m) {
            const f32x4 R = acc[m][0], I = acc[m][1];
            acc[m][0] = fma4(splat4(cc.r), R, mul4(splat4(-cc.i), I));
            acc[m][1] = fma4(splat4(-cc.r), I, mul4(splat4(-cc.i), R));
            asm volatile("" : "+v"(acc[m][0]), "+v"(acc[m][1]));
            if (m == 3) {
                SCHED();
#pragma unroll
                for (int i = 0; i < 4; ++i) asm volatile("buffer_load_dwordx4 %0, %1, %2, 0 offen sc1" : "=&v"(gq[i]) : "v"(goff[i]), "s"(grs) : "memory");
                SCHED();
            }
        }
        cf Ec;
        {
            const auto sr = __builtin_amdgcn_permlane16_swap(__float_as_uint(S.r), __float_as_uint(S.r), false, false);
            const auto si = __builtin_amdgcn_permlane16_swap(__float_as_uint(S.i), __float_as_uint(S.i), false, false);
            const cf Se = {__uint_as_float(sr[0]), __uint_as_float(si[0])}, So = {__uint_as_float(sr[1]), __uint_as_float(si[1])};
            const cf A = cfma(L32, Se, So);
            const auto ar = __builtin_amdgcn_permlane32_swap(__float_as_uint(A.r), __float_as_uint(A.r), false, false);
            const auto ai = __builtin_amdgcn_permlane32_swap(__float_as_uint(A.i), __float_as_uint(A.i), false, false);
            const cf A01 = {(q4 & 2) ? __uint_as_float(ar[0]) : 0.f, (q4 & 2) ? __uint_as_float(ai[0]) : 0.f};
            const cf kq = {(q4 & 1) ? L32.r : 1.f, (q4 & 1) ? L32.i : 0.f};
            const cf E = cfma(kq, A01, cf{(q4 & 1) ? Se.r : 0.f, (q4 & 1) ? Se.i : 0.f});
            Ec = cmul(cc, E);
            PINC(Ec);
        }
        WAIT_VM(4);
        CREAD(1);
        WAIT_LGKM0();
        CPIN(1);
        CTILE(wid, 2, cg);
        asm volatile("s_waitcnt vmcnt(8)" : "+v"(gq[0]), "+v"(gq[1]), "+v"(gq[2]), "+v"(gq[3]) :: "memory");
        STAMPT(dbg, 27, 0);
        cf carry;
        {
            unsigned spins = 0;
            for (;;) {
                unsigned bad = 0;
#pragma unroll
                for (int i = 0; i < 4; ++i) bad |= ((gq[i].y ^ tag) | (gq[i].w ^ tag)) & gmask[i];
                if (__all(bad == 0u)) break;
                if (++spins > SPIN_MAX) break;
#pragma unroll
                for (int i = 0; i < 4; ++i) asm volatile("buffer_load_dwordx4 %0, %1, %2, 0 offen sc1" : "+v"(gq[i]) : "v"(goff[i]), "s"(grs) : "memory");
                asm volatile("s_waitcnt vmcnt(0)" : "+v"(gq[0]), "+v"(gq[1]), "+v"(gq[2]), "+v"(gq[3]) :: "memory");
            }
            cf E = {__uint_as_float(gq[3].x), __uint_as_float(gq[3].z)};
#pragma unroll
            for (int i = 2; i >= 0; --i) E = cfma(L512, E, cf{__uint_as_float(gq[i].x), __uint_as_float(gq[i].z)});
            E = cmul(E, Lp);
            const float e2 = red16(red32(E.r, E.i));
            const auto eo = __builtin_amdgcn_permlane32_swap(__float_as_uint(e2), __float_as_uint(e2), false, false);
            carry = {__uint_as_float(eo[0]), __uint_as_float(eo[1])};
        }
        STAMPT(dbg, 3, 0);
        {
            const __amdgpu_buffer_rsrc_t hrs = __builtin_amdgcn_make_buffer_rsrc(h, 0, SEQ * 2048 * 2, 0x00020000);
            const cf e = cfma(cLq, carry, Ec);
            float hr = e.r, hn = -e.i;
            s16x4 fr[2][4];
#define HSTORE(m) do { _Pragma("unroll") for (int c = 0; c < 2; ++c) \
                __builtin_amdgcn_raw_buffer_store_b128(__builtin_bit_cast(u32x4, __builtin_shufflevector(fr[(m) & 1][2 * c], fr[(m) & 1][2 * c + 1], 0, 1, 2, 3, 4, 5, 6, 7)), hrs, hp, (2 * (m) + c) * 65536, WT); } while (0)
#pragma unroll
            for (int m = 0; m < 4; ++m) {
#pragma unroll
                for (int j = 0; j < 8; ++j) {
                    const float nr = fmaf(L1.r, hr, fmaf(L1.i, hn, acc[2 * m + (j >> 2)][0][j & 3]));
                    const float nn = fmaf(L1.r, hn, fmaf(-L1.i, hr, acc[2 * m + (j >> 2)][1][j & 3]));
                    hr = nr; hn = nn;
                    acc[2 * m + (j >> 2)][0][j & 3] = nr; acc[2 * m + (j >> 2)][1][j & 3] = nn;
                }
                u32x2 pk[4];
#pragma unroll
                for (int c = 0; c < 4; ++c) { const f32x4 v = acc[2 * m + (c >> 1)][c & 1]; pk[c] = (u32x2){pkbf(v[0], v[1]), pkbf(v[2], v[3])}; }
                PIN4(pk[0], pk[1], pk[2], pk[3]);
                SCHED();
#pragma unroll
                for (int c = 0; c < 4; ++c) { *(volatile __attribute__((address_space(3))) u32x2*)(size_t)(wa + c * 512) = pk[c]; SCHED(); }
                asm volatile("" ::: "memory");
#pragma unroll
                for (int c = 0; c < 2; ++c) { fr[m & 1][2 * c] = TRD(ra0 + c * 1024); SCHED(); fr[m & 1][2 * c + 1] = TRD(ra1 + c * 1024); SCHED(); }
                asm volatile("" ::: "memory");
                if (m > 0) HSTORE(m - 1);
                SCHED();
            }
            HSTORE(3);
        }
        STAMPT(dbg, 4, 0);
        {
            WAIT_VM(8);
            CREAD(2);
            WAIT_VM(0);
            {
                unsigned n;
                asm volatile("s_mov_b64 exec, 1\n\tds_add_rtn_u32 %0, %1, %2\n\ts_mov_b64 exec, -1\n\ts_waitcnt lgkmcnt(0)" : "=&v"(n) : "v"(MISC_OFF), "v"(1u) : "memory");
                if (__builtin_amdgcn_readfirstlane(n) == 3u) {
                    if (lane == 0) __hip_atomic_store((gu32*)(ws + WS_HFLAG) + (tb * 16 + nb) * 32, tag, RLX_AGENT);
                    if (DBGON && lane == 0) dbg[BIDX * 64 + 20] = __builtin_amdgcn_s_memrealtime();
                }
            }
            STAMPT(dbg, 12, 0);
            if (wid == 0 && lane < 7) CODE_TOUCH(CODE_LINES - 1 - lane - (idx & 7) * 7);
            __builtin_amdgcn_s_setprio(0);
            WAIT_LGKM0();
            CPIN(2);
            CTILE(wid, 3, cg);
            XLOAD();
            HPOLL_SETUP();
            HWAIT();
            STAMPT(dbg, 8, 0);
            const unsigned vh = (unsigned)(lane * 16);
#pragma unroll
            for (int f = 0; f < DEPTH; ++f) LOADF(f);
            __builtin_amdgcn_s_setprio(PRIO2_G);
        }
    } else {
        const int lw = wid - 4;
        const int d = cb * 4 + lw;
        const char* Krow = (const char*)(ssmk + (size_t)d * SEQ);
        cra = CRA(LA_OFF + lw * 8192); crb = cra ^ 64;
        const unsigned cst0 = lbase + LA_OFF + lw * 8192;
        CT_SETUP();
        asm volatile("" : "+v"(cra), "+v"(crb));
        u32x4 KA[4];
        unsigned draw;
        unsigned tag;
        float gacc = 0.f;
#define KLOAD() asm volatile("global_load_dwordx4 %0, %5, %7 nt\n\tglobal_load_dwordx4 %1, %5, %7 offset:1024 nt\n\tglobal_load_dwordx4 %2, %5, %7 offset:2048 nt\n\t" \
                             "global_load_dwordx4 %3, %5, %7 offset:3072 nt\n\tglobal_load_ushort %4, %6, %8" \
                             : "=&v"(KA[0]), "=&v"(KA[1]), "=&v"(KA[2]), "=&v"(KA[3]), "=&v"(draw) : "v"((unsigned)(lane * 16)), "v"((unsigned)(d * 2)), "s"(Krow), "s"(ssmD) : "memory")
#define KWAIT(n) asm volatile("s_waitcnt vmcnt(%5)" : "+v"(KA[0]), "+v"(KA[1]), "+v"(KA[2]), "+v"(KA[3]), "+v"(draw) : "i"(n) : "memory")
        if (wid < 6) {
            HWARM(voA, voB, HW3);
            STAGE_A(2, lw0 + RA(2));
            HWARM(voA, voB, HW4);
            STAGE_B(2, lw0 + RB(2));
            WAIT_VM(32);
            STAMPT(dbg, 17, 320);
            BAR();
            STAGE_B(3, lw0 + RB(3));
            unsigned long long ua_ = UNITS_A >> 12;
            unsigned sla = lw0 + RA(3), slb = lw0 + RB(4);
            int ka = ((3 + rot1) & 15) * 128, kb_ = ka;
            const char* sa = (const char*)x + ka;
            asm volatile("" : "+s"(sa), "+s"(sla), "+s"(ka), "+s"(ua_));
#define NEXT_A() do { ka = (ka + 128) & 2047; sa = (const char*)x + ka; ua_ >>= 4; sla = lw0 + (((unsigned)ua_ & 15u) << 14); asm volatile("" : "+s"(sa), "+s"(sla), "+s"(ka), "+s"(ua_)); } while (0)
#define LSTEP(n) do { \
                WAIT_VM(n); \
                BAR(); \
                GLDS4(sa, voA[0], voA[1], voA[2], voA[3], sla, 0); \
                GLDS4(sa + 16384, voA[0], voA[1], voA[2], voA[3], sla, 4096); \
                kb_ = (kb_ + 128) & 2047; \
                GLDS4((const char*)Bre + kb_, voB[0], voB[1], voB[2], voB[3], slb, 0); \
                NEXT_A(); \
                const unsigned slb0 = slb; \
                slb = (slb == lw0 + 65536) ? lw0 + 16384 : slb + 16384; \
                asm volatile("" : "+s"(slb), "+s"(kb_), "+s"(t)); \
                GLDS4((const char*)Bim + kb_, voB[0], voB[1], voB[2], voB[3], slb0, 4096); } while (0)
#pragma unroll 1
            for (int t = 1; t < 5; ++t) LSTEP(24);
            GLDS4(sa, voA[0], voA[1], voA[2], voA[3], sla, 0); GLDS4(sa + 16384, voA[0], voA[1], voA[2], voA[3], sla, 4096);
            NEXT_A();
#pragma unroll 1
            for (int t = 5; t < 13; ++t) LSTEP(32);
            KLOAD();
            WAIT_VM(37);
            BAR();
            __builtin_amdgcn_s_setprio(0);
            tag = lds_word(smem, TAG_OFF);
            CTILE(lw, 0, cst0 + GA_OFF);
            FLAGD(COK_OFF + lw * 4);
            WAIT_VM(30);
            BAR();
            CTILE(lw + 2, 0, cst0 + GA_OFF + 16384);
            FLAGD(COK_OFF + lw * 4 + 8);
            WAIT_VM(23);
            BAR();
        } else {
            const int vw = wid - 6;
            const int dch = dblk * 128 + vw * 64 + l15;
            const unsigned lk = lbase + ST_OFF + vw * ST_SZ;
            const unsigned vv_ = (unsigned)(((lane >> 4) * 4 + ((lane >> 3) & 1)) * DM * 2 + (lane & 7) * 16);
            const char* vb = (const char*)vin + (size_t)(sblk * 64) * (DM * 2) + dblk * 256 + vw * 128;
            const int grow = nb * 8 + vw * 4 + q4;
            unsigned gx0 = (unsigned)(grow * 128 + (((l15 >> 1) ^ ((grow >> 1) & 7)) * 16) + (l15 & 1) * 8), gx1 = gx0 + RA(2), gwa = (unsigned)(GW_OFF + l15 * 8);
            asm volatile("" : "+v"(gx0), "+v"(gx1), "+v"(gwa));
            u32x2 gxv, gwv;
            typedef const volatile __attribute__((address_space(3))) u32x2* l2p;
#define VBAR(t) do { SCHED(); asm volatile("" ::: "memory"); BAR(); asm volatile("" ::: "memory"); \
                     gxv = *(l2p)(size_t)(RA(t) == 0 ? gx0 : gx1 + (RA(t) - RA(2))); gwv = *(l2p)(size_t)(gwa + (t) * 128); SCHED(); } while (0)
#define GATE() do { SCHED(); gacc = fmaf(lo16(gxv.x), lo16(gwv.x), gacc); gacc = fmaf(hi16(gxv.x), hi16(gwv.x), gacc); \
                    gacc = fmaf(lo16(gxv.y), lo16(gwv.y), gacc); gacc = fmaf(hi16(gxv.y), hi16(gwv.y), gacc); asm volatile("" : "+v"(gacc)); SCHED(); } while (0)
            unsigned ka[4][2], sel[4];
            const int R0 = 4 + (l15 >> 2) * 16 + (l15 & 3) - (q4 >> 1);
            unsigned kr0 = (unsigned)(2 * R0), kr1 = (unsigned)(ST_OFF + vw * ST_SZ + R0 * 128 + (((q4 ^ (R0 >> 1)) & 1) * 16));
            asm volatile("" : "+v"(kr0), "+v"(kr1));
#pragma unroll
            for (int i = 0; i < 4; ++i)
#pragma unroll
                for (int tp = 0; tp < 2; ++tp) {
                    ka[i][tp] = (kr1 ^ (tp * 16)) + ((kr0 + 8 * i - 4 * tp) & 0xe0) + (4 * i - 2 * tp) * 128;
                    asm volatile("" : "+v"(ka[i][tp]));
                }
#pragma unroll
            for (int r = 0; r < 4; ++r) {
                const unsigned pick = (q4 & 2) ? 0x0302u : 0x0100u;
                sel[r] = ((l15 >> 1) == (q4 & 1) * 4 + r) ? ((l15 & 1) ? (pick << 16) | 0x0c0cu : 0x0c0c0000u | pick) : 0x0c0c0c0cu;
            }
            unsigned fo = lane < 8 ? (unsigned)((tb * 1024 + p_base) * 16 + lane * 128) : lane == 8 ? (unsigned)(WS_HFLAG + (tb * 16 + nb) * 128)
                        : lane == 9 ? (unsigned)(WS_CFLAG + cb * 128) : lane == 10 ? (unsigned)(WS_GATE + (tb * 16 + nb) * 128) : (unsigned)(WS_KVFLAG + (dblk * 32 + sblk) * 128);
            asm volatile("" : "+v"(fo));
            unsigned va_ = (unsigned)(ST_OFF + vw * ST_SZ + ST_V + (q4 * 2 + (l15 >> 3)) * 1056 + ((l15 >> 2) & 1) * 128 + (l15 & 3) * 8);
            unsigned so = (unsigned)(((dch >> 2) * 32 + sblk) * 512 + q4 * 64 + (dch & 3) * 16);
            PIN4(sel[0], sel[1], sel[2], sel[3]); asm volatile("" : "+v"(so), "+v"(va_));
            asm volatile("s_waitcnt vmcnt(9)" : "+v"(tw[0]), "+v"(tw[1]), "+v"(tw[2]), "+v"(tw[3]), "+v"(epoch) :: "memory");
            STAMPT(dbg, 18, 384);
            VBAR(0);
            bf16x8 wf[4][2];
#pragma unroll
            for (int ps = 0; ps < 4; ++ps)
#pragma unroll
                for (int tp = 0; tp < 2; ++tp) {
                    u32x4 w;
#pragma unroll
                    for (int r = 0; r < 4; ++r) w[r] = __builtin_amdgcn_perm(0u, tw[ps][tp], sel[r]);
                    wf[ps][tp] = __builtin_bit_cast(bf16x8, w);
                    asm volatile("" : "+v"(wf[ps][tp]));
                }
            SCHED();
            VLD(0); VLD(1); VLD(2); VLD(3); VLD(4); VLD(5); VLD(6); VLD(7);
            GATE(); VBAR(1);
            f32x4 ks[4][4];
            auto sconv = [&](int i0) __attribute__((always_inline)) {
                bf16x8 kf[2][4][2];
#pragma unroll
                for (int q = 0; q < 16; ++q) { kf[q >> 3][q & 3][(q >> 2) & 1] = RD128(ka[i0 + (q >> 3)][(q >> 2) & 1], (q & 3) * 32); SCHED(); }
                GATE();
                __builtin_amdgcn_s_setprio(PRIO_KV);
                WAIT_LGKM0();
                SCHED();
#pragma unroll
                for (int i = 0; i < 2; ++i) {
#pragma unroll
                    for (int ps = 0; ps < 4; ++ps) MF0(ks[ps][i0 + i], kf[i][ps][0], wf[ps][0]);
#pragma unroll
                    for (int ps = 0; ps < 4; ++ps) MF(ks[ps][i0 + i], kf[i][ps][1], wf[ps][1]);
                }
                __builtin_amdgcn_s_setprio(0);
                SCHED();
            };
            WAIT_VM(8);
            STAMPT(dbg, 21, 384);
#define CTCH(t) do { if (TP == (t)) { TOUCH((const char*)(vw ? Cim : Cre) + (size_t)b * 8192, (unsigned)(lane * 128)); \
                                      if (vw == 0) TOUCH(ws, fo); else TOUCH(dskip, (unsigned)(d_base * 2)); } } while (0)
#define CODE2() do { const int cl = 64 + lane + ((idx & 15) * 2 + vw) * 7; if (lane < 7 && cl < CODE_LINES) CODE_TOUCH(cl); } while (0)
            CTCH(1);
            sconv(0); if (ES) __builtin_amdgcn_s_sleep(ES); VBAR(2);
            CTCH(2);
            sconv(2); if (ES) __builtin_amdgcn_s_sleep(ES); VBAR(3);
            if (TP < 3) WAIT_VM(2); else WAIT_VM(0);
            CTCH(3);
            u32x2 vq[4][4];
#pragma unroll
            for (int ps = 0; ps < 4; ++ps)
#pragma unroll
                for (int j = 0; j < 4; ++j) vq[ps][j] = __builtin_bit_cast(u32x2, TRD(va_ + j * 256 + ps * 32));
            const __amdgpu_buffer_rsrc_t rs = __builtin_amdgcn_make_buffer_rsrc(kvT, 0, DM * SEQ * 2, 0x00020000);
            u32x4 o[8];
            auto kvout = [&](int n) __attribute__((always_inline)) {
                const int hh = n >> 2, ps = n & 3;
                asm volatile("" : "+v"(ks[ps][2 * hh]), "+v"(ks[ps][2 * hh + 1]));
#pragma unroll
                for (int e = 0; e < 4; ++e) {
                    const unsigned vv = vq[ps][2 * hh + (e >> 1)][e & 1];
                    o[n][e] = pkbf(ks[ps][2 * hh + (e >> 1)][2 * (e & 1)] * lo16(vv), ks[ps][2 * hh + (e >> 1)][2 * (e & 1) + 1] * hi16(vv));
                }
                asm volatile("" : "+v"(o[n]));
                SCHED();
            };
            GATE(); kvout(0); kvout(1);
            WAIT_LGKM0();
            if (ES) __builtin_amdgcn_s_sleep(ES);
            VBAR(4);
#define STM 2
#define SLP 0
#define KVST(n) do { __builtin_amdgcn_raw_buffer_store_b128(o[n], rs, so + (unsigned)(((n) >> 2) * 256), ((n) & 3) * 65536, WT); SCHED(); } while (0)
            CTCH(4);
            if (STM == 1) { KVST(0); KVST(1); }
            kvout(2); kvout(3); GATE(); if (SLP) __builtin_amdgcn_s_sleep(SLP); VBAR(5);
            if (STM == 1) { KVST(2); KVST(3); }
            if (STM == 2) { KVST(0); KVST(1); KVST(2); KVST(3); asm volatile("s_nop 1" ::: "memory"); SCHED(); }
            kvout(4); kvout(5); GATE(); if (SLP) __builtin_amdgcn_s_sleep(SLP); VBAR(6);
            if (STM == 1) { KVST(4); KVST(5); }
            kvout(6); kvout(7); GATE(); if (SLP) __builtin_amdgcn_s_sleep(SLP); VBAR(7);
            if (STM == 1) { KVST(6); KVST(7); }
            if (STM == 2) { KVST(4); KVST(5); KVST(6); KVST(7); }
#pragma unroll
            for (int n = 0; n < 8; ++n) if (STM == 0) __builtin_amdgcn_raw_buffer_store_b128(o[(n >> 1) + (n & 1) * 4], rs, so + (unsigned)((n & 1) * 256), (n >> 1) * 65536, WT);
            SCHED();
            asm volatile("s_nop 1" ::: "memory");
            GATE(); VBAR(8);
            if (vw == 1 && lane == 0) *(unsigned*)(smem + TAG_OFF) = (epoch + 1u) ? epoch + 1u : 1u;
            GATE();
            WAIT_LGKM0();
            WAIT_VM(0);
            VBAR(9);
            if (RP == 9) CODE2();
            tag = __builtin_amdgcn_readfirstlane(*(const unsigned*)(smem + TAG_OFF));
            if (vw == 0 && lane == 0) __hip_atomic_store((gu32*)(ws + WS_KVFLAG) + (dblk * 32 + sblk) * 32, tag, RLX_AGENT);
            if (vw == 1 && lane < 4) *(u32x4*)(smem + U_OFF + 192 + lane * 16) = (u32x4){0u, 0u, 0u, 0u};
            gu32* kfp = (gu32*)(ws + WS_KVFLAG) + ((cb >> 5) * 32 + vw * 16 + l15) * 32;
            const char* ub = (const char*)kvT + (size_t)__builtin_amdgcn_readfirstlane(cb * 16384 + vw * 8192);
            const unsigned vu = (unsigned)((lane >> 5) * 512 + ((lane >> 2) & 1) * 256 + ((lane >> 3) & 3) * 64 + ((lane - (lane >> 4)) & 3) * 16);
            const unsigned lu = lbase + U_OFF + 256 + vw * 8192;
#define U_DMA1(o) asm volatile("s_add_u32 m0, %2, %3\n\ts_nop 0\n\tglobal_load_lds_dwordx4 %0, %1 sc1\n\tglobal_load_lds_dwordx4 %0, %1 offset:1024 sc1\n\t" \
                             "global_load_lds_dwordx4 %0, %1 offset:2048 sc1\n\tglobal_load_lds_dwordx4 %0, %1 offset:3072 sc1" \
                             :: "v"(vu), "s"(ub + (o)), "s"(lu), "i"(o) : "memory", "scc")
#define U_DMA() do { U_DMA1(0); U_DMA1(4096); FLAGD(UFLAG_OFF + lw * 4); } while (0)
            GATE(); VBAR(10);
            CTCH(10);
            if (RP == 10) CODE2();
            GATE(); VBAR(11);
            GATE(); VBAR(12);
            GATE(); VBAR(13);
            if (RP == 13) CODE2();
            unsigned pv;
            asm volatile("global_load_dword %0, %1, off sc1" : "=&v"(pv) : "v"(kfp) : "memory");
            KLOAD();
            GATE(); VBAR(14);
            GATE(); VBAR(15);
            asm volatile("s_waitcnt vmcnt(5)" : "+v"(pv) :: "memory");
            const bool done = __all(pv == tag);
            if (done) U_DMA();
            if (DBGON && TIDX == 384) dbg[BIDX * 64 + 13] = (done ? 100ull : 0ull) | (1ull << 62);
            GATE();
            if (lane == 0) SETW(UFLAG_OFF, vw, 1u);
            WAIT_VM(9);
            if (!done) {
                unsigned spins = 0;
                for (;;) {
                    const unsigned v = __hip_atomic_load(kfp, RLX_AGENT);
                    if (__all(v == tag)) break;
                    if (++spins > SPIN_MAX) break;
                    __builtin_amdgcn_s_sleep(1);
                }
                U_DMA();
            }
        }
        KWAIT(18);
        STAMPT(dbg, 14, 320);
        __builtin_amdgcn_s_sleep(6);
        {
            const unsigned cp = (unsigned)(KC_OFF + lw * CV_W + 64 + lane * 16);
            if (lane == 0) KA[0].x = (KA[0].x & 0xffff0000u) | f2bf(lo16(KA[0].x) + bf2f((u16)draw));
#define WR128(v, off) asm volatile("ds_write_b128 %0, %1 offset:%2" :: "v"(cp), "v"(v), "i"(off) : "memory")
#pragma unroll
            for (int q = 3; q >= 0; --q) WR128(KA[q], q * 1024);
#pragma unroll
            for (int q = 3; q >= 0; --q) {
                const u32x2 p = q ? pred2(KA[q], RL63(KA[q ? q - 1 : 0].z), RL63(KA[q ? q - 1 : 0].w)) : pred2(KA[0], 0u, 0u);
                const u32x4 a = KA[q];
                const u32x4 s1 = {al(a.x, p.y), al(a.y, a.x), al(a.z, a.y), al(a.w, a.z)};
                WR128(s1, q * 1024 + CV_COPY);
                const u32x4 s2 = {p.y, mv(a.x), mv(a.y), mv(a.z)};
                WR128(s2, q * 1024 + 2 * CV_COPY);
                const u32x4 s3 = {al(p.y, p.x), mv(s1.x), mv(s1.y), mv(s1.z)};
                WR128(s3, q * 1024 + 3 * CV_COPY);
            }
            if (lane < 16) *(u32x4*)(smem + KC_OFF + lw * CV_W + (lane >> 2) * CV_COPY + (lane & 3) * 16) = (u32x4){0u, 0u, 0u, 0u};
            SCHED();
        }
        STAMPT(dbg, 26, 320);
        wait_lds4(smem, G1D_OFF);
        wait_lds4(smem, UFLAG_OFF);
        STAMPT(dbg, 9, 256);
        __builtin_amdgcn_s_setprio(PRIO_CV);
        STAMPT(dbg, 10, 384);
        f32x4 cv[8];
#pragma unroll
        for (int g = 0; g < 8; ++g) cv[g] = (f32x4){0.f, 0.f, 0.f, 0.f};
        PIN4(cv[0], cv[1], cv[2], cv[3]); PIN4(cv[4], cv[5], cv[6], cv[7]);
        const int uz = U_OFF + 192;
        const int ua0 = U_OFF + 256 + (2 * l15 + q4 - 2) * 64, rv0 = (2 * l15 + q4 - 2) * 4 + lw * 16;
        const unsigned ka0 = (unsigned)((l15 >> 2) * CV_COPY - 16 * q4 + 8 * (l15 & 3) - 8);
        int ua, rv, pu, pz;
        unsigned adr;
#define UNEXT1() do { pu = ua | (rv & 0x30); asm volatile("" : "+v"(pu)); } while (0)
#define UNEXT2() do { pz = (ua > uz ? ua : uz) | (rv & 0x30); asm volatile("" : "+v"(pz)); } while (0)
#define UNEXT3() do { ua -= 256; rv -= 16; asm volatile("" : "+v"(ua), "+v"(rv)); } while (0)
#define ANEXT() do { adr += 64; asm volatile("" : "+v"(adr)); } while (0)
#define RDU(dst, c) do { dst = (c) ? RD128(pu, (c) * 2048) : RD128(pz, 0); SCHED(); } while (0)
        HPOLL_SETUP();
        unsigned hpv = 0;
        {
            bf16x8 A[8], UB[8];
            ua = ua0; rv = rv0; adr = lbase + KC_OFF + lw * CV_W + ka0 + 96;
            asm volatile("" : "+v"(adr));
            UNEXT1(); UNEXT2(); UNEXT3();
#pragma unroll
            for (int i = 0; i < 8; ++i) { RDA(A[7 - i], adr, 7 - i); RDU(UB[i], i); }
            unsigned gb = lw >= 2 ? (unsigned)((tb * 16 + nb) * 128 + (lw - 2) * 32) : 0x80000000u;
            asm volatile("" : "+s"(gb));
            const unsigned gso = (l15 == 0) ? gb + (unsigned)(q4 * 8) : 0x80000000u;
            float gt = 0.f;
            unsigned vxo = 0, vdo = 0, vgo = 0;
            auto gate_out = [&](int i) __attribute__((always_inline)) {
                if (i == 0) gacc += __uint_as_float(__builtin_amdgcn_update_dpp(0u, __float_as_uint(gacc), 0xb1, 0xf, 0xf, false));
                if (i == 1) gacc += __uint_as_float(__builtin_amdgcn_update_dpp(0u, __float_as_uint(gacc), 0x4e, 0xf, 0xf, false));
                if (i == 2) gacc += __uint_as_float(__builtin_amdgcn_update_dpp(0u, __float_as_uint(gacc), 0x141, 0xf, 0xf, false));
                if (i == 3) gacc += __uint_as_float(__builtin_amdgcn_update_dpp(0u, __float_as_uint(gacc), 0x140, 0xf, 0xf, false));
                if (i == 4) gacc = rbf(gacc);
                if (i == 5) gacc = __builtin_amdgcn_exp2f(gacc * -1.44269504088896f);
                if (i == 6) gacc = __builtin_amdgcn_rcpf(1.0f + gacc);
                if (i == 7) gacc = rbf(gacc);
                if (i == 8) gt = __uint_as_float(pkbf(gacc, 1.0f - gacc));
                if (i == 9) __builtin_amdgcn_raw_buffer_store_b64((u32x2){__float_as_uint(gt), tag}, gs, gso, 0, WT);
                asm volatile("" : "+v"(gacc), "+v"(gt));
            };
            auto step = [&](auto E) __attribute__((always_inline)) {
                constexpr int e = decltype(E)::value;
                constexpr int ti = (e == CS0) ? 0 : (e == CS1) ? 1 : (e == CS2) ? 2 : -1, tj = ti < 0 ? 0 : ti;
                constexpr unsigned char GA[36] = {0x66, 0x77, 0x76, 0x44, 0x55, 0x54, 0x65, 0x64, 0x75, 0x74, 0x22, 0x33, 0x32, 0x43, 0x42, 0x53, 0x52, 0x63, 0x62, 0x73, 0x72,
                                                  0x00, 0x11, 0x10, 0x21, 0x20, 0x31, 0x30, 0x41, 0x40, 0x51, 0x50, 0x61, 0x60, 0x71, 0x70};
                const char* sb_ = (const char*)Cre + COFF(wid, tj + 1);
                const char* sc_ = (const char*)Cim + COFF(wid, tj + 1);
#pragma unroll
                for (int q = 0; q < 36; ++q) {
                    const int g = GA[q] >> 4, a = GA[q] & 15;
                    MF(cv[g], A[a], UB[g - a]); SCHED();
                    if (e < 7) {
                        if (a == 1) RDU(UB[g - 1], g - 1);
                        if (q == 35) RDU(UB[7], 7);
                        if (g == 7) RDA(A[a], adr + 64 * (e + 1), a);
                        if (q == 11) UNEXT1();
                        if (q == 13) UNEXT2();
                        if (q == 15) UNEXT3();
                    }
                    if (ti >= 0) {
                        const int n = (q >= 3 && q <= 7) ? q - 3 : q == 10 ? 5 : q == 12 ? 6 : q == 14 ? 7 : -1;
                        if (q == 3) { if (ti == 0) WAIT_VM(1); else if (e == XE + 1) WAIT_VM(7); else WAIT_VM(0); }
                        if (n >= 0) CRD(tj, n & 7);
                        if (q == 18) CPIN(tj);
                        if (q == 18 || q == 23) asm volatile("s_add_u32 m0, %3, %4\n\ts_nop 0\n\tglobal_load_lds_dwordx4 %0, %2\n\tglobal_load_lds_dwordx4 %1, %2 offset:1024"
                                                             :: "v"(vB0), "v"(vB1), "s"(q == 18 ? sb_ : sc_), "s"(cst0), "i"(q == 18 ? 0 : 4096) : "memory", "scc");
                        if (q == 21 || q == 25) asm volatile("global_load_lds_dwordx4 %0, %2 offset:2048\n\tglobal_load_lds_dwordx4 %1, %2 offset:3072"
                                                             :: "v"(vB0), "v"(vB1), "s"(q == 21 ? sb_ : sc_) : "memory");
                    }
                    if (e == 0 && q >= 3 && q <= 7) gate_out(q - 3);
                    if (e == 0 && q >= 10 && q <= 18 && !(q & 1)) gate_out(5 + (q - 10) / 2);
                    if (e == 0 && q == 4) CTILE(wid, 0, cst0);
                    if (e == XE && q >= 23 && q <= 35 && (q & 1)) {
                        const int i = (q - 23) / 2;
                        const __amdgpu_buffer_rsrc_t xs = __builtin_amdgcn_make_buffer_rsrc((void*)x, 0, SEQ * DM * 2, 0x00020000);
                        const __amdgpu_buffer_rsrc_t ks = __builtin_amdgcn_make_buffer_rsrc((void*)dskip, 0, DM * 2, 0x00020000);
                        if (i < 4) asm volatile("buffer_load_dwordx2 %0, %1, %2, %3 offen offset:%4" : "=&v"(xx[(i >> 1) & 1][i & 1]) : "v"(vxo), "s"(xs), "s"((TR((i >> 1) & 1) * DM + d_base) * 2), "i"((i & 1) * 2048) : "memory");
                        if (i == 4) asm volatile("buffer_load_dwordx2 %0, %1, %2, %3 offen" : "=&v"(dk2) : "v"(vdo), "s"(ks), "s"(d_base * 2) : "memory");
                        if (i >= 5) asm volatile("buffer_load_dwordx4 %0, %1, %2, %3 offen sc1" : "=&v"(gv[(i - 5) & 1]) : "v"(vgo), "s"(gs), "s"(GSO((i - 5) & 1)) : "memory");
                    }
                    if (e == XE && q == 16) { vxo = (unsigned)(q4 * 32 * DM * 2 + l15 * 8); vdo = (unsigned)(l15 * 8); asm volatile("" : "+v"(vxo), "+v"(vdo)); }
                    if (e == XE && q == 17) { vgo = GV(q4); asm volatile("" : "+v"(vgo)); }
                    if (e == 6 && q == 30) hpv = __hip_atomic_load(hfp, RLX_AGENT);
                    SCHED();
                }
            };
            unroll<8>(step);
            WAIT_VM(0);
            XPIN();
        }
        PAD8(cv[0], cv[1], cv[2], cv[3]); PIN4(cv[4], cv[5], cv[6], cv[7]);
        STAMPT(dbg, 11, 384);
        const unsigned vh = (unsigned)((lane ^ 2) * 16);
        if (!__all(hpv == tag)) HWAIT();
        asm volatile("" ::: "memory");
#pragma unroll
        for (int f = 0; f < DEPTH; ++f) LOADF(f);
        STAMPT(dbg, 25, 384);
        {
            const __amdgpu_buffer_rsrc_t yrs = __builtin_amdgcn_make_buffer_rsrc(y2T, 0, DM * SEQ * 2, 0x00020000);
            const unsigned yo = (unsigned)(d * 4096 + (q4 & 1) * 512 + (l15 >> 3) * 256 + (q4 >> 1) * 128 + (l15 & 7) * 16);
#pragma unroll
            for (int g = 0; g < 8; g += 2) {
                const auto r0 = __builtin_amdgcn_permlane16_swap(pkbf(cv[g][0], cv[g][1]), pkbf(cv[g + 1][0], cv[g + 1][1]), false, false);
                const auto r1 = __builtin_amdgcn_permlane16_swap(pkbf(cv[g][2], cv[g][3]), pkbf(cv[g + 1][2], cv[g + 1][3]), false, false);
                __builtin_amdgcn_raw_buffer_store_b128((u32x4){r0[0], r1[0], r0[1], r1[1]}, yrs, yo + (unsigned)(g * 512), 0, WT);
            }
        }
        asm volatile("" ::: "memory");
        STAMPT(dbg, 15, 384);
        __builtin_amdgcn_s_setprio(PRIO2_L);
    }
    {
        const int lane = fresh_lane(), l15 = lane & 15, q4 = lane >> 4;
        const int ring = wid >> 2;
        const unsigned va = (unsigned)((lane ^ (ring * 2)) * 16), vh = va;
        const unsigned tag = lds_word(smem, TAG_OFF);
        const int cb0 = ring ? KC_OFF : GA_OFF, cst = ring ? CV_W : 8192;
        f32x4 ac[2][4];
        unsigned ca[2], fa[2], fl = 0, fv = 0;
#define CHAIN(u) do { const int c_ = (wid - ((u) >> 1)) & 3; ca[(u) & 1] = va + (unsigned)(cb0 + c_ * cst + ((u) & 1) * 4096); \
                      fa[(u) & 1] = (unsigned)(RF_OFF + ring * 32 + ((u) & 1) * 16 + c_ * 4); asm volatile("" : "+v"(ca[(u) & 1]), "+v"(fa[(u) & 1])); } while (0)
#define RDIN(dst, a, f_) asm volatile("ds_read_b32 %0, %5\n\tds_read_b128 %1, %6\n\tds_read_b128 %2, %6 offset:1024\n\tds_read_b128 %3, %6 offset:2048\n\tds_read_b128 %4, %6 offset:3072" \
                     : "=&v"(fl), "=&v"(dst[0]), "=&v"(dst[1]), "=&v"(dst[2]), "=&v"(dst[3]) : "v"(f_), "v"(a) : "memory")
#define LWAIT(dst) asm volatile("s_waitcnt lgkmcnt(0)" : "+v"(fl), "+v"(dst[0]), "+v"(dst[1]), "+v"(dst[2]), "+v"(dst[3]) :: "memory")
        CHAIN(0); CHAIN(1);
        WAIT_LGKM0();
        CPIN(0); CPIN(1); CPIN(2);
        CREAD(3);
        int tr[2] = {TR(0), TR(1)};
        unsigned gw[2][2];
        {
            unsigned spins = 0;
            while (__builtin_expect(!__all((gv[0].y == tag) & (gv[0].w == tag) & (gv[1].y == tag) & (gv[1].w == tag)), 0)) {
                if (++spins > SPIN_MAX) break;
#pragma unroll
                for (int r = 0; r < 2; ++r) asm volatile("s_nop 4\n\tbuffer_load_dwordx4 %0, %1, %2, %3 offen sc1\n\ts_waitcnt vmcnt(0)" : "=&v"(gv[r]) : "v"(GV(q4)), "s"(gs), "s"(__builtin_amdgcn_readfirstlane(GSO(r))) : "memory");
            }
            gw[0][0] = gv[0].x; gw[0][1] = gv[0].z; gw[1][0] = gv[1].x; gw[1][1] = gv[1].z;
            PIN4(gw[0][0], gw[0][1], gw[1][0], gw[1][1]);
        }
        unsigned cpv = 0, yy[2][4];
        f32x2 zz[2][4];
        unsigned exo = 0, exi = 0, efo = 0, efi = 0;
        auto send = [&](int r) __attribute__((always_inline)) {
            const unsigned one = 1u;
#pragma unroll
            for (int n = 0; n < 4; ++n) { const f32x2 v = {ac[r][n][2], ac[r][n][3]}; asm volatile("ds_write_b64 %0, %1 offset:%2" :: "v"(exo), "v"(v), "i"(r * 4096 + n * 512) : "memory"); }
            asm volatile("ds_write_b32 %0, %1 offset:%2" :: "v"(efo), "v"(one), "i"(r * 4) : "memory");
        };
        STAMPT(dbg, 5, 0);
        auto body = [&](auto F) __attribute__((always_inline)) {
            constexpr int f = decltype(F)::value;
            constexpr int u = f >> 3, k = f & 7;
            f32x4* an = ac[u & 1];
            f32x4* ao = ac[(u + 1) & 1];
#pragma unroll
            for (int n = 0; n < 4; ++n) {
                if (k == 0 && u < 2) MF0(an[n], af[f % DEPTH], cf_[k][n]); else MF(an[n], af[f % DEPTH], cf_[k][n]);
                SCHED();
                const int g = k * 4 + n;
                if (u >= 1 && u < 7) {
                    if (g == 1) { fv = ((u - 1) >> 1) + 1; asm volatile("" : "+v"(fv)); }
                    if (g == 4 || g == 6 || g == 8 || g == 10) asm volatile("ds_write_b128 %0, %1 offset:%2" :: "v"(ca[(u + 1) & 1]), "v"(ao[(g - 4) / 2]), "i"((g - 4) * 512) : "memory");
                    if (g == 12) asm volatile("ds_write_b32 %0, %1" :: "v"(fa[(u + 1) & 1]), "v"(fv) : "memory");
                    if (g == 18) CHAIN(u + 1);
                    if (g == 20) asm volatile("ds_read_b32 %0, %1" : "=&v"(fl) : "v"(fa[(u + 1) & 1]) : "memory");
                    if (g > 20 && g <= 24) asm volatile("ds_read_b128 %0, %1 offset:%2" : "=&v"(ao[g - 21]) : "v"(ca[(u + 1) & 1]), "i"((g - 21) * 1024) : "memory");
                    if (g == 30) {
                        LWAIT(ao);
                        if (__builtin_expect(__builtin_amdgcn_readfirstlane(fl) != (unsigned)((u + 1) >> 1), 0)) {
                            unsigned spins = 0;
#pragma unroll 1
                            do {
                                __builtin_amdgcn_s_sleep(1);
                                RDIN(ao, ca[(u + 1) & 1], fa[(u + 1) & 1]); LWAIT(ao);
                                if (++spins > SPIN_MAX * 16u) break;
                            } while (__builtin_amdgcn_readfirstlane(fl) != (unsigned)((u + 1) >> 1));
                        }
                    }
                }
                if (u == 7 && g == 6) send(0);
                if (f >= ZF && f < ZF + 4) {
                    constexpr int r = ((f - ZF) >> 1) & 1, j = (f - ZF) & 1;
                    const unsigned xw = xx[r][j][n >> 1], dw = dk2[n >> 1];
                    const float xf = (n & 1) ? hi16(xw) : lo16(xw), df = (n & 1) ? hi16(dw) : lo16(dw), yf = j ? hi16(yy[r][n]) : lo16(yy[r][n]);
                    zz[r][n][j] = xf * fmaf(hi16(gw[r][j]), yf, lo16(gw[r][j]) * df);
                    asm volatile("" : "+v"(zz[r][n]));
                }
                if (f == ZF + 4) {
                    zz[0][n] = (f32x2){fmaf(lo16(gw[0][0]), ac[0][n][0], zz[0][n][0]), fmaf(lo16(gw[0][1]), ac[0][n][1], zz[0][n][1])};
                    asm volatile("" : "+v"(zz[0][n]));
                }
                SCHED();
            }
            if (f + DEPTH < 64) LOADF(f + DEPTH);
            if (f == DEPTH + 1 && ring) {
                WAIT_VM(6);
                if (lane == 0) __hip_atomic_store((gu32*)(ws + WS_CFLAG) + cb * 32 + (wid & 3), tag, RLX_AGENT);
                SCHED();
            }
            if (f == YPOLL && ring) { const int l15 = fresh_lane() & 15; cpv = __hip_atomic_load((gu32*)(ws + WS_CFLAG) + (nb * 16 + (wid & 3) * 4 + (l15 >> 2)) * 32 + (l15 & 3), RLX_AGENT); SCHED(); }
            if (f == YPOLL + DEPTH && ring) {
                const int ln = fresh_lane(), l15 = ln & 15, q4 = ln >> 4;
                if (__builtin_expect(!__all(cpv == tag), 0)) {
                    unsigned spins = 0;
                    while (!__all(__hip_atomic_load((gu32*)(ws + WS_CFLAG) + (nb * 16 + (wid & 3) * 4 + (l15 >> 2)) * 32 + (l15 & 3), RLX_AGENT) == tag)) {
                        if (++spins > SPIN_MAX) break;
                        __builtin_amdgcn_s_sleep(1);
                    }
                }
                const unsigned lx = lbase + LA_OFF + (wid & 3) * 8192;
#pragma unroll
                for (int i = 0; i < 4; ++i) {
                    const unsigned vo = (unsigned)((d_base + (wid & 3) * 16 + i * 4 + q4) * 4096 + tb * 256 + ((l15 ^ ((wid & 3) * 4 + i)) * 16));
                    GLDS_SC1(y2T, vo, lx, i * 1024);
                }
                SCHED();
            }
            if (f == YPOLL + 2 * DEPTH + 1 && ring) {
                WAIT_VM(6);
                SETW(YFLAG_OFF, wid & 3, 1u);
                SCHED();
            }
            if (f == 55) {
                const int ln = fresh_lane(), c_ = (wid - 3) & 3;
                const int eg = GA_OFF + c_ * 8192, el = KC_OFF + c_ * CV_W;
                exo = (unsigned)((ring ? el : eg) + ln * 8);
                exi = (unsigned)((ring ? eg : el) + ln * 8);
                efo = (unsigned)(EF_OFF + wid * 8); efi = (unsigned)(EF_OFF + (wid ^ 4) * 8);
                PIN4(exo, exi, efo, efi);
                SCHED();
            }
            if (f == ZF - 2) {
                const int ln = fresh_lane(), l15 = ln & 15, q4 = ln >> 4;
                wait_lds4(smem, YFLAG_OFF);
#pragma unroll
                for (int r = 0; r < 2; ++r) {
                    const int jb = JB(6 + r);
                    const int ya = LA_OFF + (l15 >> 2) * 8192 + (l15 & 3) * 1024 + (((((jb >> 1) & 1) * 8 + (jb >> 2) + q4 * 2) ^ l15) * 16) + (jb & 1) * 8 + ring * 4;
#pragma unroll
                    for (int e = 0; e < 4; ++e) yy[r][e] = *(const unsigned*)(smem + ya + e * 256);
                }
                SCHED();
            }
        };
        unroll<64>(body);
        PAD8(ac[1][0], ac[1][1], ac[1][2], ac[1][3]);
        send(1);
        STAMPT(dbg, 6, 0);
        const __amdgpu_buffer_rsrc_t ors = __builtin_amdgcn_make_buffer_rsrc(out, 0, SEQ * DM * 4, 0x00020000);
        const unsigned ov = (unsigned)((lane >> 4) * (32 * DM * 4) + (lane & 15) * 16);
#pragma unroll
        for (int r = 0; r < 2; ++r) {
            f32x2 in[4];
#define EXRD() asm volatile("ds_read_b32 %0, %5 offset:%7\n\tds_read_b64 %1, %6 offset:%8\n\tds_read_b64 %2, %6 offset:%9\n\tds_read_b64 %3, %6 offset:%10\n\tds_read_b64 %4, %6 offset:%11" \
                             : "=&v"(fl), "=&v"(in[0]), "=&v"(in[1]), "=&v"(in[2]), "=&v"(in[3]) \
                             : "v"(efi), "v"(exi), "i"(r * 4), "i"(r * 4096), "i"(r * 4096 + 512), "i"(r * 4096 + 1024), "i"(r * 4096 + 1536) : "memory")
            EXRD();
            if (r == 0) {
#pragma unroll
                for (int n = 0; n < 4; ++n) {
                    zz[1][n] = (f32x2){fmaf(lo16(gw[1][0]), ac[1][n][0], zz[1][n][0]), fmaf(lo16(gw[1][1]), ac[1][n][1], zz[1][n][1])};
                    asm volatile("" : "+v"(zz[1][n]));
                }
            }
            LWAIT(in);
            if (__builtin_expect(__builtin_amdgcn_readfirstlane(fl) == 0u, 0)) {
                unsigned spins = 0;
#pragma unroll 1
                do {
                    EXRD(); LWAIT(in);
                    if (++spins > SPIN_MAX * 16u) break;
                } while (__builtin_amdgcn_readfirstlane(fl) == 0u);
            }
            f32x4 o[2];
#pragma unroll
            for (int j = 0; j < 2; ++j)
#pragma unroll
                for (int e = 0; e < 4; ++e) o[j][e] = fmaf(lo16(gw[r][j]), in[e][j], zz[r][e][j]);
#pragma unroll
            for (int j = 0; j < 2; ++j)
                __builtin_amdgcn_raw_buffer_store_b128(__builtin_bit_cast(u32x4, o[j]), ors, ov + (unsigned)(((tr[r] + j) * DM + d_base) * 4), 0, WT);
        }
        STAMPT(dbg, 7, 0);
        STAMPT(dbg, 16, 384);
        if (tb == 15 && nb == 0 && wid == 0 && lane == 0) __hip_atomic_store((gu32*)(ws + WS_EPOCH), tag, RLX_AGENT);
    }
}

extern "C" void kernel_launch(void* const* d_in, const int* in_sizes, int n_in,
                              void* d_out, int out_size, void* d_ws, size_t ws_size,
                              hipStream_t stream) {
    const u16* x = (const u16*)d_in[0];
    const u16* lre = (const u16*)d_in[1];
    const u16* lim = (const u16*)d_in[2];
    const u16* Bre = (const u16*)d_in[3];
    const u16* Bim = (const u16*)d_in[4];
    const u16* Cre = (const u16*)d_in[5];
    const u16* Cim = (const u16*)d_in[6];
    const u16* logdt = (const u16*)d_in[7];
    const u16* dskip = (const u16*)d_in[8];
    const u16* kin = (const u16*)d_in[10];
    const u16* vin = (const u16*)d_in[11];
    const u16* shiftk = (const u16*)d_in[12];
    const u16* ssmk = (const u16*)d_in[13];
    const u16* ssmD = (const u16*)d_in[14];
    const u16* gatew = (const u16*)d_in[15];
    float* out = (float*)d_out;

    char* ws = (char*)d_ws;

    hipFuncSetAttribute((const void*)k1_kernel, hipFuncAttributeMaxDynamicSharedMemorySize, K1_LDS);

    k1_kernel<<<256, 512, K1_LDS, stream>>>(x, Bre, Bim, kin, vin, shiftk, gatew, ws, lre, lim, logdt, ssmk, ssmD, Cre, Cim, dskip, out);
}
#if defined(__HIP_DEVICE_COMPILE__)
#pragma clang attribute pop
#endif
```

```cpp
#if defined(__HIP_DEVICE_COMPILE__)
#pragma clang attribute push(__attribute__((target("no-packed-fp32-ops"))), apply_to = function)
#endif
#include <hip/hip_runtime.h>
#include <hip/hip_bf16.h>
#include <stdint.h>

#define TIDX ((int)__builtin_amdgcn_workitem_id_x())
#define BIDX ((int)__builtin_amdgcn_workgroup_id_x())
typedef unsigned short u16;
typedef __attribute__((ext_vector_type(8))) short bf16x8;
typedef __attribute__((ext_vector_type(4))) float f32x4;
typedef __attribute__((ext_vector_type(2))) float f32x2;
typedef __attribute__((ext_vector_type(2))) __bf16 bf16x2;
typedef __attribute__((ext_vector_type(4))) unsigned u32x4;
typedef __attribute__((ext_vector_type(2))) unsigned u32x2;
typedef __attribute__((address_space(1))) unsigned long long gu64;
typedef __attribute__((address_space(1))) unsigned gu32;
typedef __attribute__((address_space(3))) void lds_void;
typedef __attribute__((address_space(3))) unsigned lds_u32;
typedef __attribute__((ext_vector_type(4))) short s16x4;
typedef __attribute__((address_space(3))) s16x4 lds_s16x4;
#define TRD(a) __builtin_amdgcn_ds_read_tr16_b64_v4i16((lds_s16x4*)(size_t)(a))

#define DBGON 0
#define STAMPT(buf, k, t) do { } while (0)
#define CODE_LINES 264
#define SEQ 2048
#define DM 1024
#define SPIN_MAX (1u << 18)
#define PRIO_G 2
#define PSLEEP 4
#define PRIO_L 3
#define WT 16

__device__ __forceinline__ float bf2f(u16 v) { return __uint_as_float(((unsigned)v) << 16); }
__device__ __forceinline__ u16 f2bf(float f) { __bf16 b = (__bf16)f; return __builtin_bit_cast(u16, b); }
__device__ __forceinline__ float rbf(float f) { return bf2f(f2bf(f)); }
__device__ __forceinline__ unsigned pkbf(float lo, float hi) { return __builtin_bit_cast(unsigned, __builtin_convertvector((f32x2){lo, hi}, bf16x2)); }
__device__ __forceinline__ float lo16(unsigned u) { return __uint_as_float(u << 16); }
__device__ __forceinline__ float hi16(unsigned u) { return __uint_as_float(u & 0xffff0000u); }

struct cf { float r, i; };
__device__ __forceinline__ cf cmul(cf a, cf b) { return {a.r * b.r - a.i * b.i, a.r * b.i + a.i * b.r}; }
__device__ __forceinline__ cf cfma(cf a, cf b, cf c) {
    return {fmaf(a.r, b.r, fmaf(-a.i, b.i, c.r)), fmaf(a.r, b.i, fmaf(a.i, b.r, c.i))};
}
__device__ __forceinline__ f32x4 splat4(float v) { return (f32x4){v, v, v, v}; }
__device__ __forceinline__ f32x4 fma4(f32x4 a, f32x4 b, f32x4 c) { return __builtin_elementwise_fma(a, b, c); }
__device__ __forceinline__ f32x4 mul4(f32x4 a, f32x4 b) { return a * b; }

__device__ __forceinline__ float red32(float a, float b) {
    const auto r = __builtin_amdgcn_permlane32_swap(__float_as_uint(a), __float_as_uint(b), false, false);
    return __uint_as_float(r[0]) + __uint_as_float(r[1]);
}
__device__ __forceinline__ float red16(float x) {
    const auto r = __builtin_amdgcn_permlane16_swap(__float_as_uint(x), __float_as_uint(x), false, false);
    return __uint_as_float(r[0]) + __uint_as_float(r[1]);
}
__device__ __forceinline__ int fresh_lane() {
    int l;
    asm volatile("v_mbcnt_lo_u32_b32 %0, -1, 0\n\tv_mbcnt_hi_u32_b32 %0, -1, %0" : "=v"(l));
    return l;
}
#define BAR() __builtin_amdgcn_s_barrier()
#define WAIT_VM(n) asm volatile("s_waitcnt vmcnt(" #n ")" ::: "memory")
#define WAIT_LGKM0() __builtin_amdgcn_s_waitcnt(0xC07F)
#define SCHED() __builtin_amdgcn_sched_barrier(0)
#define MF(c, a, b) asm volatile("v_mfma_f32_16x16x32_bf16 %0, %1, %2, %0" : "+v"(c) : "v"(a), "v"(b))
#define PAD8(c0, c1, c2, c3) asm volatile("s_nop 7" : "+v"(c0), "+v"(c1), "+v"(c2), "+v"(c3))
#define PIN4(c0, c1, c2, c3) asm volatile("" : "+v"(c0), "+v"(c1), "+v"(c2), "+v"(c3))
#define GLDS_S(sbase, voff, sl, IMM) \
    asm volatile("s_add_u32 m0, %2, %3\n\ts_nop 0\n\tglobal_load_lds_dwordx4 %0, %1" \
                 :: "v"(voff), "s"(sbase), "s"(sl), "i"(IMM) : "memory", "scc")
#define GLDS_K(sbase, voff, sl, IMM) \
    asm volatile("s_add_u32 m0, %2, %3\n\ts_nop 0\n\tglobal_load_lds_dwordx4 %0, %1 nt" \
                 :: "v"(voff), "s"(sbase), "s"(sl), "i"(IMM) : "memory", "scc")
#define RLX_AGENT __ATOMIC_RELAXED, __HIP_MEMORY_SCOPE_AGENT

#define UNITS_A 0x0756075607560560ull
#define UNITS_B 0x1432143214321432ull
#define RA(t) ((int)((UNITS_A >> (4 * (t))) & 15) * 16384)
#define RB(t) ((int)((UNITS_B >> (4 * (t))) & 15) * 16384)
#define LA_OFF 0
#define GA_OFF 32768
#define CV_COPY 4160
#define CV_W (4 * CV_COPY)
#define KC_OFF 65536
#define ST_OFF 114688
#define ST_V 8832
#define ST_SZ (ST_V + 8 * 1056)
#define U_OFF (KC_OFF + 16 * CV_COPY)
#define HT_OFF (U_OFF + 256 + 16384)
#define GW_OFF (HT_OFF + 8192)
#define TAG_OFF (GW_OFF + 2048)
#define UFLAG_OFF (TAG_OFF + 16)
#define G1D_OFF (UFLAG_OFF + 16)
#define MISC_OFF (G1D_OFF + 16)
#define YFLAG_OFF (MISC_OFF + 16)
#define RF_OFF (YFLAG_OFF + 16)
#define EF_OFF (RF_OFF + 64)
#define COK_OFF (EF_OFF + 64)
#define NFLAGW 52
#define DUMMY_OFF (COK_OFF + 16)
#define K1_LDS (DUMMY_OFF + 256)
#define WS_GRAN 0
#define WS_KVFLAG (256u << 10)
#define WS_EPOCH (WS_KVFLAG + 65536)
#define WS_CFLAG (WS_EPOCH + 4096)
#define WS_HFLAG (384u << 10)
#define WS_GATE (512u << 10)

#define CODE_TOUCH(vl) asm volatile("s_getpc_b64 s[98:99]\n\ts_add_u32 s98, s98, k1_kernel@rel32@lo+4\n\ts_addc_u32 s99, s99, k1_kernel@rel32@hi+12\n\t" \
                                    "s_mov_b32 m0, %1\n\ts_nop 0\n\tglobal_load_lds_dword %0, s[98:99]" :: "v"((unsigned)(vl) * 128u), "i"(DUMMY_OFF) : "s98", "s99", "scc", "memory")
#define RD128(a, off) (*(const __attribute__((address_space(3))) bf16x8*)(size_t)(unsigned)((a) + (off)))
#define RDA(dst, A, F) do { const s16x4 lo_ = TRD((A) + 512 * (F) + 8); SCHED(); const s16x4 hi_ = TRD((A) + 512 * (F)); SCHED(); \
                            dst = __builtin_shufflevector(lo_, hi_, 0, 1, 2, 3, 4, 5, 6, 7); } while (0)
#define TOUCH(sbase, voff) asm volatile("s_mov_b32 m0, %2\n\ts_nop 0\n\tglobal_load_lds_dword %0, %1" :: "v"(voff), "s"(sbase), "i"(DUMMY_OFF) : "memory")
#define FLAGD(off) do { unsigned long long pc_; asm volatile("s_getpc_b64 %0\n\ts_mov_b32 m0, %2\n\ts_mov_b64 exec, 1\n\tglobal_load_lds_dword %1, %0\n\ts_mov_b64 exec, -1" \
                                                             : "=&s"(pc_) : "v"(0u), "s"(__builtin_amdgcn_readfirstlane(off)) : "memory"); } while (0)
#define GLDS_SC1(sbase, voff, sl, IMM) \
    asm volatile("s_add_u32 m0, %2, %3\n\ts_nop 0\n\tglobal_load_lds_dwordx4 %0, %1 sc1" \
                 :: "v"(voff), "s"(sbase), "s"(sl), "i"(IMM) : "memory", "scc")
__device__ __forceinline__ unsigned mv(unsigned v) { unsigned r; asm("v_mov_b32 %0, %1" : "=v"(r) : "v"(v)); return r; }
__device__ __forceinline__ unsigned al(unsigned hi, unsigned lo) { unsigned r; asm volatile("v_alignbit_b32 %0, %1, %2, 16" : "=v"(r) : "v"(hi), "v"(lo)); return r; }
__device__ __forceinline__ u32x2 pred2(const u32x4& a, unsigned f0, unsigned f1) {
    return (u32x2){(unsigned)__builtin_amdgcn_update_dpp((int)f0, (int)a.z, 0x138, 0xf, 0xf, false), (unsigned)__builtin_amdgcn_update_dpp((int)f1, (int)a.w, 0x138, 0xf, 0xf, false)};
}
#define RL63(v) ((unsigned)__builtin_amdgcn_readlane((int)(v), 63))
__device__ __forceinline__ void wait_lds4(char* smem, int off) {
    const unsigned a = (unsigned)(size_t)(lds_void*)smem + off;
    unsigned spins = 0;
    for (;;) {
        u32x4 f;
        asm volatile("ds_read_b128 %0, %1\n\ts_waitcnt lgkmcnt(0)" : "=&v"(f) : "v"(a) : "memory");
        if (__builtin_amdgcn_readfirstlane((f.x != 0u) & (f.y != 0u) & (f.z != 0u) & (f.w != 0u))) break;
        if (++spins > SPIN_MAX * 16u) break;
        __builtin_amdgcn_s_sleep(1);
    }
}
__device__ __forceinline__ unsigned lds_word(char* smem, int off) {
    const unsigned a = (unsigned)(size_t)(lds_void*)smem + off;
    unsigned f;
    asm volatile("ds_read_b32 %0, %1\n\ts_waitcnt lgkmcnt(0)" : "=&v"(f) : "v"(a) : "memory");
    return __builtin_amdgcn_readfirstlane(f);
}
__device__ __forceinline__ void wait_lds1(char* smem, int off) {
    unsigned spins = 0;
    while (lds_word(smem, off) == 0u) {
        if (++spins > SPIN_MAX * 16u) break;
        __builtin_amdgcn_s_sleep(1);
    }
}
#define SETW(off, i, v) __hip_atomic_store((lds_u32*)(smem + (off)) + (i), (v), __ATOMIC_RELAXED, __HIP_MEMORY_SCOPE_WORKGROUP)

#define PRIO_CV 1
#define PRIO_KV 3
#define CS0 1
#define CS1 3
#define CS2 5
#define XE 4
#define YPOLL 32
#define ZF 59
template <int I> struct IC { static constexpr int value = I; };
template <int N, int I = 0, class F> __device__ __forceinline__ void unroll(F& f) { if constexpr (I < N) { f(IC<I>{}); unroll<N, I + 1>(f); } }
#define MF0(c, a, b) asm volatile("v_mfma_f32_16x16x32_bf16 %0, %1, %2, 0" : "=&v"(c) : "v"(a), "v"(b))
#define DEPTH 6
#define PRIO2_G 0
#define PRIO2_L 1
__device__ __forceinline__ cf pick4(int q, cf a1, cf a2, cf a3) {
    return {q == 0 ? 1.f : q == 1 ? a1.r : q == 2 ? a2.r : a3.r, q == 0 ? 0.f : q == 1 ? a1.i : q == 2 ? a2.i : a3.i};
}
__device__ __forceinline__ void idle_mfma(unsigned a, unsigned b, int n) {
    u32x4 p = {a * 2654435761u, b * 40503u, a * 2246822519u, b * 3266489917u};
    p = (p & 0xbfffbfffu) | 0x3c003c00u;
    u32x4 q = p.yzwx ^ 0x01230456u;
    f32x4 w;
    asm volatile("s_nop 1" : "+v"(p), "+v"(q));
    MF0(w, p, q);
#pragma unroll 1
    for (int i = 0; i < n; ++i) { MF(w, q, p); MF(w, p, p); MF(w, q, q); MF(w, p, q); }
    asm volatile("s_nop 7" : "+v"(w));
}
#define PINC(c) asm volatile("" : "+v"((c).r), "+v"((c).i))

extern "C" __global__ __launch_bounds__(512, 2) void k1_kernel(
        const u16* __restrict__ x, const u16* __restrict__ Bre, const u16* __restrict__ Bim,
        const u16* __restrict__ kin, const u16* __restrict__ vin, const u16* __restrict__ shiftk, const u16* __restrict__ gatew, char* ws,
        const u16* __restrict__ lre, const u16* __restrict__ lim, const u16* __restrict__ logdt,
        const u16* __restrict__ ssmk, const u16* __restrict__ ssmD,
        const u16* __restrict__ Cre, const u16* __restrict__ Cim, const u16* __restrict__ dskip, float* __restrict__ out) {
    extern __shared__ __attribute__((aligned(16))) char smem[];
#define h ((u16*)(ws + (1u << 20)))
#define kvT ((u16*)(ws + (9u << 20)))
#define y2T ((u16*)(ws + (13u << 20)))
#define dbg ((unsigned long long*)(ws + (64u << 20)))
    const int tid = TIDX;
    if (tid < 40 && tid + ((BIDX >> 3) & 7) * 40 < CODE_LINES) CODE_TOUCH(tid + ((BIDX >> 3) & 7) * 40);
    const int lane = tid & 63;
    int wid = __builtin_amdgcn_readfirstlane(tid >> 6);
    const int b = BIDX;
    const int xcd = b & 7, idx = b >> 3;
    const int tb = ((xcd >> 1) ^ (xcd >> 2)) * 4 + (idx & 3), nb = (xcd & 1) * 8 + (idx >> 2);
    const int cb = xcd * 32 + idx;
    const int t_base = tb * 128, p_base = nb * 64, d_base = nb * 64;
    const int l15 = lane & 15, q4 = lane >> 4;
    unsigned stv = 0, sth = 0;
    STAMPT(dbg, 0, 0);
    const unsigned lbase = (unsigned)(size_t)(lds_void*)smem;
    const int rot1 = (xcd >> 1) * 4 + (xcd & 1) * 8 + (idx >> 2) + (idx & 1);
    unsigned voA[4] = {0u, 0u, 0u, 0u}, voB[4] = {0u, 0u, 0u, 0u}, lw0 = 0;
    if (wid == 4 || wid == 5) {
        __builtin_amdgcn_s_setprio(PRIO_L);
        const int lh = wid - 4;
        const unsigned ch = (unsigned)(((lane & 7) ^ (lane >> 4)) * 16);
        voA[0] = (unsigned)(t_base + lh * 16 + (lane >> 5) * 32 + ((lane >> 3) & 3)) * 2048u + ch;
        voA[1] = (voA[0] ^ 64u) + (131072u - 1024u);
        voA[2] = voA[0] + 6144u; voA[3] = voA[1] + 6144u;
        voB[0] = (unsigned)(p_base + lh * 32 + (lane >> 3)) * 2048u + ch;
        voB[1] = (voB[0] ^ 64u) + (16384u - 1024u);
        voB[2] = voB[0] + 30720u; voB[3] = voB[1] + 30720u;
        PIN4(voA[0], voA[1], voA[2], voA[3]); PIN4(voB[0], voB[1], voB[2], voB[3]);
#define GLDS4(sbase, v0, v1, v2, v3, sl, IMM) \
            asm volatile("s_add_u32 m0, %5, %6\n\ts_nop 0\n\tglobal_load_lds_dwordx4 %0, %4\n\tglobal_load_lds_dwordx4 %1, %4 offset:1024\n\t" \
                         "global_load_lds_dwordx4 %2, %4 offset:2048\n\tglobal_load_lds_dwordx4 %3, %4 offset:3072" \
                         :: "v"(v0), "v"(v1), "v"(v2), "v"(v3), "s"(sbase), "s"(sl), "i"(IMM) : "memory", "scc")
#define STAGE_A(kt, sl) do { const char* sa_ = (const char*)x + (((kt) + rot1) & 15) * 128; \
            GLDS4(sa_, voA[0], voA[1], voA[2], voA[3], sl, 0); GLDS4(sa_ + 16384, voA[0], voA[1], voA[2], voA[3], sl, 4096); } while (0)
#define STAGE_B(kt, sl) do { const int kr_ = (((kt) + rot1) & 15) * 128; const char* sb_ = (const char*)Bre + kr_; const char* sc_ = (const char*)Bim + kr_; \
            GLDS4(sb_, voB[0], voB[1], voB[2], voB[3], sl, 0); GLDS4(sc_, voB[0], voB[1], voB[2], voB[3], sl, 4096); } while (0)
        lw0 = lbase + lh * 8192;
        STAGE_A(0, lw0 + RA(0)); STAGE_B(0, lw0 + RB(0));
        idle_mfma(voA[0], voB[1], 12);
        STAGE_A(1, lw0 + RA(1)); STAGE_B(1, lw0 + RB(1));
    }
    const int sblk = b >> 3, dblk = b & 7;
    u32x2 tw[4];
    unsigned epoch;
    asm volatile("" : "=v"(tw[0]), "=v"(tw[1]), "=v"(tw[2]), "=v"(tw[3]), "=v"(epoch));
    if (wid >= 6) {
        __builtin_amdgcn_s_setprio(PRIO_L);
        const int vw = wid - 6;
        const int dch = dblk * 128 + vw * 64 + l15;
        const unsigned lk = lbase + ST_OFF + vw * ST_SZ;
        const unsigned vk = (unsigned)((lane >> 3) * DM * 2 + (((lane & 7) ^ ((lane >> 4) & 1)) * 16));
        const char* kb = (const char*)kin + (size_t)(sblk * 64 - 4) * (DM * 2) + dblk * 256 + vw * 128;
#define KLD(n) GLDS_K(kb + (n) * 8 * DM * 2, vk, lk, (n) * 1024 + ((n) >> 1) * 32)
#define VLD(n) GLDS_K(vb + (((n) >> 1) * 16 + ((n) & 1) * 2) * DM * 2, vv_, lk, ST_V + (n) * 1056)
        if (vw == 1) {
            const unsigned vg = (unsigned)((lane * 16 + rot1 * 128) & 2047), lg = lbase + GW_OFF;
            GLDS_S(gatew, vg, lg, 0); GLDS_S(gatew, vg ^ 1024u, lg, 1024);
        }
        asm volatile("global_load_dwordx2 %0, %5, %7\n\tglobal_load_dwordx2 %1, %5, %7 offset:128\n\tglobal_load_dwordx2 %2, %5, %7 offset:256\n\t"
                     "global_load_dwordx2 %3, %5, %7 offset:384\n\tglobal_load_dword %4, %6, %8 sc1"
                     : "+v"(tw[0]), "+v"(tw[1]), "+v"(tw[2]), "+v"(tw[3]), "+v"(epoch) : "v"((unsigned)(dch * 8)), "v"((unsigned)WS_EPOCH), "s"(shiftk), "s"(ws) : "memory");
        if (sblk == 0 && lane < 32) *(u32x4*)(smem + ST_OFF + vw * ST_SZ + lane * 16) = (u32x4){0u, 0u, 0u, 0u};
        if (sblk != 0 || lane >= 32) KLD(0);
        KLD(1); KLD(2); KLD(3); KLD(4); KLD(5); KLD(6); KLD(7);
        if (lane < 32) KLD(8);
        __builtin_amdgcn_s_setprio(0);
    }
    asm volatile("" : "+s"(wid));

#define WSL(w) ((((w) >> 2) ^ (nb >> 3)) * 4 + ((w) & 3))
#define COFF(w, ti) ((2 * WSL(w) + ((ti) >> 1)) * 128 + ((ti) & 1) * 4096)
    bf16x8 cf_[8][4];
    bf16x8 af[DEPTH];
    int cra, crb;
#define CT_SETUP() const unsigned vB0 = (unsigned)((d_base + (lane >> 3) * 4) * 2048 + (((lane & 7) ^ (lane >> 4)) * 16)), vB1 = (vB0 ^ 64u) + 65536u - 1024u
#define CTILE(w, ti, dst) do { const int co_ = COFF(w, ti); const unsigned ds_ = (dst); \
                               GLDS4((const char*)Cre + co_, vB0, vB1, vB0, vB1, ds_, 0); GLDS4((const char*)Cim + co_, vB0, vB1, vB0, vB1, ds_, 4096); } while (0)
#define CF(ti, n) cf_[4 * ((ti) >> 1) + ((n) >> 1)][2 * ((ti) & 1) + ((n) & 1)]
#define CRD(ti, n) CF(ti, n) = RD128((((n) & 4) ? crb : cra) ^ (((n) & 2) * 16), ((n) & 1) * 2048)
#define CREAD(ti) do { _Pragma("unroll") for (int n_ = 0; n_ < 8; ++n_) CRD(ti, n_); } while (0)
#define CPIN(ti) do { PIN4(CF(ti, 0), CF(ti, 1), CF(ti, 2), CF(ti, 3)); PIN4(CF(ti, 4), CF(ti, 5), CF(ti, 6), CF(ti, 7)); } while (0)
#define CRA(base) ((base) + (q4 >> 1) * 4096 + l15 * 128 + (((q4 & 1) ^ ((l15 >> 1) & 7)) * 16))
    const int rot2 = (idx & 16) >> 2;
#define JB(u) (((((u) ^ (rot2 >> 2)) & 1) << 2) | ((wid - ((u) >> 1) + rot2) & 3))
    const __amdgpu_buffer_rsrc_t hs = __builtin_amdgcn_make_buffer_rsrc((char*)h + (size_t)tb * 524288 + WSL(wid) * 8192, 0, 524288, 0x00020000);
#define LOADF(f) do { af[(f) % DEPTH] = __builtin_bit_cast(bf16x8, __builtin_amdgcn_raw_buffer_load_b128(hs, vh + ((f) & 3) * 1024, JB((f) >> 3) * 65536 + ((f) & 4) * 1024, 16)); SCHED(); } while (0)
#define HPOLL_SETUP() gu32* hfp = (gu32*)(ws + WS_HFLAG) + (tb * 16 + 2 * WSL(wid) + (lane & 1)) * 32
#define HWAIT() do { unsigned spins = 0; \
            while (!__all(__hip_atomic_load(hfp, RLX_AGENT) == tag)) { if (++spins > SPIN_MAX) break; __builtin_amdgcn_s_sleep(PSLEEP); } asm volatile("" ::: "memory"); } while (0)

    const __amdgpu_buffer_rsrc_t gs = __builtin_amdgcn_make_buffer_rsrc(ws + WS_GATE, 0, 32768, 0x00020000);
#define GV(q4) ((unsigned)(((q4) >> 1) * 128 + ((q4) & 1) * 32))
#define GSO(r) (tb * 2048 + JB(6 + (r)) * 256 + (wid >> 2) * 16)
#define TR(r) (t_base + JB(6 + (r)) * 4 + (wid >> 2) * 2)
    u32x2 xx[2][2], dk2;
    u32x4 gv[2];
#define XLOAD() do { \
        const __amdgpu_buffer_rsrc_t xs = __builtin_amdgcn_make_buffer_rsrc((void*)x, 0, SEQ * DM * 2, 0x00020000); \
        const __amdgpu_buffer_rsrc_t ks = __builtin_amdgcn_make_buffer_rsrc((void*)dskip, 0, DM * 2, 0x00020000); \
        dk2 = __builtin_amdgcn_raw_buffer_load_b64(ks, (unsigned)(l15 * 8), d_base * 2, 0); \
        _Pragma("unroll") for (int q = 0; q < 4; ++q) \
            xx[q >> 1][q & 1] = __builtin_amdgcn_raw_buffer_load_b64(xs, (unsigned)(q4 * 32 * DM * 2 + l15 * 8 + (q & 1) * DM * 2), (TR(q >> 1) * DM + d_base) * 2, 0); \
        _Pragma("unroll") for (int r = 0; r < 2; ++r) gv[r] = __builtin_amdgcn_raw_buffer_load_b128(gs, GV(q4), GSO(r), 16); \
        SCHED(); } while (0)
#define XPIN() do { PIN4(xx[0][0], xx[0][1], xx[1][0], xx[1][1]); asm volatile("" : "+v"(dk2), "+v"(gv[0]), "+v"(gv[1])); } while (0)

    if (wid < 4) {
        typedef __attribute__((ext_vector_type(8))) unsigned u32x8;
        typedef const __attribute__((address_space(4))) u32x8 cu8;
        const int po = (p_base + wid * 16) * 2;
        u32x8 s0 = *(cu8*)((size_t)logdt + po), s1 = *(cu8*)((size_t)lre + po), s2 = *(cu8*)((size_t)lim + po);
        SCHED();
        const int pq = p_base + wid * 16 + l15;
        unsigned goff[4], gmask[4];
#pragma unroll
        for (int i = 0; i < 4; ++i) {
            goff[i] = (unsigned)(((tb - 1 - q4 - 4 * i) * 1024 + pq) * 16);
            gmask[i] = (tb - 1 - q4 - 4 * i >= 0) ? ~0u : 0u;
            asm volatile("" : "+v"(goff[i]), "+v"(gmask[i]));
        }
        unsigned gpub = q4 == 0 ? (unsigned)((tb * 1024 + pq) * 16) : 0x80000000u;
        unsigned hp = (unsigned)(tb * 524288 + (nb * 4 + wid) * 1024 + lane * 16);
        unsigned wa = (unsigned)(HT_OFF + wid * 2048 + (q4 * 16 + (l15 ^ ((q4 >> 1) * 4))) * 8);
        unsigned ra0 = (unsigned)(HT_OFF + wid * 2048 + ((q4 >> 1) * 64 + (l15 & 3) * 16 + (((q4 & 1) * 8 + (l15 >> 2)) ^ ((l15 & 2) * 2))) * 8), ra1 = ra0 ^ 32u;
        cra = CRA(GA_OFF + wid * 8192); crb = cra ^ 64;
        unsigned vB0 = (unsigned)((d_base + (lane >> 3) * 4) * 2048 + (((lane & 7) ^ (lane >> 4)) * 16)), vB1 = (vB0 ^ 64u) + 65536u - 1024u;
        PIN4(gpub, vB0, hp, wa); PIN4(ra0, ra1, cra, crb); asm volatile("" : "+v"(vB1));
        f32x4 acc[8][2];
#pragma unroll
        for (int m = 0; m < 8; m += 2) {
#pragma unroll
            for (int c = 0; c < 4; ++c) acc[m + (c >> 1)][c & 1] = (f32x4){0.f, 0.f, 0.f, 0.f};
            PIN4(acc[m][0], acc[m][1], acc[m + 1][0], acc[m + 1][1]);
        }
        SCHED();
        cf L1, Lq3, cc, L32, cLq, Lp, L512;
        {
            unsigned w0 = 0, w1 = 0, w2 = 0;
#pragma unroll
            for (int i = 0; i < 8; ++i) {
                asm("v_writelane_b32 %0, %3, %6\n\tv_writelane_b32 %1, %4, %6\n\tv_writelane_b32 %2, %5, %6" : "+v"(w0), "+v"(w1), "+v"(w2) : "s"(s0[i]), "s"(s1[i]), "s"(s2[i]), "i"(2 * i));
            }
            asm("s_nop 1" : "+v"(w0), "+v"(w1), "+v"(w2));
            auto spread = [&](unsigned w) {
                const unsigned e = (unsigned)__builtin_amdgcn_update_dpp(0, (int)w, 0xa0, 0xf, 0xf, false);
                const unsigned a = __builtin_amdgcn_permlane16_swap(e, e, false, false)[0];
                const unsigned c = __builtin_amdgcn_permlane32_swap(a, a, false, false)[0];
                return (l15 & 1) ? hi16(c) : lo16(c);
            };
            const float ldt = spread(w0), re = spread(w1), im = spread(w2);
            if (wid == 3 && lane < NFLAGW) *(unsigned*)(smem + UFLAG_OFF + lane * 4) = 0u;
            const float dt = rbf(expf(ldt));
            const float ea = expf(dt * re);
            float sn, cs;
            sincosf(dt * im, &sn, &cs);
            L1 = {ea * cs, ea * sn};
            const float nr = L1.r - 1.0f, ni = L1.i, den = re * re + im * im;
            cc = {2.f * ((nr * re + ni * im) / den), 2.f * ((ni * re - nr * im) / den)};
            const cf T2 = cmul(L1, L1), T4 = cmul(T2, T2), L8 = cmul(T4, T4), L16 = cmul(L8, L8);
            L32 = cmul(L16, L16);
            const cf L64 = cmul(L32, L32), L96 = cmul(L64, L32), L128 = cmul(L64, L64), L256 = cmul(L128, L128), L384 = cmul(L256, L128);
            L512 = cmul(L256, L256);
            Lp = pick4(q4, L128, L256, L384);
            Lq3 = pick4(3 - q4, L32, L64, L96);
            cLq = cmul(cc, pick4(q4, L32, L64, L96));
            PINC(L1); PINC(Lq3); PINC(cc); PINC(L32); PINC(cLq); PINC(Lp); PINC(L512);
        }
        __builtin_amdgcn_s_setprio(PRIO_G);
        unsigned tagv;
        cf S;
        STAMPT(dbg, 22, 0);
        const int swz = (l15 >> 1) & 7;
        {
            const int a0 = RA(0) + l15 * 128 + ((q4 ^ swz) * 16);
            const int b0 = RB(0) + ((wid >> 1) * 64 + (wid & 1) * 16 + l15) * 128 + ((q4 ^ swz) * 16);
            WAIT_LGKM0();
            idle_mfma(hp, wa, 2);
            BAR();
            asm volatile("" ::: "memory");
            STAMPT(dbg, 19, 64);
            bf16x8 FA[8][2], FB[2][2][2];
            int pa[2] = {a0, a0 ^ 64}, pb[2] = {b0, b0 ^ 64};
            PIN4(pa[0], pa[1], pb[0], pb[1]);
#define LGW(n) __builtin_amdgcn_s_waitcnt(0xC07F | ((n) << 8))
#define RA_(j) FA[(j) >> 1][(j) & 1] = RD128(pa[(j) & 1], ((j) >> 1) * 2048)
#define RB_(bu, j) FB[bu][(j) >> 1][((j) ^ ((j) >> 1)) & 1] = RD128(pb[((j) ^ ((j) >> 1)) & 1], ((j) >> 1) * 4096)
#define RN_(bu, i) do { if ((i) == 0) RB_(bu, 0); else if ((i) < 3) RA_((i) - 1); else if ((i) < 6) RB_(bu, (i) - 2); else RA_((i) - 4); } while (0)
#define G1_H(H, bu, RD, FILL) do { \
            _Pragma("unroll") for (int i = 0; i < 16; ++i) { \
                const int m = (H) * 4 + (i >> 2), n = ((i >> 1) ^ m) & 1, ks = (i ^ (i >> 1)) & 1; \
                if ((H) == 0) { if (i == 0) LGW(6); if (i == 4) LGW(8); if (i == 8) LGW(10); if (i == 12) LGW(8); } \
                SCHED(); \
                MF(acc[m][n], FA[m][ks], FB[bu][n][ks]); SCHED(); \
                if ((H) == 0 && i < 8) RA_(8 + i); \
                if ((H) == 1 && (RD) && i < 12) RN_((bu) ^ 1, i); \
                FILL((H) * 16 + i); \
                SCHED(); \
            } } while (0)
#define NOF(g) do { } while (0)
#pragma unroll
            for (int j = 0; j < 12; ++j) { RN_(0, j); SCHED(); }
            unsigned long long ua_ = UNITS_A;
            int sb = RB(0), sa = RA(0), nx, na;
#define ADV(g) do { if ((g) == 8) { ua_ >>= 4; na = (int)((unsigned)ua_ & 15u) << 14; asm volatile("" : "+s"(na), "+s"(ua_)); } \
                    if ((g) == 9) { const int o_ = sa; sa = na; na -= o_; asm volatile("" : "+s"(na), "+s"(sa)); } \
                    if ((g) == 10) { pa[0] += na; pa[1] += na; asm volatile("" : "+v"(pa[0]), "+v"(pa[1])); } \
                    if ((g) == 11) { nx = (sb == 65536) ? -49152 : 16384; sb += nx; asm volatile("" : "+s"(nx), "+s"(sb)); } \
                    if ((g) == 13) { pb[0] += nx; pb[1] += nx; asm volatile("" : "+v"(pb[0]), "+v"(pb[1])); } } while (0)
#define G1_T(bu) do { \
                G1_H(0, bu, true, ADV); \
                WAIT_LGKM0(); \
                asm volatile("" ::: "memory"); \
                BAR();                     \
                asm volatile("" ::: "memory"); \
                G1_H(1, bu, true, NOF); } while (0)
#pragma unroll 1
            for (int t2 = 0; t2 < 7; ++t2) { G1_T(0); G1_T(1); }
            G1_T(0);
#define AGG(g) do { if ((g) == 8) { asm volatile("" ::: "memory"); SETW(G1D_OFF, wid, 1u); }        \
                    if ((g) == 9) tagv = *(volatile const lds_u32*)(size_t)(unsigned)TAG_OFF; \
                    if ((g) >= 7) { const int t = (g) - 6; \
                        if (t == 1) { asm volatile("s_nop 1" : "+v"(acc[0][0]), "+v"(acc[0][1])); S = {acc[0][0][0], acc[0][1][0]}; } \
                        if ((t & 3) == 0) asm volatile("" : "+v"(acc[t >> 2][0]), "+v"(acc[t >> 2][1])); \
                        S = cfma(L1, S, cf{acc[t >> 2][0][t & 3], acc[t >> 2][1][t & 3]}); PINC(S); } } while (0)
            G1_H(0, 1, false, AGG);
            G1_H(1, 1, false, AGG);
        }
        STAMPT(dbg, 1, 0);
        const unsigned tag = __builtin_amdgcn_readfirstlane(tagv);
        const __amdgpu_buffer_rsrc_t grs = __builtin_amdgcn_make_buffer_rsrc(ws, 0, 15 * 16384, 0x00020000);
        {
#pragma unroll
            for (int t = 26; t < 32; ++t) {
                if (t == 28) { PINC(S); asm volatile("" : "+v"(acc[7][0]), "+v"(acc[7][1])); }
                S = cfma(L1, S, cf{acc[t >> 2][0][t & 3], acc[t >> 2][1][t & 3]});
            }
            const cf W = cmul(S, Lq3);
            const float e2 = red16(red32(W.r, W.i));
            const auto eo = __builtin_amdgcn_permlane32_swap(__float_as_uint(e2), __float_as_uint(e2), false, false);
            __builtin_amdgcn_raw_buffer_store_b128((u32x4){eo[0], tag, eo[1], tag}, grs, gpub, 0, 16);
        }
        SCHED();
        STAMPT(dbg, 2, 0);
        const unsigned cg = lbase + GA_OFF + wid * 8192;
        wait_lds1(smem, COK_OFF + wid * 4);
        CREAD(0);
        WAIT_LGKM0();
        CPIN(0);
        CTILE(wid, 1, cg);
        u32x4 gq[4];
#pragma unroll
        for (int m = 0; m < 8; ++m) {
            const f32x4 R = acc[m][0], I = acc[m][1];
            acc[m][0] = fma4(splat4(cc.r), R, mul4(splat4(-cc.i), I));
            acc[m][1] = fma4(splat4(-cc.r), I, mul4(splat4(-cc.i), R));
            asm volatile("" : "+v"(acc[m][0]), "+v"(acc[m][1]));
            if (m == 3) {
                SCHED();
#pragma unroll
                for (int i = 0; i < 4; ++i) asm volatile("buffer_load_dwordx4 %0, %1, %2, 0 offen sc1" : "=&v"(gq[i]) : "v"(goff[i]), "s"(grs) : "memory");
                SCHED();
            }
        }
        cf Ec;
        {
            const auto sr = __builtin_amdgcn_permlane16_swap(__float_as_uint(S.r), __float_as_uint(S.r), false, false);
            const auto si = __builtin_amdgcn_permlane16_swap(__float_as_uint(S.i), __float_as_uint(S.i), false, false);
            const cf Se = {__uint_as_float(sr[0]), __uint_as_float(si[0])}, So = {__uint_as_float(sr[1]), __uint_as_float(si[1])};
            const cf A = cfma(L32, Se, So);
            const auto ar = __builtin_amdgcn_permlane32_swap(__float_as_uint(A.r), __float_as_uint(A.r), false, false);
            const auto ai = __builtin_amdgcn_permlane32_swap(__float_as_uint(A.i), __float_as_uint(A.i), false, false);
            const cf A01 = {(q4 & 2) ? __uint_as_float(ar[0]) : 0.f, (q4 & 2) ? __uint_as_float(ai[0]) : 0.f};
            const cf kq = {(q4 & 1) ? L32.r : 1.f, (q4 & 1) ? L32.i : 0.f};
            const cf E = cfma(kq, A01, cf{(q4 & 1) ? Se.r : 0.f, (q4 & 1) ? Se.i : 0.f});
            Ec = cmul(cc, E);
            PINC(Ec);
        }
        WAIT_VM(4);
        CREAD(1);
        WAIT_LGKM0();
        CPIN(1);
        CTILE(wid, 2, cg);
        asm volatile("s_waitcnt vmcnt(8)" : "+v"(gq[0]), "+v"(gq[1]), "+v"(gq[2]), "+v"(gq[3]) :: "memory");
        STAMPT(dbg, 27, 0);
        cf carry;
        {
            unsigned spins = 0;
            for (;;) {
                unsigned bad = 0;
#pragma unroll
                for (int i = 0; i < 4; ++i) bad |= ((gq[i].y ^ tag) | (gq[i].w ^ tag)) & gmask[i];
                if (__all(bad == 0u)) break;
                if (++spins > SPIN_MAX) break;
#pragma unroll
                for (int i = 0; i < 4; ++i) asm volatile("buffer_load_dwordx4 %0, %1, %2, 0 offen sc1" : "+v"(gq[i]) : "v"(goff[i]), "s"(grs) : "memory");
                asm volatile("s_waitcnt vmcnt(0)" : "+v"(gq[0]), "+v"(gq[1]), "+v"(gq[2]), "+v"(gq[3]) :: "memory");
            }
            cf E = {__uint_as_float(gq[3].x), __uint_as_float(gq[3].z)};
#pragma unroll
            for (int i = 2; i >= 0; --i) E = cfma(L512, E, cf{__uint_as_float(gq[i].x), __uint_as_float(gq[i].z)});
            E = cmul(E, Lp);
            const float e2 = red16(red32(E.r, E.i));
            const auto eo = __builtin_amdgcn_permlane32_swap(__float_as_uint(e2), __float_as_uint(e2), false, false);
            carry = {__uint_as_float(eo[0]), __uint_as_float(eo[1])};
        }
        STAMPT(dbg, 3, 0);
        {
            const __amdgpu_buffer_rsrc_t hrs = __builtin_amdgcn_make_buffer_rsrc(h, 0, SEQ * 2048 * 2, 0x00020000);
            const cf e = cfma(cLq, carry, Ec);
            float hr = e.r, hn = -e.i;
            s16x4 fr[2][4];
#define HSTORE(m) do { _Pragma("unroll") for (int c = 0; c < 2; ++c) \
                __builtin_amdgcn_raw_buffer_store_b128(__builtin_bit_cast(u32x4, __builtin_shufflevector(fr[(m) & 1][2 * c], fr[(m) & 1][2 * c + 1], 0, 1, 2, 3, 4, 5, 6, 7)), hrs, hp, (2 * (m) + c) * 65536, WT); } while (0)
#pragma unroll
            for (int m = 0; m < 4; ++m) {
#pragma unroll
                for (int j = 0; j < 8; ++j) {
                    const float nr = fmaf(L1.r, hr, fmaf(L1.i, hn, acc[2 * m + (j >> 2)][0][j & 3]));
                    const float nn = fmaf(L1.r, hn, fmaf(-L1.i, hr, acc[2 * m + (j >> 2)][1][j & 3]));
                    hr = nr; hn = nn;
                    acc[2 * m + (j >> 2)][0][j & 3] = nr; acc[2 * m + (j >> 2)][1][j & 3] = nn;
                }
                u32x2 pk[4];
#pragma unroll
                for (int c = 0; c < 4; ++c) { const f32x4 v = acc[2 * m + (c >> 1)][c & 1]; pk[c] = (u32x2){pkbf(v[0], v[1]), pkbf(v[2], v[3])}; }
                PIN4(pk[0], pk[1], pk[2], pk[3]);
                SCHED();
#pragma unroll
                for (int c = 0; c < 4; ++c) { *(volatile __attribute__((address_space(3))) u32x2*)(size_t)(wa + c * 512) = pk[c]; SCHED(); }
                asm volatile("" ::: "memory");
#pragma unroll
                for (int c = 0; c < 2; ++c) { fr[m & 1][2 * c] = TRD(ra0 + c * 1024); SCHED(); fr[m & 1][2 * c + 1] = TRD(ra1 + c * 1024); SCHED(); }
                asm volatile("" ::: "memory");
                if (m > 0) HSTORE(m - 1);
                SCHED();
            }
            HSTORE(3);
        }
        STAMPT(dbg, 4, 0);
        {
            WAIT_VM(8);
            CREAD(2);
            WAIT_VM(0);
            {
                unsigned n;
                asm volatile("s_mov_b64 exec, 1\n\tds_add_rtn_u32 %0, %1, %2\n\ts_mov_b64 exec, -1\n\ts_waitcnt lgkmcnt(0)" : "=&v"(n) : "v"(MISC_OFF), "v"(1u) : "memory");
                if (__builtin_amdgcn_readfirstlane(n) == 3u) {
                    if (lane == 0) __hip_atomic_store((gu32*)(ws + WS_HFLAG) + (tb * 16 + nb) * 32, tag, RLX_AGENT);
                    if (DBGON && lane == 0) dbg[BIDX * 64 + 20] = __builtin_amdgcn_s_memrealtime();
                }
            }
            STAMPT(dbg, 12, 0);
            if (wid == 0 && lane < 7) CODE_TOUCH(CODE_LINES - 1 - lane - (idx & 7) * 7);
            __builtin_amdgcn_s_setprio(0);
            WAIT_LGKM0();
            CPIN(2);
            CTILE(wid, 3, cg);
            XLOAD();
            HPOLL_SETUP();
            HWAIT();
            STAMPT(dbg, 8, 0);
            const unsigned vh = (unsigned)(lane * 16);
#pragma unroll
            for (int f = 0; f < DEPTH; ++f) LOADF(f);
            __builtin_amdgcn_s_setprio(PRIO2_G);
        }
    } else {
        const int lw = wid - 4;
        const int d = cb * 4 + lw;
        const char* Krow = (const char*)(ssmk + (size_t)d * SEQ);
        cra = CRA(LA_OFF + lw * 8192); crb = cra ^ 64;
        const unsigned cst0 = lbase + LA_OFF + lw * 8192;
        CT_SETUP();
        asm volatile("" : "+v"(cra), "+v"(crb));
        u32x4 KA[4];
        unsigned draw;
        unsigned tag;
        float gacc = 0.f;
#define KLOAD() asm volatile("global_load_dwordx4 %0, %5, %7 nt\n\tglobal_load_dwordx4 %1, %5, %7 offset:1024 nt\n\tglobal_load_dwordx4 %2, %5, %7 offset:2048 nt\n\t" \
                             "global_load_dwordx4 %3, %5, %7 offset:3072 nt\n\tglobal_load_ushort %4, %6, %8" \
                             : "=&v"(KA[0]), "=&v"(KA[1]), "=&v"(KA[2]), "=&v"(KA[3]), "=&v"(draw) : "v"((unsigned)(lane * 16)), "v"((unsigned)(d * 2)), "s"(Krow), "s"(ssmD) : "memory")
#define KWAIT(n) asm volatile("s_waitcnt vmcnt(%5)" : "+v"(KA[0]), "+v"(KA[1]), "+v"(KA[2]), "+v"(KA[3]), "+v"(draw) : "i"(n) : "memory")
        if (wid < 6) {
            WAIT_VM(16);
            STAMPT(dbg, 17, 320);
            BAR();
            STAGE_A(2, lw0 + RA(2)); STAGE_B(2, lw0 + RB(2));
            unsigned long long ua_ = UNITS_A >> 12;
            unsigned sla = lw0 + RA(3), slb = lw0 + RB(4);
            int ka = ((3 + rot1) & 15) * 128, kb_ = ka;
            const char* sa = (const char*)x + ka;
            asm volatile("" : "+s"(sa), "+s"(sla), "+s"(ka), "+s"(ua_));
#define NEXT_A() do { ka = (ka + 128) & 2047; sa = (const char*)x + ka; ua_ >>= 4; sla = lw0 + (((unsigned)ua_ & 15u) << 14); asm volatile("" : "+s"(sa), "+s"(sla), "+s"(ka), "+s"(ua_)); } while (0)
#define LSTEP(n) do { \
                if ((n) == 24 && t == 1) { WAIT_VM(16); BAR(); STAGE_B(3, lw0 + RB(3)); } else { WAIT_VM(n); BAR(); } \
                GLDS4(sa, voA[0], voA[1], voA[2], voA[3], sla, 0); \
                GLDS4(sa + 16384, voA[0], voA[1], voA[2], voA[3], sla, 4096); \
                kb_ = (kb_ + 128) & 2047; \
                GLDS4((const char*)Bre + kb_, voB[0], voB[1], voB[2], voB[3], slb, 0); \
                NEXT_A(); \
                const unsigned slb0 = slb; \
                slb = (slb == lw0 + 65536) ? lw0 + 16384 : slb + 16384; \
                asm volatile("" : "+s"(slb), "+s"(kb_), "+s"(t)); \
                GLDS4((const char*)Bim + kb_, voB[0], voB[1], voB[2], voB[3], slb0, 4096); } while (0)
#pragma unroll 1
            for (int t = 1; t < 5; ++t) LSTEP(24);
            GLDS4(sa, voA[0], voA[1], voA[2], voA[3], sla, 0); GLDS4(sa + 16384, voA[0], voA[1], voA[2], voA[3], sla, 4096);
            NEXT_A();
#pragma unroll 1
            for (int t = 5; t < 13; ++t) LSTEP(32);
            KLOAD();
            WAIT_VM(37);
            BAR();
            __builtin_amdgcn_s_setprio(0);
            tag = lds_word(smem, TAG_OFF);
            CTILE(lw, 0, cst0 + GA_OFF);
            FLAGD(COK_OFF + lw * 4);
            WAIT_VM(30);
            BAR();
            CTILE(lw + 2, 0, cst0 + GA_OFF + 16384);
            FLAGD(COK_OFF + lw * 4 + 8);
            WAIT_VM(23);
            BAR();
        } else {
            const int vw = wid - 6;
            const int dch = dblk * 128 + vw * 64 + l15;
            const unsigned lk = lbase + ST_OFF + vw * ST_SZ;
            const unsigned vv_ = (unsigned)(((lane >> 4) * 4 + ((lane >> 3) & 1)) * DM * 2 + (lane & 7) * 16);
            const char* vb = (const char*)vin + (size_t)(sblk * 64) * (DM * 2) + dblk * 256 + vw * 128;
            const int grow = nb * 8 + vw * 4 + q4;
            unsigned gx0 = (unsigned)(grow * 128 + (((l15 >> 1) ^ ((grow >> 1) & 7)) * 16) + (l15 & 1) * 8), gx1 = gx0 + RA(2), gwa = (unsigned)(GW_OFF + l15 * 8);
            asm volatile("" : "+v"(gx0), "+v"(gx1), "+v"(gwa));
            u32x2 gxv, gwv;
            typedef const volatile __attribute__((address_space(3))) u32x2* l2p;
#define VBAR(t) do { SCHED(); asm volatile("" ::: "memory"); BAR(); asm volatile("" ::: "memory"); \
                     gxv = *(l2p)(size_t)(RA(t) == 0 ? gx0 : gx1 + (RA(t) - RA(2))); gwv = *(l2p)(size_t)(gwa + (t) * 128); SCHED(); } while (0)
#define GATE() do { SCHED(); gacc = fmaf(lo16(gxv.x), lo16(gwv.x), gacc); gacc = fmaf(hi16(gxv.x), hi16(gwv.x), gacc); \
                    gacc = fmaf(lo16(gxv.y), lo16(gwv.y), gacc); gacc = fmaf(hi16(gxv.y), hi16(gwv.y), gacc); asm volatile("" : "+v"(gacc)); SCHED(); } while (0)
            unsigned ka[4][2], sel[4];
            const int R0 = 4 + (l15 >> 2) * 16 + (l15 & 3) - (q4 >> 1);
            unsigned kr0 = (unsigned)(2 * R0), kr1 = (unsigned)(ST_OFF + vw * ST_SZ + R0 * 128 + (((q4 ^ (R0 >> 1)) & 1) * 16));
            asm volatile("" : "+v"(kr0), "+v"(kr1));
#pragma unroll
            for (int i = 0; i < 4; ++i)
#pragma unroll
                for (int tp = 0; tp < 2; ++tp) {
                    ka[i][tp] = (kr1 ^ (tp * 16)) + ((kr0 + 8 * i - 4 * tp) & 0xe0) + (4 * i - 2 * tp) * 128;
                    asm volatile("" : "+v"(ka[i][tp]));
                }
#pragma unroll
            for (int r = 0; r < 4; ++r) {
                const unsigned pick = (q4 & 2) ? 0x0302u : 0x0100u;
                sel[r] = ((l15 >> 1) == (q4 & 1) * 4 + r) ? ((l15 & 1) ? (pick << 16) | 0x0c0cu : 0x0c0c0000u | pick) : 0x0c0c0c0cu;
            }
            unsigned va_ = (unsigned)(ST_OFF + vw * ST_SZ + ST_V + (q4 * 2 + (l15 >> 3)) * 1056 + ((l15 >> 2) & 1) * 128 + (l15 & 3) * 8);
            unsigned so = (unsigned)(((dch >> 2) * 32 + sblk) * 512 + q4 * 64 + (dch & 3) * 16);
            PIN4(sel[0], sel[1], sel[2], sel[3]); asm volatile("" : "+v"(so), "+v"(va_));
            asm volatile("s_waitcnt vmcnt(9)" : "+v"(tw[0]), "+v"(tw[1]), "+v"(tw[2]), "+v"(tw[3]), "+v"(epoch) :: "memory");
            STAMPT(dbg, 18, 384);
            VBAR(0);
            bf16x8 wf[4][2];
#pragma unroll
            for (int ps = 0; ps < 4; ++ps)
#pragma unroll
                for (int tp = 0; tp < 2; ++tp) {
                    u32x4 w;
#pragma unroll
                    for (int r = 0; r < 4; ++r) w[r] = __builtin_amdgcn_perm(0u, tw[ps][tp], sel[r]);
                    wf[ps][tp] = __builtin_bit_cast(bf16x8, w);
                    asm volatile("" : "+v"(wf[ps][tp]));
                }
            SCHED();
            VLD(0); VLD(1); VLD(2); VLD(3); VLD(4); VLD(5); VLD(6); VLD(7);
            GATE(); VBAR(1);
            f32x4 ks[4][4];
            auto sconv = [&](int i0) __attribute__((always_inline)) {
                bf16x8 kf[2][4][2];
#pragma unroll
                for (int q = 0; q < 16; ++q) { kf[q >> 3][q & 3][(q >> 2) & 1] = RD128(ka[i0 + (q >> 3)][(q >> 2) & 1], (q & 3) * 32); SCHED(); }
                GATE();
                __builtin_amdgcn_s_setprio(PRIO_KV);
                WAIT_LGKM0();
                SCHED();
#pragma unroll
                for (int i = 0; i < 2; ++i) {
#pragma unroll
                    for (int ps = 0; ps < 4; ++ps) MF0(ks[ps][i0 + i], kf[i][ps][0], wf[ps][0]);
#pragma unroll
                    for (int ps = 0; ps < 4; ++ps) MF(ks[ps][i0 + i], kf[i][ps][1], wf[ps][1]);
                }
                __builtin_amdgcn_s_setprio(0);
                SCHED();
            };
            WAIT_VM(8);
            STAMPT(dbg, 21, 384);
#define EASY() __builtin_amdgcn_s_sleep(2)
            sconv(0); EASY(); VBAR(2);
            sconv(2); EASY(); VBAR(3);
            WAIT_VM(0);
            u32x2 vq[4][4];
#pragma unroll
            for (int ps = 0; ps < 4; ++ps)
#pragma unroll
                for (int j = 0; j < 4; ++j) vq[ps][j] = __builtin_bit_cast(u32x2, TRD(va_ + j * 256 + ps * 32));
            const __amdgpu_buffer_rsrc_t rs = __builtin_amdgcn_make_buffer_rsrc(kvT, 0, DM * SEQ * 2, 0x00020000);
            u32x4 o[8];
            auto kvout = [&](int n) __attribute__((always_inline)) {
                const int hh = n >> 2, ps = n & 3;
                asm volatile("" : "+v"(ks[ps][2 * hh]), "+v"(ks[ps][2 * hh + 1]));
#pragma unroll
                for (int e = 0; e < 4; ++e) {
                    const unsigned vv = vq[ps][2 * hh + (e >> 1)][e & 1];
                    o[n][e] = pkbf(ks[ps][2 * hh + (e >> 1)][2 * (e & 1)] * lo16(vv), ks[ps][2 * hh + (e >> 1)][2 * (e & 1) + 1] * hi16(vv));
                }
                asm volatile("" : "+v"(o[n]));
                SCHED();
            };
            GATE(); kvout(0); kvout(1);
            WAIT_LGKM0();
            EASY();
            VBAR(4);
#define KVST(h) do { _Pragma("unroll") for (int n = 0; n < 4; ++n) __builtin_amdgcn_raw_buffer_store_b128(o[4 * (h) + n], rs, so + (unsigned)((h) * 256), n * 65536, WT); \
                     SCHED(); asm volatile("s_nop 1" ::: "memory"); SCHED(); } while (0)
            kvout(2); kvout(3); GATE(); VBAR(5);
            KVST(0);
            kvout(4); kvout(5); GATE(); VBAR(6);
            kvout(6); kvout(7); GATE(); VBAR(7);
            KVST(1);
            GATE(); VBAR(8);
            if (vw == 1 && lane == 0) *(unsigned*)(smem + TAG_OFF) = (epoch + 1u) ? epoch + 1u : 1u;
            GATE();
            WAIT_LGKM0();
            WAIT_VM(0);
            VBAR(9);
            tag = __builtin_amdgcn_readfirstlane(*(const unsigned*)(smem + TAG_OFF));
            if (vw == 0 && lane == 0) __hip_atomic_store((gu32*)(ws + WS_KVFLAG) + (dblk * 32 + sblk) * 32, tag, RLX_AGENT);
            if (vw == 1 && lane < 4) *(u32x4*)(smem + U_OFF + 192 + lane * 16) = (u32x4){0u, 0u, 0u, 0u};
            gu32* kfp = (gu32*)(ws + WS_KVFLAG) + ((cb >> 5) * 32 + vw * 16 + l15) * 32;
            const char* ub = (const char*)kvT + (size_t)__builtin_amdgcn_readfirstlane(cb * 16384 + vw * 8192);
            const unsigned vu = (unsigned)((lane >> 5) * 512 + ((lane >> 2) & 1) * 256 + ((lane >> 3) & 3) * 64 + ((lane - (lane >> 4)) & 3) * 16);
            const unsigned lu = lbase + U_OFF + 256 + vw * 8192;
#define U_DMA1(o) asm volatile("s_add_u32 m0, %2, %3\n\ts_nop 0\n\tglobal_load_lds_dwordx4 %0, %1 sc1\n\tglobal_load_lds_dwordx4 %0, %1 offset:1024 sc1\n\t" \
                             "global_load_lds_dwordx4 %0, %1 offset:2048 sc1\n\tglobal_load_lds_dwordx4 %0, %1 offset:3072 sc1" \
                             :: "v"(vu), "s"(ub + (o)), "s"(lu), "i"(o) : "memory", "scc")
#define U_DMA() do { U_DMA1(0); U_DMA1(4096); FLAGD(UFLAG_OFF + lw * 4); } while (0)
            unsigned fo = lane < 8 ? (unsigned)((tb * 1024 + p_base) * 16 + lane * 128) : lane == 8 ? (unsigned)(WS_HFLAG + (tb * 16 + nb) * 128)
                        : lane == 9 ? (unsigned)(WS_CFLAG + cb * 128) : (unsigned)(WS_GATE + (tb * 16 + nb) * 128);
            asm volatile("" : "+v"(fo));
            GATE(); VBAR(10);
            TOUCH((const char*)(vw ? Cim : Cre) + (size_t)b * 8192, (unsigned)(lane * 128));
            if (vw == 0) TOUCH(ws, fo); else TOUCH(dskip, (unsigned)(d_base * 2));
            {
                const int cl = 64 + lane + ((idx & 15) * 2 + vw) * 7;
                if (lane < 7 && cl < CODE_LINES) CODE_TOUCH(cl);
            }
            GATE(); VBAR(11);
            GATE(); VBAR(12);
            GATE(); VBAR(13);
            unsigned pv;
            asm volatile("global_load_dword %0, %1, off sc1" : "=&v"(pv) : "v"(kfp) : "memory");
            KLOAD();
            GATE(); VBAR(14);
            GATE(); VBAR(15);
            asm volatile("s_waitcnt vmcnt(5)" : "+v"(pv) :: "memory");
            const bool done = __all(pv == tag);
            if (done) U_DMA();
            if (DBGON && TIDX == 384) dbg[BIDX * 64 + 13] = (done ? 100ull : 0ull) | (1ull << 62);
            GATE();
            if (lane == 0) SETW(UFLAG_OFF, vw, 1u);
            WAIT_VM(9);
            if (!done) {
                unsigned spins = 0;
                for (;;) {
                    const unsigned v = __hip_atomic_load(kfp, RLX_AGENT);
                    if (__all(v == tag)) break;
                    if (++spins > SPIN_MAX) break;
                    __builtin_amdgcn_s_sleep(1);
                }
                U_DMA();
            }
        }
        KWAIT(18);
        STAMPT(dbg, 14, 320);
        __builtin_amdgcn_s_sleep(6);
        {
            const unsigned cp = (unsigned)(KC_OFF + lw * CV_W + 64 + lane * 16);
            if (lane == 0) KA[0].x = (KA[0].x & 0xffff0000u) | f2bf(lo16(KA[0].x) + bf2f((u16)draw));
#define WR128(v, off) asm volatile("ds_write_b128 %0, %1 offset:%2" :: "v"(cp), "v"(v), "i"(off) : "memory")
#pragma unroll
            for (int q = 3; q >= 0; --q) WR128(KA[q], q * 1024);
#pragma unroll
            for (int q = 3; q >= 0; --q) {
                const u32x2 p = q ? pred2(KA[q], RL63(KA[q ? q - 1 : 0].z), RL63(KA[q ? q - 1 : 0].w)) : pred2(KA[0], 0u, 0u);
                const u32x4 a = KA[q];
                const u32x4 s1 = {al(a.x, p.y), al(a.y, a.x), al(a.z, a.y), al(a.w, a.z)};
                WR128(s1, q * 1024 + CV_COPY);
                const u32x4 s2 = {p.y, mv(a.x), mv(a.y), mv(a.z)};
                WR128(s2, q * 1024 + 2 * CV_COPY);
                const u32x4 s3 = {al(p.y, p.x), mv(s1.x), mv(s1.y), mv(s1.z)};
                WR128(s3, q * 1024 + 3 * CV_COPY);
            }
            if (lane < 16) *(u32x4*)(smem + KC_OFF + lw * CV_W + (lane >> 2) * CV_COPY + (lane & 3) * 16) = (u32x4){0u, 0u, 0u, 0u};
            SCHED();
        }
        STAMPT(dbg, 26, 320);
        wait_lds4(smem, G1D_OFF);
        wait_lds4(smem, UFLAG_OFF);
        STAMPT(dbg, 9, 256);
        __builtin_amdgcn_s_setprio(PRIO_CV);
        STAMPT(dbg, 10, 384);
        f32x4 cv[8];
#pragma unroll
        for (int g = 0; g < 8; ++g) cv[g] = (f32x4){0.f, 0.f, 0.f, 0.f};
        PIN4(cv[0], cv[1], cv[2], cv[3]); PIN4(cv[4], cv[5], cv[6], cv[7]);
        const int uz = U_OFF + 192;
        const int ua0 = U_OFF + 256 + (2 * l15 + q4 - 2) * 64, rv0 = (2 * l15 + q4 - 2) * 4 + lw * 16;
        const unsigned ka0 = (unsigned)((l15 >> 2) * CV_COPY - 16 * q4 + 8 * (l15 & 3) - 8);
        int ua, rv, pu, pz;
        unsigned adr;
#define UNEXT1() do { pu = ua | (rv & 0x30); asm volatile("" : "+v"(pu)); } while (0)
#define UNEXT2() do { pz = (ua > uz ? ua : uz) | (rv & 0x30); asm volatile("" : "+v"(pz)); } while (0)
#define UNEXT3() do { ua -= 256; rv -= 16; asm volatile("" : "+v"(ua), "+v"(rv)); } while (0)
#define ANEXT() do { adr += 64; asm volatile("" : "+v"(adr)); } while (0)
#define RDU(dst, c) do { dst = (c) ? RD128(pu, (c) * 2048) : RD128(pz, 0); SCHED(); } while (0)
        HPOLL_SETUP();
        unsigned hpv = 0;
        {
            bf16x8 A[8], UB[8];
            ua = ua0; rv = rv0; adr = lbase + KC_OFF + lw * CV_W + ka0 + 96;
            asm volatile("" : "+v"(adr));
            UNEXT1(); UNEXT2(); UNEXT3();
#pragma unroll
            for (int i = 0; i < 8; ++i) { RDA(A[7 - i], adr, 7 - i); RDU(UB[i], i); }
            unsigned gb = lw >= 2 ? (unsigned)((tb * 16 + nb) * 128 + (lw - 2) * 32) : 0x80000000u;
            asm volatile("" : "+s"(gb));
            const unsigned gso = (l15 == 0) ? gb + (unsigned)(q4 * 8) : 0x80000000u;
            float gt = 0.f;
            unsigned vxo = 0, vdo = 0, vgo = 0;
            auto gate_out = [&](int i) __attribute__((always_inline)) {
                if (i == 0) gacc += __uint_as_float(__builtin_amdgcn_update_dpp(0u, __float_as_uint(gacc), 0xb1, 0xf, 0xf, false));
                if (i == 1) gacc += __uint_as_float(__builtin_amdgcn_update_dpp(0u, __float_as_uint(gacc), 0x4e, 0xf, 0xf, false));
                if (i == 2) gacc += __uint_as_float(__builtin_amdgcn_update_dpp(0u, __float_as_uint(gacc), 0x141, 0xf, 0xf, false));
                if (i == 3) gacc += __uint_as_float(__builtin_amdgcn_update_dpp(0u, __float_as_uint(gacc), 0x140, 0xf, 0xf, false));
                if (i == 4) gacc = rbf(gacc);
                if (i == 5) gacc = __builtin_amdgcn_exp2f(gacc * -1.44269504088896f);
                if (i == 6) gacc = __builtin_amdgcn_rcpf(1.0f + gacc);
                if (i == 7) gacc = rbf(gacc);
                if (i == 8) gt = __uint_as_float(pkbf(gacc, 1.0f - gacc));
                if (i == 9) __builtin_amdgcn_raw_buffer_store_b64((u32x2){__float_as_uint(gt), tag}, gs, gso, 0, WT);
                asm volatile("" : "+v"(gacc), "+v"(gt));
            };
            auto step = [&](auto E) __attribute__((always_inline)) {
                constexpr int e = decltype(E)::value;
                constexpr int ti = (e == CS0) ? 0 : (e == CS1) ? 1 : (e == CS2) ? 2 : -1, tj = ti < 0 ? 0 : ti;
                constexpr unsigned char GA[36] = {0x66, 0x77, 0x76, 0x44, 0x55, 0x54, 0x65, 0x64, 0x75, 0x74, 0x22, 0x33, 0x32, 0x43, 0x42, 0x53, 0x52, 0x63, 0x62, 0x73, 0x72,
                                                  0x00, 0x11, 0x10, 0x21, 0x20, 0x31, 0x30, 0x41, 0x40, 0x51, 0x50, 0x61, 0x60, 0x71, 0x70};
                const char* sb_ = (const char*)Cre + COFF(wid, tj + 1);
                const char* sc_ = (const char*)Cim + COFF(wid, tj + 1);
#pragma unroll
                for (int q = 0; q < 36; ++q) {
                    const int g = GA[q] >> 4, a = GA[q] & 15;
                    MF(cv[g], A[a], UB[g - a]); SCHED();
                    if (e < 7) {
                        if (a == 1) RDU(UB[g - 1], g - 1);
                        if (q == 35) RDU(UB[7], 7);
                        if (g == 7) RDA(A[a], adr + 64 * (e + 1), a);
                        if (q == 11) UNEXT1();
                        if (q == 13) UNEXT2();
                        if (q == 15) UNEXT3();
                    }
                    if (ti >= 0) {
                        const int n = (q >= 3 && q <= 7) ? q - 3 : q == 10 ? 5 : q == 12 ? 6 : q == 14 ? 7 : -1;
                        if (q == 3) { if (ti == 0) WAIT_VM(1); else if (e == XE + 1) WAIT_VM(7); else WAIT_VM(0); }
                        if (n >= 0) CRD(tj, n & 7);
                        if (q == 18) CPIN(tj);
                        if (q == 18 || q == 23) asm volatile("s_add_u32 m0, %3, %4\n\ts_nop 0\n\tglobal_load_lds_dwordx4 %0, %2\n\tglobal_load_lds_dwordx4 %1, %2 offset:1024"
                                                             :: "v"(vB0), "v"(vB1), "s"(q == 18 ? sb_ : sc_), "s"(cst0), "i"(q == 18 ? 0 : 4096) : "memory", "scc");
                        if (q == 21 || q == 25) asm volatile("global_load_lds_dwordx4 %0, %2 offset:2048\n\tglobal_load_lds_dwordx4 %1, %2 offset:3072"
                                                             :: "v"(vB0), "v"(vB1), "s"(q == 21 ? sb_ : sc_) : "memory");
                    }
                    if (e == 0 && q >= 3 && q <= 7) gate_out(q - 3);
                    if (e == 0 && q >= 10 && q <= 18 && !(q & 1)) gate_out(5 + (q - 10) / 2);
                    if (e == 0 && q == 4) CTILE(wid, 0, cst0);
                    if (e == XE && q >= 23 && q <= 35 && (q & 1)) {
                        const int i = (q - 23) / 2;
                        const __amdgpu_buffer_rsrc_t xs = __builtin_amdgcn_make_buffer_rsrc((void*)x, 0, SEQ * DM * 2, 0x00020000);
                        const __amdgpu_buffer_rsrc_t ks = __builtin_amdgcn_make_buffer_rsrc((void*)dskip, 0, DM * 2, 0x00020000);
                        if (i < 4) asm volatile("buffer_load_dwordx2 %0, %1, %2, %3 offen offset:%4" : "=&v"(xx[(i >> 1) & 1][i & 1]) : "v"(vxo), "s"(xs), "s"((TR((i >> 1) & 1) * DM + d_base) * 2), "i"((i & 1) * 2048) : "memory");
                        if (i == 4) asm volatile("buffer_load_dwordx2 %0, %1, %2, %3 offen" : "=&v"(dk2) : "v"(vdo), "s"(ks), "s"(d_base * 2) : "memory");
                        if (i >= 5) asm volatile("buffer_load_dwordx4 %0, %1, %2, %3 offen sc1" : "=&v"(gv[(i - 5) & 1]) : "v"(vgo), "s"(gs), "s"(GSO((i - 5) & 1)) : "memory");
                    }
                    if (e == XE && q == 16) { vxo = (unsigned)(q4 * 32 * DM * 2 + l15 * 8); vdo = (unsigned)(l15 * 8); asm volatile("" : "+v"(vxo), "+v"(vdo)); }
                    if (e == XE && q == 17) { vgo = GV(q4); asm volatile("" : "+v"(vgo)); }
                    if (e == 6 && q == 30) hpv = __hip_atomic_load(hfp, RLX_AGENT);
                    SCHED();
                }
            };
            unroll<8>(step);
            WAIT_VM(0);
            XPIN();
        }
        PAD8(cv[0], cv[1], cv[2], cv[3]); PIN4(cv[4], cv[5], cv[6], cv[7]);
        STAMPT(dbg, 11, 384);
        const unsigned vh = (unsigned)((lane ^ 2) * 16);
        if (!__all(hpv == tag)) HWAIT();
        asm volatile("" ::: "memory");
#pragma unroll
        for (int f = 0; f < DEPTH; ++f) LOADF(f);
        STAMPT(dbg, 25, 384);
        {
            const __amdgpu_buffer_rsrc_t yrs = __builtin_amdgcn_make_buffer_rsrc(y2T, 0, DM * SEQ * 2, 0x00020000);
            const unsigned yo = (unsigned)(d * 4096 + (q4 & 1) * 512 + (l15 >> 3) * 256 + (q4 >> 1) * 128 + (l15 & 7) * 16);
#pragma unroll
            for (int g = 0; g < 8; g += 2) {
                const auto r0 = __builtin_amdgcn_permlane16_swap(pkbf(cv[g][0], cv[g][1]), pkbf(cv[g + 1][0], cv[g + 1][1]), false, false);
                const auto r1 = __builtin_amdgcn_permlane16_swap(pkbf(cv[g][2], cv[g][3]), pkbf(cv[g + 1][2], cv[g + 1][3]), false, false);
                __builtin_amdgcn_raw_buffer_store_b128((u32x4){r0[0], r1[0], r0[1], r1[1]}, yrs, yo + (unsigned)(g * 512), 0, WT);
            }
        }
        asm volatile("" ::: "memory");
        STAMPT(dbg, 15, 384);
        __builtin_amdgcn_s_setprio(PRIO2_L);
    }
    {
        const int lane = fresh_lane(), l15 = lane & 15, q4 = lane >> 4;
        const int ring = wid >> 2;
        const unsigned va = (unsigned)((lane ^ (ring * 2)) * 16), vh = va;
        const unsigned tag = lds_word(smem, TAG_OFF);
        const int cb0 = ring ? KC_OFF : GA_OFF, cst = ring ? CV_W : 8192;
        f32x4 ac[2][4];
        unsigned ca[2], fa[2], fl = 0, fv = 0;
#define CHAIN(u) do { const int c_ = (wid - ((u) >> 1)) & 3; ca[(u) & 1] = va + (unsigned)(cb0 + c_ * cst + ((u) & 1) * 4096); \
                      fa[(u) & 1] = (unsigned)(RF_OFF + ring * 32 + ((u) & 1) * 16 + c_ * 4); asm volatile("" : "+v"(ca[(u) & 1]), "+v"(fa[(u) & 1])); } while (0)
#define RDIN(dst, a, f_) asm volatile("ds_read_b32 %0, %5\n\tds_read_b128 %1, %6\n\tds_read_b128 %2, %6 offset:1024\n\tds_read_b128 %3, %6 offset:2048\n\tds_read_b128 %4, %6 offset:3072" \
                     : "=&v"(fl), "=&v"(dst[0]), "=&v"(dst[1]), "=&v"(dst[2]), "=&v"(dst[3]) : "v"(f_), "v"(a) : "memory")
#define LWAIT(dst) asm volatile("s_waitcnt lgkmcnt(0)" : "+v"(fl), "+v"(dst[0]), "+v"(dst[1]), "+v"(dst[2]), "+v"(dst[3]) :: "memory")
        CHAIN(0); CHAIN(1);
        WAIT_LGKM0();
        CPIN(0); CPIN(1); CPIN(2);
        CREAD(3);
        int tr[2] = {TR(0), TR(1)};
        unsigned gw[2][2];
        {
            unsigned spins = 0;
            while (__builtin_expect(!__all((gv[0].y == tag) & (gv[0].w == tag) & (gv[1].y == tag) & (gv[1].w == tag)), 0)) {
                if (++spins > SPIN_MAX) break;
#pragma unroll
                for (int r = 0; r < 2; ++r) asm volatile("s_nop 4\n\tbuffer_load_dwordx4 %0, %1, %2, %3 offen sc1\n\ts_waitcnt vmcnt(0)" : "=&v"(gv[r]) : "v"(GV(q4)), "s"(gs), "s"(__builtin_amdgcn_readfirstlane(GSO(r))) : "memory");
            }
            gw[0][0] = gv[0].x; gw[0][1] = gv[0].z; gw[1][0] = gv[1].x; gw[1][1] = gv[1].z;
            PIN4(gw[0][0], gw[0][1], gw[1][0], gw[1][1]);
        }
        unsigned cpv = 0, yy[2][4];
        f32x2 zz[2][4];
        unsigned exo = 0, exi = 0, efo = 0, efi = 0;
        auto send = [&](int r) __attribute__((always_inline)) {
            const unsigned one = 1u;
#pragma unroll
            for (int n = 0; n < 4; ++n) { const f32x2 v = {ac[r][n][2], ac[r][n][3]}; asm volatile("ds_write_b64 %0, %1 offset:%2" :: "v"(exo), "v"(v), "i"(r * 4096 + n * 512) : "memory"); }
            asm volatile("ds_write_b32 %0, %1 offset:%2" :: "v"(efo), "v"(one), "i"(r * 4) : "memory");
        };
        STAMPT(dbg, 5, 0);
        auto body = [&](auto F) __attribute__((always_inline)) {
            constexpr int f = decltype(F)::value;
            constexpr int u = f >> 3, k = f & 7;
            f32x4* an = ac[u & 1];
            f32x4* ao = ac[(u + 1) & 1];
#pragma unroll
            for (int n = 0; n < 4; ++n) {
                if (k == 0 && u < 2) MF0(an[n], af[f % DEPTH], cf_[k][n]); else MF(an[n], af[f % DEPTH], cf_[k][n]);
                SCHED();
                const int g = k * 4 + n;
                if (u >= 1 && u < 7) {
                    if (g == 1) { fv = ((u - 1) >> 1) + 1; asm volatile("" : "+v"(fv)); }
                    if (g == 4 || g == 6 || g == 8 || g == 10) asm volatile("ds_write_b128 %0, %1 offset:%2" :: "v"(ca[(u + 1) & 1]), "v"(ao[(g - 4) / 2]), "i"((g - 4) * 512) : "memory");
                    if (g == 12) asm volatile("ds_write_b32 %0, %1" :: "v"(fa[(u + 1) & 1]), "v"(fv) : "memory");
                    if (g == 18) CHAIN(u + 1);
                    if (g == 20) asm volatile("ds_read_b32 %0, %1" : "=&v"(fl) : "v"(fa[(u + 1) & 1]) : "memory");
                    if (g > 20 && g <= 24) asm volatile("ds_read_b128 %0, %1 offset:%2" : "=&v"(ao[g - 21]) : "v"(ca[(u + 1) & 1]), "i"((g - 21) * 1024) : "memory");
                    if (g == 30) {
                        LWAIT(ao);
                        if (__builtin_expect(__builtin_amdgcn_readfirstlane(fl) != (unsigned)((u + 1) >> 1), 0)) {
                            unsigned spins = 0;
#pragma unroll 1
                            do {
                                __builtin_amdgcn_s_sleep(1);
                                RDIN(ao, ca[(u + 1) & 1], fa[(u + 1) & 1]); LWAIT(ao);
                                if (++spins > SPIN_MAX * 16u) break;
                            } while (__builtin_amdgcn_readfirstlane(fl) != (unsigned)((u + 1) >> 1));
                        }
                    }
                }
                if (u == 7 && g == 6) send(0);
                if (f >= ZF && f < ZF + 4) {
                    constexpr int r = ((f - ZF) >> 1) & 1, j = (f - ZF) & 1;
                    const unsigned xw = xx[r][j][n >> 1], dw = dk2[n >> 1];
                    const float xf = (n & 1) ? hi16(xw) : lo16(xw), df = (n & 1) ? hi16(dw) : lo16(dw), yf = j ? hi16(yy[r][n]) : lo16(yy[r][n]);
                    zz[r][n][j] = xf * fmaf(hi16(gw[r][j]), yf, lo16(gw[r][j]) * df);
                    asm volatile("" : "+v"(zz[r][n]));
                }
                if (f == ZF + 4) {
                    zz[0][n] = (f32x2){fmaf(lo16(gw[0][0]), ac[0][n][0], zz[0][n][0]), fmaf(lo16(gw[0][1]), ac[0][n][1], zz[0][n][1])};
                    asm volatile("" : "+v"(zz[0][n]));
                }
                SCHED();
            }
            if (f + DEPTH < 64) LOADF(f + DEPTH);
            if (f == DEPTH + 1 && ring) {
                WAIT_VM(6);
                if (lane == 0) __hip_atomic_store((gu32*)(ws + WS_CFLAG) + cb * 32 + (wid & 3), tag, RLX_AGENT);
                SCHED();
            }
            if (f == YPOLL && ring) { const int l15 = fresh_lane() & 15; cpv = __hip_atomic_load((gu32*)(ws + WS_CFLAG) + (nb * 16 + (wid & 3) * 4 + (l15 >> 2)) * 32 + (l15 & 3), RLX_AGENT); SCHED(); }
            if (f == YPOLL + DEPTH && ring) {
                const int ln = fresh_lane(), l15 = ln & 15, q4 = ln >> 4;
                if (__builtin_expect(!__all(cpv == tag), 0)) {
                    unsigned spins = 0;
                    while (!__all(__hip_atomic_load((gu32*)(ws + WS_CFLAG) + (nb * 16 + (wid & 3) * 4 + (l15 >> 2)) * 32 + (l15 & 3), RLX_AGENT) == tag)) {
                        if (++spins > SPIN_MAX) break;
                        __builtin_amdgcn_s_sleep(1);
                    }
                }
                const unsigned lx = lbase + LA_OFF + (wid & 3) * 8192;
#pragma unroll
                for (int i = 0; i < 4; ++i) {
                    const unsigned vo = (unsigned)((d_base + (wid & 3) * 16 + i * 4 + q4) * 4096 + tb * 256 + ((l15 ^ ((wid & 3) * 4 + i)) * 16));
                    GLDS_SC1(y2T, vo, lx, i * 1024);
                }
                SCHED();
            }
            if (f == YPOLL + 2 * DEPTH + 1 && ring) {
                WAIT_VM(6);
                SETW(YFLAG_OFF, wid & 3, 1u);
                SCHED();
            }
            if (f == 55) {
                const int ln = fresh_lane(), c_ = (wid - 3) & 3;
                const int eg = GA_OFF + c_ * 8192, el = KC_OFF + c_ * CV_W;
                exo = (unsigned)((ring ? el : eg) + ln * 8);
                exi = (unsigned)((ring ? eg : el) + ln * 8);
                efo = (unsigned)(EF_OFF + wid * 8); efi = (unsigned)(EF_OFF + (wid ^ 4) * 8);
                PIN4(exo, exi, efo, efi);
                SCHED();
            }
            if (f == ZF - 2) {
                const int ln = fresh_lane(), l15 = ln & 15, q4 = ln >> 4;
                wait_lds4(smem, YFLAG_OFF);
#pragma unroll
                for (int r = 0; r < 2; ++r) {
                    const int jb = JB(6 + r);
                    const int ya = LA_OFF + (l15 >> 2) * 8192 + (l15 & 3) * 1024 + (((((jb >> 1) & 1) * 8 + (jb >> 2) + q4 * 2) ^ l15) * 16) + (jb & 1) * 8 + ring * 4;
#pragma unroll
                    for (int e = 0; e < 4; ++e) yy[r][e] = *(const unsigned*)(smem + ya + e * 256);
                }
                SCHED();
            }
        };
        unroll<64>(body);
        PAD8(ac[1][0], ac[1][1], ac[1][2], ac[1][3]);
        send(1);
        STAMPT(dbg, 6, 0);
        const __amdgpu_buffer_rsrc_t ors = __builtin_amdgcn_make_buffer_rsrc(out, 0, SEQ * DM * 4, 0x00020000);
        const unsigned ov = (unsigned)((lane >> 4) * (32 * DM * 4) + (lane & 15) * 16);
#pragma unroll
        for (int r = 0; r < 2; ++r) {
            f32x2 in[4];
#define EXRD() asm volatile("ds_read_b32 %0, %5 offset:%7\n\tds_read_b64 %1, %6 offset:%8\n\tds_read_b64 %2, %6 offset:%9\n\tds_read_b64 %3, %6 offset:%10\n\tds_read_b64 %4, %6 offset:%11" \
                             : "=&v"(fl), "=&v"(in[0]), "=&v"(in[1]), "=&v"(in[2]), "=&v"(in[3]) \
                             : "v"(efi), "v"(exi), "i"(r * 4), "i"(r * 4096), "i"(r * 4096 + 512), "i"(r * 4096 + 1024), "i"(r * 4096 + 1536) : "memory")
            EXRD();
            if (r == 0) {
#pragma unroll
                for (int n = 0; n < 4; ++n) {
                    zz[1][n] = (f32x2){fmaf(lo16(gw[1][0]), ac[1][n][0], zz[1][n][0]), fmaf(lo16(gw[1][1]), ac[1][n][1], zz[1][n][1])};
                    asm volatile("" : "+v"(zz[1][n]));
                }
            }
            LWAIT(in);
            if (__builtin_expect(__builtin_amdgcn_readfirstlane(fl) == 0u, 0)) {
                unsigned spins = 0;
#pragma unroll 1
                do {
                    EXRD(); LWAIT(in);
                    if (++spins > SPIN_MAX * 16u) break;
                } while (__builtin_amdgcn_readfirstlane(fl) == 0u);
            }
            f32x4 o[2];
#pragma unroll
            for (int j = 0; j < 2; ++j)
#pragma unroll
                for (int e = 0; e < 4; ++e) o[j][e] = fmaf(lo16(gw[r][j]), in[e][j], zz[r][e][j]);
#pragma unroll
            for (int j = 0; j < 2; ++j)
                __builtin_amdgcn_raw_buffer_store_b128(__builtin_bit_cast(u32x4, o[j]), ors, ov + (unsigned)(((tr[r] + j) * DM + d_base) * 4), 0, WT);
        }
        STAMPT(dbg, 7, 0);
        STAMPT(dbg, 16, 384);
        if (tb == 15 && nb == 0 && wid == 0 && lane == 0) __hip_atomic_store((gu32*)(ws + WS_EPOCH), tag, RLX_AGENT);
    }
}

extern "C" void kernel_launch(void* const* d_in, const int* in_sizes, int n_in,
                              void* d_out, int out_size, void* d_ws, size_t ws_size,
                              hipStream_t stream) {
    const u16* x = (const u16*)d_in[0];
    const u16* lre = (const u16*)d_in[1];
    const u16* lim = (const u16*)d_in[2];
    const u16* Bre = (const u16*)d_in[3];
    const u16* Bim = (const u16*)d_in[4];
    const u16* Cre = (const u16*)d_in[5];
    const u16* Cim = (const u16*)d_in[6];
    const u16* logdt = (const u16*)d_in[7];
    const u16* dskip = (const u16*)d_in[8];
    const u16* kin = (const u16*)d_in[10];
    const u16* vin = (const u16*)d_in[11];
    const u16* shiftk = (const u16*)d_in[12];
    const u16* ssmk = (const u16*)d_in[13];
    const u16* ssmD = (const u16*)d_in[14];
    const u16* gatew = (const u16*)d_in[15];
    float* out = (float*)d_out;

    char* ws = (char*)d_ws;

    hipFuncSetAttribute((const void*)k1_kernel, hipFuncAttributeMaxDynamicSharedMemorySize, K1_LDS);

    k1_kernel<<<256, 512, K1_LDS, stream>>>(x, Bre, Bim, kin, vin, shiftk, gatew, ws, lre, lim, logdt, ssmk, ssmD, Cre, Cim, dskip, out);
}
#if defined(__HIP_DEVICE_COMPILE__)
#pragma clang attribute pop
#endif
```

```cpp
#define IM0 0
#define IM1 0
#define EZ 0
#if defined(__HIP_DEVICE_COMPILE__)
#pragma clang attribute push(__attribute__((target("no-packed-fp32-ops"))), apply_to = function)
#endif
#include <hip/hip_runtime.h>
#include <hip/hip_bf16.h>
#include <stdint.h>

#define TIDX ((int)__builtin_amdgcn_workitem_id_x())
#define BIDX ((int)__builtin_amdgcn_workgroup_id_x())
typedef unsigned short u16;
typedef __attribute__((ext_vector_type(8))) short bf16x8;
typedef __attribute__((ext_vector_type(4))) float f32x4;
typedef __attribute__((ext_vector_type(2))) float f32x2;
typedef __attribute__((ext_vector_type(2))) __bf16 bf16x2;
typedef __attribute__((ext_vector_type(4))) unsigned u32x4;
typedef __attribute__((ext_vector_type(2))) unsigned u32x2;
typedef __attribute__((address_space(1))) unsigned long long gu64;
typedef __attribute__((address_space(1))) unsigned gu32;
typedef __attribute__((address_space(3))) void lds_void;
typedef __attribute__((address_space(3))) unsigned lds_u32;
typedef __attribute__((ext_vector_type(4))) short s16x4;
typedef __attribute__((address_space(3))) s16x4 lds_s16x4;
#define TRD(a) __builtin_amdgcn_ds_read_tr16_b64_v4i16((lds_s16x4*)(size_t)(a))

#define DBGON 0
#define STAMPT(buf, k, t) do { } while (0)
#define CODE_LINES 260
#define SEQ 2048
#define DM 1024
#define SPIN_MAX (1u << 18)
#define PRIO_G 2
#define PSLEEP 4
#define PRIO_L 3
#define WT 16

__device__ __forceinline__ float bf2f(u16 v) { return __uint_as_float(((unsigned)v) << 16); }
__device__ __forceinline__ u16 f2bf(float f) { __bf16 b = (__bf16)f; return __builtin_bit_cast(u16, b); }
__device__ __forceinline__ float rbf(float f) { return bf2f(f2bf(f)); }
__device__ __forceinline__ unsigned pkbf(float lo, float hi) { return __builtin_bit_cast(unsigned, __builtin_convertvector((f32x2){lo, hi}, bf16x2)); }
__device__ __forceinline__ float lo16(unsigned u) { return __uint_as_float(u << 16); }
__device__ __forceinline__ float hi16(unsigned u) { return __uint_as_float(u & 0xffff0000u); }

struct cf { float r, i; };
__device__ __forceinline__ cf cmul(cf a, cf b) { return {a.r * b.r - a.i * b.i, a.r * b.i + a.i * b.r}; }
__device__ __forceinline__ cf cfma(cf a, cf b, cf c) {
    return {fmaf(a.r, b.r, fmaf(-a.i, b.i, c.r)), fmaf(a.r, b.i, fmaf(a.i, b.r, c.i))};
}
__device__ __forceinline__ f32x4 splat4(float v) { return (f32x4){v, v, v, v}; }
__device__ __forceinline__ f32x4 fma4(f32x4 a, f32x4 b, f32x4 c) { return __builtin_elementwise_fma(a, b, c); }
__device__ __forceinline__ f32x4 mul4(f32x4 a, f32x4 b) { return a * b; }

__device__ __forceinline__ float red32(float a, float b) {
    const auto r = __builtin_amdgcn_permlane32_swap(__float_as_uint(a), __float_as_uint(b), false, false);
    return __uint_as_float(r[0]) + __uint_as_float(r[1]);
}
__device__ __forceinline__ float red16(float x) {
    const auto r = __builtin_amdgcn_permlane16_swap(__float_as_uint(x), __float_as_uint(x), false, false);
    return __uint_as_float(r[0]) + __uint_as_float(r[1]);
}
__device__ __forceinline__ int fresh_lane() {
    int l;
    asm volatile("v_mbcnt_lo_u32_b32 %0, -1, 0\n\tv_mbcnt_hi_u32_b32 %0, -1, %0" : "=v"(l));
    return l;
}
#define BAR() __builtin_amdgcn_s_barrier()
#define WAIT_VM(n) asm volatile("s_waitcnt vmcnt(" #n ")" ::: "memory")
#define WAIT_LGKM0() __builtin_amdgcn_s_waitcnt(0xC07F)
#define SCHED() __builtin_amdgcn_sched_barrier(0)
#define MF(c, a, b) asm volatile("v_mfma_f32_16x16x32_bf16 %0, %1, %2, %0" : "+v"(c) : "v"(a), "v"(b))
#define PAD8(c0, c1, c2, c3) asm volatile("s_nop 7" : "+v"(c0), "+v"(c1), "+v"(c2), "+v"(c3))
#define PIN4(c0, c1, c2, c3) asm volatile("" : "+v"(c0), "+v"(c1), "+v"(c2), "+v"(c3))
#define GLDS_S(sbase, voff, sl, IMM) \
    asm volatile("s_add_u32 m0, %2, %3\n\ts_nop 0\n\tglobal_load_lds_dwordx4 %0, %1" \
                 :: "v"(voff), "s"(sbase), "s"(sl), "i"(IMM) : "memory", "scc")
#define GLDS_K(sbase, voff, sl, IMM) \
    asm volatile("s_add_u32 m0, %2, %3\n\ts_nop 0\n\tglobal_load_lds_dwordx4 %0, %1 nt" \
                 :: "v"(voff), "s"(sbase), "s"(sl), "i"(IMM) : "memory", "scc")
#define RLX_AGENT __ATOMIC_RELAXED, __HIP_MEMORY_SCOPE_AGENT

#define UNITS_A 0x0756075607560560ull
#define UNITS_B 0x1432143214321432ull
#define RA(t) ((int)((UNITS_A >> (4 * (t))) & 15) * 16384)
#define RB(t) ((int)((UNITS_B >> (4 * (t))) & 15) * 16384)
#define LA_OFF 0
#define GA_OFF 32768
#define CV_COPY 4160
#define CV_W (4 * CV_COPY)
#define KC_OFF 65536
#define ST_OFF 114688
#define ST_V 8832
#define ST_SZ (ST_V + 8 * 1056)
#define U_OFF (KC_OFF + 16 * CV_COPY)
#define HT_OFF (U_OFF + 256 + 16384)
#define GW_OFF (HT_OFF + 8192)
#define TAG_OFF (GW_OFF + 2048)
#define UFLAG_OFF (TAG_OFF + 16)
#define G1D_OFF (UFLAG_OFF + 16)
#define MISC_OFF (G1D_OFF + 16)
#define YFLAG_OFF (MISC_OFF + 16)
#define RF_OFF (YFLAG_OFF + 16)
#define EF_OFF (RF_OFF + 64)
#define COK_OFF (EF_OFF + 64)
#define NFLAGW 52
#define DUMMY_OFF (COK_OFF + 16)
#define K1_LDS (DUMMY_OFF + 256)
#define WS_GRAN 0
#define WS_KVFLAG (256u << 10)
#define WS_EPOCH (WS_KVFLAG + 65536)
#define WS_CFLAG (WS_EPOCH + 4096)
#define WS_HFLAG (384u << 10)
#define WS_GATE (512u << 10)

#define CODE_TOUCH(vl) asm volatile("s_getpc_b64 s[98:99]\n\ts_add_u32 s98, s98, k1_kernel@rel32@lo+4\n\ts_addc_u32 s99, s99, k1_kernel@rel32@hi+12\n\t" \
                                    "s_mov_b32 m0, %1\n\ts_nop 0\n\tglobal_load_lds_dword %0, s[98:99]" :: "v"((unsigned)(vl) * 128u), "i"(DUMMY_OFF) : "s98", "s99", "scc", "memory")
#define RD128(a, off) (*(const __attribute__((address_space(3))) bf16x8*)(size_t)(unsigned)((a) + (off)))
#define RDA(dst, A, F) do { const s16x4 lo_ = TRD((A) + 512 * (F) + 8); SCHED(); const s16x4 hi_ = TRD((A) + 512 * (F)); SCHED(); \
                            dst = __builtin_shufflevector(lo_, hi_, 0, 1, 2, 3, 4, 5, 6, 7); } while (0)
#define TOUCH(sbase, voff) asm volatile("s_mov_b32 m0, %2\n\ts_nop 0\n\tglobal_load_lds_dword %0, %1" :: "v"(voff), "s"(sbase), "i"(DUMMY_OFF) : "memory")
#define FLAGD(off) do { unsigned long long pc_; asm volatile("s_getpc_b64 %0\n\ts_mov_b32 m0, %2\n\ts_mov_b64 exec, 1\n\tglobal_load_lds_dword %1, %0\n\ts_mov_b64 exec, -1" \
                                                             : "=&s"(pc_) : "v"(0u), "s"(__builtin_amdgcn_readfirstlane(off)) : "memory"); } while (0)
#define GLDS_SC1(sbase, voff, sl, IMM) \
    asm volatile("s_add_u32 m0, %2, %3\n\ts_nop 0\n\tglobal_load_lds_dwordx4 %0, %1 sc1" \
                 :: "v"(voff), "s"(sbase), "s"(sl), "i"(IMM) : "memory", "scc")
__device__ __forceinline__ unsigned mv(unsigned v) { unsigned r; asm("v_mov_b32 %0, %1" : "=v"(r) : "v"(v)); return r; }
__device__ __forceinline__ unsigned al(unsigned hi, unsigned lo) { unsigned r; asm volatile("v_alignbit_b32 %0, %1, %2, 16" : "=v"(r) : "v"(hi), "v"(lo)); return r; }
__device__ __forceinline__ u32x2 pred2(const u32x4& a, unsigned f0, unsigned f1) {
    return (u32x2){(unsigned)__builtin_amdgcn_update_dpp((int)f0, (int)a.z, 0x138, 0xf, 0xf, false), (unsigned)__builtin_amdgcn_update_dpp((int)f1, (int)a.w, 0x138, 0xf, 0xf, false)};
}
#define RL63(v) ((unsigned)__builtin_amdgcn_readlane((int)(v), 63))
__device__ __forceinline__ void wait_lds4(char* smem, int off) {
    const unsigned a = (unsigned)(size_t)(lds_void*)smem + off;
    unsigned spins = 0;
    for (;;) {
        u32x4 f;
        asm volatile("ds_read_b128 %0, %1\n\ts_waitcnt lgkmcnt(0)" : "=&v"(f) : "v"(a) : "memory");
        if (__builtin_amdgcn_readfirstlane((f.x != 0u) & (f.y != 0u) & (f.z != 0u) & (f.w != 0u))) break;
        if (++spins > SPIN_MAX * 16u) break;
        __builtin_amdgcn_s_sleep(1);
    }
}
__device__ __forceinline__ unsigned lds_word(char* smem, int off) {
    const unsigned a = (unsigned)(size_t)(lds_void*)smem + off;
    unsigned f;
    asm volatile("ds_read_b32 %0, %1\n\ts_waitcnt lgkmcnt(0)" : "=&v"(f) : "v"(a) : "memory");
    return __builtin_amdgcn_readfirstlane(f);
}
__device__ __forceinline__ void wait_lds1(char* smem, int off) {
    unsigned spins = 0;
    while (lds_word(smem, off) == 0u) {
        if (++spins > SPIN_MAX * 16u) break;
        __builtin_amdgcn_s_sleep(1);
    }
}
#define SETW(off, i, v) __hip_atomic_store((lds_u32*)(smem + (off)) + (i), (v), __ATOMIC_RELAXED, __HIP_MEMORY_SCOPE_WORKGROUP)

#define PRIO_CV 1
#define PRIO_KV 3
#define CS0 1
#define CS1 3
#define CS2 5
#define XE 4
#define YPOLL 32
#define ZF 59
template <int I> struct IC { static constexpr int value = I; };
template <int N, int I = 0, class F> __device__ __forceinline__ void unroll(F& f) { if constexpr (I < N) { f(IC<I>{}); unroll<N, I + 1>(f); } }
#define MF0(c, a, b) asm volatile("v_mfma_f32_16x16x32_bf16 %0, %1, %2, 0" : "=&v"(c) : "v"(a), "v"(b))
#define DEPTH 6
#define PRIO2_G 0
#define PRIO2_L 1
__device__ __forceinline__ cf pick4(int q, cf a1, cf a2, cf a3) {
    return {q == 0 ? 1.f : q == 1 ? a1.r : q == 2 ? a2.r : a3.r, q == 0 ? 0.f : q == 1 ? a1.i : q == 2 ? a2.i : a3.i};
}
__device__ __forceinline__ void idle_mfma(unsigned a, unsigned b, int n) {
    u32x4 p = {a * 2654435761u, b * 40503u, a * 2246822519u, b * 3266489917u};
    p = (p & 0xbfffbfffu) | 0x3c003c00u;
    u32x4 q = p.yzwx ^ 0x01230456u;
    f32x4 w;
    asm volatile("s_nop 1" : "+v"(p), "+v"(q));
    MF0(w, p, q);
#pragma unroll 1
    for (int i = 0; i < n; ++i) { MF(w, q, p); MF(w, p, p); MF(w, q, q); MF(w, p, q); }
    asm volatile("s_nop 7" : "+v"(w));
}
#define PINC(c) asm volatile("" : "+v"((c).r), "+v"((c).i))

extern "C" __global__ __launch_bounds__(512, 2) void k1_kernel(
        const u16* __restrict__ x, const u16* __restrict__ Bre, const u16* __restrict__ Bim,
        const u16* __restrict__ kin, const u16* __restrict__ vin, const u16* __restrict__ shiftk, const u16* __restrict__ gatew, char* ws,
        const u16* __restrict__ lre, const u16* __restrict__ lim, const u16* __restrict__ logdt,
        const u16* __restrict__ ssmk, const u16* __restrict__ ssmD,
        const u16* __restrict__ Cre, const u16* __restrict__ Cim, const u16* __restrict__ dskip, float* __restrict__ out) {
    extern __shared__ __attribute__((aligned(16))) char smem[];
#define h ((u16*)(ws + (1u << 20)))
#define kvT ((u16*)(ws + (9u << 20)))
#define y2T ((u16*)(ws + (13u << 20)))
#define dbg ((unsigned long long*)(ws + (64u << 20)))
    const int tid = TIDX;
    if (tid < 40 && tid + ((BIDX >> 3) & 7) * 40 < CODE_LINES) CODE_TOUCH(tid + ((BIDX >> 3) & 7) * 40);
    const int lane = tid & 63;
    int wid = __builtin_amdgcn_readfirstlane(tid >> 6);
    const int b = BIDX;
    const int xcd = b & 7, idx = b >> 3;
    const int tb = ((xcd >> 1) ^ (xcd >> 2)) * 4 + (idx & 3), nb = (xcd & 1) * 8 + (idx >> 2);
    const int cb = xcd * 32 + idx;
    const int t_base = tb * 128, p_base = nb * 64, d_base = nb * 64;
    const int l15 = lane & 15, q4 = lane >> 4;
    unsigned stv = 0, sth = 0;
    STAMPT(dbg, 0, 0);
    const unsigned lbase = (unsigned)(size_t)(lds_void*)smem;
    const int rot1 = (xcd >> 1) * 4 + (xcd & 1) * 8 + (idx >> 2) + (idx & 1);
    unsigned voA[4] = {0u, 0u, 0u, 0u}, voB[4] = {0u, 0u, 0u, 0u}, lw0 = 0;
    if (wid == 4 || wid == 5) {
        __builtin_amdgcn_s_setprio(PRIO_L);
        const int lh = wid - 4;
        const unsigned ch = (unsigned)(((lane & 7) ^ (lane >> 4)) * 16);
        voA[0] = (unsigned)(t_base + lh * 16 + (lane >> 5) * 32 + ((lane >> 3) & 3)) * 2048u + ch;
        voA[1] = (voA[0] ^ 64u) + (131072u - 1024u);
        voA[2] = voA[0] + 6144u; voA[3] = voA[1] + 6144u;
        voB[0] = (unsigned)(p_base + lh * 32 + (lane >> 3)) * 2048u + ch;
        voB[1] = (voB[0] ^ 64u) + (16384u - 1024u);
        voB[2] = voB[0] + 30720u; voB[3] = voB[1] + 30720u;
        PIN4(voA[0], voA[1], voA[2], voA[3]); PIN4(voB[0], voB[1], voB[2], voB[3]);
#define GLDS4(sbase, v0, v1, v2, v3, sl, IMM) \
            asm volatile("s_add_u32 m0, %5, %6\n\ts_nop 0\n\tglobal_load_lds_dwordx4 %0, %4\n\tglobal_load_lds_dwordx4 %1, %4 offset:1024\n\t" \
                         "global_load_lds_dwordx4 %2, %4 offset:2048\n\tglobal_load_lds_dwordx4 %3, %4 offset:3072" \
                         :: "v"(v0), "v"(v1), "v"(v2), "v"(v3), "s"(sbase), "s"(sl), "i"(IMM) : "memory", "scc")
#define STAGE_A(kt, sl) do { const char* sa_ = (const char*)x + (((kt) + rot1) & 15) * 128; \
            GLDS4(sa_, voA[0], voA[1], voA[2], voA[3], sl, 0); GLDS4(sa_ + 16384, voA[0], voA[1], voA[2], voA[3], sl, 4096); } while (0)
#define STAGE_B(kt, sl) do { const int kr_ = (((kt) + rot1) & 15) * 128; const char* sb_ = (const char*)Bre + kr_; const char* sc_ = (const char*)Bim + kr_; \
            GLDS4(sb_, voB[0], voB[1], voB[2], voB[3], sl, 0); GLDS4(sc_, voB[0], voB[1], voB[2], voB[3], sl, 4096); } while (0)
        lw0 = lbase + lh * 8192;
        STAGE_A(0, lw0 + RA(0)); STAGE_B(0, lw0 + RB(0));
        if (IM0) idle_mfma(voA[0], voB[1], IM0);
        STAGE_A(1, lw0 + RA(1)); STAGE_B(1, lw0 + RB(1));
    }
    const int sblk = b >> 3, dblk = b & 7;
    u32x2 tw[4];
    unsigned epoch;
    asm volatile("" : "=v"(tw[0]), "=v"(tw[1]), "=v"(tw[2]), "=v"(tw[3]), "=v"(epoch));
    if (wid >= 6) {
        __builtin_amdgcn_s_setprio(PRIO_L);
        const int vw = wid - 6;
        const int dch = dblk * 128 + vw * 64 + l15;
        const unsigned lk = lbase + ST_OFF + vw * ST_SZ;
        const unsigned vk = (unsigned)((lane >> 3) * DM * 2 + (((lane & 7) ^ ((lane >> 4) & 1)) * 16));
        const char* kb = (const char*)kin + (size_t)(sblk * 64 - 4) * (DM * 2) + dblk * 256 + vw * 128;
#define KLD(n) GLDS_K(kb + (n) * 8 * DM * 2, vk, lk, (n) * 1024 + ((n) >> 1) * 32)
#define VLD(n) GLDS_K(vb + (((n) >> 1) * 16 + ((n) & 1) * 2) * DM * 2, vv_, lk, ST_V + (n) * 1056)
        if (vw == 1) {
            const unsigned vg = (unsigned)((lane * 16 + rot1 * 128) & 2047), lg = lbase + GW_OFF;
            GLDS_S(gatew, vg, lg, 0); GLDS_S(gatew, vg ^ 1024u, lg, 1024);
        }
        asm volatile("global_load_dwordx2 %0, %5, %7\n\tglobal_load_dwordx2 %1, %5, %7 offset:128\n\tglobal_load_dwordx2 %2, %5, %7 offset:256\n\t"
                     "global_load_dwordx2 %3, %5, %7 offset:384\n\tglobal_load_dword %4, %6, %8 sc1"
                     : "+v"(tw[0]), "+v"(tw[1]), "+v"(tw[2]), "+v"(tw[3]), "+v"(epoch) : "v"((unsigned)(dch * 8)), "v"((unsigned)WS_EPOCH), "s"(shiftk), "s"(ws) : "memory");
        if (sblk == 0 && lane < 32) *(u32x4*)(smem + ST_OFF + vw * ST_SZ + lane * 16) = (u32x4){0u, 0u, 0u, 0u};
        if (sblk != 0 || lane >= 32) KLD(0);
        KLD(1); KLD(2); KLD(3); KLD(4); KLD(5); KLD(6); KLD(7);
        if (lane < 32) KLD(8);
        __builtin_amdgcn_s_setprio(0);
    }
    asm volatile("" : "+s"(wid));

#define WSL(w) ((((w) >> 2) ^ (nb >> 3)) * 4 + ((w) & 3))
#define COFF(w, ti) ((2 * WSL(w) + ((ti) >> 1)) * 128 + ((ti) & 1) * 4096)
    bf16x8 cf_[8][4];
    bf16x8 af[DEPTH];
    int cra, crb;
#define CT_SETUP() const unsigned vB0 = (unsigned)((d_base + (lane >> 3) * 4) * 2048 + (((lane & 7) ^ (lane >> 4)) * 16)), vB1 = (vB0 ^ 64u) + 65536u - 1024u
#define CTILE(w, ti, dst) do { const int co_ = COFF(w, ti); const unsigned ds_ = (dst); \
                               GLDS4((const char*)Cre + co_, vB0, vB1, vB0, vB1, ds_, 0); GLDS4((const char*)Cim + co_, vB0, vB1, vB0, vB1, ds_, 4096); } while (0)
#define CF(ti, n) cf_[4 * ((ti) >> 1) + ((n) >> 1)][2 * ((ti) & 1) + ((n) & 1)]
#define CRD(ti, n) CF(ti, n) = RD128((((n) & 4) ? crb : cra) ^ (((n) & 2) * 16), ((n) & 1) * 2048)
#define CREAD(ti) do { _Pragma("unroll") for (int n_ = 0; n_ < 8; ++n_) CRD(ti, n_); } while (0)
#define CPIN(ti) do { PIN4(CF(ti, 0), CF(ti, 1), CF(ti, 2), CF(ti, 3)); PIN4(CF(ti, 4), CF(ti, 5), CF(ti, 6), CF(ti, 7)); } while (0)
#define CRA(base) ((base) + (q4 >> 1) * 4096 + l15 * 128 + (((q4 & 1) ^ ((l15 >> 1) & 7)) * 16))
    const int rot2 = (idx & 16) >> 2;
#define JB(u) (((((u) ^ (rot2 >> 2)) & 1) << 2) | ((wid - ((u) >> 1) + rot2) & 3))
    const __amdgpu_buffer_rsrc_t hs = __builtin_amdgcn_make_buffer_rsrc((char*)h + (size_t)tb * 524288 + WSL(wid) * 8192, 0, 524288, 0x00020000);
#define LOADF(f) do { af[(f) % DEPTH] = __builtin_bit_cast(bf16x8, __builtin_amdgcn_raw_buffer_load_b128(hs, vh + ((f) & 3) * 1024, JB((f) >> 3) * 65536 + ((f) & 4) * 1024, 16)); SCHED(); } while (0)
#define HPOLL_SETUP() gu32* hfp = (gu32*)(ws + WS_HFLAG) + (tb * 16 + 2 * WSL(wid) + (lane & 1)) * 32
#define HWAIT() do { unsigned spins = 0; \
            while (!__all(__hip_atomic_load(hfp, RLX_AGENT) == tag)) { if (++spins > SPIN_MAX) break; __builtin_amdgcn_s_sleep(PSLEEP); } asm volatile("" ::: "memory"); } while (0)

    const __amdgpu_buffer_rsrc_t gs = __builtin_amdgcn_make_buffer_rsrc(ws + WS_GATE, 0, 32768, 0x00020000);
#define GV(q4) ((unsigned)(((q4) >> 1) * 128 + ((q4) & 1) * 32))
#define GSO(r) (tb * 2048 + JB(6 + (r)) * 256 + (wid >> 2) * 16)
#define TR(r) (t_base + JB(6 + (r)) * 4 + (wid >> 2) * 2)
    u32x2 xx[2][2], dk2;
    u32x4 gv[2];
#define XLOAD() do { \
        const __amdgpu_buffer_rsrc_t xs = __builtin_amdgcn_make_buffer_rsrc((void*)x, 0, SEQ * DM * 2, 0x00020000); \
        const __amdgpu_buffer_rsrc_t ks = __builtin_amdgcn_make_buffer_rsrc((void*)dskip, 0, DM * 2, 0x00020000); \
        dk2 = __builtin_amdgcn_raw_buffer_load_b64(ks, (unsigned)(l15 * 8), d_base * 2, 0); \
        _Pragma("unroll") for (int q = 0; q < 4; ++q) \
            xx[q >> 1][q & 1] = __builtin_amdgcn_raw_buffer_load_b64(xs, (unsigned)(q4 * 32 * DM * 2 + l15 * 8 + (q & 1) * DM * 2), (TR(q >> 1) * DM + d_base) * 2, 0); \
        _Pragma("unroll") for (int r = 0; r < 2; ++r) gv[r] = __builtin_amdgcn_raw_buffer_load_b128(gs, GV(q4), GSO(r), 16); \
        SCHED(); } while (0)
#define XPIN() do { PIN4(xx[0][0], xx[0][1], xx[1][0], xx[1][1]); asm volatile("" : "+v"(dk2), "+v"(gv[0]), "+v"(gv[1])); } while (0)

    if (wid < 4) {
        typedef __attribute__((ext_vector_type(8))) unsigned u32x8;
        typedef const __attribute__((address_space(4))) u32x8 cu8;
        const int po = (p_base + wid * 16) * 2;
        u32x8 s0 = *(cu8*)((size_t)logdt + po), s1 = *(cu8*)((size_t)lre + po), s2 = *(cu8*)((size_t)lim + po);
        SCHED();
        const int pq = p_base + wid * 16 + l15;
        unsigned goff[4], gmask[4];
#pragma unroll
        for (int i = 0; i < 4; ++i) {
            goff[i] = (unsigned)(((tb - 1 - q4 - 4 * i) * 1024 + pq) * 16);
            gmask[i] = (tb - 1 - q4 - 4 * i >= 0) ? ~0u : 0u;
            asm volatile("" : "+v"(goff[i]), "+v"(gmask[i]));
        }
        unsigned gpub = q4 == 0 ? (unsigned)((tb * 1024 + pq) * 16) : 0x80000000u;
        unsigned hp = (unsigned)(tb * 524288 + (nb * 4 + wid) * 1024 + lane * 16);
        unsigned wa = (unsigned)(HT_OFF + wid * 2048 + (q4 * 16 + (l15 ^ ((q4 >> 1) * 4))) * 8);
        unsigned ra0 = (unsigned)(HT_OFF + wid * 2048 + ((q4 >> 1) * 64 + (l15 & 3) * 16 + (((q4 & 1) * 8 + (l15 >> 2)) ^ ((l15 & 2) * 2))) * 8), ra1 = ra0 ^ 32u;
        cra = CRA(GA_OFF + wid * 8192); crb = cra ^ 64;
        unsigned vB0 = (unsigned)((d_base + (lane >> 3) * 4) * 2048 + (((lane & 7) ^ (lane >> 4)) * 16)), vB1 = (vB0 ^ 64u) + 65536u - 1024u;
        PIN4(gpub, vB0, hp, wa); PIN4(ra0, ra1, cra, crb); asm volatile("" : "+v"(vB1));
        f32x4 acc[8][2];
#pragma unroll
        for (int m = 0; m < 8; m += 2) {
#pragma unroll
            for (int c = 0; c < 4; ++c) acc[m + (c >> 1)][c & 1] = (f32x4){0.f, 0.f, 0.f, 0.f};
            PIN4(acc[m][0], acc[m][1], acc[m + 1][0], acc[m + 1][1]);
        }
        SCHED();
        cf L1, Lq3, cc, L32, cLq, Lp, L512;
        {
            unsigned w0 = 0, w1 = 0, w2 = 0;
#pragma unroll
            for (int i = 0; i < 8; ++i) {
                asm("v_writelane_b32 %0, %3, %6\n\tv_writelane_b32 %1, %4, %6\n\tv_writelane_b32 %2, %5, %6" : "+v"(w0), "+v"(w1), "+v"(w2) : "s"(s0[i]), "s"(s1[i]), "s"(s2[i]), "i"(2 * i));
            }
            asm("s_nop 1" : "+v"(w0), "+v"(w1), "+v"(w2));
            auto spread = [&](unsigned w) {
                const unsigned e = (unsigned)__builtin_amdgcn_update_dpp(0, (int)w, 0xa0, 0xf, 0xf, false);
                const unsigned a = __builtin_amdgcn_permlane16_swap(e, e, false, false)[0];
                const unsigned c = __builtin_amdgcn_permlane32_swap(a, a, false, false)[0];
                return (l15 & 1) ? hi16(c) : lo16(c);
            };
            const float ldt = spread(w0), re = spread(w1), im = spread(w2);
            if (wid == 3 && lane < NFLAGW) *(unsigned*)(smem + UFLAG_OFF + lane * 4) = 0u;
            const float dt = rbf(expf(ldt));
            const float ea = expf(dt * re);
            float sn, cs;
            sincosf(dt * im, &sn, &cs);
            L1 = {ea * cs, ea * sn};
            const float nr = L1.r - 1.0f, ni = L1.i, den = re * re + im * im;
            cc = {2.f * ((nr * re + ni * im) / den), 2.f * ((ni * re - nr * im) / den)};
            const cf T2 = cmul(L1, L1), T4 = cmul(T2, T2), L8 = cmul(T4, T4), L16 = cmul(L8, L8);
            L32 = cmul(L16, L16);
            const cf L64 = cmul(L32, L32), L96 = cmul(L64, L32), L128 = cmul(L64, L64), L256 = cmul(L128, L128), L384 = cmul(L256, L128);
            L512 = cmul(L256, L256);
            Lp = pick4(q4, L128, L256, L384);
            Lq3 = pick4(3 - q4, L32, L64, L96);
            cLq = cmul(cc, pick4(q4, L32, L64, L96));
            PINC(L1); PINC(Lq3); PINC(cc); PINC(L32); PINC(cLq); PINC(Lp); PINC(L512);
        }
        __builtin_amdgcn_s_setprio(PRIO_G);
        unsigned tagv;
        cf S;
        STAMPT(dbg, 22, 0);
        const int swz = (l15 >> 1) & 7;
        {
            const int a0 = RA(0) + l15 * 128 + ((q4 ^ swz) * 16);
            const int b0 = RB(0) + ((wid >> 1) * 64 + (wid & 1) * 16 + l15) * 128 + ((q4 ^ swz) * 16);
            WAIT_LGKM0();
            if (IM1) idle_mfma(hp, wa, IM1);
            BAR();
            asm volatile("" ::: "memory");
            STAMPT(dbg, 19, 64);
            bf16x8 FA[8][2], FB[2][2][2];
            int pa[2] = {a0, a0 ^ 64}, pb[2] = {b0, b0 ^ 64};
            PIN4(pa[0], pa[1], pb[0], pb[1]);
#define LGW(n) __builtin_amdgcn_s_waitcnt(0xC07F | ((n) << 8))
#define RA_(j) FA[(j) >> 1][(j) & 1] = RD128(pa[(j) & 1], ((j) >> 1) * 2048)
#define RB_(bu, j) FB[bu][(j) >> 1][((j) ^ ((j) >> 1)) & 1] = RD128(pb[((j) ^ ((j) >> 1)) & 1], ((j) >> 1) * 4096)
#define RN_(bu, i) do { if ((i) == 0) RB_(bu, 0); else if ((i) < 3) RA_((i) - 1); else if ((i) < 6) RB_(bu, (i) - 2); else RA_((i) - 4); } while (0)
#define G1_H(H, bu, RD, FILL) do { \
            _Pragma("unroll") for (int i = 0; i < 16; ++i) { \
                const int m = (H) * 4 + (i >> 2), n = ((i >> 1) ^ m) & 1, ks = (i ^ (i >> 1)) & 1; \
                if ((H) == 0) { if (i == 0) LGW(6); if (i == 4) LGW(8); if (i == 8) LGW(10); if (i == 12) LGW(8); } \
                SCHED(); \
                MF(acc[m][n], FA[m][ks], FB[bu][n][ks]); SCHED(); \
                if ((H) == 0 && i < 8) RA_(8 + i); \
                if ((H) == 1 && (RD) && i < 12) RN_((bu) ^ 1, i); \
                FILL((H) * 16 + i); \
                SCHED(); \
            } } while (0)
#define NOF(g) do { } while (0)
#pragma unroll
            for (int j = 0; j < 12; ++j) { RN_(0, j); SCHED(); }
            unsigned long long ua_ = UNITS_A;
            int sb = RB(0), sa = RA(0), nx, na;
#define ADV(g) do { if ((g) == 8) { ua_ >>= 4; na = (int)((unsigned)ua_ & 15u) << 14; asm volatile("" : "+s"(na), "+s"(ua_)); } \
                    if ((g) == 9) { const int o_ = sa; sa = na; na -= o_; asm volatile("" : "+s"(na), "+s"(sa)); } \
                    if ((g) == 10) { pa[0] += na; pa[1] += na; asm volatile("" : "+v"(pa[0]), "+v"(pa[1])); } \
                    if ((g) == 11) { nx = (sb == 65536) ? -49152 : 16384; sb += nx; asm volatile("" : "+s"(nx), "+s"(sb)); } \
                    if ((g) == 13) { pb[0] += nx; pb[1] += nx; asm volatile("" : "+v"(pb[0]), "+v"(pb[1])); } } while (0)
#define G1_T(bu) do { \
                G1_H(0, bu, true, ADV); \
                WAIT_LGKM0(); \
                asm volatile("" ::: "memory"); \
                BAR();                     \
                asm volatile("" ::: "memory"); \
                G1_H(1, bu, true, NOF); } while (0)
#pragma unroll 1
            for (int t2 = 0; t2 < 7; ++t2) { G1_T(0); G1_T(1); }
            G1_T(0);
#define AGG(g) do { if ((g) == 8) { asm volatile("" ::: "memory"); SETW(G1D_OFF, wid, 1u); }        \
                    if ((g) == 9) tagv = *(volatile const lds_u32*)(size_t)(unsigned)TAG_OFF; \
                    if ((g) >= 7) { const int t = (g) - 6; \
                        if (t == 1) { asm volatile("s_nop 1" : "+v"(acc[0][0]), "+v"(acc[0][1])); S = {acc[0][0][0], acc[0][1][0]}; } \
                        if ((t & 3) == 0) asm volatile("" : "+v"(acc[t >> 2][0]), "+v"(acc[t >> 2][1])); \
                        S = cfma(L1, S, cf{acc[t >> 2][0][t & 3], acc[t >> 2][1][t & 3]}); PINC(S); } } while (0)
            G1_H(0, 1, false, AGG);
            G1_H(1, 1, false, AGG);
        }
        STAMPT(dbg, 1, 0);
        const unsigned tag = __builtin_amdgcn_readfirstlane(tagv);
        const __amdgpu_buffer_rsrc_t grs = __builtin_amdgcn_make_buffer_rsrc(ws, 0, 15 * 16384, 0x00020000);
        {
#pragma unroll
            for (int t = 26; t < 32; ++t) {
                if (t == 28) { PINC(S); asm volatile("" : "+v"(acc[7][0]), "+v"(acc[7][1])); }
                S = cfma(L1, S, cf{acc[t >> 2][0][t & 3], acc[t >> 2][1][t & 3]});
            }
            const cf W = cmul(S, Lq3);
            const float e2 = red16(red32(W.r, W.i));
            const auto eo = __builtin_amdgcn_permlane32_swap(__float_as_uint(e2), __float_as_uint(e2), false, false);
            __builtin_amdgcn_raw_buffer_store_b128((u32x4){eo[0], tag, eo[1], tag}, grs, gpub, 0, 16);
        }
        SCHED();
        STAMPT(dbg, 2, 0);
        const unsigned cg = lbase + GA_OFF + wid * 8192;
        wait_lds1(smem, COK_OFF + wid * 4);
        CREAD(0);
        WAIT_LGKM0();
        CPIN(0);
        CTILE(wid, 1, cg);
        u32x4 gq[4];
#pragma unroll
        for (int m = 0; m < 8; ++m) {
            const f32x4 R = acc[m][0], I = acc[m][1];
            acc[m][0] = fma4(splat4(cc.r), R, mul4(splat4(-cc.i), I));
            acc[m][1] = fma4(splat4(-cc.r), I, mul4(splat4(-cc.i), R));
            asm volatile("" : "+v"(acc[m][0]), "+v"(acc[m][1]));
            if (m == 3) {
                SCHED();
#pragma unroll
                for (int i = 0; i < 4; ++i) asm volatile("buffer_load_dwordx4 %0, %1, %2, 0 offen sc1" : "=&v"(gq[i]) : "v"(goff[i]), "s"(grs) : "memory");
                SCHED();
            }
        }
        cf Ec;
        {
            const auto sr = __builtin_amdgcn_permlane16_swap(__float_as_uint(S.r), __float_as_uint(S.r), false, false);
            const auto si = __builtin_amdgcn_permlane16_swap(__float_as_uint(S.i), __float_as_uint(S.i), false, false);
            const cf Se = {__uint_as_float(sr[0]), __uint_as_float(si[0])}, So = {__uint_as_float(sr[1]), __uint_as_float(si[1])};
            const cf A = cfma(L32, Se, So);
            const auto ar = __builtin_amdgcn_permlane32_swap(__float_as_uint(A.r), __float_as_uint(A.r), false, false);
            const auto ai = __builtin_amdgcn_permlane32_swap(__float_as_uint(A.i), __float_as_uint(A.i), false, false);
            const cf A01 = {(q4 & 2) ? __uint_as_float(ar[0]) : 0.f, (q4 & 2) ? __uint_as_float(ai[0]) : 0.f};
            const cf kq = {(q4 & 1) ? L32.r : 1.f, (q4 & 1) ? L32.i : 0.f};
            const cf E = cfma(kq, A01, cf{(q4 & 1) ? Se.r : 0.f, (q4 & 1) ? Se.i : 0.f});
            Ec = cmul(cc, E);
            PINC(Ec);
        }
        WAIT_VM(4);
        CREAD(1);
        WAIT_LGKM0();
        CPIN(1);
        CTILE(wid, 2, cg);
        asm volatile("s_waitcnt vmcnt(8)" : "+v"(gq[0]), "+v"(gq[1]), "+v"(gq[2]), "+v"(gq[3]) :: "memory");
        STAMPT(dbg, 27, 0);
        cf carry;
        {
            unsigned spins = 0;
            for (;;) {
                unsigned bad = 0;
#pragma unroll
                for (int i = 0; i < 4; ++i) bad |= ((gq[i].y ^ tag) | (gq[i].w ^ tag)) & gmask[i];
                if (__all(bad == 0u)) break;
                if (++spins > SPIN_MAX) break;
#pragma unroll
                for (int i = 0; i < 4; ++i) asm volatile("buffer_load_dwordx4 %0, %1, %2, 0 offen sc1" : "+v"(gq[i]) : "v"(goff[i]), "s"(grs) : "memory");
                asm volatile("s_waitcnt vmcnt(0)" : "+v"(gq[0]), "+v"(gq[1]), "+v"(gq[2]), "+v"(gq[3]) :: "memory");
            }
            cf E = {__uint_as_float(gq[3].x), __uint_as_float(gq[3].z)};
#pragma unroll
            for (int i = 2; i >= 0; --i) E = cfma(L512, E, cf{__uint_as_float(gq[i].x), __uint_as_float(gq[i].z)});
            E = cmul(E, Lp);
            const float e2 = red16(red32(E.r, E.i));
            const auto eo = __builtin_amdgcn_permlane32_swap(__float_as_uint(e2), __float_as_uint(e2), false, false);
            carry = {__uint_as_float(eo[0]), __uint_as_float(eo[1])};
        }
        STAMPT(dbg, 3, 0);
        {
            const __amdgpu_buffer_rsrc_t hrs = __builtin_amdgcn_make_buffer_rsrc(h, 0, SEQ * 2048 * 2, 0x00020000);
            const cf e = cfma(cLq, carry, Ec);
            float hr = e.r, hn = -e.i;
            s16x4 fr[2][4];
#define HSTORE(m) do { _Pragma("unroll") for (int c = 0; c < 2; ++c) \
                __builtin_amdgcn_raw_buffer_store_b128(__builtin_bit_cast(u32x4, __builtin_shufflevector(fr[(m) & 1][2 * c], fr[(m) & 1][2 * c + 1], 0, 1, 2, 3, 4, 5, 6, 7)), hrs, hp, (2 * (m) + c) * 65536, WT); } while (0)
#pragma unroll
            for (int m = 0; m < 4; ++m) {
#pragma unroll
                for (int j = 0; j < 8; ++j) {
                    const float nr = fmaf(L1.r, hr, fmaf(L1.i, hn, acc[2 * m + (j >> 2)][0][j & 3]));
                    const float nn = fmaf(L1.r, hn, fmaf(-L1.i, hr, acc[2 * m + (j >> 2)][1][j & 3]));
                    hr = nr; hn = nn;
                    acc[2 * m + (j >> 2)][0][j & 3] = nr; acc[2 * m + (j >> 2)][1][j & 3] = nn;
                }
                u32x2 pk[4];
#pragma unroll
                for (int c = 0; c < 4; ++c) { const f32x4 v = acc[2 * m + (c >> 1)][c & 1]; pk[c] = (u32x2){pkbf(v[0], v[1]), pkbf(v[2], v[3])}; }
                PIN4(pk[0], pk[1], pk[2], pk[3]);
                SCHED();
#pragma unroll
                for (int c = 0; c < 4; ++c) { *(volatile __attribute__((address_space(3))) u32x2*)(size_t)(wa + c * 512) = pk[c]; SCHED(); }
                asm volatile("" ::: "memory");
#pragma unroll
                for (int c = 0; c < 2; ++c) { fr[m & 1][2 * c] = TRD(ra0 + c * 1024); SCHED(); fr[m & 1][2 * c + 1] = TRD(ra1 + c * 1024); SCHED(); }
                asm volatile("" ::: "memory");
                if (m > 0) HSTORE(m - 1);
                SCHED();
            }
            HSTORE(3);
        }
        STAMPT(dbg, 4, 0);
        {
            WAIT_VM(8);
            CREAD(2);
            WAIT_VM(0);
            {
                unsigned n;
                asm volatile("s_mov_b64 exec, 1\n\tds_add_rtn_u32 %0, %1, %2\n\ts_mov_b64 exec, -1\n\ts_waitcnt lgkmcnt(0)" : "=&v"(n) : "v"(MISC_OFF), "v"(1u) : "memory");
                if (__builtin_amdgcn_readfirstlane(n) == 3u) {
                    if (lane == 0) __hip_atomic_store((gu32*)(ws + WS_HFLAG) + (tb * 16 + nb) * 32, tag, RLX_AGENT);
                    if (DBGON && lane == 0) dbg[BIDX * 64 + 20] = __builtin_amdgcn_s_memrealtime();
                }
            }
            STAMPT(dbg, 12, 0);
            if (wid == 0 && lane < 7) CODE_TOUCH(CODE_LINES - 1 - lane - (idx & 7) * 7);
            __builtin_amdgcn_s_setprio(0);
            WAIT_LGKM0();
            CPIN(2);
            CTILE(wid, 3, cg);
            XLOAD();
            HPOLL_SETUP();
            HWAIT();
            STAMPT(dbg, 8, 0);
            const unsigned vh = (unsigned)(lane * 16);
#pragma unroll
            for (int f = 0; f < DEPTH; ++f) LOADF(f);
            __builtin_amdgcn_s_setprio(PRIO2_G);
        }
    } else {
        const int lw = wid - 4;
        const int d = cb * 4 + lw;
        const char* Krow = (const char*)(ssmk + (size_t)d * SEQ);
        cra = CRA(LA_OFF + lw * 8192); crb = cra ^ 64;
        const unsigned cst0 = lbase + LA_OFF + lw * 8192;
        CT_SETUP();
        asm volatile("" : "+v"(cra), "+v"(crb));
        u32x4 KA[4];
        unsigned draw;
        unsigned tag;
        float gacc = 0.f;
#define KLOAD() asm volatile("global_load_dwordx4 %0, %5, %7 nt\n\tglobal_load_dwordx4 %1, %5, %7 offset:1024 nt\n\tglobal_load_dwordx4 %2, %5, %7 offset:2048 nt\n\t" \
                             "global_load_dwordx4 %3, %5, %7 offset:3072 nt\n\tglobal_load_ushort %4, %6, %8" \
                             : "=&v"(KA[0]), "=&v"(KA[1]), "=&v"(KA[2]), "=&v"(KA[3]), "=&v"(draw) : "v"((unsigned)(lane * 16)), "v"((unsigned)(d * 2)), "s"(Krow), "s"(ssmD) : "memory")
#define KWAIT(n) asm volatile("s_waitcnt vmcnt(%5)" : "+v"(KA[0]), "+v"(KA[1]), "+v"(KA[2]), "+v"(KA[3]), "+v"(draw) : "i"(n) : "memory")
        if (wid < 6) {
            WAIT_VM(16);
            STAMPT(dbg, 17, 320);
            BAR();
            STAGE_A(2, lw0 + RA(2)); STAGE_B(2, lw0 + RB(2));
            unsigned long long ua_ = UNITS_A >> 12;
            unsigned sla = lw0 + RA(3), slb = lw0 + RB(4);
            int ka = ((3 + rot1) & 15) * 128, kb_ = ka;
            const char* sa = (const char*)x + ka;
            asm volatile("" : "+s"(sa), "+s"(sla), "+s"(ka), "+s"(ua_));
#define NEXT_A() do { ka = (ka + 128) & 2047; sa = (const char*)x + ka; ua_ >>= 4; sla = lw0 + (((unsigned)ua_ & 15u) << 14); asm volatile("" : "+s"(sa), "+s"(sla), "+s"(ka), "+s"(ua_)); } while (0)
#define LSTEP(n) do { \
                if ((n) == 24 && t == 1) { WAIT_VM(16); BAR(); STAGE_B(3, lw0 + RB(3)); } else { WAIT_VM(n); BAR(); } \
                GLDS4(sa, voA[0], voA[1], voA[2], voA[3], sla, 0); \
                GLDS4(sa + 16384, voA[0], voA[1], voA[2], voA[3], sla, 4096); \
                kb_ = (kb_ + 128) & 2047; \
                GLDS4((const char*)Bre + kb_, voB[0], voB[1], voB[2], voB[3], slb, 0); \
                NEXT_A(); \
                const unsigned slb0 = slb; \
                slb = (slb == lw0 + 65536) ? lw0 + 16384 : slb + 16384; \
                asm volatile("" : "+s"(slb), "+s"(kb_), "+s"(t)); \
                GLDS4((const char*)Bim + kb_, voB[0], voB[1], voB[2], voB[3], slb0, 4096); } while (0)
#pragma unroll 1
            for (int t = 1; t < 5; ++t) LSTEP(24);
            GLDS4(sa, voA[0], voA[1], voA[2], voA[3], sla, 0); GLDS4(sa + 16384, voA[0], voA[1], voA[2], voA[3], sla, 4096);
            NEXT_A();
#pragma unroll 1
            for (int t = 5; t < 13; ++t) LSTEP(32);
            KLOAD();
            WAIT_VM(37);
            BAR();
            __builtin_amdgcn_s_setprio(0);
            tag = lds_word(smem, TAG_OFF);
            CTILE(lw, 0, cst0 + GA_OFF);
            FLAGD(COK_OFF + lw * 4);
            WAIT_VM(30);
            BAR();
            CTILE(lw + 2, 0, cst0 + GA_OFF + 16384);
            FLAGD(COK_OFF + lw * 4 + 8);
            WAIT_VM(23);
            BAR();
        } else {
            const int vw = wid - 6;
            const int dch = dblk * 128 + vw * 64 + l15;
            const unsigned lk = lbase + ST_OFF + vw * ST_SZ;
            const unsigned vv_ = (unsigned)(((lane >> 4) * 4 + ((lane >> 3) & 1)) * DM * 2 + (lane & 7) * 16);
            const char* vb = (const char*)vin + (size_t)(sblk * 64) * (DM * 2) + dblk * 256 + vw * 128;
            const int grow = nb * 8 + vw * 4 + q4;
            unsigned gx0 = (unsigned)(grow * 128 + (((l15 >> 1) ^ ((grow >> 1) & 7)) * 16) + (l15 & 1) * 8), gx1 = gx0 + RA(2), gwa = (unsigned)(GW_OFF + l15 * 8);
            asm volatile("" : "+v"(gx0), "+v"(gx1), "+v"(gwa));
            u32x2 gxv, gwv;
            typedef const volatile __attribute__((address_space(3))) u32x2* l2p;
#define VBAR(t) do { SCHED(); asm volatile("" ::: "memory"); BAR(); asm volatile("" ::: "memory"); \
                     gxv = *(l2p)(size_t)(RA(t) == 0 ? gx0 : gx1 + (RA(t) - RA(2))); gwv = *(l2p)(size_t)(gwa + (t) * 128); SCHED(); } while (0)
#define GATE() do { SCHED(); gacc = fmaf(lo16(gxv.x), lo16(gwv.x), gacc); gacc = fmaf(hi16(gxv.x), hi16(gwv.x), gacc); \
                    gacc = fmaf(lo16(gxv.y), lo16(gwv.y), gacc); gacc = fmaf(hi16(gxv.y), hi16(gwv.y), gacc); asm volatile("" : "+v"(gacc)); SCHED(); } while (0)
            unsigned ka[4][2], sel[4];
            const int R0 = 4 + (l15 >> 2) * 16 + (l15 & 3) - (q4 >> 1);
            unsigned kr0 = (unsigned)(2 * R0), kr1 = (unsigned)(ST_OFF + vw * ST_SZ + R0 * 128 + (((q4 ^ (R0 >> 1)) & 1) * 16));
            asm volatile("" : "+v"(kr0), "+v"(kr1));
#pragma unroll
            for (int i = 0; i < 4; ++i)
#pragma unroll
                for (int tp = 0; tp < 2; ++tp) {
                    ka[i][tp] = (kr1 ^ (tp * 16)) + ((kr0 + 8 * i - 4 * tp) & 0xe0) + (4 * i - 2 * tp) * 128;
                    asm volatile("" : "+v"(ka[i][tp]));
                }
#pragma unroll
            for (int r = 0; r < 4; ++r) {
                const unsigned pick = (q4 & 2) ? 0x0302u : 0x0100u;
                sel[r] = ((l15 >> 1) == (q4 & 1) * 4 + r) ? ((l15 & 1) ? (pick << 16) | 0x0c0cu : 0x0c0c0000u | pick) : 0x0c0c0c0cu;
            }
            unsigned va_ = (unsigned)(ST_OFF + vw * ST_SZ + ST_V + (q4 * 2 + (l15 >> 3)) * 1056 + ((l15 >> 2) & 1) * 128 + (l15 & 3) * 8);
            unsigned so = (unsigned)(((dch >> 2) * 32 + sblk) * 512 + q4 * 64 + (dch & 3) * 16);
            PIN4(sel[0], sel[1], sel[2], sel[3]); asm volatile("" : "+v"(so), "+v"(va_));
            asm volatile("s_waitcnt vmcnt(9)" : "+v"(tw[0]), "+v"(tw[1]), "+v"(tw[2]), "+v"(tw[3]), "+v"(epoch) :: "memory");
            STAMPT(dbg, 18, 384);
            VBAR(0);
            bf16x8 wf[4][2];
#pragma unroll
            for (int ps = 0; ps < 4; ++ps)
#pragma unroll
                for (int tp = 0; tp < 2; ++tp) {
                    u32x4 w;
#pragma unroll
                    for (int r = 0; r < 4; ++r) w[r] = __builtin_amdgcn_perm(0u, tw[ps][tp], sel[r]);
                    wf[ps][tp] = __builtin_bit_cast(bf16x8, w);
                    asm volatile("" : "+v"(wf[ps][tp]));
                }
            SCHED();
            VLD(0); VLD(1); VLD(2); VLD(3); VLD(4); VLD(5); VLD(6); VLD(7);
            GATE(); VBAR(1);
            f32x4 ks[4][4];
            auto sconv = [&](int i0) __attribute__((always_inline)) {
                bf16x8 kf[2][4][2];
#pragma unroll
                for (int q = 0; q < 16; ++q) { kf[q >> 3][q & 3][(q >> 2) & 1] = RD128(ka[i0 + (q >> 3)][(q >> 2) & 1], (q & 3) * 32); SCHED(); }
                GATE();
                __builtin_amdgcn_s_setprio(PRIO_KV);
                WAIT_LGKM0();
                SCHED();
#pragma unroll
                for (int i = 0; i < 2; ++i) {
#pragma unroll
                    for (int ps = 0; ps < 4; ++ps) MF0(ks[ps][i0 + i], kf[i][ps][0], wf[ps][0]);
#pragma unroll
                    for (int ps = 0; ps < 4; ++ps) MF(ks[ps][i0 + i], kf[i][ps][1], wf[ps][1]);
                }
                __builtin_amdgcn_s_setprio(0);
                SCHED();
            };
            WAIT_VM(8);
            STAMPT(dbg, 21, 384);
#define EASY() do { if (EZ) __builtin_amdgcn_s_sleep(EZ); } while (0)
            sconv(0); EASY(); VBAR(2);
            sconv(2); EASY(); VBAR(3);
            WAIT_VM(0);
            u32x2 vq[4][4];
#pragma unroll
            for (int ps = 0; ps < 4; ++ps)
#pragma unroll
                for (int j = 0; j < 4; ++j) vq[ps][j] = __builtin_bit_cast(u32x2, TRD(va_ + j * 256 + ps * 32));
            const __amdgpu_buffer_rsrc_t rs = __builtin_amdgcn_make_buffer_rsrc(kvT, 0, DM * SEQ * 2, 0x00020000);
            u32x4 o[8];
            auto kvout = [&](int n) __attribute__((always_inline)) {
                const int hh = n >> 2, ps = n & 3;
                asm volatile("" : "+v"(ks[ps][2 * hh]), "+v"(ks[ps][2 * hh + 1]));
#pragma unroll
                for (int e = 0; e < 4; ++e) {
                    const unsigned vv = vq[ps][2 * hh + (e >> 1)][e & 1];
                    o[n][e] = pkbf(ks[ps][2 * hh + (e >> 1)][2 * (e & 1)] * lo16(vv), ks[ps][2 * hh + (e >> 1)][2 * (e & 1) + 1] * hi16(vv));
                }
                asm volatile("" : "+v"(o[n]));
                SCHED();
            };
            GATE(); kvout(0); kvout(1);
            WAIT_LGKM0();
            EASY();
            VBAR(4);
#define KVST(h) do { _Pragma("unroll") for (int n = 0; n < 4; ++n) __builtin_amdgcn_raw_buffer_store_b128(o[4 * (h) + n], rs, so + (unsigned)((h) * 256), n * 65536, WT); \
                     SCHED(); asm volatile("s_nop 1" ::: "memory"); SCHED(); } while (0)
            kvout(2); kvout(3); GATE(); VBAR(5);
            KVST(0);
            kvout(4); kvout(5); GATE(); VBAR(6);
            kvout(6); kvout(7); GATE(); VBAR(7);
            KVST(1);
            GATE(); VBAR(8);
            if (vw == 1 && lane == 0) *(unsigned*)(smem + TAG_OFF) = (epoch + 1u) ? epoch + 1u : 1u;
            GATE();
            WAIT_LGKM0();
            WAIT_VM(0);
            VBAR(9);
            tag = __builtin_amdgcn_readfirstlane(*(const unsigned*)(smem + TAG_OFF));
            if (vw == 0 && lane == 0) __hip_atomic_store((gu32*)(ws + WS_KVFLAG) + (dblk * 32 + sblk) * 32, tag, RLX_AGENT);
            if (vw == 1 && lane < 4) *(u32x4*)(smem + U_OFF + 192 + lane * 16) = (u32x4){0u, 0u, 0u, 0u};
            gu32* kfp = (gu32*)(ws + WS_KVFLAG) + ((cb >> 5) * 32 + vw * 16 + l15) * 32;
            const char* ub = (const char*)kvT + (size_t)__builtin_amdgcn_readfirstlane(cb * 16384 + vw * 8192);
            const unsigned vu = (unsigned)((lane >> 5) * 512 + ((lane >> 2) & 1) * 256 + ((lane >> 3) & 3) * 64 + ((lane - (lane >> 4)) & 3) * 16);
            const unsigned lu = lbase + U_OFF + 256 + vw * 8192;
#define U_DMA1(o) asm volatile("s_add_u32 m0, %2, %3\n\ts_nop 0\n\tglobal_load_lds_dwordx4 %0, %1 sc1\n\tglobal_load_lds_dwordx4 %0, %1 offset:1024 sc1\n\t" \
                             "global_load_lds_dwordx4 %0, %1 offset:2048 sc1\n\tglobal_load_lds_dwordx4 %0, %1 offset:3072 sc1" \
                             :: "v"(vu), "s"(ub + (o)), "s"(lu), "i"(o) : "memory", "scc")
#define U_DMA() do { U_DMA1(0); U_DMA1(4096); FLAGD(UFLAG_OFF + lw * 4); } while (0)
            unsigned fo = lane < 8 ? (unsigned)((tb * 1024 + p_base) * 16 + lane * 128) : lane == 8 ? (unsigned)(WS_HFLAG + (tb * 16 + nb) * 128)
                        : lane == 9 ? (unsigned)(WS_CFLAG + cb * 128) : (unsigned)(WS_GATE + (tb * 16 + nb) * 128);
            asm volatile("" : "+v"(fo));
            GATE(); VBAR(10);
            TOUCH((const char*)(vw ? Cim : Cre) + (size_t)b * 8192, (unsigned)(lane * 128));
            if (vw == 0) TOUCH(ws, fo); else TOUCH(dskip, (unsigned)(d_base * 2));
            {
                const int cl = 64 + lane + ((idx & 15) * 2 + vw) * 7;
                if (lane < 7 && cl < CODE_LINES) CODE_TOUCH(cl);
            }
            GATE(); VBAR(11);
            GATE(); VBAR(12);
            GATE(); VBAR(13);
            unsigned pv;
            asm volatile("global_load_dword %0, %1, off sc1" : "=&v"(pv) : "v"(kfp) : "memory");
            KLOAD();
            GATE(); VBAR(14);
            GATE(); VBAR(15);
            asm volatile("s_waitcnt vmcnt(5)" : "+v"(pv) :: "memory");
            const bool done = __all(pv == tag);
            if (done) U_DMA();
            if (DBGON && TIDX == 384) dbg[BIDX * 64 + 13] = (done ? 100ull : 0ull) | (1ull << 62);
            GATE();
            if (lane == 0) SETW(UFLAG_OFF, vw, 1u);
            WAIT_VM(9);
            if (!done) {
                unsigned spins = 0;
                for (;;) {
                    const unsigned v = __hip_atomic_load(kfp, RLX_AGENT);
                    if (__all(v == tag)) break;
                    if (++spins > SPIN_MAX) break;
                    __builtin_amdgcn_s_sleep(1);
                }
                U_DMA();
            }
        }
        KWAIT(18);
        STAMPT(dbg, 14, 320);
        __builtin_amdgcn_s_sleep(6);
        {
            const unsigned cp = (unsigned)(KC_OFF + lw * CV_W + 64 + lane * 16);
            if (lane == 0) KA[0].x = (KA[0].x & 0xffff0000u) | f2bf(lo16(KA[0].x) + bf2f((u16)draw));
#define WR128(v, off) asm volatile("ds_write_b128 %0, %1 offset:%2" :: "v"(cp), "v"(v), "i"(off) : "memory")
#pragma unroll
            for (int q = 3; q >= 0; --q) WR128(KA[q], q * 1024);
#pragma unroll
            for (int q = 3; q >= 0; --q) {
                const u32x2 p = q ? pred2(KA[q], RL63(KA[q ? q - 1 : 0].z), RL63(KA[q ? q - 1 : 0].w)) : pred2(KA[0], 0u, 0u);
                const u32x4 a = KA[q];
                const u32x4 s1 = {al(a.x, p.y), al(a.y, a.x), al(a.z, a.y), al(a.w, a.z)};
                WR128(s1, q * 1024 + CV_COPY);
                const u32x4 s2 = {p.y, mv(a.x), mv(a.y), mv(a.z)};
                WR128(s2, q * 1024 + 2 * CV_COPY);
                const u32x4 s3 = {al(p.y, p.x), mv(s1.x), mv(s1.y), mv(s1.z)};
                WR128(s3, q * 1024 + 3 * CV_COPY);
            }
            if (lane < 16) *(u32x4*)(smem + KC_OFF + lw * CV_W + (lane >> 2) * CV_COPY + (lane & 3) * 16) = (u32x4){0u, 0u, 0u, 0u};
            SCHED();
        }
        STAMPT(dbg, 26, 320);
        wait_lds4(smem, G1D_OFF);
        wait_lds4(smem, UFLAG_OFF);
        STAMPT(dbg, 9, 256);
        __builtin_amdgcn_s_setprio(PRIO_CV);
        STAMPT(dbg, 10, 384);
        f32x4 cv[8];
#pragma unroll
        for (int g = 0; g < 8; ++g) cv[g] = (f32x4){0.f, 0.f, 0.f, 0.f};
        PIN4(cv[0], cv[1], cv[2], cv[3]); PIN4(cv[4], cv[5], cv[6], cv[7]);
        const int uz = U_OFF + 192;
        const int ua0 = U_OFF + 256 + (2 * l15 + q4 - 2) * 64, rv0 = (2 * l15 + q4 - 2) * 4 + lw * 16;
        const unsigned ka0 = (unsigned)((l15 >> 2) * CV_COPY - 16 * q4 + 8 * (l15 & 3) - 8);
        int ua, rv, pu, pz;
        unsigned adr;
#define UNEXT1() do { pu = ua | (rv & 0x30); asm volatile("" : "+v"(pu)); } while (0)
#define UNEXT2() do { pz = (ua > uz ? ua : uz) | (rv & 0x30); asm volatile("" : "+v"(pz)); } while (0)
#define UNEXT3() do { ua -= 256; rv -= 16; asm volatile("" : "+v"(ua), "+v"(rv)); } while (0)
#define ANEXT() do { adr += 64; asm volatile("" : "+v"(adr)); } while (0)
#define RDU(dst, c) do { dst = (c) ? RD128(pu, (c) * 2048) : RD128(pz, 0); SCHED(); } while (0)
        HPOLL_SETUP();
        unsigned hpv = 0;
        {
            bf16x8 A[8], UB[8];
            ua = ua0; rv = rv0; adr = lbase + KC_OFF + lw * CV_W + ka0 + 96;
            asm volatile("" : "+v"(adr));
            UNEXT1(); UNEXT2(); UNEXT3();
#pragma unroll
            for (int i = 0; i < 8; ++i) { RDA(A[7 - i], adr, 7 - i); RDU(UB[i], i); }
            unsigned gb = lw >= 2 ? (unsigned)((tb * 16 + nb) * 128 + (lw - 2) * 32) : 0x80000000u;
            asm volatile("" : "+s"(gb));
            const unsigned gso = (l15 == 0) ? gb + (unsigned)(q4 * 8) : 0x80000000u;
            float gt = 0.f;
            unsigned vxo = 0, vdo = 0, vgo = 0;
            auto gate_out = [&](int i) __attribute__((always_inline)) {
                if (i == 0) gacc += __uint_as_float(__builtin_amdgcn_update_dpp(0u, __float_as_uint(gacc), 0xb1, 0xf, 0xf, false));
                if (i == 1) gacc += __uint_as_float(__builtin_amdgcn_update_dpp(0u, __float_as_uint(gacc), 0x4e, 0xf, 0xf, false));
                if (i == 2) gacc += __uint_as_float(__builtin_amdgcn_update_dpp(0u, __float_as_uint(gacc), 0x141, 0xf, 0xf, false));
                if (i == 3) gacc += __uint_as_float(__builtin_amdgcn_update_dpp(0u, __float_as_uint(gacc), 0x140, 0xf, 0xf, false));
                if (i == 4) gacc = rbf(gacc);
                if (i == 5) gacc = __builtin_amdgcn_exp2f(gacc * -1.44269504088896f);
                if (i == 6) gacc = __builtin_amdgcn_rcpf(1.0f + gacc);
                if (i == 7) gacc = rbf(gacc);
                if (i == 8) gt = __uint_as_float(pkbf(gacc, 1.0f - gacc));
                if (i == 9) __builtin_amdgcn_raw_buffer_store_b64((u32x2){__float_as_uint(gt), tag}, gs, gso, 0, WT);
                asm volatile("" : "+v"(gacc), "+v"(gt));
            };
            auto step = [&](auto E) __attribute__((always_inline)) {
                constexpr int e = decltype(E)::value;
                constexpr int ti = (e == CS0) ? 0 : (e == CS1) ? 1 : (e == CS2) ? 2 : -1, tj = ti < 0 ? 0 : ti;
                constexpr unsigned char GA[36] = {0x66, 0x77, 0x76, 0x44, 0x55, 0x54, 0x65, 0x64, 0x75, 0x74, 0x22, 0x33, 0x32, 0x43, 0x42, 0x53, 0x52, 0x63, 0x62, 0x73, 0x72,
                                                  0x00, 0x11, 0x10, 0x21, 0x20, 0x31, 0x30, 0x41, 0x40, 0x51, 0x50, 0x61, 0x60, 0x71, 0x70};
                const char* sb_ = (const char*)Cre + COFF(wid, tj + 1);
                const char* sc_ = (const char*)Cim + COFF(wid, tj + 1);
#pragma unroll
                for (int q = 0; q < 36; ++q) {
                    const int g = GA[q] >> 4, a = GA[q] & 15;
                    MF(cv[g], A[a], UB[g - a]); SCHED();
                    if (e < 7) {
                        if (a == 1) RDU(UB[g - 1], g - 1);
                        if (q == 35) RDU(UB[7], 7);
                        if (g == 7) RDA(A[a], adr + 64 * (e + 1), a);
                        if (q == 11) UNEXT1();
                        if (q == 13) UNEXT2();
                        if (q == 15) UNEXT3();
                    }
                    if (ti >= 0) {
                        const int n = (q >= 3 && q <= 7) ? q - 3 : q == 10 ? 5 : q == 12 ? 6 : q == 14 ? 7 : -1;
                        if (q == 3) { if (ti == 0) WAIT_VM(1); else if (e == XE + 1) WAIT_VM(7); else WAIT_VM(0); }
                        if (n >= 0) CRD(tj, n & 7);
                        if (q == 18) CPIN(tj);
                        if (q == 18 || q == 23) asm volatile("s_add_u32 m0, %3, %4\n\ts_nop 0\n\tglobal_load_lds_dwordx4 %0, %2\n\tglobal_load_lds_dwordx4 %1, %2 offset:1024"
                                                             :: "v"(vB0), "v"(vB1), "s"(q == 18 ? sb_ : sc_), "s"(cst0), "i"(q == 18 ? 0 : 4096) : "memory", "scc");
                        if (q == 21 || q == 25) asm volatile("global_load_lds_dwordx4 %0, %2 offset:2048\n\tglobal_load_lds_dwordx4 %1, %2 offset:3072"
                                                             :: "v"(vB0), "v"(vB1), "s"(q == 21 ? sb_ : sc_) : "memory");
                    }
                    if (e == 0 && q >= 3 && q <= 7) gate_out(q - 3);
                    if (e == 0 && q >= 10 && q <= 18 && !(q & 1)) gate_out(5 + (q - 10) / 2);
                    if (e == 0 && q == 4) CTILE(wid, 0, cst0);
                    if (e == XE && q >= 23 && q <= 35 && (q & 1)) {
                        const int i = (q - 23) / 2;
                        const __amdgpu_buffer_rsrc_t xs = __builtin_amdgcn_make_buffer_rsrc((void*)x, 0, SEQ * DM * 2, 0x00020000);
                        const __amdgpu_buffer_rsrc_t ks = __builtin_amdgcn_make_buffer_rsrc((void*)dskip, 0, DM * 2, 0x00020000);
                        if (i < 4) asm volatile("buffer_load_dwordx2 %0, %1, %2, %3 offen offset:%4" : "=&v"(xx[(i >> 1) & 1][i & 1]) : "v"(vxo), "s"(xs), "s"((TR((i >> 1) & 1) * DM + d_base) * 2), "i"((i & 1) * 2048) : "memory");
                        if (i == 4) asm volatile("buffer_load_dwordx2 %0, %1, %2, %3 offen" : "=&v"(dk2) : "v"(vdo), "s"(ks), "s"(d_base * 2) : "memory");
                        if (i >= 5) asm volatile("buffer_load_dwordx4 %0, %1, %2, %3 offen sc1" : "=&v"(gv[(i - 5) & 1]) : "v"(vgo), "s"(gs), "s"(GSO((i - 5) & 1)) : "memory");
                    }
                    if (e == XE && q == 16) { vxo = (unsigned)(q4 * 32 * DM * 2 + l15 * 8); vdo = (unsigned)(l15 * 8); asm volatile("" : "+v"(vxo), "+v"(vdo)); }
                    if (e == XE && q == 17) { vgo = GV(q4); asm volatile("" : "+v"(vgo)); }
                    if (e == 6 && q == 30) hpv = __hip_atomic_load(hfp, RLX_AGENT);
                    SCHED();
                }
            };
            unroll<8>(step);
            WAIT_VM(0);
            XPIN();
        }
        PAD8(cv[0], cv[1], cv[2], cv[3]); PIN4(cv[4], cv[5], cv[6], cv[7]);
        STAMPT(dbg, 11, 384);
        const unsigned vh = (unsigned)((lane ^ 2) * 16);
        if (!__all(hpv == tag)) HWAIT();
        asm volatile("" ::: "memory");
#pragma unroll
        for (int f = 0; f < DEPTH; ++f) LOADF(f);
        STAMPT(dbg, 25, 384);
        {
            const __amdgpu_buffer_rsrc_t yrs = __builtin_amdgcn_make_buffer_rsrc(y2T, 0, DM * SEQ * 2, 0x00020000);
            const unsigned yo = (unsigned)(d * 4096 + (q4 & 1) * 512 + (l15 >> 3) * 256 + (q4 >> 1) * 128 + (l15 & 7) * 16);
#pragma unroll
            for (int g = 0; g < 8; g += 2) {
                const auto r0 = __builtin_amdgcn_permlane16_swap(pkbf(cv[g][0], cv[g][1]), pkbf(cv[g + 1][0], cv[g + 1][1]), false, false);
                const auto r1 = __builtin_amdgcn_permlane16_swap(pkbf(cv[g][2], cv[g][3]), pkbf(cv[g + 1][2], cv[g + 1][3]), false, false);
                __builtin_amdgcn_raw_buffer_store_b128((u32x4){r0[0], r1[0], r0[1], r1[1]}, yrs, yo + (unsigned)(g * 512), 0, WT);
            }
        }
        asm volatile("" ::: "memory");
        STAMPT(dbg, 15, 384);
        __builtin_amdgcn_s_setprio(PRIO2_L);
    }
    {
        const int lane = fresh_lane(), l15 = lane & 15, q4 = lane >> 4;
        const int ring = wid >> 2;
        const unsigned va = (unsigned)((lane ^ (ring * 2)) * 16), vh = va;
        const unsigned tag = lds_word(smem, TAG_OFF);
        const int cb0 = ring ? KC_OFF : GA_OFF, cst = ring ? CV_W : 8192;
        f32x4 ac[2][4];
        unsigned ca[2], fa[2], fl = 0, fv = 0;
#define CHAIN(u) do { const int c_ = (wid - ((u) >> 1)) & 3; ca[(u) & 1] = va + (unsigned)(cb0 + c_ * cst + ((u) & 1) * 4096); \
                      fa[(u) & 1] = (unsigned)(RF_OFF + ring * 32 + ((u) & 1) * 16 + c_ * 4); asm volatile("" : "+v"(ca[(u) & 1]), "+v"(fa[(u) & 1])); } while (0)
#define RDIN(dst, a, f_) asm volatile("ds_read_b32 %0, %5\n\tds_read_b128 %1, %6\n\tds_read_b128 %2, %6 offset:1024\n\tds_read_b128 %3, %6 offset:2048\n\tds_read_b128 %4, %6 offset:3072" \
                     : "=&v"(fl), "=&v"(dst[0]), "=&v"(dst[1]), "=&v"(dst[2]), "=&v"(dst[3]) : "v"(f_), "v"(a) : "memory")
#define LWAIT(dst) asm volatile("s_waitcnt lgkmcnt(0)" : "+v"(fl), "+v"(dst[0]), "+v"(dst[1]), "+v"(dst[2]), "+v"(dst[3]) :: "memory")
        CHAIN(0); CHAIN(1);
        WAIT_LGKM0();
        CPIN(0); CPIN(1); CPIN(2);
        CREAD(3);
        int tr[2] = {TR(0), TR(1)};
        unsigned gw[2][2];
        {
            unsigned spins = 0;
            while (__builtin_expect(!__all((gv[0].y == tag) & (gv[0].w == tag) & (gv[1].y == tag) & (gv[1].w == tag)), 0)) {
                if (++spins > SPIN_MAX) break;
#pragma unroll
                for (int r = 0; r < 2; ++r) asm volatile("s_nop 4\n\tbuffer_load_dwordx4 %0, %1, %2, %3 offen sc1\n\ts_waitcnt vmcnt(0)" : "=&v"(gv[r]) : "v"(GV(q4)), "s"(gs), "s"(__builtin_amdgcn_readfirstlane(GSO(r))) : "memory");
            }
            gw[0][0] = gv[0].x; gw[0][1] = gv[0].z; gw[1][0] = gv[1].x; gw[1][1] = gv[1].z;
            PIN4(gw[0][0], gw[0][1], gw[1][0], gw[1][1]);
        }
        unsigned cpv = 0, yy[2][4];
        f32x2 zz[2][4];
        unsigned exo = 0, exi = 0, efo = 0, efi = 0;
        auto send = [&](int r) __attribute__((always_inline)) {
            const unsigned one = 1u;
#pragma unroll
            for (int n = 0; n < 4; ++n) { const f32x2 v = {ac[r][n][2], ac[r][n][3]}; asm volatile("ds_write_b64 %0, %1 offset:%2" :: "v"(exo), "v"(v), "i"(r * 4096 + n * 512) : "memory"); }
            asm volatile("ds_write_b32 %0, %1 offset:%2" :: "v"(efo), "v"(one), "i"(r * 4) : "memory");
        };
        STAMPT(dbg, 5, 0);
        auto body = [&](auto F) __attribute__((always_inline)) {
            constexpr int f = decltype(F)::value;
            constexpr int u = f >> 3, k = f & 7;
            f32x4* an = ac[u & 1];
            f32x4* ao = ac[(u + 1) & 1];
#pragma unroll
            for (int n = 0; n < 4; ++n) {
                if (k == 0 && u < 2) MF0(an[n], af[f % DEPTH], cf_[k][n]); else MF(an[n], af[f % DEPTH], cf_[k][n]);
                SCHED();
                const int g = k * 4 + n;
                if (u >= 1 && u < 7) {
                    if (g == 1) { fv = ((u - 1) >> 1) + 1; asm volatile("" : "+v"(fv)); }
                    if (g == 4 || g == 6 || g == 8 || g == 10) asm volatile("ds_write_b128 %0, %1 offset:%2" :: "v"(ca[(u + 1) & 1]), "v"(ao[(g - 4) / 2]), "i"((g - 4) * 512) : "memory");
                    if (g == 12) asm volatile("ds_write_b32 %0, %1" :: "v"(fa[(u + 1) & 1]), "v"(fv) : "memory");
                    if (g == 18) CHAIN(u + 1);
                    if (g == 20) asm volatile("ds_read_b32 %0, %1" : "=&v"(fl) : "v"(fa[(u + 1) & 1]) : "memory");
                    if (g > 20 && g <= 24) asm volatile("ds_read_b128 %0, %1 offset:%2" : "=&v"(ao[g - 21]) : "v"(ca[(u + 1) & 1]), "i"((g - 21) * 1024) : "memory");
                    if (g == 30) {
                        LWAIT(ao);
                        if (__builtin_expect(__builtin_amdgcn_readfirstlane(fl) != (unsigned)((u + 1) >> 1), 0)) {
                            unsigned spins = 0;
#pragma unroll 1
                            do {
                                __builtin_amdgcn_s_sleep(1);
                                RDIN(ao, ca[(u + 1) & 1], fa[(u + 1) & 1]); LWAIT(ao);
                                if (++spins > SPIN_MAX * 16u) break;
                            } while (__builtin_amdgcn_readfirstlane(fl) != (unsigned)((u + 1) >> 1));
                        }
                    }
                }
                if (u == 7 && g == 6) send(0);
                if (f >= ZF && f < ZF + 4) {
                    constexpr int r = ((f - ZF) >> 1) & 1, j = (f - ZF) & 1;
                    const unsigned xw = xx[r][j][n >> 1], dw = dk2[n >> 1];
                    const float xf = (n & 1) ? hi16(xw) : lo16(xw), df = (n & 1) ? hi16(dw) : lo16(dw), yf = j ? hi16(yy[r][n]) : lo16(yy[r][n]);
                    zz[r][n][j] = xf * fmaf(hi16(gw[r][j]), yf, lo16(gw[r][j]) * df);
                    asm volatile("" : "+v"(zz[r][n]));
                }
                if (f == ZF + 4) {
                    zz[0][n] = (f32x2){fmaf(lo16(gw[0][0]), ac[0][n][0], zz[0][n][0]), fmaf(lo16(gw[0][1]), ac[0][n][1], zz[0][n][1])};
                    asm volatile("" : "+v"(zz[0][n]));
                }
                SCHED();
            }
            if (f + DEPTH < 64) LOADF(f + DEPTH);
            if (f == DEPTH + 1 && ring) {
                WAIT_VM(6);
                if (lane == 0) __hip_atomic_store((gu32*)(ws + WS_CFLAG) + cb * 32 + (wid & 3), tag, RLX_AGENT);
                SCHED();
            }
            if (f == YPOLL && ring) { const int l15 = fresh_lane() & 15; cpv = __hip_atomic_load((gu32*)(ws + WS_CFLAG) + (nb * 16 + (wid & 3) * 4 + (l15 >> 2)) * 32 + (l15 & 3), RLX_AGENT); SCHED(); }
            if (f == YPOLL + DEPTH && ring) {
                const int ln = fresh_lane(), l15 = ln & 15, q4 = ln >> 4;
                if (__builtin_expect(!__all(cpv == tag), 0)) {
                    unsigned spins = 0;
                    while (!__all(__hip_atomic_load((gu32*)(ws + WS_CFLAG) + (nb * 16 + (wid & 3) * 4 + (l15 >> 2)) * 32 + (l15 & 3), RLX_AGENT) == tag)) {
                        if (++spins > SPIN_MAX) break;
                        __builtin_amdgcn_s_sleep(1);
                    }
                }
                const unsigned lx = lbase + LA_OFF + (wid & 3) * 8192;
#pragma unroll
                for (int i = 0; i < 4; ++i) {
                    const unsigned vo = (unsigned)((d_base + (wid & 3) * 16 + i * 4 + q4) * 4096 + tb * 256 + ((l15 ^ ((wid & 3) * 4 + i)) * 16));
                    GLDS_SC1(y2T, vo, lx, i * 1024);
                }
                SCHED();
            }
            if (f == YPOLL + 2 * DEPTH + 1 && ring) {
                WAIT_VM(6);
                SETW(YFLAG_OFF, wid & 3, 1u);
                SCHED();
            }
            if (f == 55) {
                const int ln = fresh_lane(), c_ = (wid - 3) & 3;
                const int eg = GA_OFF + c_ * 8192, el = KC_OFF + c_ * CV_W;
                exo = (unsigned)((ring ? el : eg) + ln * 8);
                exi = (unsigned)((ring ? eg : el) + ln * 8);
                efo = (unsigned)(EF_OFF + wid * 8); efi = (unsigned)(EF_OFF + (wid ^ 4) * 8);
                PIN4(exo, exi, efo, efi);
                SCHED();
            }
            if (f == ZF - 2) {
                const int ln = fresh_lane(), l15 = ln & 15, q4 = ln >> 4;
                wait_lds4(smem, YFLAG_OFF);
#pragma unroll
                for (int r = 0; r < 2; ++r) {
                    const int jb = JB(6 + r);
                    const int ya = LA_OFF + (l15 >> 2) * 8192 + (l15 & 3) * 1024 + (((((jb >> 1) & 1) * 8 + (jb >> 2) + q4 * 2) ^ l15) * 16) + (jb & 1) * 8 + ring * 4;
#pragma unroll
                    for (int e = 0; e < 4; ++e) yy[r][e] = *(const unsigned*)(smem + ya + e * 256);
                }
                SCHED();
            }
        };
        unroll<64>(body);
        PAD8(ac[1][0], ac[1][1], ac[1][2], ac[1][3]);
        send(1);
        STAMPT(dbg, 6, 0);
        const __amdgpu_buffer_rsrc_t ors = __builtin_amdgcn_make_buffer_rsrc(out, 0, SEQ * DM * 4, 0x00020000);
        const unsigned ov = (unsigned)((lane >> 4) * (32 * DM * 4) + (lane & 15) * 16);
#pragma unroll
        for (int r = 0; r < 2; ++r) {
            f32x2 in[4];
#define EXRD() asm volatile("ds_read_b32 %0, %5 offset:%7\n\tds_read_b64 %1, %6 offset:%8\n\tds_read_b64 %2, %6 offset:%9\n\tds_read_b64 %3, %6 offset:%10\n\tds_read_b64 %4, %6 offset:%11" \
                             : "=&v"(fl), "=&v"(in[0]), "=&v"(in[1]), "=&v"(in[2]), "=&v"(in[3]) \
                             : "v"(efi), "v"(exi), "i"(r * 4), "i"(r * 4096), "i"(r * 4096 + 512), "i"(r * 4096 + 1024), "i"(r * 4096 + 1536) : "memory")
            EXRD();
            if (r == 0) {
#pragma unroll
                for (int n = 0; n < 4; ++n) {
                    zz[1][n] = (f32x2){fmaf(lo16(gw[1][0]), ac[1][n][0], zz[1][n][0]), fmaf(lo16(gw[1][1]), ac[1][n][1], zz[1][n][1])};
                    asm volatile("" : "+v"(zz[1][n]));
                }
            }
            LWAIT(in);
            if (__builtin_expect(__builtin_amdgcn_readfirstlane(fl) == 0u, 0)) {
                unsigned spins = 0;
#pragma unroll 1
                do {
                    EXRD(); LWAIT(in);
                    if (++spins > SPIN_MAX * 16u) break;
                } while (__builtin_amdgcn_readfirstlane(fl) == 0u);
            }
            f32x4 o[2];
#pragma unroll
            for (int j = 0; j < 2; ++j)
#pragma unroll
                for (int e = 0; e < 4; ++e) o[j][e] = fmaf(lo16(gw[r][j]), in[e][j], zz[r][e][j]);
#pragma unroll
            for (int j = 0; j < 2; ++j)
                __builtin_amdgcn_raw_buffer_store_b128(__builtin_bit_cast(u32x4, o[j]), ors, ov + (unsigned)(((tr[r] + j) * DM + d_base) * 4), 0, WT);
        }
        STAMPT(dbg, 7, 0);
        STAMPT(dbg, 16, 384);
        if (tb == 15 && nb == 0 && wid == 0 && lane == 0) __hip_atomic_store((gu32*)(ws + WS_EPOCH), tag, RLX_AGENT);
    }
}

extern "C" void kernel_launch(void* const* d_in, const int* in_sizes, int n_in,
                              void* d_out, int out_size, void* d_ws, size_t ws_size,
                              hipStream_t stream) {
    const u16* x = (const u16*)d_in[0];
    const u16* lre = (const u16*)d_in[1];
    const u16* lim = (const u16*)d_in[2];
    const u16* Bre = (const u16*)d_in[3];
    const u16* Bim = (const u16*)d_in[4];
    const u16* Cre = (const u16*)d_in[5];
    const u16* Cim = (const u16*)d_in[6];
    const u16* logdt = (const u16*)d_in[7];
    const u16* dskip = (const u16*)d_in[8];
    const u16* kin = (const u16*)d_in[10];
    const u16* vin = (const u16*)d_in[11];
    const u16* shiftk = (const u16*)d_in[12];
    const u16* ssmk = (const u16*)d_in[13];
    const u16* ssmD = (const u16*)d_in[14];
    const u16* gatew = (const u16*)d_in[15];
    float* out = (float*)d_out;

    char* ws = (char*)d_ws;

    hipFuncSetAttribute((const void*)k1_kernel, hipFuncAttributeMaxDynamicSharedMemorySize, K1_LDS);

    k1_kernel<<<256, 512, K1_LDS, stream>>>(x, Bre, Bim, kin, vin, shiftk, gatew, ws, lre, lim, logdt, ssmk, ssmD, Cre, Cim, dskip, out);
}
#if defined(__HIP_DEVICE_COMPILE__)
#pragma clang attribute pop
#endif
```

```cpp
#if defined(__HIP_DEVICE_COMPILE__)
#pragma clang attribute push(__attribute__((target("no-packed-fp32-ops"))), apply_to = function)
#endif
#include <hip/hip_runtime.h>
#include <hip/hip_bf16.h>
#include <stdint.h>

#define TIDX ((int)__builtin_amdgcn_workitem_id_x())
#define BIDX ((int)__builtin_amdgcn_workgroup_id_x())
typedef unsigned short u16;
typedef __attribute__((ext_vector_type(8))) short bf16x8;
typedef __attribute__((ext_vector_type(4))) float f32x4;
typedef __attribute__((ext_vector_type(2))) float f32x2;
typedef __attribute__((ext_vector_type(2))) __bf16 bf16x2;
typedef __attribute__((ext_vector_type(4))) unsigned u32x4;
typedef __attribute__((ext_vector_type(2))) unsigned u32x2;
typedef __attribute__((address_space(1))) unsigned long long gu64;
typedef __attribute__((address_space(1))) unsigned gu32;
typedef __attribute__((address_space(3))) void lds_void;
typedef __attribute__((address_space(3))) unsigned lds_u32;
typedef __attribute__((ext_vector_type(4))) short s16x4;
typedef __attribute__((address_space(3))) s16x4 lds_s16x4;
#define TRD(a) __builtin_amdgcn_ds_read_tr16_b64_v4i16((lds_s16x4*)(size_t)(a))

#define DBGON 0
#define STAMPT(buf, k, t) do { } while (0)
#define CODE_LINES 260
#define SEQ 2048
#define DM 1024
#define SPIN_MAX (1u << 18)
#define PRIO_G 2
#define PSLEEP 4
#define PRIO_L 3
#define WT 16

__device__ __forceinline__ float bf2f(u16 v) { return __uint_as_float(((unsigned)v) << 16); }
__device__ __forceinline__ u16 f2bf(float f) { __bf16 b = (__bf16)f; return __builtin_bit_cast(u16, b); }
__device__ __forceinline__ float rbf(float f) { return bf2f(f2bf(f)); }
__device__ __forceinline__ unsigned pkbf(float lo, float hi) { return __builtin_bit_cast(unsigned, __builtin_convertvector((f32x2){lo, hi}, bf16x2)); }
__device__ __forceinline__ float lo16(unsigned u) { return __uint_as_float(u << 16); }
__device__ __forceinline__ float hi16(unsigned u) { return __uint_as_float(u & 0xffff0000u); }

struct cf { float r, i; };
__device__ __forceinline__ cf cmul(cf a, cf b) { return {a.r * b.r - a.i * b.i, a.r * b.i + a.i * b.r}; }
__device__ __forceinline__ cf cfma(cf a, cf b, cf c) {
    return {fmaf(a.r, b.r, fmaf(-a.i, b.i, c.r)), fmaf(a.r, b.i, fmaf(a.i, b.r, c.i))};
}
__device__ __forceinline__ f32x4 splat4(float v) { return (f32x4){v, v, v, v}; }
__device__ __forceinline__ f32x4 fma4(f32x4 a, f32x4 b, f32x4 c) { return __builtin_elementwise_fma(a, b, c); }
__device__ __forceinline__ f32x4 mul4(f32x4 a, f32x4 b) { return a * b; }

__device__ __forceinline__ float red32(float a, float b) {
    const auto r = __builtin_amdgcn_permlane32_swap(__float_as_uint(a), __float_as_uint(b), false, false);
    return __uint_as_float(r[0]) + __uint_as_float(r[1]);
}
__device__ __forceinline__ float red16(float x) {
    const auto r = __builtin_amdgcn_permlane16_swap(__float_as_uint(x), __float_as_uint(x), false, false);
    return __uint_as_float(r[0]) + __uint_as_float(r[1]);
}
__device__ __forceinline__ int fresh_lane() {
    int l;
    asm volatile("v_mbcnt_lo_u32_b32 %0, -1, 0\n\tv_mbcnt_hi_u32_b32 %0, -1, %0" : "=v"(l));
    return l;
}
#define BAR() __builtin_amdgcn_s_barrier()
#define WAIT_VM(n) asm volatile("s_waitcnt vmcnt(" #n ")" ::: "memory")
#define WAIT_LGKM0() __builtin_amdgcn_s_waitcnt(0xC07F)
#define SCHED() __builtin_amdgcn_sched_barrier(0)
#define MF(c, a, b) asm volatile("v_mfma_f32_16x16x32_bf16 %0, %1, %2, %0" : "+v"(c) : "v"(a), "v"(b))
#define PAD8(c0, c1, c2, c3) asm volatile("s_nop 7" : "+v"(c0), "+v"(c1), "+v"(c2), "+v"(c3))
#define PIN4(c0, c1, c2, c3) asm volatile("" : "+v"(c0), "+v"(c1), "+v"(c2), "+v"(c3))
#define GLDS_S(sbase, voff, sl, IMM) \
    asm volatile("s_add_u32 m0, %2, %3\n\ts_nop 0\n\tglobal_load_lds_dwordx4 %0, %1" \
                 :: "v"(voff), "s"(sbase), "s"(sl), "i"(IMM) : "memory", "scc")
#define GLDS_K(sbase, voff, sl, IMM) \
    asm volatile("s_add_u32 m0, %2, %3\n\ts_nop 0\n\tglobal_load_lds_dwordx4 %0, %1 nt" \
                 :: "v"(voff), "s"(sbase), "s"(sl), "i"(IMM) : "memory", "scc")
#define RLX_AGENT __ATOMIC_RELAXED, __HIP_MEMORY_SCOPE_AGENT

#define UNITS_A 0x0756075607560560ull
#define UNITS_B 0x1432143214321432ull
#define RA(t) ((int)((UNITS_A >> (4 * (t))) & 15) * 16384)
#define RB(t) ((int)((UNITS_B >> (4 * (t))) & 15) * 16384)
#define LA_OFF 0
#define GA_OFF 32768
#define CV_COPY 4160
#define CV_W (4 * CV_COPY)
#define KC_OFF 65536
#define ST_OFF 114688
#define ST_V 8832
#define ST_SZ (ST_V + 8 * 1056)
#define U_OFF (KC_OFF + 16 * CV_COPY)
#define HT_OFF (U_OFF + 256 + 16384)
#define GW_OFF (HT_OFF + 8192)
#define TAG_OFF (GW_OFF + 2048)
#define UFLAG_OFF (TAG_OFF + 16)
#define G1D_OFF (UFLAG_OFF + 16)
#define MISC_OFF (G1D_OFF + 16)
#define YFLAG_OFF (MISC_OFF + 16)
#define RF_OFF (YFLAG_OFF + 16)
#define EF_OFF (RF_OFF + 64)
#define COK_OFF (EF_OFF + 64)
#define NFLAGW 52
#define DUMMY_OFF (COK_OFF + 16)
#define K1_LDS (DUMMY_OFF + 256)
#define WS_GRAN 0
#define WS_KVFLAG (256u << 10)
#define WS_EPOCH (WS_KVFLAG + 65536)
#define WS_CFLAG (WS_EPOCH + 4096)
#define WS_HFLAG (384u << 10)
#define WS_GATE (512u << 10)

#define CODE_TOUCH(vl) asm volatile("s_getpc_b64 s[98:99]\n\ts_add_u32 s98, s98, k1_kernel@rel32@lo+4\n\ts_addc_u32 s99, s99, k1_kernel@rel32@hi+12\n\t" \
                                    "s_mov_b32 m0, %1\n\ts_nop 0\n\tglobal_load_lds_dword %0, s[98:99]" :: "v"((unsigned)(vl) * 128u), "i"(DUMMY_OFF) : "s98", "s99", "scc", "memory")
#define RD128(a, off) (*(const __attribute__((address_space(3))) bf16x8*)(size_t)(unsigned)((a) + (off)))
#define RDA(dst, A, F) do { const s16x4 lo_ = TRD((A) + 512 * (F) + 8); SCHED(); const s16x4 hi_ = TRD((A) + 512 * (F)); SCHED(); \
                            dst = __builtin_shufflevector(lo_, hi_, 0, 1, 2, 3, 4, 5, 6, 7); } while (0)
#define TOUCH(sbase, voff) asm volatile("s_mov_b32 m0, %2\n\ts_nop 0\n\tglobal_load_lds_dword %0, %1" :: "v"(voff), "s"(sbase), "i"(DUMMY_OFF) : "memory")
#define FLAGD(off) do { unsigned long long pc_; asm volatile("s_getpc_b64 %0\n\ts_mov_b32 m0, %2\n\ts_mov_b64 exec, 1\n\tglobal_load_lds_dword %1, %0\n\ts_mov_b64 exec, -1" \
                                                             : "=&s"(pc_) : "v"(0u), "s"(__builtin_amdgcn_readfirstlane(off)) : "memory"); } while (0)
#define GLDS_SC1(sbase, voff, sl, IMM) \
    asm volatile("s_add_u32 m0, %2, %3\n\ts_nop 0\n\tglobal_load_lds_dwordx4 %0, %1 sc1" \
                 :: "v"(voff), "s"(sbase), "s"(sl), "i"(IMM) : "memory", "scc")
__device__ __forceinline__ unsigned mv(unsigned v) { unsigned r; asm("v_mov_b32 %0, %1" : "=v"(r) : "v"(v)); return r; }
__device__ __forceinline__ unsigned al(unsigned hi, unsigned lo) { unsigned r; asm volatile("v_alignbit_b32 %0, %1, %2, 16" : "=v"(r) : "v"(hi), "v"(lo)); return r; }
__device__ __forceinline__ u32x2 pred2(const u32x4& a, unsigned f0, unsigned f1) {
    return (u32x2){(unsigned)__builtin_amdgcn_update_dpp((int)f0, (int)a.z, 0x138, 0xf, 0xf, false), (unsigned)__builtin_amdgcn_update_dpp((int)f1, (int)a.w, 0x138, 0xf, 0xf, false)};
}
#define RL63(v) ((unsigned)__builtin_amdgcn_readlane((int)(v), 63))
__device__ __forceinline__ void wait_lds4(char* smem, int off) {
    const unsigned a = (unsigned)(size_t)(lds_void*)smem + off;
    unsigned spins = 0;
    for (;;) {
        u32x4 f;
        asm volatile("ds_read_b128 %0, %1\n\ts_waitcnt lgkmcnt(0)" : "=&v"(f) : "v"(a) : "memory");
        if (__builtin_amdgcn_readfirstlane((f.x != 0u) & (f.y != 0u) & (f.z != 0u) & (f.w != 0u))) break;
        if (++spins > SPIN_MAX * 16u) break;
        __builtin_amdgcn_s_sleep(1);
    }
}
__device__ __forceinline__ unsigned lds_word(char* smem, int off) {
    const unsigned a = (unsigned)(size_t)(lds_void*)smem + off;
    unsigned f;
    asm volatile("ds_read_b32 %0, %1\n\ts_waitcnt lgkmcnt(0)" : "=&v"(f) : "v"(a) : "memory");
    return __builtin_amdgcn_readfirstlane(f);
}
__device__ __forceinline__ void wait_lds1(char* smem, int off) {
    unsigned spins = 0;
    while (lds_word(smem, off) == 0u) {
        if (++spins > SPIN_MAX * 16u) break;
        __builtin_amdgcn_s_sleep(1);
    }
}
#define SETW(off, i, v) __hip_atomic_store((lds_u32*)(smem + (off)) + (i), (v), __ATOMIC_RELAXED, __HIP_MEMORY_SCOPE_WORKGROUP)

#define PRIO_CV 1
#define PRIO_KV 3
#define CS0 1
#define CS1 3
#define CS2 5
#define XE 4
#define YPOLL 32
#define ZF 59
template <int I> struct IC { static constexpr int value = I; };
template <int N, int I = 0, class F> __device__ __forceinline__ void unroll(F& f) { if constexpr (I < N) { f(IC<I>{}); unroll<N, I + 1>(f); } }
#define MF0(c, a, b) asm volatile("v_mfma_f32_16x16x32_bf16 %0, %1, %2, 0" : "=&v"(c) : "v"(a), "v"(b))
#define DEPTH 6
#define PRIO2_G 0
#define PRIO2_L 1
__device__ __forceinline__ cf pick4(int q, cf a1, cf a2, cf a3) {
    return {q == 0 ? 1.f : q == 1 ? a1.r : q == 2 ? a2.r : a3.r, q == 0 ? 0.f : q == 1 ? a1.i : q == 2 ? a2.i : a3.i};
}
#define PINC(c) asm volatile("" : "+v"((c).r), "+v"((c).i))

extern "C" __global__ __launch_bounds__(512, 2) void k1_kernel(
        const u16* __restrict__ x, const u16* __restrict__ Bre, const u16* __restrict__ Bim,
        const u16* __restrict__ kin, const u16* __restrict__ vin, const u16* __restrict__ shiftk, const u16* __restrict__ gatew, char* ws,
        const u16* __restrict__ lre, const u16* __restrict__ lim, const u16* __restrict__ logdt,
        const u16* __restrict__ ssmk, const u16* __restrict__ ssmD,
        const u16* __restrict__ Cre, const u16* __restrict__ Cim, const u16* __restrict__ dskip, float* __restrict__ out) {
    extern __shared__ __attribute__((aligned(16))) char smem[];
#define h ((u16*)(ws + (1u << 20)))
#define kvT ((u16*)(ws + (9u << 20)))
#define y2T ((u16*)(ws + (13u << 20)))
#define dbg ((unsigned long long*)(ws + (64u << 20)))
    const int tid = TIDX;
    if (tid < 40 && tid + ((BIDX >> 3) & 7) * 40 < CODE_LINES) CODE_TOUCH(tid + ((BIDX >> 3) & 7) * 40);
    const int lane = tid & 63;
    int wid = __builtin_amdgcn_readfirstlane(tid >> 6);
    const int b = BIDX;
    const int xcd = b & 7, idx = b >> 3;
    const int tb = ((xcd >> 1) ^ (xcd >> 2)) * 4 + (idx & 3), nb = (xcd & 1) * 8 + (idx >> 2);
    const int cb = xcd * 32 + idx;
    const int t_base = tb * 128, p_base = nb * 64, d_base = nb * 64;
    const int l15 = lane & 15, q4 = lane >> 4;
    unsigned stv = 0, sth = 0;
    STAMPT(dbg, 0, 0);
    const unsigned lbase = (unsigned)(size_t)(lds_void*)smem;
    const int rot1 = (xcd >> 1) * 4 + (xcd & 1) * 8 + (idx >> 2) + (idx & 1);
    unsigned voA[4] = {0u, 0u, 0u, 0u}, voB[4] = {0u, 0u, 0u, 0u}, lw0 = 0;
    if (wid == 4 || wid == 5) {
        __builtin_amdgcn_s_setprio(PRIO_L);
        const int lh = wid - 4;
        const unsigned ch = (unsigned)(((lane & 7) ^ (lane >> 4)) * 16);
        voA[0] = (unsigned)(t_base + lh * 16 + (lane >> 5) * 32 + ((lane >> 3) & 3)) * 2048u + ch;
        voA[1] = (voA[0] ^ 64u) + (131072u - 1024u);
        voA[2] = voA[0] + 6144u; voA[3] = voA[1] + 6144u;
        voB[0] = (unsigned)(p_base + lh * 32 + (lane >> 3)) * 2048u + ch;
        voB[1] = (voB[0] ^ 64u) + (16384u - 1024u);
        voB[2] = voB[0] + 30720u; voB[3] = voB[1] + 30720u;
        PIN4(voA[0], voA[1], voA[2], voA[3]); PIN4(voB[0], voB[1], voB[2], voB[3]);
#define GLDS4(sbase, v0, v1, v2, v3, sl, IMM) \
            asm volatile("s_add_u32 m0, %5, %6\n\ts_nop 0\n\tglobal_load_lds_dwordx4 %0, %4\n\tglobal_load_lds_dwordx4 %1, %4 offset:1024\n\t" \
                         "global_load_lds_dwordx4 %2, %4 offset:2048\n\tglobal_load_lds_dwordx4 %3, %4 offset:3072" \
                         :: "v"(v0), "v"(v1), "v"(v2), "v"(v3), "s"(sbase), "s"(sl), "i"(IMM) : "memory", "scc")
#define STAGE_A(kt, sl) do { const char* sa_ = (const char*)x + (((kt) + rot1) & 15) * 128; \
            GLDS4(sa_, voA[0], voA[1], voA[2], voA[3], sl, 0); GLDS4(sa_ + 16384, voA[0], voA[1], voA[2], voA[3], sl, 4096); } while (0)
#define STAGE_B(kt, sl) do { const int kr_ = (((kt) + rot1) & 15) * 128; const char* sb_ = (const char*)Bre + kr_; const char* sc_ = (const char*)Bim + kr_; \
            GLDS4(sb_, voB[0], voB[1], voB[2], voB[3], sl, 0); GLDS4(sc_, voB[0], voB[1], voB[2], voB[3], sl, 4096); } while (0)
        lw0 = lbase + lh * 8192;
        STAGE_A(0, lw0 + RA(0)); STAGE_B(0, lw0 + RB(0)); STAGE_A(1, lw0 + RA(1)); STAGE_B(1, lw0 + RB(1));
    }
    const int sblk = b >> 3, dblk = b & 7;
    u32x2 tw[4];
    unsigned epoch;
    asm volatile("" : "=v"(tw[0]), "=v"(tw[1]), "=v"(tw[2]), "=v"(tw[3]), "=v"(epoch));
    if (wid >= 6) {
        __builtin_amdgcn_s_setprio(PRIO_L);
        const int vw = wid - 6;
        const int dch = dblk * 128 + vw * 64 + l15;
        const unsigned lk = lbase + ST_OFF + vw * ST_SZ;
        const unsigned vk = (unsigned)((lane >> 3) * DM * 2 + (((lane & 7) ^ ((lane >> 4) & 1)) * 16));
        const char* kb = (const char*)kin + (size_t)(sblk * 64 - 4) * (DM * 2) + dblk * 256 + vw * 128;
#define KLD(n) GLDS_K(kb + (n) * 8 * DM * 2, vk, lk, (n) * 1024 + ((n) >> 1) * 32)
#define VLD(n) GLDS_K(vb + (((n) >> 1) * 16 + ((n) & 1) * 2) * DM * 2, vv_, lk, ST_V + (n) * 1056)
        if (vw == 1) {
            const unsigned vg = (unsigned)((lane * 16 + rot1 * 128) & 2047), lg = lbase + GW_OFF;
            GLDS_S(gatew, vg, lg, 0); GLDS_S(gatew, vg ^ 1024u, lg, 1024);
        }
        asm volatile("global_load_dwordx2 %0, %5, %7\n\tglobal_load_dwordx2 %1, %5, %7 offset:128\n\tglobal_load_dwordx2 %2, %5, %7 offset:256\n\t"
                     "global_load_dwordx2 %3, %5, %7 offset:384\n\tglobal_load_dword %4, %6, %8 sc1"
                     : "+v"(tw[0]), "+v"(tw[1]), "+v"(tw[2]), "+v"(tw[3]), "+v"(epoch) : "v"((unsigned)(dch * 8)), "v"((unsigned)WS_EPOCH), "s"(shiftk), "s"(ws) : "memory");
        if (sblk == 0 && lane < 32) *(u32x4*)(smem + ST_OFF + vw * ST_SZ + lane * 16) = (u32x4){0u, 0u, 0u, 0u};
        if (sblk != 0 || lane >= 32) KLD(0);
        KLD(1); KLD(2); KLD(3); KLD(4); KLD(5); KLD(6); KLD(7);
        if (lane < 32) KLD(8);
        __builtin_amdgcn_s_setprio(0);
    }
    asm volatile("" : "+s"(wid));

#define WSL(w) ((((w) >> 2) ^ (nb >> 3)) * 4 + ((w) & 3))
#define COFF(w, ti) ((2 * WSL(w) + ((ti) >> 1)) * 128 + ((ti) & 1) * 4096)
    bf16x8 cf_[8][4];
    bf16x8 af[DEPTH];
    int cra, crb;
#define CT_SETUP() const unsigned vB0 = (unsigned)((d_base + (lane >> 3) * 4) * 2048 + (((lane & 7) ^ (lane >> 4)) * 16)), vB1 = (vB0 ^ 64u) + 65536u - 1024u
#define CTILE(w, ti, dst) do { const int co_ = COFF(w, ti); const unsigned ds_ = (dst); \
                               GLDS4((const char*)Cre + co_, vB0, vB1, vB0, vB1, ds_, 0); GLDS4((const char*)Cim + co_, vB0, vB1, vB0, vB1, ds_, 4096); } while (0)
#define CF(ti, n) cf_[4 * ((ti) >> 1) + ((n) >> 1)][2 * ((ti) & 1) + ((n) & 1)]
#define CRD(ti, n) CF(ti, n) = RD128((((n) & 4) ? crb : cra) ^ (((n) & 2) * 16), ((n) & 1) * 2048)
#define CREAD(ti) do { _Pragma("unroll") for (int n_ = 0; n_ < 8; ++n_) CRD(ti, n_); } while (0)
#define CPIN(ti) do { PIN4(CF(ti, 0), CF(ti, 1), CF(ti, 2), CF(ti, 3)); PIN4(CF(ti, 4), CF(ti, 5), CF(ti, 6), CF(ti, 7)); } while (0)
#define CRA(base) ((base) + (q4 >> 1) * 4096 + l15 * 128 + (((q4 & 1) ^ ((l15 >> 1) & 7)) * 16))
    const int rot2 = (idx & 16) >> 2;
#define JB(u) (((((u) ^ (rot2 >> 2)) & 1) << 2) | ((wid - ((u) >> 1) + rot2) & 3))
    const __amdgpu_buffer_rsrc_t hs = __builtin_amdgcn_make_buffer_rsrc((char*)h + (size_t)tb * 524288 + WSL(wid) * 8192, 0, 524288, 0x00020000);
#define LOADF(f) do { af[(f) % DEPTH] = __builtin_bit_cast(bf16x8, __builtin_amdgcn_raw_buffer_load_b128(hs, vh + ((f) & 3) * 1024, JB((f) >> 3) * 65536 + ((f) & 4) * 1024, 16)); SCHED(); } while (0)
#define HPOLL_SETUP() gu32* hfp = (gu32*)(ws + WS_HFLAG) + (tb * 16 + 2 * WSL(wid) + (lane & 1)) * 32
#define HWAIT() do { unsigned spins = 0; \
            while (!__all(__hip_atomic_load(hfp, RLX_AGENT) == tag)) { if (++spins > SPIN_MAX) break; __builtin_amdgcn_s_sleep(PSLEEP); } asm volatile("" ::: "memory"); } while (0)

    const __amdgpu_buffer_rsrc_t gs = __builtin_amdgcn_make_buffer_rsrc(ws + WS_GATE, 0, 32768, 0x00020000);
#define GV(q4) ((unsigned)(((q4) >> 1) * 128 + ((q4) & 1) * 32))
#define GSO(r) (tb * 2048 + JB(6 + (r)) * 256 + (wid >> 2) * 16)
#define TR(r) (t_base + JB(6 + (r)) * 4 + (wid >> 2) * 2)
    u32x2 xx[2][2], dk2;
    u32x4 gv[2];
#define XLOAD() do { \
        const __amdgpu_buffer_rsrc_t xs = __builtin_amdgcn_make_buffer_rsrc((void*)x, 0, SEQ * DM * 2, 0x00020000); \
        const __amdgpu_buffer_rsrc_t ks = __builtin_amdgcn_make_buffer_rsrc((void*)dskip, 0, DM * 2, 0x00020000); \
        dk2 = __builtin_amdgcn_raw_buffer_load_b64(ks, (unsigned)(l15 * 8), d_base * 2, 0); \
        _Pragma("unroll") for (int q = 0; q < 4; ++q) \
            xx[q >> 1][q & 1] = __builtin_amdgcn_raw_buffer_load_b64(xs, (unsigned)(q4 * 32 * DM * 2 + l15 * 8 + (q & 1) * DM * 2), (TR(q >> 1) * DM + d_base) * 2, 0); \
        _Pragma("unroll") for (int r = 0; r < 2; ++r) gv[r] = __builtin_amdgcn_raw_buffer_load_b128(gs, GV(q4), GSO(r), 16); \
        SCHED(); } while (0)
#define XPIN() do { PIN4(xx[0][0], xx[0][1], xx[1][0], xx[1][1]); asm volatile("" : "+v"(dk2), "+v"(gv[0]), "+v"(gv[1])); } while (0)

    if (wid < 4) {
        typedef __attribute__((ext_vector_type(8))) unsigned u32x8;
        typedef const __attribute__((address_space(4))) u32x8 cu8;
        const int po = (p_base + wid * 16) * 2;
        u32x8 s0 = *(cu8*)((size_t)logdt + po), s1 = *(cu8*)((size_t)lre + po), s2 = *(cu8*)((size_t)lim + po);
        SCHED();
        const int pq = p_base + wid * 16 + l15;
        unsigned goff[4], gmask[4];
#pragma unroll
        for (int i = 0; i < 4; ++i) {
            goff[i] = (unsigned)(((tb - 1 - q4 - 4 * i) * 1024 + pq) * 16);
            gmask[i] = (tb - 1 - q4 - 4 * i >= 0) ? ~0u : 0u;
            asm volatile("" : "+v"(goff[i]), "+v"(gmask[i]));
        }
        unsigned gpub = q4 == 0 ? (unsigned)((tb * 1024 + pq) * 16) : 0x80000000u;
        unsigned hp = (unsigned)(tb * 524288 + (nb * 4 + wid) * 1024 + lane * 16);
        unsigned wa = (unsigned)(HT_OFF + wid * 2048 + (q4 * 16 + (l15 ^ ((q4 >> 1) * 4))) * 8);
        unsigned ra0 = (unsigned)(HT_OFF + wid * 2048 + ((q4 >> 1) * 64 + (l15 & 3) * 16 + (((q4 & 1) * 8 + (l15 >> 2)) ^ ((l15 & 2) * 2))) * 8), ra1 = ra0 ^ 32u;
        cra = CRA(GA_OFF + wid * 8192); crb = cra ^ 64;
        unsigned vB0 = (unsigned)((d_base + (lane >> 3) * 4) * 2048 + (((lane & 7) ^ (lane >> 4)) * 16)), vB1 = (vB0 ^ 64u) + 65536u - 1024u;
        PIN4(gpub, vB0, hp, wa); PIN4(ra0, ra1, cra, crb); asm volatile("" : "+v"(vB1));
        f32x4 acc[8][2];
#pragma unroll
        for (int m = 0; m < 8; m += 2) {
#pragma unroll
            for (int c = 0; c < 4; ++c) acc[m + (c >> 1)][c & 1] = (f32x4){0.f, 0.f, 0.f, 0.f};
            PIN4(acc[m][0], acc[m][1], acc[m + 1][0], acc[m + 1][1]);
        }
        SCHED();
        cf L1, Lq3, cc, L32, cLq, Lp, L512;
        {
            unsigned w0 = 0, w1 = 0, w2 = 0;
#pragma unroll
            for (int i = 0; i < 8; ++i) {
                asm("v_writelane_b32 %0, %3, %6\n\tv_writelane_b32 %1, %4, %6\n\tv_writelane_b32 %2, %5, %6" : "+v"(w0), "+v"(w1), "+v"(w2) : "s"(s0[i]), "s"(s1[i]), "s"(s2[i]), "i"(2 * i));
            }
            asm("s_nop 1" : "+v"(w0), "+v"(w1), "+v"(w2));
            auto spread = [&](unsigned w) {
                const unsigned e = (unsigned)__builtin_amdgcn_update_dpp(0, (int)w, 0xa0, 0xf, 0xf, false);
                const unsigned a = __builtin_amdgcn_permlane16_swap(e, e, false, false)[0];
                const unsigned c = __builtin_amdgcn_permlane32_swap(a, a, false, false)[0];
                return (l15 & 1) ? hi16(c) : lo16(c);
            };
            const float ldt = spread(w0), re = spread(w1), im = spread(w2);
            if (wid == 3 && lane < NFLAGW) *(unsigned*)(smem + UFLAG_OFF + lane * 4) = 0u;
            const float dt = rbf(expf(ldt));
            const float ea = expf(dt * re);
            float sn, cs;
            sincosf(dt * im, &sn, &cs);
            L1 = {ea * cs, ea * sn};
            const float nr = L1.r - 1.0f, ni = L1.i, den = re * re + im * im;
            cc = {2.f * ((nr * re + ni * im) / den), 2.f * ((ni * re - nr * im) / den)};
            const cf T2 = cmul(L1, L1), T4 = cmul(T2, T2), L8 = cmul(T4, T4), L16 = cmul(L8, L8);
            L32 = cmul(L16, L16);
            const cf L64 = cmul(L32, L32), L96 = cmul(L64, L32), L128 = cmul(L64, L64), L256 = cmul(L128, L128), L384 = cmul(L256, L128);
            L512 = cmul(L256, L256);
            Lp = pick4(q4, L128, L256, L384);
            Lq3 = pick4(3 - q4, L32, L64, L96);
            cLq = cmul(cc, pick4(q4, L32, L64, L96));
            PINC(L1); PINC(Lq3); PINC(cc); PINC(L32); PINC(cLq); PINC(Lp); PINC(L512);
        }
        __builtin_amdgcn_s_setprio(PRIO_G);
        unsigned tagv;
        cf S;
        STAMPT(dbg, 22, 0);
        const int swz = (l15 >> 1) & 7;
        {
            const int a0 = RA(0) + l15 * 128 + ((q4 ^ swz) * 16);
            const int b0 = RB(0) + ((wid >> 1) * 64 + (wid & 1) * 16 + l15) * 128 + ((q4 ^ swz) * 16);
            WAIT_LGKM0();
            BAR();
            asm volatile("" ::: "memory");
            STAMPT(dbg, 19, 64);
            bf16x8 FA[8][2], FB[2][2][2];
            int pa[2] = {a0, a0 ^ 64}, pb[2] = {b0, b0 ^ 64};
            PIN4(pa[0], pa[1], pb[0], pb[1]);
#define LGW(n) __builtin_amdgcn_s_waitcnt(0xC07F | ((n) << 8))
#define RA_(j) FA[(j) >> 1][(j) & 1] = RD128(pa[(j) & 1], ((j) >> 1) * 2048)
#define RB_(bu, j) FB[bu][(j) >> 1][((j) ^ ((j) >> 1)) & 1] = RD128(pb[((j) ^ ((j) >> 1)) & 1], ((j) >> 1) * 4096)
#define RN_(bu, i) do { if ((i) == 0) RB_(bu, 0); else if ((i) < 3) RA_((i) - 1); else if ((i) < 6) RB_(bu, (i) - 2); else RA_((i) - 4); } while (0)
#define G1_H(H, bu, RD, FILL) do { \
            _Pragma("unroll") for (int i = 0; i < 16; ++i) { \
                const int m = (H) * 4 + (i >> 2), n = ((i >> 1) ^ m) & 1, ks = (i ^ (i >> 1)) & 1; \
                if ((H) == 0) { if (i == 0) LGW(6); if (i == 4) LGW(8); if (i == 8) LGW(10); if (i == 12) LGW(8); } \
                SCHED(); \
                MF(acc[m][n], FA[m][ks], FB[bu][n][ks]); SCHED(); \
                if ((H) == 0 && i < 8) RA_(8 + i); \
                if ((H) == 1 && (RD) && i < 12) RN_((bu) ^ 1, i); \
                FILL((H) * 16 + i); \
                SCHED(); \
            } } while (0)
#define NOF(g) do { } while (0)
#pragma unroll
            for (int j = 0; j < 12; ++j) { RN_(0, j); SCHED(); }
            unsigned long long ua_ = UNITS_A;
            int sb = RB(0), sa = RA(0), nx, na;
#define ADV(g) do { if ((g) == 8) { ua_ >>= 4; na = (int)((unsigned)ua_ & 15u) << 14; asm volatile("" : "+s"(na), "+s"(ua_)); } \
                    if ((g) == 9) { const int o_ = sa; sa = na; na -= o_; asm volatile("" : "+s"(na), "+s"(sa)); } \
                    if ((g) == 10) { pa[0] += na; pa[1] += na; asm volatile("" : "+v"(pa[0]), "+v"(pa[1])); } \
                    if ((g) == 11) { nx = (sb == 65536) ? -49152 : 16384; sb += nx; asm volatile("" : "+s"(nx), "+s"(sb)); } \
                    if ((g) == 13) { pb[0] += nx; pb[1] += nx; asm volatile("" : "+v"(pb[0]), "+v"(pb[1])); } } while (0)
#define G1_T(bu) do { \
                G1_H(0, bu, true, ADV); \
                WAIT_LGKM0(); \
                asm volatile("" ::: "memory"); \
                BAR();                     \
                asm volatile("" ::: "memory"); \
                G1_H(1, bu, true, NOF); } while (0)
#pragma unroll 1
            for (int t2 = 0; t2 < 7; ++t2) { G1_T(0); G1_T(1); }
            G1_T(0);
#define AGG(g) do { if ((g) == 8) { asm volatile("" ::: "memory"); SETW(G1D_OFF, wid, 1u); }        \
                    if ((g) == 9) tagv = *(volatile const lds_u32*)(size_t)(unsigned)TAG_OFF; \
                    if ((g) >= 7) { const int t = (g) - 6; \
                        if (t == 1) { asm volatile("s_nop 1" : "+v"(acc[0][0]), "+v"(acc[0][1])); S = {acc[0][0][0], acc[0][1][0]}; } \
                        if ((t & 3) == 0) asm volatile("" : "+v"(acc[t >> 2][0]), "+v"(acc[t >> 2][1])); \
                        S = cfma(L1, S, cf{acc[t >> 2][0][t & 3], acc[t >> 2][1][t & 3]}); PINC(S); } } while (0)
            G1_H(0, 1, false, AGG);
            G1_H(1, 1, false, AGG);
        }
        STAMPT(dbg, 1, 0);
        const unsigned tag = __builtin_amdgcn_readfirstlane(tagv);
        const __amdgpu_buffer_rsrc_t grs = __builtin_amdgcn_make_buffer_rsrc(ws, 0, 15 * 16384, 0x00020000);
        {
#pragma unroll
            for (int t = 26; t < 32; ++t) {
                if (t == 28) { PINC(S); asm volatile("" : "+v"(acc[7][0]), "+v"(acc[7][1])); }
                S = cfma(L1, S, cf{acc[t >> 2][0][t & 3], acc[t >> 2][1][t & 3]});
            }
            const cf W = cmul(S, Lq3);
            const float e2 = red16(red32(W.r, W.i));
            const auto eo = __builtin_amdgcn_permlane32_swap(__float_as_uint(e2), __float_as_uint(e2), false, false);
            __builtin_amdgcn_raw_buffer_store_b128((u32x4){eo[0], tag, eo[1], tag}, grs, gpub, 0, 16);
        }
        SCHED();
        STAMPT(dbg, 2, 0);
        const unsigned cg = lbase + GA_OFF + wid * 8192;
        wait_lds1(smem, COK_OFF + wid * 4);
        CREAD(0);
        WAIT_LGKM0();
        CPIN(0);
        CTILE(wid, 1, cg);
        u32x4 gq[4];
#pragma unroll
        for (int m = 0; m < 8; ++m) {
            const f32x4 R = acc[m][0], I = acc[m][1];
            acc[m][0] = fma4(splat4(cc.r), R, mul4(splat4(-cc.i), I));
            acc[m][1] = fma4(splat4(-cc.r), I, mul4(splat4(-cc.i), R));
            asm volatile("" : "+v"(acc[m][0]), "+v"(acc[m][1]));
            if (m == 3) {
                SCHED();
#pragma unroll
                for (int i = 0; i < 4; ++i) asm volatile("buffer_load_dwordx4 %0, %1, %2, 0 offen sc1" : "=&v"(gq[i]) : "v"(goff[i]), "s"(grs) : "memory");
                SCHED();
            }
        }
        cf Ec;
        {
            const auto sr = __builtin_amdgcn_permlane16_swap(__float_as_uint(S.r), __float_as_uint(S.r), false, false);
            const auto si = __builtin_amdgcn_permlane16_swap(__float_as_uint(S.i), __float_as_uint(S.i), false, false);
            const cf Se = {__uint_as_float(sr[0]), __uint_as_float(si[0])}, So = {__uint_as_float(sr[1]), __uint_as_float(si[1])};
            const cf A = cfma(L32, Se, So);
            const auto ar = __builtin_amdgcn_permlane32_swap(__float_as_uint(A.r), __float_as_uint(A.r), false, false);
            const auto ai = __builtin_amdgcn_permlane32_swap(__float_as_uint(A.i), __float_as_uint(A.i), false, false);
            const cf A01 = {(q4 & 2) ? __uint_as_float(ar[0]) : 0.f, (q4 & 2) ? __uint_as_float(ai[0]) : 0.f};
            const cf kq = {(q4 & 1) ? L32.r : 1.f, (q4 & 1) ? L32.i : 0.f};
            const cf E = cfma(kq, A01, cf{(q4 & 1) ? Se.r : 0.f, (q4 & 1) ? Se.i : 0.f});
            Ec = cmul(cc, E);
            PINC(Ec);
        }
        WAIT_VM(4);
        CREAD(1);
        WAIT_LGKM0();
        CPIN(1);
        CTILE(wid, 2, cg);
        asm volatile("s_waitcnt vmcnt(8)" : "+v"(gq[0]), "+v"(gq[1]), "+v"(gq[2]), "+v"(gq[3]) :: "memory");
        STAMPT(dbg, 27, 0);
        cf carry;
        {
            unsigned spins = 0;
            for (;;) {
                unsigned bad = 0;
#pragma unroll
                for (int i = 0; i < 4; ++i) bad |= ((gq[i].y ^ tag) | (gq[i].w ^ tag)) & gmask[i];
                if (__all(bad == 0u)) break;
                if (++spins > SPIN_MAX) break;
#pragma unroll
                for (int i = 0; i < 4; ++i) asm volatile("buffer_load_dwordx4 %0, %1, %2, 0 offen sc1" : "+v"(gq[i]) : "v"(goff[i]), "s"(grs) : "memory");
                asm volatile("s_waitcnt vmcnt(0)" : "+v"(gq[0]), "+v"(gq[1]), "+v"(gq[2]), "+v"(gq[3]) :: "memory");
            }
            cf E = {__uint_as_float(gq[3].x), __uint_as_float(gq[3].z)};
#pragma unroll
            for (int i = 2; i >= 0; --i) E = cfma(L512, E, cf{__uint_as_float(gq[i].x), __uint_as_float(gq[i].z)});
            E = cmul(E, Lp);
            const float e2 = red16(red32(E.r, E.i));
            const auto eo = __builtin_amdgcn_permlane32_swap(__float_as_uint(e2), __float_as_uint(e2), false, false);
            carry = {__uint_as_float(eo[0]), __uint_as_float(eo[1])};
        }
        STAMPT(dbg, 3, 0);
        {
            const __amdgpu_buffer_rsrc_t hrs = __builtin_amdgcn_make_buffer_rsrc(h, 0, SEQ * 2048 * 2, 0x00020000);
            const cf e = cfma(cLq, carry, Ec);
            float hr = e.r, hn = -e.i;
            s16x4 fr[2][4];
#define HSTORE(m) do { _Pragma("unroll") for (int c = 0; c < 2; ++c) \
                __builtin_amdgcn_raw_buffer_store_b128(__builtin_bit_cast(u32x4, __builtin_shufflevector(fr[(m) & 1][2 * c], fr[(m) & 1][2 * c + 1], 0, 1, 2, 3, 4, 5, 6, 7)), hrs, hp, (2 * (m) + c) * 65536, WT); } while (0)
#pragma unroll
            for (int m = 0; m < 4; ++m) {
#pragma unroll
                for (int j = 0; j < 8; ++j) {
                    const float nr = fmaf(L1.r, hr, fmaf(L1.i, hn, acc[2 * m + (j >> 2)][0][j & 3]));
                    const float nn = fmaf(L1.r, hn, fmaf(-L1.i, hr, acc[2 * m + (j >> 2)][1][j & 3]));
                    hr = nr; hn = nn;
                    acc[2 * m + (j >> 2)][0][j & 3] = nr; acc[2 * m + (j >> 2)][1][j & 3] = nn;
                }
                u32x2 pk[4];
#pragma unroll
                for (int c = 0; c < 4; ++c) { const f32x4 v = acc[2 * m + (c >> 1)][c & 1]; pk[c] = (u32x2){pkbf(v[0], v[1]), pkbf(v[2], v[3])}; }
                PIN4(pk[0], pk[1], pk[2], pk[3]);
                SCHED();
#pragma unroll
                for (int c = 0; c < 4; ++c) { *(volatile __attribute__((address_space(3))) u32x2*)(size_t)(wa + c * 512) = pk[c]; SCHED(); }
                asm volatile("" ::: "memory");
#pragma unroll
                for (int c = 0; c < 2; ++c) { fr[m & 1][2 * c] = TRD(ra0 + c * 1024); SCHED(); fr[m & 1][2 * c + 1] = TRD(ra1 + c * 1024); SCHED(); }
                asm volatile("" ::: "memory");
                if (m > 0) HSTORE(m - 1);
                SCHED();
            }
            HSTORE(3);
        }
        STAMPT(dbg, 4, 0);
        {
            WAIT_VM(8);
            CREAD(2);
            WAIT_VM(0);
            {
                unsigned n;
                asm volatile("s_mov_b64 exec, 1\n\tds_add_rtn_u32 %0, %1, %2\n\ts_mov_b64 exec, -1\n\ts_waitcnt lgkmcnt(0)" : "=&v"(n) : "v"(MISC_OFF), "v"(1u) : "memory");
                if (__builtin_amdgcn_readfirstlane(n) == 3u) {
                    if (lane == 0) __hip_atomic_store((gu32*)(ws + WS_HFLAG) + (tb * 16 + nb) * 32, tag, RLX_AGENT);
                    if (DBGON && lane == 0) dbg[BIDX * 64 + 20] = __builtin_amdgcn_s_memrealtime();
                }
            }
            STAMPT(dbg, 12, 0);
            if (wid == 0 && lane < 7) CODE_TOUCH(CODE_LINES - 1 - lane - (idx & 7) * 7);
            __builtin_amdgcn_s_setprio(0);
            WAIT_LGKM0();
            CPIN(2);
            CTILE(wid, 3, cg);
            XLOAD();
            HPOLL_SETUP();
            HWAIT();
            STAMPT(dbg, 8, 0);
            const unsigned vh = (unsigned)(lane * 16);
#pragma unroll
            for (int f = 0; f < DEPTH; ++f) LOADF(f);
            __builtin_amdgcn_s_setprio(PRIO2_G);
        }
    } else {
        const int lw = wid - 4;
        const int d = cb * 4 + lw;
        const char* Krow = (const char*)(ssmk + (size_t)d * SEQ);
        cra = CRA(LA_OFF + lw * 8192); crb = cra ^ 64;
        const unsigned cst0 = lbase + LA_OFF + lw * 8192;
        CT_SETUP();
        asm volatile("" : "+v"(cra), "+v"(crb));
        u32x4 KA[4];
        unsigned draw;
        unsigned tag;
        float gacc = 0.f;
#define KLOAD() asm volatile("global_load_dwordx4 %0, %5, %7 nt\n\tglobal_load_dwordx4 %1, %5, %7 offset:1024 nt\n\tglobal_load_dwordx4 %2, %5, %7 offset:2048 nt\n\t" \
                             "global_load_dwordx4 %3, %5, %7 offset:3072 nt\n\tglobal_load_ushort %4, %6, %8" \
                             : "=&v"(KA[0]), "=&v"(KA[1]), "=&v"(KA[2]), "=&v"(KA[3]), "=&v"(draw) : "v"((unsigned)(lane * 16)), "v"((unsigned)(d * 2)), "s"(Krow), "s"(ssmD) : "memory")
#define KWAIT(n) asm volatile("s_waitcnt vmcnt(%5)" : "+v"(KA[0]), "+v"(KA[1]), "+v"(KA[2]), "+v"(KA[3]), "+v"(draw) : "i"(n) : "memory")
        if (wid < 6) {
            WAIT_VM(16);
            STAMPT(dbg, 17, 320);
            BAR();
            STAGE_A(2, lw0 + RA(2)); STAGE_B(2, lw0 + RB(2));
            unsigned long long ua_ = UNITS_A >> 12;
            unsigned sla = lw0 + RA(3), slb = lw0 + RB(4);
            int ka = ((3 + rot1) & 15) * 128, kb_ = ka;
            const char* sa = (const char*)x + ka;
            asm volatile("" : "+s"(sa), "+s"(sla), "+s"(ka), "+s"(ua_));
#define NEXT_A() do { ka = (ka + 128) & 2047; sa = (const char*)x + ka; ua_ >>= 4; sla = lw0 + (((unsigned)ua_ & 15u) << 14); asm volatile("" : "+s"(sa), "+s"(sla), "+s"(ka), "+s"(ua_)); } while (0)
#define LSTEP(n) do { \
                if ((n) == 24 && t == 1) { WAIT_VM(16); BAR(); STAGE_B(3, lw0 + RB(3)); } else { WAIT_VM(n); BAR(); } \
                GLDS4(sa, voA[0], voA[1], voA[2], voA[3], sla, 0); \
                GLDS4(sa + 16384, voA[0], voA[1], voA[2], voA[3], sla, 4096); \
                kb_ = (kb_ + 128) & 2047; \
                GLDS4((const char*)Bre + kb_, voB[0], voB[1], voB[2], voB[3], slb, 0); \
                NEXT_A(); \
                const unsigned slb0 = slb; \
                slb = (slb == lw0 + 65536) ? lw0 + 16384 : slb + 16384; \
                asm volatile("" : "+s"(slb), "+s"(kb_), "+s"(t)); \
                GLDS4((const char*)Bim + kb_, voB[0], voB[1], voB[2], voB[3], slb0, 4096); } while (0)
#pragma unroll 1
            for (int t = 1; t < 5; ++t) LSTEP(24);
            GLDS4(sa, voA[0], voA[1], voA[2], voA[3], sla, 0); GLDS4(sa + 16384, voA[0], voA[1], voA[2], voA[3], sla, 4096);
            NEXT_A();
#pragma unroll 1
            for (int t = 5; t < 13; ++t) LSTEP(32);
            KLOAD();
            WAIT_VM(37);
            BAR();
            __builtin_amdgcn_s_setprio(0);
            tag = lds_word(smem, TAG_OFF);
            CTILE(lw, 0, cst0 + GA_OFF);
            FLAGD(COK_OFF + lw * 4);
            WAIT_VM(30);
            BAR();
            CTILE(lw + 2, 0, cst0 + GA_OFF + 16384);
            FLAGD(COK_OFF + lw * 4 + 8);
            WAIT_VM(23);
            BAR();
        } else {
            const int vw = wid - 6;
            const int dch = dblk * 128 + vw * 64 + l15;
            const unsigned lk = lbase + ST_OFF + vw * ST_SZ;
            const unsigned vv_ = (unsigned)(((lane >> 4) * 4 + ((lane >> 3) & 1)) * DM * 2 + (lane & 7) * 16);
            const char* vb = (const char*)vin + (size_t)(sblk * 64) * (DM * 2) + dblk * 256 + vw * 128;
            const int grow = nb * 8 + vw * 4 + q4;
            unsigned gx0 = (unsigned)(grow * 128 + (((l15 >> 1) ^ ((grow >> 1) & 7)) * 16) + (l15 & 1) * 8), gx1 = gx0 + RA(2), gwa = (unsigned)(GW_OFF + l15 * 8);
            asm volatile("" : "+v"(gx0), "+v"(gx1), "+v"(gwa));
            u32x2 gxv, gwv;
            typedef const volatile __attribute__((address_space(3))) u32x2* l2p;
#define VBAR(t) do { SCHED(); asm volatile("" ::: "memory"); BAR(); asm volatile("" ::: "memory"); \
                     gxv = *(l2p)(size_t)(RA(t) == 0 ? gx0 : gx1 + (RA(t) - RA(2))); gwv = *(l2p)(size_t)(gwa + (t) * 128); SCHED(); } while (0)
#define GATE() do { SCHED(); gacc = fmaf(lo16(gxv.x), lo16(gwv.x), gacc); gacc = fmaf(hi16(gxv.x), hi16(gwv.x), gacc); \
                    gacc = fmaf(lo16(gxv.y), lo16(gwv.y), gacc); gacc = fmaf(hi16(gxv.y), hi16(gwv.y), gacc); asm volatile("" : "+v"(gacc)); SCHED(); } while (0)
            unsigned ka[4][2], sel[4];
            const int R0 = 4 + (l15 >> 2) * 16 + (l15 & 3) - (q4 >> 1);
            unsigned kr0 = (unsigned)(2 * R0), kr1 = (unsigned)(ST_OFF + vw * ST_SZ + R0 * 128 + (((q4 ^ (R0 >> 1)) & 1) * 16));
            asm volatile("" : "+v"(kr0), "+v"(kr1));
#pragma unroll
            for (int i = 0; i < 4; ++i)
#pragma unroll
                for (int tp = 0; tp < 2; ++tp) {
                    ka[i][tp] = (kr1 ^ (tp * 16)) + ((kr0 + 8 * i - 4 * tp) & 0xe0) + (4 * i - 2 * tp) * 128;
                    asm volatile("" : "+v"(ka[i][tp]));
                }
#pragma unroll
            for (int r = 0; r < 4; ++r) {
                const unsigned pick = (q4 & 2) ? 0x0302u : 0x0100u;
                sel[r] = ((l15 >> 1) == (q4 & 1) * 4 + r) ? ((l15 & 1) ? (pick << 16) | 0x0c0cu : 0x0c0c0000u | pick) : 0x0c0c0c0cu;
            }
            unsigned va_ = (unsigned)(ST_OFF + vw * ST_SZ + ST_V + (q4 * 2 + (l15 >> 3)) * 1056 + ((l15 >> 2) & 1) * 128 + (l15 & 3) * 8);
            unsigned so = (unsigned)(((dch >> 2) * 32 + sblk) * 512 + q4 * 64 + (dch & 3) * 16);
            PIN4(sel[0], sel[1], sel[2], sel[3]); asm volatile("" : "+v"(so), "+v"(va_));
            asm volatile("s_waitcnt vmcnt(9)" : "+v"(tw[0]), "+v"(tw[1]), "+v"(tw[2]), "+v"(tw[3]), "+v"(epoch) :: "memory");
            STAMPT(dbg, 18, 384);
            VBAR(0);
            bf16x8 wf[4][2];
#pragma unroll
            for (int ps = 0; ps < 4; ++ps)
#pragma unroll
                for (int tp = 0; tp < 2; ++tp) {
                    u32x4 w;
#pragma unroll
                    for (int r = 0; r < 4; ++r) w[r] = __builtin_amdgcn_perm(0u, tw[ps][tp], sel[r]);
                    wf[ps][tp] = __builtin_bit_cast(bf16x8, w);
                    asm volatile("" : "+v"(wf[ps][tp]));
                }
            SCHED();
            VLD(0); VLD(1); VLD(2); VLD(3); VLD(4); VLD(5); VLD(6); VLD(7);
            GATE(); VBAR(1);
            f32x4 ks[4][4];
            auto sconv = [&](int i0) __attribute__((always_inline)) {
                bf16x8 kf[2][4][2];
#pragma unroll
                for (int q = 0; q < 16; ++q) { kf[q >> 3][q & 3][(q >> 2) & 1] = RD128(ka[i0 + (q >> 3)][(q >> 2) & 1], (q & 3) * 32); SCHED(); }
                GATE();
                __builtin_amdgcn_s_setprio(PRIO_KV);
                WAIT_LGKM0();
                SCHED();
#pragma unroll
                for (int i = 0; i < 2; ++i) {
#pragma unroll
                    for (int ps = 0; ps < 4; ++ps) MF0(ks[ps][i0 + i], kf[i][ps][0], wf[ps][0]);
#pragma unroll
                    for (int ps = 0; ps < 4; ++ps) MF(ks[ps][i0 + i], kf[i][ps][1], wf[ps][1]);
                }
                __builtin_amdgcn_s_setprio(0);
                SCHED();
            };
            WAIT_VM(8);
            STAMPT(dbg, 21, 384);
            sconv(0); VBAR(2);
            sconv(2); VBAR(3);
            WAIT_VM(0);
            u32x2 vq[4][4];
#pragma unroll
            for (int ps = 0; ps < 4; ++ps)
#pragma unroll
                for (int j = 0; j < 4; ++j) vq[ps][j] = __builtin_bit_cast(u32x2, TRD(va_ + j * 256 + ps * 32));
            const __amdgpu_buffer_rsrc_t rs = __builtin_amdgcn_make_buffer_rsrc(kvT, 0, DM * SEQ * 2, 0x00020000);
            u32x4 o[8];
            auto kvout = [&](int n) __attribute__((always_inline)) {
                const int hh = n >> 2, ps = n & 3;
                asm volatile("" : "+v"(ks[ps][2 * hh]), "+v"(ks[ps][2 * hh + 1]));
#pragma unroll
                for (int e = 0; e < 4; ++e) {
                    const unsigned vv = vq[ps][2 * hh + (e >> 1)][e & 1];
                    o[n][e] = pkbf(ks[ps][2 * hh + (e >> 1)][2 * (e & 1)] * lo16(vv), ks[ps][2 * hh + (e >> 1)][2 * (e & 1) + 1] * hi16(vv));
                }
                asm volatile("" : "+v"(o[n]));
                SCHED();
            };
            GATE(); kvout(0); kvout(1);
            WAIT_LGKM0();

            VBAR(4);
#define KVST(h) do { _Pragma("unroll") for (int n = 0; n < 4; ++n) __builtin_amdgcn_raw_buffer_store_b128(o[4 * (h) + n], rs, so + (unsigned)((h) * 256), n * 65536, WT); \
                     SCHED(); asm volatile("s_nop 1" ::: "memory"); SCHED(); } while (0)
            kvout(2); kvout(3); GATE(); VBAR(5);
            KVST(0);
            kvout(4); kvout(5); GATE(); VBAR(6);
            kvout(6); kvout(7); GATE(); VBAR(7);
            KVST(1);
            GATE(); VBAR(8);
            if (vw == 1 && lane == 0) *(unsigned*)(smem + TAG_OFF) = (epoch + 1u) ? epoch + 1u : 1u;
            GATE();
            WAIT_LGKM0();
            WAIT_VM(0);
            VBAR(9);
            tag = __builtin_amdgcn_readfirstlane(*(const unsigned*)(smem + TAG_OFF));
            if (vw == 0 && lane == 0) __hip_atomic_store((gu32*)(ws + WS_KVFLAG) + (dblk * 32 + sblk) * 32, tag, RLX_AGENT);
            if (vw == 1 && lane < 4) *(u32x4*)(smem + U_OFF + 192 + lane * 16) = (u32x4){0u, 0u, 0u, 0u};
            gu32* kfp = (gu32*)(ws + WS_KVFLAG) + ((cb >> 5) * 32 + vw * 16 + l15) * 32;
            const char* ub = (const char*)kvT + (size_t)__builtin_amdgcn_readfirstlane(cb * 16384 + vw * 8192);
            const unsigned vu = (unsigned)((lane >> 5) * 512 + ((lane >> 2) & 1) * 256 + ((lane >> 3) & 3) * 64 + ((lane - (lane >> 4)) & 3) * 16);
            const unsigned lu = lbase + U_OFF + 256 + vw * 8192;
#define U_DMA1(o) asm volatile("s_add_u32 m0, %2, %3\n\ts_nop 0\n\tglobal_load_lds_dwordx4 %0, %1 sc1\n\tglobal_load_lds_dwordx4 %0, %1 offset:1024 sc1\n\t" \
                             "global_load_lds_dwordx4 %0, %1 offset:2048 sc1\n\tglobal_load_lds_dwordx4 %0, %1 offset:3072 sc1" \
                             :: "v"(vu), "s"(ub + (o)), "s"(lu), "i"(o) : "memory", "scc")
#define U_DMA() do { U_DMA1(0); U_DMA1(4096); FLAGD(UFLAG_OFF + lw * 4); } while (0)
            unsigned fo = lane < 8 ? (unsigned)((tb * 1024 + p_base) * 16 + lane * 128) : lane == 8 ? (unsigned)(WS_HFLAG + (tb * 16 + nb) * 128)
                        : lane == 9 ? (unsigned)(WS_CFLAG + cb * 128) : (unsigned)(WS_GATE + (tb * 16 + nb) * 128);
            asm volatile("" : "+v"(fo));
            GATE(); VBAR(10);
            TOUCH((const char*)(vw ? Cim : Cre) + (size_t)b * 8192, (unsigned)(lane * 128));
            if (vw == 0) TOUCH(ws, fo); else TOUCH(dskip, (unsigned)(d_base * 2));
            {
                const int cl = 64 + lane + ((idx & 15) * 2 + vw) * 7;
                if (lane < 7 && cl < CODE_LINES) CODE_TOUCH(cl);
            }
            GATE(); VBAR(11);
            GATE(); VBAR(12);
            GATE(); VBAR(13);
            unsigned pv;
            asm volatile("global_load_dword %0, %1, off sc1" : "=&v"(pv) : "v"(kfp) : "memory");
            KLOAD();
            GATE(); VBAR(14);
            GATE(); VBAR(15);
            asm volatile("s_waitcnt vmcnt(5)" : "+v"(pv) :: "memory");
            const bool done = __all(pv == tag);
            if (done) U_DMA();
            if (DBGON && TIDX == 384) dbg[BIDX * 64 + 13] = (done ? 100ull : 0ull) | (1ull << 62);
            GATE();
            if (lane == 0) SETW(UFLAG_OFF, vw, 1u);
            WAIT_VM(9);
            if (!done) {
                unsigned spins = 0;
                for (;;) {
                    const unsigned v = __hip_atomic_load(kfp, RLX_AGENT);
                    if (__all(v == tag)) break;
                    if (++spins > SPIN_MAX) break;
                    __builtin_amdgcn_s_sleep(1);
                }
                U_DMA();
            }
        }
        KWAIT(18);
        STAMPT(dbg, 14, 320);
        __builtin_amdgcn_s_sleep(6);
        {
            const unsigned cp = (unsigned)(KC_OFF + lw * CV_W + 64 + lane * 16);
            if (lane == 0) KA[0].x = (KA[0].x & 0xffff0000u) | f2bf(lo16(KA[0].x) + bf2f((u16)draw));
#define WR128(v, off) asm volatile("ds_write_b128 %0, %1 offset:%2" :: "v"(cp), "v"(v), "i"(off) : "memory")
#pragma unroll
            for (int q = 3; q >= 0; --q) WR128(KA[q], q * 1024);
#pragma unroll
            for (int q = 3; q >= 0; --q) {
                const u32x2 p = q ? pred2(KA[q], RL63(KA[q ? q - 1 : 0].z), RL63(KA[q ? q - 1 : 0].w)) : pred2(KA[0], 0u, 0u);
                const u32x4 a = KA[q];
                const u32x4 s1 = {al(a.x, p.y), al(a.y, a.x), al(a.z, a.y), al(a.w, a.z)};
                WR128(s1, q * 1024 + CV_COPY);
                const u32x4 s2 = {p.y, mv(a.x), mv(a.y), mv(a.z)};
                WR128(s2, q * 1024 + 2 * CV_COPY);
                const u32x4 s3 = {al(p.y, p.x), mv(s1.x), mv(s1.y), mv(s1.z)};
                WR128(s3, q * 1024 + 3 * CV_COPY);
            }
            if (lane < 16) *(u32x4*)(smem + KC_OFF + lw * CV_W + (lane >> 2) * CV_COPY + (lane & 3) * 16) = (u32x4){0u, 0u, 0u, 0u};
            SCHED();
        }
        STAMPT(dbg, 26, 320);
        wait_lds4(smem, G1D_OFF);
        wait_lds4(smem, UFLAG_OFF);
        STAMPT(dbg, 9, 256);
        __builtin_amdgcn_s_setprio(PRIO_CV);
        STAMPT(dbg, 10, 384);
        f32x4 cv[8];
#pragma unroll
        for (int g = 0; g < 8; ++g) cv[g] = (f32x4){0.f, 0.f, 0.f, 0.f};
        PIN4(cv[0], cv[1], cv[2], cv[3]); PIN4(cv[4], cv[5], cv[6], cv[7]);
        const int uz = U_OFF + 192;
        const int ua0 = U_OFF + 256 + (2 * l15 + q4 - 2) * 64, rv0 = (2 * l15 + q4 - 2) * 4 + lw * 16;
        const unsigned ka0 = (unsigned)((l15 >> 2) * CV_COPY - 16 * q4 + 8 * (l15 & 3) - 8);
        int ua, rv, pu, pz;
        unsigned adr;
#define UNEXT1() do { pu = ua | (rv & 0x30); asm volatile("" : "+v"(pu)); } while (0)
#define UNEXT2() do { pz = (ua > uz ? ua : uz) | (rv & 0x30); asm volatile("" : "+v"(pz)); } while (0)
#define UNEXT3() do { ua -= 256; rv -= 16; asm volatile("" : "+v"(ua), "+v"(rv)); } while (0)
#define ANEXT() do { adr += 64; asm volatile("" : "+v"(adr)); } while (0)
#define RDU(dst, c) do { dst = (c) ? RD128(pu, (c) * 2048) : RD128(pz, 0); SCHED(); } while (0)
        HPOLL_SETUP();
        unsigned hpv = 0;
        {
            bf16x8 A[8], UB[8];
            ua = ua0; rv = rv0; adr = lbase + KC_OFF + lw * CV_W + ka0 + 96;
            asm volatile("" : "+v"(adr));
            UNEXT1(); UNEXT2(); UNEXT3();
#pragma unroll
            for (int i = 0; i < 8; ++i) { RDA(A[7 - i], adr, 7 - i); RDU(UB[i], i); }
            unsigned gb = lw >= 2 ? (unsigned)((tb * 16 + nb) * 128 + (lw - 2) * 32) : 0x80000000u;
            asm volatile("" : "+s"(gb));
            const unsigned gso = (l15 == 0) ? gb + (unsigned)(q4 * 8) : 0x80000000u;
            float gt = 0.f;
            unsigned vxo = 0, vdo = 0, vgo = 0;
            auto gate_out = [&](int i) __attribute__((always_inline)) {
                if (i == 0) gacc += __uint_as_float(__builtin_amdgcn_update_dpp(0u, __float_as_uint(gacc), 0xb1, 0xf, 0xf, false));
                if (i == 1) gacc += __uint_as_float(__builtin_amdgcn_update_dpp(0u, __float_as_uint(gacc), 0x4e, 0xf, 0xf, false));
                if (i == 2) gacc += __uint_as_float(__builtin_amdgcn_update_dpp(0u, __float_as_uint(gacc), 0x141, 0xf, 0xf, false));
                if (i == 3) gacc += __uint_as_float(__builtin_amdgcn_update_dpp(0u, __float_as_uint(gacc), 0x140, 0xf, 0xf, false));
                if (i == 4) gacc = rbf(gacc);
                if (i == 5) gacc = __builtin_amdgcn_exp2f(gacc * -1.44269504088896f);
                if (i == 6) gacc = __builtin_amdgcn_rcpf(1.0f + gacc);
                if (i == 7) gacc = rbf(gacc);
                if (i == 8) gt = __uint_as_float(pkbf(gacc, 1.0f - gacc));
                if (i == 9) __builtin_amdgcn_raw_buffer_store_b64((u32x2){__float_as_uint(gt), tag}, gs, gso, 0, WT);
                asm volatile("" : "+v"(gacc), "+v"(gt));
            };
            auto step = [&](auto E) __attribute__((always_inline)) {
                constexpr int e = decltype(E)::value;
                constexpr int ti = (e == CS0) ? 0 : (e == CS1) ? 1 : (e == CS2) ? 2 : -1, tj = ti < 0 ? 0 : ti;
                constexpr unsigned char GA[36] = {0x66, 0x77, 0x76, 0x44, 0x55, 0x54, 0x65, 0x64, 0x75, 0x74, 0x22, 0x33, 0x32, 0x43, 0x42, 0x53, 0x52, 0x63, 0x62, 0x73, 0x72,
                                                  0x00, 0x11, 0x10, 0x21, 0x20, 0x31, 0x30, 0x41, 0x40, 0x51, 0x50, 0x61, 0x60, 0x71, 0x70};
                const char* sb_ = (const char*)Cre + COFF(wid, tj + 1);
                const char* sc_ = (const char*)Cim + COFF(wid, tj + 1);
#pragma unroll
                for (int q = 0; q < 36; ++q) {
                    const int g = GA[q] >> 4, a = GA[q] & 15;
                    MF(cv[g], A[a], UB[g - a]); SCHED();
                    if (e < 7) {
                        if (a == 1) RDU(UB[g - 1], g - 1);
                        if (q == 35) RDU(UB[7], 7);
                        if (g == 7) RDA(A[a], adr + 64 * (e + 1), a);
                        if (q == 11) UNEXT1();
                        if (q == 13) UNEXT2();
                        if (q == 15) UNEXT3();
                    }
                    if (ti >= 0) {
                        const int n = (q >= 3 && q <= 7) ? q - 3 : q == 10 ? 5 : q == 12 ? 6 : q == 14 ? 7 : -1;
                        if (q == 3) { if (ti == 0) WAIT_VM(1); else if (e == XE + 1) WAIT_VM(7); else WAIT_VM(0); }
                        if (n >= 0) CRD(tj, n & 7);
                        if (q == 18) CPIN(tj);
                        if (q == 18 || q == 23) asm volatile("s_add_u32 m0, %3, %4\n\ts_nop 0\n\tglobal_load_lds_dwordx4 %0, %2\n\tglobal_load_lds_dwordx4 %1, %2 offset:1024"
                                                             :: "v"(vB0), "v"(vB1), "s"(q == 18 ? sb_ : sc_), "s"(cst0), "i"(q == 18 ? 0 : 4096) : "memory", "scc");
                        if (q == 21 || q == 25) asm volatile("global_load_lds_dwordx4 %0, %2 offset:2048\n\tglobal_load_lds_dwordx4 %1, %2 offset:3072"
                                                             :: "v"(vB0), "v"(vB1), "s"(q == 21 ? sb_ : sc_) : "memory");
                    }
                    if (e == 0 && q >= 3 && q <= 7) gate_out(q - 3);
                    if (e == 0 && q >= 10 && q <= 18 && !(q & 1)) gate_out(5 + (q - 10) / 2);
                    if (e == 0 && q == 4) CTILE(wid, 0, cst0);
                    if (e == XE && q >= 23 && q <= 35 && (q & 1)) {
                        const int i = (q - 23) / 2;
                        const __amdgpu_buffer_rsrc_t xs = __builtin_amdgcn_make_buffer_rsrc((void*)x, 0, SEQ * DM * 2, 0x00020000);
                        const __amdgpu_buffer_rsrc_t ks = __builtin_amdgcn_make_buffer_rsrc((void*)dskip, 0, DM * 2, 0x00020000);
                        if (i < 4) asm volatile("buffer_load_dwordx2 %0, %1, %2, %3 offen offset:%4" : "=&v"(xx[(i >> 1) & 1][i & 1]) : "v"(vxo), "s"(xs), "s"((TR((i >> 1) & 1) * DM + d_base) * 2), "i"((i & 1) * 2048) : "memory");
                        if (i == 4) asm volatile("buffer_load_dwordx2 %0, %1, %2, %3 offen" : "=&v"(dk2) : "v"(vdo), "s"(ks), "s"(d_base * 2) : "memory");
                        if (i >= 5) asm volatile("buffer_load_dwordx4 %0, %1, %2, %3 offen sc1" : "=&v"(gv[(i - 5) & 1]) : "v"(vgo), "s"(gs), "s"(GSO((i - 5) & 1)) : "memory");
                    }
                    if (e == XE && q == 16) { vxo = (unsigned)(q4 * 32 * DM * 2 + l15 * 8); vdo = (unsigned)(l15 * 8); asm volatile("" : "+v"(vxo), "+v"(vdo)); }
                    if (e == XE && q == 17) { vgo = GV(q4); asm volatile("" : "+v"(vgo)); }
                    if (e == 6 && q == 30) hpv = __hip_atomic_load(hfp, RLX_AGENT);
                    SCHED();
                }
            };
            unroll<8>(step);
            WAIT_VM(0);
            XPIN();
        }
        PAD8(cv[0], cv[1], cv[2], cv[3]); PIN4(cv[4], cv[5], cv[6], cv[7]);
        STAMPT(dbg, 11, 384);
        const unsigned vh = (unsigned)((lane ^ 2) * 16);
        if (!__all(hpv == tag)) HWAIT();
        asm volatile("" ::: "memory");
#pragma unroll
        for (int f = 0; f < DEPTH; ++f) LOADF(f);
        STAMPT(dbg, 25, 384);
        {
            const __amdgpu_buffer_rsrc_t yrs = __builtin_amdgcn_make_buffer_rsrc(y2T, 0, DM * SEQ * 2, 0x00020000);
            const unsigned yo = (unsigned)(d * 4096 + (q4 & 1) * 512 + (l15 >> 3) * 256 + (q4 >> 1) * 128 + (l15 & 7) * 16);
#pragma unroll
            for (int g = 0; g < 8; g += 2) {
                const auto r0 = __builtin_amdgcn_permlane16_swap(pkbf(cv[g][0], cv[g][1]), pkbf(cv[g + 1][0], cv[g + 1][1]), false, false);
                const auto r1 = __builtin_amdgcn_permlane16_swap(pkbf(cv[g][2], cv[g][3]), pkbf(cv[g + 1][2], cv[g + 1][3]), false, false);
                __builtin_amdgcn_raw_buffer_store_b128((u32x4){r0[0], r1[0], r0[1], r1[1]}, yrs, yo + (unsigned)(g * 512), 0, WT);
            }
        }
        asm volatile("" ::: "memory");
        STAMPT(dbg, 15, 384);
        __builtin_amdgcn_s_setprio(PRIO2_L);
    }
    {
        const int lane = fresh_lane(), l15 = lane & 15, q4 = lane >> 4;
        const int ring = wid >> 2;
        const unsigned va = (unsigned)((lane ^ (ring * 2)) * 16), vh = va;
        const unsigned tag = lds_word(smem, TAG_OFF);
        const int cb0 = ring ? KC_OFF : GA_OFF, cst = ring ? CV_W : 8192;
        f32x4 ac[2][4];
        unsigned ca[2], fa[2], fl = 0, fv = 0;
#define CHAIN(u) do { const int c_ = (wid - ((u) >> 1)) & 3; ca[(u) & 1] = va + (unsigned)(cb0 + c_ * cst + ((u) & 1) * 4096); \
                      fa[(u) & 1] = (unsigned)(RF_OFF + ring * 32 + ((u) & 1) * 16 + c_ * 4); asm volatile("" : "+v"(ca[(u) & 1]), "+v"(fa[(u) & 1])); } while (0)
#define RDIN(dst, a, f_) asm volatile("ds_read_b32 %0, %5\n\tds_read_b128 %1, %6\n\tds_read_b128 %2, %6 offset:1024\n\tds_read_b128 %3, %6 offset:2048\n\tds_read_b128 %4, %6 offset:3072" \
                     : "=&v"(fl), "=&v"(dst[0]), "=&v"(dst[1]), "=&v"(dst[2]), "=&v"(dst[3]) : "v"(f_), "v"(a) : "memory")
#define LWAIT(dst) asm volatile("s_waitcnt lgkmcnt(0)" : "+v"(fl), "+v"(dst[0]), "+v"(dst[1]), "+v"(dst[2]), "+v"(dst[3]) :: "memory")
        CHAIN(0); CHAIN(1);
        WAIT_LGKM0();
        CPIN(0); CPIN(1); CPIN(2);
        CREAD(3);
        int tr[2] = {TR(0), TR(1)};
        unsigned gw[2][2];
        {
            unsigned spins = 0;
            while (__builtin_expect(!__all((gv[0].y == tag) & (gv[0].w == tag) & (gv[1].y == tag) & (gv[1].w == tag)), 0)) {
                if (++spins > SPIN_MAX) break;
#pragma unroll
                for (int r = 0; r < 2; ++r) asm volatile("s_nop 4\n\tbuffer_load_dwordx4 %0, %1, %2, %3 offen sc1\n\ts_waitcnt vmcnt(0)" : "=&v"(gv[r]) : "v"(GV(q4)), "s"(gs), "s"(__builtin_amdgcn_readfirstlane(GSO(r))) : "memory");
            }
            gw[0][0] = gv[0].x; gw[0][1] = gv[0].z; gw[1][0] = gv[1].x; gw[1][1] = gv[1].z;
            PIN4(gw[0][0], gw[0][1], gw[1][0], gw[1][1]);
        }
        unsigned cpv = 0, yy[2][4];
        f32x2 zz[2][4];
        unsigned exo = 0, exi = 0, efo = 0, efi = 0;
        auto send = [&](int r) __attribute__((always_inline)) {
            const unsigned one = 1u;
#pragma unroll
            for (int n = 0; n < 4; ++n) { const f32x2 v = {ac[r][n][2], ac[r][n][3]}; asm volatile("ds_write_b64 %0, %1 offset:%2" :: "v"(exo), "v"(v), "i"(r * 4096 + n * 512) : "memory"); }
            asm volatile("ds_write_b32 %0, %1 offset:%2" :: "v"(efo), "v"(one), "i"(r * 4) : "memory");
        };
        STAMPT(dbg, 5, 0);
        auto body = [&](auto F) __attribute__((always_inline)) {
            constexpr int f = decltype(F)::value;
            constexpr int u = f >> 3, k = f & 7;
            f32x4* an = ac[u & 1];
            f32x4* ao = ac[(u + 1) & 1];
#pragma unroll
            for (int n = 0; n < 4; ++n) {
                if (k == 0 && u < 2) MF0(an[n], af[f % DEPTH], cf_[k][n]); else MF(an[n], af[f % DEPTH], cf_[k][n]);
                SCHED();
                const int g = k * 4 + n;
                if (u >= 1 && u < 7) {
                    if (g == 1) { fv = ((u - 1) >> 1) + 1; asm volatile("" : "+v"(fv)); }
                    if (g == 4 || g == 6 || g == 8 || g == 10) asm volatile("ds_write_b128 %0, %1 offset:%2" :: "v"(ca[(u + 1) & 1]), "v"(ao[(g - 4) / 2]), "i"((g - 4) * 512) : "memory");
                    if (g == 12) asm volatile("ds_write_b32 %0, %1" :: "v"(fa[(u + 1) & 1]), "v"(fv) : "memory");
                    if (g == 18) CHAIN(u + 1);
                    if (g == 20) asm volatile("ds_read_b32 %0, %1" : "=&v"(fl) : "v"(fa[(u + 1) & 1]) : "memory");
                    if (g > 20 && g <= 24) asm volatile("ds_read_b128 %0, %1 offset:%2" : "=&v"(ao[g - 21]) : "v"(ca[(u + 1) & 1]), "i"((g - 21) * 1024) : "memory");
                    if (g == 30) {
                        LWAIT(ao);
                        if (__builtin_expect(__builtin_amdgcn_readfirstlane(fl) != (unsigned)((u + 1) >> 1), 0)) {
                            unsigned spins = 0;
#pragma unroll 1
                            do {
                                __builtin_amdgcn_s_sleep(1);
                                RDIN(ao, ca[(u + 1) & 1], fa[(u + 1) & 1]); LWAIT(ao);
                                if (++spins > SPIN_MAX * 16u) break;
                            } while (__builtin_amdgcn_readfirstlane(fl) != (unsigned)((u + 1) >> 1));
                        }
                    }
                }
                if (u == 7 && g == 6) send(0);
                if (f >= ZF && f < ZF + 4) {
                    constexpr int r = ((f - ZF) >> 1) & 1, j = (f - ZF) & 1;
                    const unsigned xw = xx[r][j][n >> 1], dw = dk2[n >> 1];
                    const float xf = (n & 1) ? hi16(xw) : lo16(xw), df = (n & 1) ? hi16(dw) : lo16(dw), yf = j ? hi16(yy[r][n]) : lo16(yy[r][n]);
                    zz[r][n][j] = xf * fmaf(hi16(gw[r][j]), yf, lo16(gw[r][j]) * df);
                    asm volatile("" : "+v"(zz[r][n]));
                }
                if (f == ZF + 4) {
                    zz[0][n] = (f32x2){fmaf(lo16(gw[0][0]), ac[0][n][0], zz[0][n][0]), fmaf(lo16(gw[0][1]), ac[0][n][1], zz[0][n][1])};
                    asm volatile("" : "+v"(zz[0][n]));
                }
                SCHED();
            }
            if (f + DEPTH < 64) LOADF(f + DEPTH);
            if (f == DEPTH + 1 && ring) {
                WAIT_VM(6);
                if (lane == 0) __hip_atomic_store((gu32*)(ws + WS_CFLAG) + cb * 32 + (wid & 3), tag, RLX_AGENT);
                SCHED();
            }
            if (f == YPOLL && ring) { const int l15 = fresh_lane() & 15; cpv = __hip_atomic_load((gu32*)(ws + WS_CFLAG) + (nb * 16 + (wid & 3) * 4 + (l15 >> 2)) * 32 + (l15 & 3), RLX_AGENT); SCHED(); }
            if (f == YPOLL + DEPTH && ring) {
                const int ln = fresh_lane(), l15 = ln & 15, q4 = ln >> 4;
                if (__builtin_expect(!__all(cpv == tag), 0)) {
                    unsigned spins = 0;
                    while (!__all(__hip_atomic_load((gu32*)(ws + WS_CFLAG) + (nb * 16 + (wid & 3) * 4 + (l15 >> 2)) * 32 + (l15 & 3), RLX_AGENT) == tag)) {
                        if (++spins > SPIN_MAX) break;
                        __builtin_amdgcn_s_sleep(1);
                    }
                }
                const unsigned lx = lbase + LA_OFF + (wid & 3) * 8192;
#pragma unroll
                for (int i = 0; i < 4; ++i) {
                    const unsigned vo = (unsigned)((d_base + (wid & 3) * 16 + i * 4 + q4) * 4096 + tb * 256 + ((l15 ^ ((wid & 3) * 4 + i)) * 16));
                    GLDS_SC1(y2T, vo, lx, i * 1024);
                }
                SCHED();
            }
            if (f == YPOLL + 2 * DEPTH + 1 && ring) {
                WAIT_VM(6);
                SETW(YFLAG_OFF, wid & 3, 1u);
                SCHED();
            }
            if (f == 55) {
                const int ln = fresh_lane(), c_ = (wid - 3) & 3;
                const int eg = GA_OFF + c_ * 8192, el = KC_OFF + c_ * CV_W;
                exo = (unsigned)((ring ? el : eg) + ln * 8);
                exi = (unsigned)((ring ? eg : el) + ln * 8);
                efo = (unsigned)(EF_OFF + wid * 8); efi = (unsigned)(EF_OFF + (wid ^ 4) * 8);
                PIN4(exo, exi, efo, efi);
                SCHED();
            }
            if (f == ZF - 2) {
                const int ln = fresh_lane(), l15 = ln & 15, q4 = ln >> 4;
                wait_lds4(smem, YFLAG_OFF);
#pragma unroll
                for (int r = 0; r < 2; ++r) {
                    const int jb = JB(6 + r);
                    const int ya = LA_OFF + (l15 >> 2) * 8192 + (l15 & 3) * 1024 + (((((jb >> 1) & 1) * 8 + (jb >> 2) + q4 * 2) ^ l15) * 16) + (jb & 1) * 8 + ring * 4;
#pragma unroll
                    for (int e = 0; e < 4; ++e) yy[r][e] = *(const unsigned*)(smem + ya + e * 256);
                }
                SCHED();
            }
        };
        unroll<64>(body);
        PAD8(ac[1][0], ac[1][1], ac[1][2], ac[1][3]);
        send(1);
        STAMPT(dbg, 6, 0);
        const __amdgpu_buffer_rsrc_t ors = __builtin_amdgcn_make_buffer_rsrc(out, 0, SEQ * DM * 4, 0x00020000);
        const unsigned ov = (unsigned)((lane >> 4) * (32 * DM * 4) + (lane & 15) * 16);
#pragma unroll
        for (int r = 0; r < 2; ++r) {
            f32x2 in[4];
#define EXRD() asm volatile("ds_read_b32 %0, %5 offset:%7\n\tds_read_b64 %1, %6 offset:%8\n\tds_read_b64 %2, %6 offset:%9\n\tds_read_b64 %3, %6 offset:%10\n\tds_read_b64 %4, %6 offset:%11" \
                             : "=&v"(fl), "=&v"(in[0]), "=&v"(in[1]), "=&v"(in[2]), "=&v"(in[3]) \
                             : "v"(efi), "v"(exi), "i"(r * 4), "i"(r * 4096), "i"(r * 4096 + 512), "i"(r * 4096 + 1024), "i"(r * 4096 + 1536) : "memory")
            EXRD();
            if (r == 0) {
#pragma unroll
                for (int n = 0; n < 4; ++n) {
                    zz[1][n] = (f32x2){fmaf(lo16(gw[1][0]), ac[1][n][0], zz[1][n][0]), fmaf(lo16(gw[1][1]), ac[1][n][1], zz[1][n][1])};
                    asm volatile("" : "+v"(zz[1][n]));
                }
            }
            LWAIT(in);
            if (__builtin_expect(__builtin_amdgcn_readfirstlane(fl) == 0u, 0)) {
                unsigned spins = 0;
#pragma unroll 1
                do {
                    EXRD(); LWAIT(in);
                    if (++spins > SPIN_MAX * 16u) break;
                } while (__builtin_amdgcn_readfirstlane(fl) == 0u);
            }
            f32x4 o[2];
#pragma unroll
            for (int j = 0; j < 2; ++j)
#pragma unroll
                for (int e = 0; e < 4; ++e) o[j][e] = fmaf(lo16(gw[r][j]), in[e][j], zz[r][e][j]);
#pragma unroll
            for (int j = 0; j < 2; ++j)
                __builtin_amdgcn_raw_buffer_store_b128(__builtin_bit_cast(u32x4, o[j]), ors, ov + (unsigned)(((tr[r] + j) * DM + d_base) * 4), 0, WT);
        }
        STAMPT(dbg, 7, 0);
        STAMPT(dbg, 16, 384);
        if (tb == 15 && nb == 0 && wid == 0 && lane == 0) __hip_atomic_store((gu32*)(ws + WS_EPOCH), tag, RLX_AGENT);
    }
}

extern "C" void kernel_launch(void* const* d_in, const int* in_sizes, int n_in,
                              void* d_out, int out_size, void* d_ws, size_t ws_size,
                              hipStream_t stream) {
    const u16* x = (const u16*)d_in[0];
    const u16* lre = (const u16*)d_in[1];
    const u16* lim = (const u16*)d_in[2];
    const u16* Bre = (const u16*)d_in[3];
    const u16* Bim = (const u16*)d_in[4];
    const u16* Cre = (const u16*)d_in[5];
    const u16* Cim = (const u16*)d_in[6];
    const u16* logdt = (const u16*)d_in[7];
    const u16* dskip = (const u16*)d_in[8];
    const u16* kin = (const u16*)d_in[10];
    const u16* vin = (const u16*)d_in[11];
    const u16* shiftk = (const u16*)d_in[12];
    const u16* ssmk = (const u16*)d_in[13];
    const u16* ssmD = (const u16*)d_in[14];
    const u16* gatew = (const u16*)d_in[15];
    float* out = (float*)d_out;

    char* ws = (char*)d_ws;

    hipFuncSetAttribute((const void*)k1_kernel, hipFuncAttributeMaxDynamicSharedMemorySize, K1_LDS);

    k1_kernel<<<256, 512, K1_LDS, stream>>>(x, Bre, Bim, kin, vin, shiftk, gatew, ws, lre, lim, logdt, ssmk, ssmD, Cre, Cim, dskip, out);
}
#if defined(__HIP_DEVICE_COMPILE__)
#pragma clang attribute pop
#endif
```

```cpp
#if defined(__HIP_DEVICE_COMPILE__)
#pragma clang attribute push(__attribute__((target("no-packed-fp32-ops"))), apply_to = function)
#endif
#include <hip/hip_runtime.h>
#include <hip/hip_bf16.h>
#include <stdint.h>

#define TIDX ((int)__builtin_amdgcn_workitem_id_x())
#define BIDX ((int)__builtin_amdgcn_workgroup_id_x())
typedef unsigned short u16;
typedef __attribute__((ext_vector_type(8))) short bf16x8;
typedef __attribute__((ext_vector_type(4))) float f32x4;
typedef __attribute__((ext_vector_type(2))) float f32x2;
typedef __attribute__((ext_vector_type(2))) __bf16 bf16x2;
typedef __attribute__((ext_vector_type(4))) unsigned u32x4;
typedef __attribute__((ext_vector_type(2))) unsigned u32x2;
typedef __attribute__((address_space(1))) unsigned long long gu64;
typedef __attribute__((address_space(1))) unsigned gu32;
typedef __attribute__((address_space(3))) void lds_void;
typedef __attribute__((address_space(3))) unsigned lds_u32;
typedef __attribute__((ext_vector_type(4))) short s16x4;
typedef __attribute__((address_space(3))) s16x4 lds_s16x4;
#define TRD(a) __builtin_amdgcn_ds_read_tr16_b64_v4i16((lds_s16x4*)(size_t)(a))

#define DBGON 0
#define STAMPT(buf, k, t) do { } while (0)
#define CODE_LINES 262
#define SEQ 2048
#define DM 1024
#define SPIN_MAX (1u << 18)
#define PRIO_G 2
#define PSLEEP 4
#define PRIO_L 3
#define WT 16

__device__ __forceinline__ float bf2f(u16 v) { return __uint_as_float(((unsigned)v) << 16); }
__device__ __forceinline__ u16 f2bf(float f) { __bf16 b = (__bf16)f; return __builtin_bit_cast(u16, b); }
__device__ __forceinline__ float rbf(float f) { return bf2f(f2bf(f)); }
__device__ __forceinline__ unsigned pkbf(float lo, float hi) { return __builtin_bit_cast(unsigned, __builtin_convertvector((f32x2){lo, hi}, bf16x2)); }
__device__ __forceinline__ float lo16(unsigned u) { return __uint_as_float(u << 16); }
__device__ __forceinline__ float hi16(unsigned u) { return __uint_as_float(u & 0xffff0000u); }

struct cf { float r, i; };
__device__ __forceinline__ cf cmul(cf a, cf b) { return {a.r * b.r - a.i * b.i, a.r * b.i + a.i * b.r}; }
__device__ __forceinline__ cf cfma(cf a, cf b, cf c) {
    return {fmaf(a.r, b.r, fmaf(-a.i, b.i, c.r)), fmaf(a.r, b.i, fmaf(a.i, b.r, c.i))};
}
__device__ __forceinline__ f32x4 splat4(float v) { return (f32x4){v, v, v, v}; }
__device__ __forceinline__ f32x4 fma4(f32x4 a, f32x4 b, f32x4 c) { return __builtin_elementwise_fma(a, b, c); }
__device__ __forceinline__ f32x4 mul4(f32x4 a, f32x4 b) { return a * b; }

__device__ __forceinline__ float red32(float a, float b) {
    const auto r = __builtin_amdgcn_permlane32_swap(__float_as_uint(a), __float_as_uint(b), false, false);
    return __uint_as_float(r[0]) + __uint_as_float(r[1]);
}
__device__ __forceinline__ float red16(float x) {
    const auto r = __builtin_amdgcn_permlane16_swap(__float_as_uint(x), __float_as_uint(x), false, false);
    return __uint_as_float(r[0]) + __uint_as_float(r[1]);
}
__device__ __forceinline__ int fresh_lane() {
    int l;
    asm volatile("v_mbcnt_lo_u32_b32 %0, -1, 0\n\tv_mbcnt_hi_u32_b32 %0, -1, %0" : "=v"(l));
    return l;
}
#define BAR() __builtin_amdgcn_s_barrier()
#define WAIT_VM(n) asm volatile("s_waitcnt vmcnt(" #n ")" ::: "memory")
#define WAIT_LGKM0() __builtin_amdgcn_s_waitcnt(0xC07F)
#define SCHED() __builtin_amdgcn_sched_barrier(0)
#define MF(c, a, b) asm volatile("v_mfma_f32_16x16x32_bf16 %0, %1, %2, %0" : "+v"(c) : "v"(a), "v"(b))
#define PAD8(c0, c1, c2, c3) asm volatile("s_nop 7" : "+v"(c0), "+v"(c1), "+v"(c2), "+v"(c3))
#define PIN4(c0, c1, c2, c3) asm volatile("" : "+v"(c0), "+v"(c1), "+v"(c2), "+v"(c3))
#define GLDS_S(sbase, voff, sl, IMM) \
    asm volatile("s_add_u32 m0, %2, %3\n\ts_nop 0\n\tglobal_load_lds_dwordx4 %0, %1" \
                 :: "v"(voff), "s"(sbase), "s"(sl), "i"(IMM) : "memory", "scc")
#define GLDS_K(sbase, voff, sl, IMM) \
    asm volatile("s_add_u32 m0, %2, %3\n\ts_nop 0\n\tglobal_load_lds_dwordx4 %0, %1 nt" \
                 :: "v"(voff), "s"(sbase), "s"(sl), "i"(IMM) : "memory", "scc")
#define RLX_AGENT __ATOMIC_RELAXED, __HIP_MEMORY_SCOPE_AGENT

#define UNITS_A 0x0756075607560560ull
#define UNITS_B 0x1432143214321432ull
#define RA(t) ((int)((UNITS_A >> (4 * (t))) & 15) * 16384)
#define RB(t) ((int)((UNITS_B >> (4 * (t))) & 15) * 16384)
#define LA_OFF 0
#define GA_OFF 32768
#define CV_COPY 4160
#define CV_W (4 * CV_COPY)
#define KC_OFF 65536
#define ST_OFF 114688
#define ST_V 8832
#define ST_SZ (ST_V + 8 * 1056)
#define U_OFF (KC_OFF + 16 * CV_COPY)
#define HT_OFF (U_OFF + 256 + 16384)
#define GW_OFF (HT_OFF + 8192)
#define TAG_OFF (GW_OFF + 2048)
#define UFLAG_OFF (TAG_OFF + 16)
#define G1D_OFF (UFLAG_OFF + 16)
#define MISC_OFF (G1D_OFF + 16)
#define YFLAG_OFF (MISC_OFF + 16)
#define RF_OFF (YFLAG_OFF + 16)
#define EF_OFF (RF_OFF + 64)
#define COK_OFF (EF_OFF + 64)
#define NFLAGW 52
#define DUMMY_OFF (COK_OFF + 16)
#define K1_LDS (DUMMY_OFF + 256)
#define WS_GRAN 0
#define WS_KVFLAG (256u << 10)
#define WS_EPOCH (WS_KVFLAG + 65536)
#define WS_CFLAG (WS_EPOCH + 4096)
#define WS_HFLAG (384u << 10)
#define WS_GATE (512u << 10)

#define CODE_TOUCH(vl) asm volatile("s_getpc_b64 s[98:99]\n\ts_add_u32 s98, s98, k1_kernel@rel32@lo+4\n\ts_addc_u32 s99, s99, k1_kernel@rel32@hi+12\n\t" \
                                    "s_mov_b32 m0, %1\n\ts_nop 0\n\tglobal_load_lds_dword %0, s[98:99]" :: "v"((unsigned)(vl) * 128u), "i"(DUMMY_OFF) : "s98", "s99", "scc", "memory")
#define RD128(a, off) (*(const __attribute__((address_space(3))) bf16x8*)(size_t)(unsigned)((a) + (off)))
#define RDA(dst, A, F) do { const s16x4 lo_ = TRD((A) + 512 * (F) + 8); SCHED(); const s16x4 hi_ = TRD((A) + 512 * (F)); SCHED(); \
                            dst = __builtin_shufflevector(lo_, hi_, 0, 1, 2, 3, 4, 5, 6, 7); } while (0)
#define TOUCH(sbase, voff) asm volatile("s_mov_b32 m0, %2\n\ts_nop 0\n\tglobal_load_lds_dword %0, %1" :: "v"(voff), "s"(sbase), "i"(DUMMY_OFF) : "memory")
#define FLAGD(off) do { unsigned long long pc_; asm volatile("s_getpc_b64 %0\n\ts_mov_b32 m0, %2\n\ts_mov_b64 exec, 1\n\tglobal_load_lds_dword %1, %0\n\ts_mov_b64 exec, -1" \
                                                             : "=&s"(pc_) : "v"(0u), "s"(__builtin_amdgcn_readfirstlane(off)) : "memory"); } while (0)
#define GLDS_SC1(sbase, voff, sl, IMM) \
    asm volatile("s_add_u32 m0, %2, %3\n\ts_nop 0\n\tglobal_load_lds_dwordx4 %0, %1 sc1" \
                 :: "v"(voff), "s"(sbase), "s"(sl), "i"(IMM) : "memory", "scc")
__device__ __forceinline__ unsigned mv(unsigned v) { unsigned r; asm("v_mov_b32 %0, %1" : "=v"(r) : "v"(v)); return r; }
__device__ __forceinline__ unsigned al(unsigned hi, unsigned lo) { unsigned r; asm volatile("v_alignbit_b32 %0, %1, %2, 16" : "=v"(r) : "v"(hi), "v"(lo)); return r; }
__device__ __forceinline__ u32x2 pred2(const u32x4& a, unsigned f0, unsigned f1) {
    return (u32x2){(unsigned)__builtin_amdgcn_update_dpp((int)f0, (int)a.z, 0x138, 0xf, 0xf, false), (unsigned)__builtin_amdgcn_update_dpp((int)f1, (int)a.w, 0x138, 0xf, 0xf, false)};
}
#define RL63(v) ((unsigned)__builtin_amdgcn_readlane((int)(v), 63))
__device__ __forceinline__ void wait_lds4(char* smem, int off) {
    const unsigned a = (unsigned)(size_t)(lds_void*)smem + off;
    unsigned spins = 0;
    for (;;) {
        u32x4 f;
        asm volatile("ds_read_b128 %0, %1\n\ts_waitcnt lgkmcnt(0)" : "=&v"(f) : "v"(a) : "memory");
        if (__builtin_amdgcn_readfirstlane((f.x != 0u) & (f.y != 0u) & (f.z != 0u) & (f.w != 0u))) break;
        if (++spins > SPIN_MAX * 16u) break;
        __builtin_amdgcn_s_sleep(1);
    }
}
__device__ __forceinline__ unsigned lds_word(char* smem, int off) {
    const unsigned a = (unsigned)(size_t)(lds_void*)smem + off;
    unsigned f;
    asm volatile("ds_read_b32 %0, %1\n\ts_waitcnt lgkmcnt(0)" : "=&v"(f) : "v"(a) : "memory");
    return __builtin_amdgcn_readfirstlane(f);
}
__device__ __forceinline__ void wait_lds1(char* smem, int off) {
    unsigned spins = 0;
    while (lds_word(smem, off) == 0u) {
        if (++spins > SPIN_MAX * 16u) break;
        __builtin_amdgcn_s_sleep(1);
    }
}
#define SETW(off, i, v) __hip_atomic_store((lds_u32*)(smem + (off)) + (i), (v), __ATOMIC_RELAXED, __HIP_MEMORY_SCOPE_WORKGROUP)

#define PRIO_CV 1
#define PRIO_KV 3
#define CS0 1
#define CS1 3
#define CS2 5
#define XE 4
#define YPOLL 32
#define ZF 59
template <int I> struct IC { static constexpr int value = I; };
template <int N, int I = 0, class F> __device__ __forceinline__ void unroll(F& f) { if constexpr (I < N) { f(IC<I>{}); unroll<N, I + 1>(f); } }
#define MF0(c, a, b) asm volatile("v_mfma_f32_16x16x32_bf16 %0, %1, %2, 0" : "=&v"(c) : "v"(a), "v"(b))
#define DEPTH 6
#define PRIO2_G 0
#define PRIO2_L 1
__device__ __forceinline__ cf pick4(int q, cf a1, cf a2, cf a3) {
    return {q == 0 ? 1.f : q == 1 ? a1.r : q == 2 ? a2.r : a3.r, q == 0 ? 0.f : q == 1 ? a1.i : q == 2 ? a2.i : a3.i};
}
__device__ __forceinline__ void idle_mfma(unsigned a, unsigned b, int n) {
    u32x4 p = {a * 2654435761u, b * 40503u, a * 2246822519u, b * 3266489917u};
    p = (p & 0xbfffbfffu) | 0x3c003c00u;
    u32x4 q = p.yzwx ^ 0x01230456u;
    f32x4 w;
    asm volatile("s_nop 1" : "+v"(p), "+v"(q));
    MF0(w, p, q);
#pragma unroll 1
    for (int i = 0; i < n; ++i) { MF(w, q, p); MF(w, p, p); MF(w, q, q); MF(w, p, q); }
    asm volatile("s_nop 7" : "+v"(w));
}
#define PINC(c) asm volatile("" : "+v"((c).r), "+v"((c).i))

extern "C" __global__ __launch_bounds__(512, 2) void k1_kernel(
        const u16* __restrict__ x, const u16* __restrict__ Bre, const u16* __restrict__ Bim,
        const u16* __restrict__ kin, const u16* __restrict__ vin, const u16* __restrict__ shiftk, const u16* __restrict__ gatew, char* ws,
        const u16* __restrict__ lre, const u16* __restrict__ lim, const u16* __restrict__ logdt,
        const u16* __restrict__ ssmk, const u16* __restrict__ ssmD,
        const u16* __restrict__ Cre, const u16* __restrict__ Cim, const u16* __restrict__ dskip, float* __restrict__ out) {
    extern __shared__ __attribute__((aligned(16))) char smem[];
#define h ((u16*)(ws + (1u << 20)))
#define kvT ((u16*)(ws + (9u << 20)))
#define y2T ((u16*)(ws + (13u << 20)))
#define dbg ((unsigned long long*)(ws + (64u << 20)))
    const int tid = TIDX;
    if (tid < 40 && tid + ((BIDX >> 3) & 7) * 40 < CODE_LINES) CODE_TOUCH(tid + ((BIDX >> 3) & 7) * 40);
    const int lane = tid & 63;
    int wid = __builtin_amdgcn_readfirstlane(tid >> 6);
    const int b = BIDX;
    const int xcd = b & 7, idx = b >> 3;
    const int tb = ((xcd >> 1) ^ (xcd >> 2)) * 4 + (idx & 3), nb = (xcd & 1) * 8 + (idx >> 2);
    const int cb = xcd * 32 + idx;
    const int t_base = tb * 128, p_base = nb * 64, d_base = nb * 64;
    const int l15 = lane & 15, q4 = lane >> 4;
    unsigned stv = 0, sth = 0;
    STAMPT(dbg, 0, 0);
    const unsigned lbase = (unsigned)(size_t)(lds_void*)smem;
    const int rot1 = (xcd >> 1) * 4 + (xcd & 1) * 8 + (idx >> 2) + (idx & 1);
    unsigned voA[4] = {0u, 0u, 0u, 0u}, voB[4] = {0u, 0u, 0u, 0u}, lw0 = 0;
    if (wid == 4 || wid == 5) {
        __builtin_amdgcn_s_setprio(PRIO_L);
        const int lh = wid - 4;
        const unsigned ch = (unsigned)(((lane & 7) ^ (lane >> 4)) * 16);
        voA[0] = (unsigned)(t_base + lh * 16 + (lane >> 5) * 32 + ((lane >> 3) & 3)) * 2048u + ch;
        voA[1] = (voA[0] ^ 64u) + (131072u - 1024u);
        voA[2] = voA[0] + 6144u; voA[3] = voA[1] + 6144u;
        voB[0] = (unsigned)(p_base + lh * 32 + (lane >> 3)) * 2048u + ch;
        voB[1] = (voB[0] ^ 64u) + (16384u - 1024u);
        voB[2] = voB[0] + 30720u; voB[3] = voB[1] + 30720u;
        PIN4(voA[0], voA[1], voA[2], voA[3]); PIN4(voB[0], voB[1], voB[2], voB[3]);
#define GLDS4(sbase, v0, v1, v2, v3, sl, IMM) \
            asm volatile("s_add_u32 m0, %5, %6\n\ts_nop 0\n\tglobal_load_lds_dwordx4 %0, %4\n\tglobal_load_lds_dwordx4 %1, %4 offset:1024\n\t" \
                         "global_load_lds_dwordx4 %2, %4 offset:2048\n\tglobal_load_lds_dwordx4 %3, %4 offset:3072" \
                         :: "v"(v0), "v"(v1), "v"(v2), "v"(v3), "s"(sbase), "s"(sl), "i"(IMM) : "memory", "scc")
#define STAGE_A(kt, sl) do { const char* sa_ = (const char*)x + (((kt) + rot1) & 15) * 128; \
            GLDS4(sa_, voA[0], voA[1], voA[2], voA[3], sl, 0); GLDS4(sa_ + 16384, voA[0], voA[1], voA[2], voA[3], sl, 4096); } while (0)
#define STAGE_B(kt, sl) do { const int kr_ = (((kt) + rot1) & 15) * 128; const char* sb_ = (const char*)Bre + kr_; const char* sc_ = (const char*)Bim + kr_; \
            GLDS4(sb_, voB[0], voB[1], voB[2], voB[3], sl, 0); GLDS4(sc_, voB[0], voB[1], voB[2], voB[3], sl, 4096); } while (0)
        lw0 = lbase + lh * 8192;
        STAGE_A(0, lw0 + RA(0)); STAGE_B(0, lw0 + RB(0));
        idle_mfma(voA[0], voB[1], 12);
        STAGE_A(1, lw0 + RA(1)); STAGE_B(1, lw0 + RB(1));
    }
    const int sblk = b >> 3, dblk = b & 7;
    u32x2 tw[4];
    unsigned epoch;
    asm volatile("" : "=v"(tw[0]), "=v"(tw[1]), "=v"(tw[2]), "=v"(tw[3]), "=v"(epoch));
    if (wid >= 6) {
        __builtin_amdgcn_s_setprio(PRIO_L);
        const int vw = wid - 6;
        const int dch = dblk * 128 + vw * 64 + l15;
        const unsigned lk = lbase + ST_OFF + vw * ST_SZ;
        const unsigned vk = (unsigned)((lane >> 3) * DM * 2 + (((lane & 7) ^ ((lane >> 4) & 1)) * 16));
        const char* kb = (const char*)kin + (size_t)(sblk * 64 - 4) * (DM * 2) + dblk * 256 + vw * 128;
#define KLD(n) GLDS_K(kb + (n) * 8 * DM * 2, vk, lk, (n) * 1024 + ((n) >> 1) * 32)
#define VLD(n) GLDS_K(vb + (((n) >> 1) * 16 + ((n) & 1) * 2) * DM * 2, vv_, lk, ST_V + (n) * 1056)
        if (vw == 1) {
            const unsigned vg = (unsigned)((lane * 16 + rot1 * 128) & 2047), lg = lbase + GW_OFF;
            GLDS_S(gatew, vg, lg, 0); GLDS_S(gatew, vg ^ 1024u, lg, 1024);
        }
        asm volatile("global_load_dwordx2 %0, %5, %7\n\tglobal_load_dwordx2 %1, %5, %7 offset:128\n\tglobal_load_dwordx2 %2, %5, %7 offset:256\n\t"
                     "global_load_dwordx2 %3, %5, %7 offset:384\n\tglobal_load_dword %4, %6, %8 sc1"
                     : "+v"(tw[0]), "+v"(tw[1]), "+v"(tw[2]), "+v"(tw[3]), "+v"(epoch) : "v"((unsigned)(dch * 8)), "v"((unsigned)WS_EPOCH), "s"(shiftk), "s"(ws) : "memory");
        if (sblk == 0 && lane < 32) *(u32x4*)(smem + ST_OFF + vw * ST_SZ + lane * 16) = (u32x4){0u, 0u, 0u, 0u};
        if (sblk != 0 || lane >= 32) KLD(0);
        KLD(1); KLD(2); KLD(3); KLD(4); KLD(5); KLD(6); KLD(7);
        if (lane < 32) KLD(8);
        __builtin_amdgcn_s_setprio(0);
    }
    asm volatile("" : "+s"(wid));

#define WSL(w) ((((w) >> 2) ^ (nb >> 3)) * 4 + ((w) & 3))
#define COFF(w, ti) ((2 * WSL(w) + ((ti) >> 1)) * 128 + ((ti) & 1) * 4096)
    bf16x8 cf_[8][4];
    bf16x8 af[DEPTH];
    int cra, crb;
#define CT_SETUP() const unsigned vB0 = (unsigned)((d_base + (lane >> 3) * 4) * 2048 + (((lane & 7) ^ (lane >> 4)) * 16)), vB1 = (vB0 ^ 64u) + 65536u - 1024u
#define CTILE(w, ti, dst) do { const int co_ = COFF(w, ti); const unsigned ds_ = (dst); \
                               GLDS4((const char*)Cre + co_, vB0, vB1, vB0, vB1, ds_, 0); GLDS4((const char*)Cim + co_, vB0, vB1, vB0, vB1, ds_, 4096); } while (0)
#define CF(ti, n) cf_[4 * ((ti) >> 1) + ((n) >> 1)][2 * ((ti) & 1) + ((n) & 1)]
#define CRD(ti, n) CF(ti, n) = RD128((((n) & 4) ? crb : cra) ^ (((n) & 2) * 16), ((n) & 1) * 2048)
#define CREAD(ti) do { _Pragma("unroll") for (int n_ = 0; n_ < 8; ++n_) CRD(ti, n_); } while (0)
#define CPIN(ti) do { PIN4(CF(ti, 0), CF(ti, 1), CF(ti, 2), CF(ti, 3)); PIN4(CF(ti, 4), CF(ti, 5), CF(ti, 6), CF(ti, 7)); } while (0)
#define CRA(base) ((base) + (q4 >> 1) * 4096 + l15 * 128 + (((q4 & 1) ^ ((l15 >> 1) & 7)) * 16))
    const int rot2 = (idx & 16) >> 2;
#define JB(u) (((((u) ^ (rot2 >> 2)) & 1) << 2) | ((wid - ((u) >> 1) + rot2) & 3))
    const __amdgpu_buffer_rsrc_t hs = __builtin_amdgcn_make_buffer_rsrc((char*)h + (size_t)tb * 524288 + WSL(wid) * 8192, 0, 524288, 0x00020000);
#define LOADF(f) do { af[(f) % DEPTH] = __builtin_bit_cast(bf16x8, __builtin_amdgcn_raw_buffer_load_b128(hs, vh + ((f) & 3) * 1024, JB((f) >> 3) * 65536 + ((f) & 4) * 1024, 16)); SCHED(); } while (0)
#define HPOLL_SETUP() gu32* hfp = (gu32*)(ws + WS_HFLAG) + (tb * 16 + 2 * WSL(wid) + (lane & 1)) * 32
#define HWAIT() do { unsigned spins = 0; \
            while (!__all(__hip_atomic_load(hfp, RLX_AGENT) == tag)) { if (++spins > SPIN_MAX) break; __builtin_amdgcn_s_sleep(PSLEEP); } asm volatile("" ::: "memory"); } while (0)

    const __amdgpu_buffer_rsrc_t gs = __builtin_amdgcn_make_buffer_rsrc(ws + WS_GATE, 0, 32768, 0x00020000);
#define GV(q4) ((unsigned)(((q4) >> 1) * 128 + ((q4) & 1) * 32))
#define GSO(r) (tb * 2048 + JB(6 + (r)) * 256 + (wid >> 2) * 16)
#define TR(r) (t_base + JB(6 + (r)) * 4 + (wid >> 2) * 2)
    u32x2 xx[2][2], dk2;
    u32x4 gv[2];
#define XLOAD() do { \
        const __amdgpu_buffer_rsrc_t xs = __builtin_amdgcn_make_buffer_rsrc((void*)x, 0, SEQ * DM * 2, 0x00020000); \
        const __amdgpu_buffer_rsrc_t ks = __builtin_amdgcn_make_buffer_rsrc((void*)dskip, 0, DM * 2, 0x00020000); \
        dk2 = __builtin_amdgcn_raw_buffer_load_b64(ks, (unsigned)(l15 * 8), d_base * 2, 0); \
        _Pragma("unroll") for (int q = 0; q < 4; ++q) \
            xx[q >> 1][q & 1] = __builtin_amdgcn_raw_buffer_load_b64(xs, (unsigned)(q4 * 32 * DM * 2 + l15 * 8 + (q & 1) * DM * 2), (TR(q >> 1) * DM + d_base) * 2, 0); \
        _Pragma("unroll") for (int r = 0; r < 2; ++r) gv[r] = __builtin_amdgcn_raw_buffer_load_b128(gs, GV(q4), GSO(r), 16); \
        SCHED(); } while (0)
#define XPIN() do { PIN4(xx[0][0], xx[0][1], xx[1][0], xx[1][1]); asm volatile("" : "+v"(dk2), "+v"(gv[0]), "+v"(gv[1])); } while (0)

    if (wid < 4) {
        typedef __attribute__((ext_vector_type(8))) unsigned u32x8;
        typedef const __attribute__((address_space(4))) u32x8 cu8;
        const int po = (p_base + wid * 16) * 2;
        u32x8 s0 = *(cu8*)((size_t)logdt + po), s1 = *(cu8*)((size_t)lre + po), s2 = *(cu8*)((size_t)lim + po);
        SCHED();
        const int pq = p_base + wid * 16 + l15;
        unsigned goff[4], gmask[4];
#pragma unroll
        for (int i = 0; i < 4; ++i) {
            goff[i] = (unsigned)(((tb - 1 - q4 - 4 * i) * 1024 + pq) * 16);
            gmask[i] = (tb - 1 - q4 - 4 * i >= 0) ? ~0u : 0u;
            asm volatile("" : "+v"(goff[i]), "+v"(gmask[i]));
        }
        unsigned gpub = q4 == 0 ? (unsigned)((tb * 1024 + pq) * 16) : 0x80000000u;
        unsigned hp = (unsigned)(tb * 524288 + (nb * 4 + wid) * 1024 + lane * 16);
        unsigned wa = (unsigned)(HT_OFF + wid * 2048 + (q4 * 16 + (l15 ^ ((q4 >> 1) * 4))) * 8);
        unsigned ra0 = (unsigned)(HT_OFF + wid * 2048 + ((q4 >> 1) * 64 + (l15 & 3) * 16 + (((q4 & 1) * 8 + (l15 >> 2)) ^ ((l15 & 2) * 2))) * 8), ra1 = ra0 ^ 32u;
        cra = CRA(GA_OFF + wid * 8192); crb = cra ^ 64;
        unsigned vB0 = (unsigned)((d_base + (lane >> 3) * 4) * 2048 + (((lane & 7) ^ (lane >> 4)) * 16)), vB1 = (vB0 ^ 64u) + 65536u - 1024u;
        PIN4(gpub, vB0, hp, wa); PIN4(ra0, ra1, cra, crb); asm volatile("" : "+v"(vB1));
        f32x4 acc[8][2];
#pragma unroll
        for (int m = 0; m < 8; m += 2) {
#pragma unroll
            for (int c = 0; c < 4; ++c) acc[m + (c >> 1)][c & 1] = (f32x4){0.f, 0.f, 0.f, 0.f};
            PIN4(acc[m][0], acc[m][1], acc[m + 1][0], acc[m + 1][1]);
        }
        SCHED();
        cf L1, Lq3, cc, L32, cLq, Lp, L512;
        {
            unsigned w0 = 0, w1 = 0, w2 = 0;
#pragma unroll
            for (int i = 0; i < 8; ++i) {
                asm("v_writelane_b32 %0, %3, %6\n\tv_writelane_b32 %1, %4, %6\n\tv_writelane_b32 %2, %5, %6" : "+v"(w0), "+v"(w1), "+v"(w2) : "s"(s0[i]), "s"(s1[i]), "s"(s2[i]), "i"(2 * i));
            }
            asm("s_nop 1" : "+v"(w0), "+v"(w1), "+v"(w2));
            auto spread = [&](unsigned w) {
                const unsigned e = (unsigned)__builtin_amdgcn_update_dpp(0, (int)w, 0xa0, 0xf, 0xf, false);
                const unsigned a = __builtin_amdgcn_permlane16_swap(e, e, false, false)[0];
                const unsigned c = __builtin_amdgcn_permlane32_swap(a, a, false, false)[0];
                return (l15 & 1) ? hi16(c) : lo16(c);
            };
            const float ldt = spread(w0), re = spread(w1), im = spread(w2);
            if (wid == 3 && lane < NFLAGW) *(unsigned*)(smem + UFLAG_OFF + lane * 4) = 0u;
            const float dt = rbf(expf(ldt));
            const float ea = expf(dt * re);
            float sn, cs;
            sincosf(dt * im, &sn, &cs);
            L1 = {ea * cs, ea * sn};
            const float nr = L1.r - 1.0f, ni = L1.i, den = re * re + im * im;
            cc = {2.f * ((nr * re + ni * im) / den), 2.f * ((ni * re - nr * im) / den)};
            const cf T2 = cmul(L1, L1), T4 = cmul(T2, T2), L8 = cmul(T4, T4), L16 = cmul(L8, L8);
            L32 = cmul(L16, L16);
            const cf L64 = cmul(L32, L32), L96 = cmul(L64, L32), L128 = cmul(L64, L64), L256 = cmul(L128, L128), L384 = cmul(L256, L128);
            L512 = cmul(L256, L256);
            Lp = pick4(q4, L128, L256, L384);
            Lq3 = pick4(3 - q4, L32, L64, L96);
            cLq = cmul(cc, pick4(q4, L32, L64, L96));
            PINC(L1); PINC(Lq3); PINC(cc); PINC(L32); PINC(cLq); PINC(Lp); PINC(L512);
        }
        __builtin_amdgcn_s_setprio(PRIO_G);
        unsigned tagv;
        cf S;
        STAMPT(dbg, 22, 0);
        const int swz = (l15 >> 1) & 7;
        {
            const int a0 = RA(0) + l15 * 128 + ((q4 ^ swz) * 16);
            const int b0 = RB(0) + ((wid >> 1) * 64 + (wid & 1) * 16 + l15) * 128 + ((q4 ^ swz) * 16);
            WAIT_LGKM0();
            BAR();
            asm volatile("" ::: "memory");
            STAMPT(dbg, 19, 64);
            bf16x8 FA[8][2], FB[2][2][2];
            int pa[2] = {a0, a0 ^ 64}, pb[2] = {b0, b0 ^ 64};
            PIN4(pa[0], pa[1], pb[0], pb[1]);
#define LGW(n) __builtin_amdgcn_s_waitcnt(0xC07F | ((n) << 8))
#define RA_(j) FA[(j) >> 1][(j) & 1] = RD128(pa[(j) & 1], ((j) >> 1) * 2048)
#define RB_(bu, j) FB[bu][(j) >> 1][((j) ^ ((j) >> 1)) & 1] = RD128(pb[((j) ^ ((j) >> 1)) & 1], ((j) >> 1) * 4096)
#define RN_(bu, i) do { if ((i) == 0) RB_(bu, 0); else if ((i) < 3) RA_((i) - 1); else if ((i) < 6) RB_(bu, (i) - 2); else RA_((i) - 4); } while (0)
#define G1_H(H, bu, RD, FILL) do { \
            _Pragma("unroll") for (int i = 0; i < 16; ++i) { \
                const int m = (H) * 4 + (i >> 2), n = ((i >> 1) ^ m) & 1, ks = (i ^ (i >> 1)) & 1; \
                if ((H) == 0) { if (i == 0) LGW(6); if (i == 4) LGW(8); if (i == 8) LGW(10); if (i == 12) LGW(8); } \
                SCHED(); \
                MF(acc[m][n], FA[m][ks], FB[bu][n][ks]); SCHED(); \
                if ((H) == 0 && i < 8) RA_(8 + i); \
                if ((H) == 1 && (RD) && i < 12) RN_((bu) ^ 1, i); \
                FILL((H) * 16 + i); \
                SCHED(); \
            } } while (0)
#define NOF(g) do { } while (0)
#pragma unroll
            for (int j = 0; j < 12; ++j) { RN_(0, j); SCHED(); }
            unsigned long long ua_ = UNITS_A;
            int sb = RB(0), sa = RA(0), nx, na;
#define ADV(g) do { if ((g) == 8) { ua_ >>= 4; na = (int)((unsigned)ua_ & 15u) << 14; asm volatile("" : "+s"(na), "+s"(ua_)); } \
                    if ((g) == 9) { const int o_ = sa; sa = na; na -= o_; asm volatile("" : "+s"(na), "+s"(sa)); } \
                    if ((g) == 10) { pa[0] += na; pa[1] += na; asm volatile("" : "+v"(pa[0]), "+v"(pa[1])); } \
                    if ((g) == 11) { nx = (sb == 65536) ? -49152 : 16384; sb += nx; asm volatile("" : "+s"(nx), "+s"(sb)); } \
                    if ((g) == 13) { pb[0] += nx; pb[1] += nx; asm volatile("" : "+v"(pb[0]), "+v"(pb[1])); } } while (0)
#define G1_T(bu) do { \
                G1_H(0, bu, true, ADV); \
                WAIT_LGKM0(); \
                asm volatile("" ::: "memory"); \
                BAR();                     \
                asm volatile("" ::: "memory"); \
                G1_H(1, bu, true, NOF); } while (0)
#pragma unroll 1
            for (int t2 = 0; t2 < 7; ++t2) { G1_T(0); G1_T(1); }
            G1_T(0);
#define AGG(g) do { if ((g) == 8) { asm volatile("" ::: "memory"); SETW(G1D_OFF, wid, 1u); }        \
                    if ((g) == 9) tagv = *(volatile const lds_u32*)(size_t)(unsigned)TAG_OFF; \
                    if ((g) >= 7) { const int t = (g) - 6; \
                        if (t == 1) { asm volatile("s_nop 1" : "+v"(acc[0][0]), "+v"(acc[0][1])); S = {acc[0][0][0], acc[0][1][0]}; } \
                        if ((t & 3) == 0) asm volatile("" : "+v"(acc[t >> 2][0]), "+v"(acc[t >> 2][1])); \
                        S = cfma(L1, S, cf{acc[t >> 2][0][t & 3], acc[t >> 2][1][t & 3]}); PINC(S); } } while (0)
            G1_H(0, 1, false, AGG);
            G1_H(1, 1, false, AGG);
        }
        STAMPT(dbg, 1, 0);
        const unsigned tag = __builtin_amdgcn_readfirstlane(tagv);
        const __amdgpu_buffer_rsrc_t grs = __builtin_amdgcn_make_buffer_rsrc(ws, 0, 15 * 16384, 0x00020000);
        {
#pragma unroll
            for (int t = 26; t < 32; ++t) {
                if (t == 28) { PINC(S); asm volatile("" : "+v"(acc[7][0]), "+v"(acc[7][1])); }
                S = cfma(L1, S, cf{acc[t >> 2][0][t & 3], acc[t >> 2][1][t & 3]});
            }
            const cf W = cmul(S, Lq3);
            const float e2 = red16(red32(W.r, W.i));
            const auto eo = __builtin_amdgcn_permlane32_swap(__float_as_uint(e2), __float_as_uint(e2), false, false);
            __builtin_amdgcn_raw_buffer_store_b128((u32x4){eo[0], tag, eo[1], tag}, grs, gpub, 0, 16);
        }
        SCHED();
        STAMPT(dbg, 2, 0);
        const unsigned cg = lbase + GA_OFF + wid * 8192;
        wait_lds1(smem, COK_OFF + wid * 4);
        CREAD(0);
        WAIT_LGKM0();
        CPIN(0);
        CTILE(wid, 1, cg);
        u32x4 gq[4];
#pragma unroll
        for (int m = 0; m < 8; ++m) {
            const f32x4 R = acc[m][0], I = acc[m][1];
            acc[m][0] = fma4(splat4(cc.r), R, mul4(splat4(-cc.i), I));
            acc[m][1] = fma4(splat4(-cc.r), I, mul4(splat4(-cc.i), R));
            asm volatile("" : "+v"(acc[m][0]), "+v"(acc[m][1]));
            if (m == 3) {
                SCHED();
#pragma unroll
                for (int i = 0; i < 4; ++i) asm volatile("buffer_load_dwordx4 %0, %1, %2, 0 offen sc1" : "=&v"(gq[i]) : "v"(goff[i]), "s"(grs) : "memory");
                SCHED();
            }
        }
        cf Ec;
        {
            const auto sr = __builtin_amdgcn_permlane16_swap(__float_as_uint(S.r), __float_as_uint(S.r), false, false);
            const auto si = __builtin_amdgcn_permlane16_swap(__float_as_uint(S.i), __float_as_uint(S.i), false, false);
            const cf Se = {__uint_as_float(sr[0]), __uint_as_float(si[0])}, So = {__uint_as_float(sr[1]), __uint_as_float(si[1])};
            const cf A = cfma(L32, Se, So);
            const auto ar = __builtin_amdgcn_permlane32_swap(__float_as_uint(A.r), __float_as_uint(A.r), false, false);
            const auto ai = __builtin_amdgcn_permlane32_swap(__float_as_uint(A.i), __float_as_uint(A.i), false, false);
            const cf A01 = {(q4 & 2) ? __uint_as_float(ar[0]) : 0.f, (q4 & 2) ? __uint_as_float(ai[0]) : 0.f};
            const cf kq = {(q4 & 1) ? L32.r : 1.f, (q4 & 1) ? L32.i : 0.f};
            const cf E = cfma(kq, A01, cf{(q4 & 1) ? Se.r : 0.f, (q4 & 1) ? Se.i : 0.f});
            Ec = cmul(cc, E);
            PINC(Ec);
        }
        WAIT_VM(4);
        CREAD(1);
        WAIT_LGKM0();
        CPIN(1);
        CTILE(wid, 2, cg);
        asm volatile("s_waitcnt vmcnt(8)" : "+v"(gq[0]), "+v"(gq[1]), "+v"(gq[2]), "+v"(gq[3]) :: "memory");
        STAMPT(dbg, 27, 0);
        cf carry;
        {
            unsigned spins = 0;
            for (;;) {
                unsigned bad = 0;
#pragma unroll
                for (int i = 0; i < 4; ++i) bad |= ((gq[i].y ^ tag) | (gq[i].w ^ tag)) & gmask[i];
                if (__all(bad == 0u)) break;
                if (++spins > SPIN_MAX) break;
#pragma unroll
                for (int i = 0; i < 4; ++i) asm volatile("buffer_load_dwordx4 %0, %1, %2, 0 offen sc1" : "+v"(gq[i]) : "v"(goff[i]), "s"(grs) : "memory");
                asm volatile("s_waitcnt vmcnt(0)" : "+v"(gq[0]), "+v"(gq[1]), "+v"(gq[2]), "+v"(gq[3]) :: "memory");
            }
            cf E = {__uint_as_float(gq[3].x), __uint_as_float(gq[3].z)};
#pragma unroll
            for (int i = 2; i >= 0; --i) E = cfma(L512, E, cf{__uint_as_float(gq[i].x), __uint_as_float(gq[i].z)});
            E = cmul(E, Lp);
            const float e2 = red16(red32(E.r, E.i));
            const auto eo = __builtin_amdgcn_permlane32_swap(__float_as_uint(e2), __float_as_uint(e2), false, false);
            carry = {__uint_as_float(eo[0]), __uint_as_float(eo[1])};
        }
        STAMPT(dbg, 3, 0);
        {
            const __amdgpu_buffer_rsrc_t hrs = __builtin_amdgcn_make_buffer_rsrc(h, 0, SEQ * 2048 * 2, 0x00020000);
            const cf e = cfma(cLq, carry, Ec);
            float hr = e.r, hn = -e.i;
            s16x4 fr[2][4];
#define HSTORE(m) do { _Pragma("unroll") for (int c = 0; c < 2; ++c) \
                __builtin_amdgcn_raw_buffer_store_b128(__builtin_bit_cast(u32x4, __builtin_shufflevector(fr[(m) & 1][2 * c], fr[(m) & 1][2 * c + 1], 0, 1, 2, 3, 4, 5, 6, 7)), hrs, hp, (2 * (m) + c) * 65536, WT); } while (0)
#pragma unroll
            for (int m = 0; m < 4; ++m) {
#pragma unroll
                for (int j = 0; j < 8; ++j) {
                    const float nr = fmaf(L1.r, hr, fmaf(L1.i, hn, acc[2 * m + (j >> 2)][0][j & 3]));
                    const float nn = fmaf(L1.r, hn, fmaf(-L1.i, hr, acc[2 * m + (j >> 2)][1][j & 3]));
                    hr = nr; hn = nn;
                    acc[2 * m + (j >> 2)][0][j & 3] = nr; acc[2 * m + (j >> 2)][1][j & 3] = nn;
                }
                u32x2 pk[4];
#pragma unroll
                for (int c = 0; c < 4; ++c) { const f32x4 v = acc[2 * m + (c >> 1)][c & 1]; pk[c] = (u32x2){pkbf(v[0], v[1]), pkbf(v[2], v[3])}; }
                PIN4(pk[0], pk[1], pk[2], pk[3]);
                SCHED();
#pragma unroll
                for (int c = 0; c < 4; ++c) { *(volatile __attribute__((address_space(3))) u32x2*)(size_t)(wa + c * 512) = pk[c]; SCHED(); }
                asm volatile("" ::: "memory");
#pragma unroll
                for (int c = 0; c < 2; ++c) { fr[m & 1][2 * c] = TRD(ra0 + c * 1024); SCHED(); fr[m & 1][2 * c + 1] = TRD(ra1 + c * 1024); SCHED(); }
                asm volatile("" ::: "memory");
                if (m > 0) HSTORE(m - 1);
                SCHED();
            }
            HSTORE(3);
        }
        STAMPT(dbg, 4, 0);
        {
            WAIT_VM(8);
            CREAD(2);
            WAIT_VM(0);
            {
                unsigned n;
                asm volatile("s_mov_b64 exec, 1\n\tds_add_rtn_u32 %0, %1, %2\n\ts_mov_b64 exec, -1\n\ts_waitcnt lgkmcnt(0)" : "=&v"(n) : "v"(MISC_OFF), "v"(1u) : "memory");
                if (__builtin_amdgcn_readfirstlane(n) == 3u) {
                    if (lane == 0) __hip_atomic_store((gu32*)(ws + WS_HFLAG) + (tb * 16 + nb) * 32, tag, RLX_AGENT);
                    if (DBGON && lane == 0) dbg[BIDX * 64 + 20] = __builtin_amdgcn_s_memrealtime();
                }
            }
            STAMPT(dbg, 12, 0);
            if (wid == 0 && lane < 7) CODE_TOUCH(CODE_LINES - 1 - lane - (idx & 7) * 7);
            __builtin_amdgcn_s_setprio(0);
            WAIT_LGKM0();
            CPIN(2);
            CTILE(wid, 3, cg);
            XLOAD();
            HPOLL_SETUP();
            HWAIT();
            STAMPT(dbg, 8, 0);
            const unsigned vh = (unsigned)(lane * 16);
#pragma unroll
            for (int f = 0; f < DEPTH; ++f) LOADF(f);
            __builtin_amdgcn_s_setprio(PRIO2_G);
        }
    } else {
        const int lw = wid - 4;
        const int d = cb * 4 + lw;
        const char* Krow = (const char*)(ssmk + (size_t)d * SEQ);
        cra = CRA(LA_OFF + lw * 8192); crb = cra ^ 64;
        const unsigned cst0 = lbase + LA_OFF + lw * 8192;
        CT_SETUP();
        asm volatile("" : "+v"(cra), "+v"(crb));
        u32x4 KA[4];
        unsigned draw;
        unsigned tag;
        float gacc = 0.f;
#define KLOAD() asm volatile("global_load_dwordx4 %0, %5, %7 nt\n\tglobal_load_dwordx4 %1, %5, %7 offset:1024 nt\n\tglobal_load_dwordx4 %2, %5, %7 offset:2048 nt\n\t" \
                             "global_load_dwordx4 %3, %5, %7 offset:3072 nt\n\tglobal_load_ushort %4, %6, %8" \
                             : "=&v"(KA[0]), "=&v"(KA[1]), "=&v"(KA[2]), "=&v"(KA[3]), "=&v"(draw) : "v"((unsigned)(lane * 16)), "v"((unsigned)(d * 2)), "s"(Krow), "s"(ssmD) : "memory")
#define KWAIT(n) asm volatile("s_waitcnt vmcnt(%5)" : "+v"(KA[0]), "+v"(KA[1]), "+v"(KA[2]), "+v"(KA[3]), "+v"(draw) : "i"(n) : "memory")
        if (wid < 6) {
            WAIT_VM(16);
            STAMPT(dbg, 17, 320);
            BAR();
            STAGE_A(2, lw0 + RA(2)); STAGE_B(2, lw0 + RB(2));
            unsigned long long ua_ = UNITS_A >> 12;
            unsigned sla = lw0 + RA(3), slb = lw0 + RB(4);
            int ka = ((3 + rot1) & 15) * 128, kb_ = ka;
            const char* sa = (const char*)x + ka;
            asm volatile("" : "+s"(sa), "+s"(sla), "+s"(ka), "+s"(ua_));
#define NEXT_A() do { ka = (ka + 128) & 2047; sa = (const char*)x + ka; ua_ >>= 4; sla = lw0 + (((unsigned)ua_ & 15u) << 14); asm volatile("" : "+s"(sa), "+s"(sla), "+s"(ka), "+s"(ua_)); } while (0)
#define LSTEP(n) do { \
                if ((n) == 24 && t == 1) { WAIT_VM(16); BAR(); STAGE_B(3, lw0 + RB(3)); } else { WAIT_VM(n); BAR(); } \
                GLDS4(sa, voA[0], voA[1], voA[2], voA[3], sla, 0); \
                GLDS4(sa + 16384, voA[0], voA[1], voA[2], voA[3], sla, 4096); \
                kb_ = (kb_ + 128) & 2047; \
                GLDS4((const char*)Bre + kb_, voB[0], voB[1], voB[2], voB[3], slb, 0); \
                NEXT_A(); \
                const unsigned slb0 = slb; \
                slb = (slb == lw0 + 65536) ? lw0 + 16384 : slb + 16384; \
                asm volatile("" : "+s"(slb), "+s"(kb_), "+s"(t)); \
                GLDS4((const char*)Bim + kb_, voB[0], voB[1], voB[2], voB[3], slb0, 4096); } while (0)
#pragma unroll 1
            for (int t = 1; t < 5; ++t) LSTEP(24);
            GLDS4(sa, voA[0], voA[1], voA[2], voA[3], sla, 0); GLDS4(sa + 16384, voA[0], voA[1], voA[2], voA[3], sla, 4096);
            NEXT_A();
#pragma unroll 1
            for (int t = 5; t < 13; ++t) LSTEP(32);
            KLOAD();
            WAIT_VM(37);
            BAR();
            __builtin_amdgcn_s_setprio(0);
            tag = lds_word(smem, TAG_OFF);
            CTILE(lw, 0, cst0 + GA_OFF);
            FLAGD(COK_OFF + lw * 4);
            WAIT_VM(30);
            BAR();
            CTILE(lw + 2, 0, cst0 + GA_OFF + 16384);
            FLAGD(COK_OFF + lw * 4 + 8);
            WAIT_VM(23);
            BAR();
        } else {
            const int vw = wid - 6;
            const int dch = dblk * 128 + vw * 64 + l15;
            const unsigned lk = lbase + ST_OFF + vw * ST_SZ;
            const unsigned vv_ = (unsigned)(((lane >> 4) * 4 + ((lane >> 3) & 1)) * DM * 2 + (lane & 7) * 16);
            const char* vb = (const char*)vin + (size_t)(sblk * 64) * (DM * 2) + dblk * 256 + vw * 128;
            const int grow = nb * 8 + vw * 4 + q4;
            unsigned gx0 = (unsigned)(grow * 128 + (((l15 >> 1) ^ ((grow >> 1) & 7)) * 16) + (l15 & 1) * 8), gx1 = gx0 + RA(2), gwa = (unsigned)(GW_OFF + l15 * 8);
            asm volatile("" : "+v"(gx0), "+v"(gx1), "+v"(gwa));
            u32x2 gxv, gwv;
            typedef const volatile __attribute__((address_space(3))) u32x2* l2p;
#define VBAR(t) do { SCHED(); asm volatile("" ::: "memory"); BAR(); asm volatile("" ::: "memory"); \
                     gxv = *(l2p)(size_t)(RA(t) == 0 ? gx0 : gx1 + (RA(t) - RA(2))); gwv = *(l2p)(size_t)(gwa + (t) * 128); SCHED(); } while (0)
#define GATE() do { SCHED(); gacc = fmaf(lo16(gxv.x), lo16(gwv.x), gacc); gacc = fmaf(hi16(gxv.x), hi16(gwv.x), gacc); \
                    gacc = fmaf(lo16(gxv.y), lo16(gwv.y), gacc); gacc = fmaf(hi16(gxv.y), hi16(gwv.y), gacc); asm volatile("" : "+v"(gacc)); SCHED(); } while (0)
            unsigned ka[4][2], sel[4];
            const int R0 = 4 + (l15 >> 2) * 16 + (l15 & 3) - (q4 >> 1);
            unsigned kr0 = (unsigned)(2 * R0), kr1 = (unsigned)(ST_OFF + vw * ST_SZ + R0 * 128 + (((q4 ^ (R0 >> 1)) & 1) * 16));
            asm volatile("" : "+v"(kr0), "+v"(kr1));
#pragma unroll
            for (int i = 0; i < 4; ++i)
#pragma unroll
                for (int tp = 0; tp < 2; ++tp) {
                    ka[i][tp] = (kr1 ^ (tp * 16)) + ((kr0 + 8 * i - 4 * tp) & 0xe0) + (4 * i - 2 * tp) * 128;
                    asm volatile("" : "+v"(ka[i][tp]));
                }
#pragma unroll
            for (int r = 0; r < 4; ++r) {
                const unsigned pick = (q4 & 2) ? 0x0302u : 0x0100u;
                sel[r] = ((l15 >> 1) == (q4 & 1) * 4 + r) ? ((l15 & 1) ? (pick << 16) | 0x0c0cu : 0x0c0c0000u | pick) : 0x0c0c0c0cu;
            }
            unsigned va_ = (unsigned)(ST_OFF + vw * ST_SZ + ST_V + (q4 * 2 + (l15 >> 3)) * 1056 + ((l15 >> 2) & 1) * 128 + (l15 & 3) * 8);
            unsigned so = (unsigned)(((dch >> 2) * 32 + sblk) * 512 + q4 * 64 + (dch & 3) * 16);
            PIN4(sel[0], sel[1], sel[2], sel[3]); asm volatile("" : "+v"(so), "+v"(va_));
            asm volatile("s_waitcnt vmcnt(9)" : "+v"(tw[0]), "+v"(tw[1]), "+v"(tw[2]), "+v"(tw[3]), "+v"(epoch) :: "memory");
            STAMPT(dbg, 18, 384);
            VBAR(0);
            bf16x8 wf[4][2];
#pragma unroll
            for (int ps = 0; ps < 4; ++ps)
#pragma unroll
                for (int tp = 0; tp < 2; ++tp) {
                    u32x4 w;
#pragma unroll
                    for (int r = 0; r < 4; ++r) w[r] = __builtin_amdgcn_perm(0u, tw[ps][tp], sel[r]);
                    wf[ps][tp] = __builtin_bit_cast(bf16x8, w);
                    asm volatile("" : "+v"(wf[ps][tp]));
                }
            SCHED();
            GATE(); VBAR(1);
            VLD(0); VLD(1); VLD(2); VLD(3); VLD(4); VLD(5); VLD(6); VLD(7);
            f32x4 ks[4][4];
            auto sconv = [&](int i0) __attribute__((always_inline)) {
                bf16x8 kf[2][4][2];
#pragma unroll
                for (int q = 0; q < 16; ++q) { kf[q >> 3][q & 3][(q >> 2) & 1] = RD128(ka[i0 + (q >> 3)][(q >> 2) & 1], (q & 3) * 32); SCHED(); }
                GATE();
                __builtin_amdgcn_s_setprio(PRIO_KV);
                WAIT_LGKM0();
                SCHED();
#pragma unroll
                for (int i = 0; i < 2; ++i) {
#pragma unroll
                    for (int ps = 0; ps < 4; ++ps) MF0(ks[ps][i0 + i], kf[i][ps][0], wf[ps][0]);
#pragma unroll
                    for (int ps = 0; ps < 4; ++ps) MF(ks[ps][i0 + i], kf[i][ps][1], wf[ps][1]);
                }
                __builtin_amdgcn_s_setprio(0);
                SCHED();
            };
            WAIT_VM(8);
            STAMPT(dbg, 21, 384);
            sconv(0); VBAR(2);
            sconv(2); VBAR(3);
            WAIT_VM(0);
            u32x2 vq[4][4];
#pragma unroll
            for (int ps = 0; ps < 4; ++ps)
#pragma unroll
                for (int j = 0; j < 4; ++j) vq[ps][j] = __builtin_bit_cast(u32x2, TRD(va_ + j * 256 + ps * 32));
            const __amdgpu_buffer_rsrc_t rs = __builtin_amdgcn_make_buffer_rsrc(kvT, 0, DM * SEQ * 2, 0x00020000);
            u32x4 o[8];
            auto kvout = [&](int n) __attribute__((always_inline)) {
                const int hh = n >> 2, ps = n & 3;
                asm volatile("" : "+v"(ks[ps][2 * hh]), "+v"(ks[ps][2 * hh + 1]));
#pragma unroll
                for (int e = 0; e < 4; ++e) {
                    const unsigned vv = vq[ps][2 * hh + (e >> 1)][e & 1];
                    o[n][e] = pkbf(ks[ps][2 * hh + (e >> 1)][2 * (e & 1)] * lo16(vv), ks[ps][2 * hh + (e >> 1)][2 * (e & 1) + 1] * hi16(vv));
                }
                asm volatile("" : "+v"(o[n]));
                SCHED();
            };
            GATE(); kvout(0); kvout(1);
            WAIT_LGKM0();

            VBAR(4);
#define KVST(h) do { _Pragma("unroll") for (int n = 0; n < 4; ++n) __builtin_amdgcn_raw_buffer_store_b128(o[4 * (h) + n], rs, so + (unsigned)((h) * 256), n * 65536, WT); \
                     SCHED(); asm volatile("s_nop 1" ::: "memory"); SCHED(); } while (0)
            kvout(2); kvout(3); GATE(); VBAR(5);
            KVST(0);
            kvout(4); kvout(5); GATE(); VBAR(6);
            kvout(6); kvout(7); GATE(); VBAR(7);
            KVST(1);
            GATE(); VBAR(8);
            if (vw == 1 && lane == 0) *(unsigned*)(smem + TAG_OFF) = (epoch + 1u) ? epoch + 1u : 1u;
            GATE();
            WAIT_LGKM0();
            WAIT_VM(0);
            VBAR(9);
            tag = __builtin_amdgcn_readfirstlane(*(const unsigned*)(smem + TAG_OFF));
            if (vw == 0 && lane == 0) __hip_atomic_store((gu32*)(ws + WS_KVFLAG) + (dblk * 32 + sblk) * 32, tag, RLX_AGENT);
            if (vw == 1 && lane < 4) *(u32x4*)(smem + U_OFF + 192 + lane * 16) = (u32x4){0u, 0u, 0u, 0u};
            gu32* kfp = (gu32*)(ws + WS_KVFLAG) + ((cb >> 5) * 32 + vw * 16 + l15) * 32;
            const char* ub = (const char*)kvT + (size_t)__builtin_amdgcn_readfirstlane(cb * 16384 + vw * 8192);
            const unsigned vu = (unsigned)((lane >> 5) * 512 + ((lane >> 2) & 1) * 256 + ((lane >> 3) & 3) * 64 + ((lane - (lane >> 4)) & 3) * 16);
            const unsigned lu = lbase + U_OFF + 256 + vw * 8192;
#define U_DMA1(o) asm volatile("s_add_u32 m0, %2, %3\n\ts_nop 0\n\tglobal_load_lds_dwordx4 %0, %1 sc1\n\tglobal_load_lds_dwordx4 %0, %1 offset:1024 sc1\n\t" \
                             "global_load_lds_dwordx4 %0, %1 offset:2048 sc1\n\tglobal_load_lds_dwordx4 %0, %1 offset:3072 sc1" \
                             :: "v"(vu), "s"(ub + (o)), "s"(lu), "i"(o) : "memory", "scc")
#define U_DMA() do { U_DMA1(0); U_DMA1(4096); FLAGD(UFLAG_OFF + lw * 4); } while (0)
            unsigned fo = lane < 8 ? (unsigned)((tb * 1024 + p_base) * 16 + lane * 128) : lane == 8 ? (unsigned)(WS_HFLAG + (tb * 16 + nb) * 128)
                        : lane == 9 ? (unsigned)(WS_CFLAG + cb * 128) : (unsigned)(WS_GATE + (tb * 16 + nb) * 128);
            asm volatile("" : "+v"(fo));
            GATE(); VBAR(10);
            TOUCH((const char*)(vw ? Cim : Cre) + (size_t)b * 8192, (unsigned)(lane * 128));
            if (vw == 0) TOUCH(ws, fo); else TOUCH(dskip, (unsigned)(d_base * 2));
            {
                const int cl = 64 + lane + ((idx & 15) * 2 + vw) * 7;
                if (lane < 7 && cl < CODE_LINES) CODE_TOUCH(cl);
            }
            GATE(); VBAR(11);
            GATE(); VBAR(12);
            GATE(); VBAR(13);
            unsigned pv;
            asm volatile("global_load_dword %0, %1, off sc1" : "=&v"(pv) : "v"(kfp) : "memory");
            KLOAD();
            GATE(); VBAR(14);
            GATE(); VBAR(15);
            asm volatile("s_waitcnt vmcnt(5)" : "+v"(pv) :: "memory");
            const bool done = __all(pv == tag);
            if (done) U_DMA();
            if (DBGON && TIDX == 384) dbg[BIDX * 64 + 13] = (done ? 100ull : 0ull) | (1ull << 62);
            GATE();
            if (lane == 0) SETW(UFLAG_OFF, vw, 1u);
            WAIT_VM(9);
            if (!done) {
                unsigned spins = 0;
                for (;;) {
                    const unsigned v = __hip_atomic_load(kfp, RLX_AGENT);
                    if (__all(v == tag)) break;
                    if (++spins > SPIN_MAX) break;
                    __builtin_amdgcn_s_sleep(1);
                }
                U_DMA();
            }
        }
        KWAIT(18);
        STAMPT(dbg, 14, 320);
        __builtin_amdgcn_s_sleep(6);
        {
            const unsigned cp = (unsigned)(KC_OFF + lw * CV_W + 64 + lane * 16);
            if (lane == 0) KA[0].x = (KA[0].x & 0xffff0000u) | f2bf(lo16(KA[0].x) + bf2f((u16)draw));
#define WR128(v, off) asm volatile("ds_write_b128 %0, %1 offset:%2" :: "v"(cp), "v"(v), "i"(off) : "memory")
#pragma unroll
            for (int q = 3; q >= 0; --q) WR128(KA[q], q * 1024);
#pragma unroll
            for (int q = 3; q >= 0; --q) {
                const u32x2 p = q ? pred2(KA[q], RL63(KA[q ? q - 1 : 0].z), RL63(KA[q ? q - 1 : 0].w)) : pred2(KA[0], 0u, 0u);
                const u32x4 a = KA[q];
                const u32x4 s1 = {al(a.x, p.y), al(a.y, a.x), al(a.z, a.y), al(a.w, a.z)};
                WR128(s1, q * 1024 + CV_COPY);
                const u32x4 s2 = {p.y, mv(a.x), mv(a.y), mv(a.z)};
                WR128(s2, q * 1024 + 2 * CV_COPY);
                const u32x4 s3 = {al(p.y, p.x), mv(s1.x), mv(s1.y), mv(s1.z)};
                WR128(s3, q * 1024 + 3 * CV_COPY);
            }
            if (lane < 16) *(u32x4*)(smem + KC_OFF + lw * CV_W + (lane >> 2) * CV_COPY + (lane & 3) * 16) = (u32x4){0u, 0u, 0u, 0u};
            SCHED();
        }
        STAMPT(dbg, 26, 320);
        wait_lds4(smem, G1D_OFF);
        wait_lds4(smem, UFLAG_OFF);
        STAMPT(dbg, 9, 256);
        __builtin_amdgcn_s_setprio(PRIO_CV);
        STAMPT(dbg, 10, 384);
        f32x4 cv[8];
#pragma unroll
        for (int g = 0; g < 8; ++g) cv[g] = (f32x4){0.f, 0.f, 0.f, 0.f};
        PIN4(cv[0], cv[1], cv[2], cv[3]); PIN4(cv[4], cv[5], cv[6], cv[7]);
        const int uz = U_OFF + 192;
        const int ua0 = U_OFF + 256 + (2 * l15 + q4 - 2) * 64, rv0 = (2 * l15 + q4 - 2) * 4 + lw * 16;
        const unsigned ka0 = (unsigned)((l15 >> 2) * CV_COPY - 16 * q4 + 8 * (l15 & 3) - 8);
        int ua, rv, pu, pz;
        unsigned adr;
#define UNEXT1() do { pu = ua | (rv & 0x30); asm volatile("" : "+v"(pu)); } while (0)
#define UNEXT2() do { pz = (ua > uz ? ua : uz) | (rv & 0x30); asm volatile("" : "+v"(pz)); } while (0)
#define UNEXT3() do { ua -= 256; rv -= 16; asm volatile("" : "+v"(ua), "+v"(rv)); } while (0)
#define ANEXT() do { adr += 64; asm volatile("" : "+v"(adr)); } while (0)
#define RDU(dst, c) do { dst = (c) ? RD128(pu, (c) * 2048) : RD128(pz, 0); SCHED(); } while (0)
        HPOLL_SETUP();
        unsigned hpv = 0;
        {
            bf16x8 A[8], UB[8];
            ua = ua0; rv = rv0; adr = lbase + KC_OFF + lw * CV_W + ka0 + 96;
            asm volatile("" : "+v"(adr));
            UNEXT1(); UNEXT2(); UNEXT3();
#pragma unroll
            for (int i = 0; i < 8; ++i) { RDA(A[7 - i], adr, 7 - i); RDU(UB[i], i); }
            unsigned gb = lw >= 2 ? (unsigned)((tb * 16 + nb) * 128 + (lw - 2) * 32) : 0x80000000u;
            asm volatile("" : "+s"(gb));
            const unsigned gso = (l15 == 0) ? gb + (unsigned)(q4 * 8) : 0x80000000u;
            float gt = 0.f;
            unsigned vxo = 0, vdo = 0, vgo = 0;
            auto gate_out = [&](int i) __attribute__((always_inline)) {
                if (i == 0) gacc += __uint_as_float(__builtin_amdgcn_update_dpp(0u, __float_as_uint(gacc), 0xb1, 0xf, 0xf, false));
                if (i == 1) gacc += __uint_as_float(__builtin_amdgcn_update_dpp(0u, __float_as_uint(gacc), 0x4e, 0xf, 0xf, false));
                if (i == 2) gacc += __uint_as_float(__builtin_amdgcn_update_dpp(0u, __float_as_uint(gacc), 0x141, 0xf, 0xf, false));
                if (i == 3) gacc += __uint_as_float(__builtin_amdgcn_update_dpp(0u, __float_as_uint(gacc), 0x140, 0xf, 0xf, false));
                if (i == 4) gacc = rbf(gacc);
                if (i == 5) gacc = __builtin_amdgcn_exp2f(gacc * -1.44269504088896f);
                if (i == 6) gacc = __builtin_amdgcn_rcpf(1.0f + gacc);
                if (i == 7) gacc = rbf(gacc);
                if (i == 8) gt = __uint_as_float(pkbf(gacc, 1.0f - gacc));
                if (i == 9) __builtin_amdgcn_raw_buffer_store_b64((u32x2){__float_as_uint(gt), tag}, gs, gso, 0, WT);
                asm volatile("" : "+v"(gacc), "+v"(gt));
            };
            auto step = [&](auto E) __attribute__((always_inline)) {
                constexpr int e = decltype(E)::value;
                constexpr int ti = (e == CS0) ? 0 : (e == CS1) ? 1 : (e == CS2) ? 2 : -1, tj = ti < 0 ? 0 : ti;
                constexpr unsigned char GA[36] = {0x66, 0x77, 0x76, 0x44, 0x55, 0x54, 0x65, 0x64, 0x75, 0x74, 0x22, 0x33, 0x32, 0x43, 0x42, 0x53, 0x52, 0x63, 0x62, 0x73, 0x72,
                                                  0x00, 0x11, 0x10, 0x21, 0x20, 0x31, 0x30, 0x41, 0x40, 0x51, 0x50, 0x61, 0x60, 0x71, 0x70};
                const char* sb_ = (const char*)Cre + COFF(wid, tj + 1);
                const char* sc_ = (const char*)Cim + COFF(wid, tj + 1);
#pragma unroll
                for (int q = 0; q < 36; ++q) {
                    const int g = GA[q] >> 4, a = GA[q] & 15;
                    MF(cv[g], A[a], UB[g - a]); SCHED();
                    if (e < 7) {
                        if (a == 1) RDU(UB[g - 1], g - 1);
                        if (q == 35) RDU(UB[7], 7);
                        if (g == 7) RDA(A[a], adr + 64 * (e + 1), a);
                        if (q == 11) UNEXT1();
                        if (q == 13) UNEXT2();
                        if (q == 15) UNEXT3();
                    }
                    if (ti >= 0) {
                        const int n = (q >= 3 && q <= 7) ? q - 3 : q == 10 ? 5 : q == 12 ? 6 : q == 14 ? 7 : -1;
                        if (q == 3) { if (ti == 0) WAIT_VM(1); else if (e == XE + 1) WAIT_VM(7); else WAIT_VM(0); }
                        if (n >= 0) CRD(tj, n & 7);
                        if (q == 18) CPIN(tj);
                        if (q == 18 || q == 23) asm volatile("s_add_u32 m0, %3, %4\n\ts_nop 0\n\tglobal_load_lds_dwordx4 %0, %2\n\tglobal_load_lds_dwordx4 %1, %2 offset:1024"
                                                             :: "v"(vB0), "v"(vB1), "s"(q == 18 ? sb_ : sc_), "s"(cst0), "i"(q == 18 ? 0 : 4096) : "memory", "scc");
                        if (q == 21 || q == 25) asm volatile("global_load_lds_dwordx4 %0, %2 offset:2048\n\tglobal_load_lds_dwordx4 %1, %2 offset:3072"
                                                             :: "v"(vB0), "v"(vB1), "s"(q == 21 ? sb_ : sc_) : "memory");
                    }
                    if (e == 0 && q >= 3 && q <= 7) gate_out(q - 3);
                    if (e == 0 && q >= 10 && q <= 18 && !(q & 1)) gate_out(5 + (q - 10) / 2);
                    if (e == 0 && q == 4) CTILE(wid, 0, cst0);
                    if (e == XE && q >= 23 && q <= 35 && (q & 1)) {
                        const int i = (q - 23) / 2;
                        const __amdgpu_buffer_rsrc_t xs = __builtin_amdgcn_make_buffer_rsrc((void*)x, 0, SEQ * DM * 2, 0x00020000);
                        const __amdgpu_buffer_rsrc_t ks = __builtin_amdgcn_make_buffer_rsrc((void*)dskip, 0, DM * 2, 0x00020000);
                        if (i < 4) asm volatile("buffer_load_dwordx2 %0, %1, %2, %3 offen offset:%4" : "=&v"(xx[(i >> 1) & 1][i & 1]) : "v"(vxo), "s"(xs), "s"((TR((i >> 1) & 1) * DM + d_base) * 2), "i"((i & 1) * 2048) : "memory");
                        if (i == 4) asm volatile("buffer_load_dwordx2 %0, %1, %2, %3 offen" : "=&v"(dk2) : "v"(vdo), "s"(ks), "s"(d_base * 2) : "memory");
                        if (i >= 5) asm volatile("buffer_load_dwordx4 %0, %1, %2, %3 offen sc1" : "=&v"(gv[(i - 5) & 1]) : "v"(vgo), "s"(gs), "s"(GSO((i - 5) & 1)) : "memory");
                    }
                    if (e == XE && q == 16) { vxo = (unsigned)(q4 * 32 * DM * 2 + l15 * 8); vdo = (unsigned)(l15 * 8); asm volatile("" : "+v"(vxo), "+v"(vdo)); }
                    if (e == XE && q == 17) { vgo = GV(q4); asm volatile("" : "+v"(vgo)); }
                    if (e == 6 && q == 30) hpv = __hip_atomic_load(hfp, RLX_AGENT);
                    SCHED();
                }
            };
            unroll<8>(step);
            WAIT_VM(0);
            XPIN();
        }
        PAD8(cv[0], cv[1], cv[2], cv[3]); PIN4(cv[4], cv[5], cv[6], cv[7]);
        STAMPT(dbg, 11, 384);
        const unsigned vh = (unsigned)((lane ^ 2) * 16);
        if (!__all(hpv == tag)) HWAIT();
        asm volatile("" ::: "memory");
#pragma unroll
        for (int f = 0; f < DEPTH; ++f) LOADF(f);
        STAMPT(dbg, 25, 384);
        {
            const __amdgpu_buffer_rsrc_t yrs = __builtin_amdgcn_make_buffer_rsrc(y2T, 0, DM * SEQ * 2, 0x00020000);
            const unsigned yo = (unsigned)(d * 4096 + (q4 & 1) * 512 + (l15 >> 3) * 256 + (q4 >> 1) * 128 + (l15 & 7) * 16);
#pragma unroll
            for (int g = 0; g < 8; g += 2) {
                const auto r0 = __builtin_amdgcn_permlane16_swap(pkbf(cv[g][0], cv[g][1]), pkbf(cv[g + 1][0], cv[g + 1][1]), false, false);
                const auto r1 = __builtin_amdgcn_permlane16_swap(pkbf(cv[g][2], cv[g][3]), pkbf(cv[g + 1][2], cv[g + 1][3]), false, false);
                __builtin_amdgcn_raw_buffer_store_b128((u32x4){r0[0], r1[0], r0[1], r1[1]}, yrs, yo + (unsigned)(g * 512), 0, WT);
            }
        }
        asm volatile("" ::: "memory");
        STAMPT(dbg, 15, 384);
        __builtin_amdgcn_s_setprio(PRIO2_L);
    }
    {
        const int lane = fresh_lane(), l15 = lane & 15, q4 = lane >> 4;
        const int ring = wid >> 2;
        const unsigned va = (unsigned)((lane ^ (ring * 2)) * 16), vh = va;
        const unsigned tag = lds_word(smem, TAG_OFF);
        const int cb0 = ring ? KC_OFF : GA_OFF, cst = ring ? CV_W : 8192;
        f32x4 ac[2][4];
        unsigned ca[2], fa[2], fl = 0, fv = 0;
#define CHAIN(u) do { const int c_ = (wid - ((u) >> 1)) & 3; ca[(u) & 1] = va + (unsigned)(cb0 + c_ * cst + ((u) & 1) * 4096); \
                      fa[(u) & 1] = (unsigned)(RF_OFF + ring * 32 + ((u) & 1) * 16 + c_ * 4); asm volatile("" : "+v"(ca[(u) & 1]), "+v"(fa[(u) & 1])); } while (0)
#define RDIN(dst, a, f_) asm volatile("ds_read_b32 %0, %5\n\tds_read_b128 %1, %6\n\tds_read_b128 %2, %6 offset:1024\n\tds_read_b128 %3, %6 offset:2048\n\tds_read_b128 %4, %6 offset:3072" \
                     : "=&v"(fl), "=&v"(dst[0]), "=&v"(dst[1]), "=&v"(dst[2]), "=&v"(dst[3]) : "v"(f_), "v"(a) : "memory")
#define LWAIT(dst) asm volatile("s_waitcnt lgkmcnt(0)" : "+v"(fl), "+v"(dst[0]), "+v"(dst[1]), "+v"(dst[2]), "+v"(dst[3]) :: "memory")
        CHAIN(0); CHAIN(1);
        WAIT_LGKM0();
        CPIN(0); CPIN(1); CPIN(2);
        CREAD(3);
        int tr[2] = {TR(0), TR(1)};
        unsigned gw[2][2];
        {
            unsigned spins = 0;
            while (__builtin_expect(!__all((gv[0].y == tag) & (gv[0].w == tag) & (gv[1].y == tag) & (gv[1].w == tag)), 0)) {
                if (++spins > SPIN_MAX) break;
#pragma unroll
                for (int r = 0; r < 2; ++r) asm volatile("s_nop 4\n\tbuffer_load_dwordx4 %0, %1, %2, %3 offen sc1\n\ts_waitcnt vmcnt(0)" : "=&v"(gv[r]) : "v"(GV(q4)), "s"(gs), "s"(__builtin_amdgcn_readfirstlane(GSO(r))) : "memory");
            }
            gw[0][0] = gv[0].x; gw[0][1] = gv[0].z; gw[1][0] = gv[1].x; gw[1][1] = gv[1].z;
            PIN4(gw[0][0], gw[0][1], gw[1][0], gw[1][1]);
        }
        unsigned cpv = 0, yy[2][4];
        f32x2 zz[2][4];
        unsigned exo = 0, exi = 0, efo = 0, efi = 0;
        auto send = [&](int r) __attribute__((always_inline)) {
            const unsigned one = 1u;
#pragma unroll
            for (int n = 0; n < 4; ++n) { const f32x2 v = {ac[r][n][2], ac[r][n][3]}; asm volatile("ds_write_b64 %0, %1 offset:%2" :: "v"(exo), "v"(v), "i"(r * 4096 + n * 512) : "memory"); }
            asm volatile("ds_write_b32 %0, %1 offset:%2" :: "v"(efo), "v"(one), "i"(r * 4) : "memory");
        };
        STAMPT(dbg, 5, 0);
        auto body = [&](auto F) __attribute__((always_inline)) {
            constexpr int f = decltype(F)::value;
            constexpr int u = f >> 3, k = f & 7;
            f32x4* an = ac[u & 1];
            f32x4* ao = ac[(u + 1) & 1];
#pragma unroll
            for (int n = 0; n < 4; ++n) {
                if (k == 0 && u < 2) MF0(an[n], af[f % DEPTH], cf_[k][n]); else MF(an[n], af[f % DEPTH], cf_[k][n]);
                SCHED();
                const int g = k * 4 + n;
                if (u >= 1 && u < 7) {
                    if (g == 1) { fv = ((u - 1) >> 1) + 1; asm volatile("" : "+v"(fv)); }
                    if (g == 4 || g == 6 || g == 8 || g == 10) asm volatile("ds_write_b128 %0, %1 offset:%2" :: "v"(ca[(u + 1) & 1]), "v"(ao[(g - 4) / 2]), "i"((g - 4) * 512) : "memory");
                    if (g == 12) asm volatile("ds_write_b32 %0, %1" :: "v"(fa[(u + 1) & 1]), "v"(fv) : "memory");
                    if (g == 18) CHAIN(u + 1);
                    if (g == 20) asm volatile("ds_read_b32 %0, %1" : "=&v"(fl) : "v"(fa[(u + 1) & 1]) : "memory");
                    if (g > 20 && g <= 24) asm volatile("ds_read_b128 %0, %1 offset:%2" : "=&v"(ao[g - 21]) : "v"(ca[(u + 1) & 1]), "i"((g - 21) * 1024) : "memory");
                    if (g == 30) {
                        LWAIT(ao);
                        if (__builtin_expect(__builtin_amdgcn_readfirstlane(fl) != (unsigned)((u + 1) >> 1), 0)) {
                            unsigned spins = 0;
#pragma unroll 1
                            do {
                                __builtin_amdgcn_s_sleep(1);
                                RDIN(ao, ca[(u + 1) & 1], fa[(u + 1) & 1]); LWAIT(ao);
                                if (++spins > SPIN_MAX * 16u) break;
                            } while (__builtin_amdgcn_readfirstlane(fl) != (unsigned)((u + 1) >> 1));
                        }
                    }
                }
                if (u == 7 && g == 6) send(0);
                if (f >= ZF && f < ZF + 4) {
                    constexpr int r = ((f - ZF) >> 1) & 1, j = (f - ZF) & 1;
                    const unsigned xw = xx[r][j][n >> 1], dw = dk2[n >> 1];
                    const float xf = (n & 1) ? hi16(xw) : lo16(xw), df = (n & 1) ? hi16(dw) : lo16(dw), yf = j ? hi16(yy[r][n]) : lo16(yy[r][n]);
                    zz[r][n][j] = xf * fmaf(hi16(gw[r][j]), yf, lo16(gw[r][j]) * df);
                    asm volatile("" : "+v"(zz[r][n]));
                }
                if (f == ZF + 4) {
                    zz[0][n] = (f32x2){fmaf(lo16(gw[0][0]), ac[0][n][0], zz[0][n][0]), fmaf(lo16(gw[0][1]), ac[0][n][1], zz[0][n][1])};
                    asm volatile("" : "+v"(zz[0][n]));
                }
                SCHED();
            }
            if (f + DEPTH < 64) LOADF(f + DEPTH);
            if (f == DEPTH + 1 && ring) {
                WAIT_VM(6);
                if (lane == 0) __hip_atomic_store((gu32*)(ws + WS_CFLAG) + cb * 32 + (wid & 3), tag, RLX_AGENT);
                SCHED();
            }
            if (f == YPOLL && ring) { const int l15 = fresh_lane() & 15; cpv = __hip_atomic_load((gu32*)(ws + WS_CFLAG) + (nb * 16 + (wid & 3) * 4 + (l15 >> 2)) * 32 + (l15 & 3), RLX_AGENT); SCHED(); }
            if (f == YPOLL + DEPTH && ring) {
                const int ln = fresh_lane(), l15 = ln & 15, q4 = ln >> 4;
                if (__builtin_expect(!__all(cpv == tag), 0)) {
                    unsigned spins = 0;
                    while (!__all(__hip_atomic_load((gu32*)(ws + WS_CFLAG) + (nb * 16 + (wid & 3) * 4 + (l15 >> 2)) * 32 + (l15 & 3), RLX_AGENT) == tag)) {
                        if (++spins > SPIN_MAX) break;
                        __builtin_amdgcn_s_sleep(1);
                    }
                }
                const unsigned lx = lbase + LA_OFF + (wid & 3) * 8192;
#pragma unroll
                for (int i = 0; i < 4; ++i) {
                    const unsigned vo = (unsigned)((d_base + (wid & 3) * 16 + i * 4 + q4) * 4096 + tb * 256 + ((l15 ^ ((wid & 3) * 4 + i)) * 16));
                    GLDS_SC1(y2T, vo, lx, i * 1024);
                }
                SCHED();
            }
            if (f == YPOLL + 2 * DEPTH + 1 && ring) {
                WAIT_VM(6);
                SETW(YFLAG_OFF, wid & 3, 1u);
                SCHED();
            }
            if (f == 55) {
                const int ln = fresh_lane(), c_ = (wid - 3) & 3;
                const int eg = GA_OFF + c_ * 8192, el = KC_OFF + c_ * CV_W;
                exo = (unsigned)((ring ? el : eg) + ln * 8);
                exi = (unsigned)((ring ? eg : el) + ln * 8);
                efo = (unsigned)(EF_OFF + wid * 8); efi = (unsigned)(EF_OFF + (wid ^ 4) * 8);
                PIN4(exo, exi, efo, efi);
                SCHED();
            }
            if (f == ZF - 2) {
                const int ln = fresh_lane(), l15 = ln & 15, q4 = ln >> 4;
                wait_lds4(smem, YFLAG_OFF);
#pragma unroll
                for (int r = 0; r < 2; ++r) {
                    const int jb = JB(6 + r);
                    const int ya = LA_OFF + (l15 >> 2) * 8192 + (l15 & 3) * 1024 + (((((jb >> 1) & 1) * 8 + (jb >> 2) + q4 * 2) ^ l15) * 16) + (jb & 1) * 8 + ring * 4;
#pragma unroll
                    for (int e = 0; e < 4; ++e) yy[r][e] = *(const unsigned*)(smem + ya + e * 256);
                }
                SCHED();
            }
        };
        unroll<64>(body);
        PAD8(ac[1][0], ac[1][1], ac[1][2], ac[1][3]);
        send(1);
        STAMPT(dbg, 6, 0);
        const __amdgpu_buffer_rsrc_t ors = __builtin_amdgcn_make_buffer_rsrc(out, 0, SEQ * DM * 4, 0x00020000);
        const unsigned ov = (unsigned)((lane >> 4) * (32 * DM * 4) + (lane & 15) * 16);
#pragma unroll
        for (int r = 0; r < 2; ++r) {
            f32x2 in[4];
#define EXRD() asm volatile("ds_read_b32 %0, %5 offset:%7\n\tds_read_b64 %1, %6 offset:%8\n\tds_read_b64 %2, %6 offset:%9\n\tds_read_b64 %3, %6 offset:%10\n\tds_read_b64 %4, %6 offset:%11" \
                             : "=&v"(fl), "=&v"(in[0]), "=&v"(in[1]), "=&v"(in[2]), "=&v"(in[3]) \
                             : "v"(efi), "v"(exi), "i"(r * 4), "i"(r * 4096), "i"(r * 4096 + 512), "i"(r * 4096 + 1024), "i"(r * 4096 + 1536) : "memory")
            EXRD();
            if (r == 0) {
#pragma unroll
                for (int n = 0; n < 4; ++n) {
                    zz[1][n] = (f32x2){fmaf(lo16(gw[1][0]), ac[1][n][0], zz[1][n][0]), fmaf(lo16(gw[1][1]), ac[1][n][1], zz[1][n][1])};
                    asm volatile("" : "+v"(zz[1][n]));
                }
            }
            LWAIT(in);
            if (__builtin_expect(__builtin_amdgcn_readfirstlane(fl) == 0u, 0)) {
                unsigned spins = 0;
#pragma unroll 1
                do {
                    EXRD(); LWAIT(in);
                    if (++spins > SPIN_MAX * 16u) break;
                } while (__builtin_amdgcn_readfirstlane(fl) == 0u);
            }
            f32x4 o[2];
#pragma unroll
            for (int j = 0; j < 2; ++j)
#pragma unroll
                for (int e = 0; e < 4; ++e) o[j][e] = fmaf(lo16(gw[r][j]), in[e][j], zz[r][e][j]);
#pragma unroll
            for (int j = 0; j < 2; ++j)
                __builtin_amdgcn_raw_buffer_store_b128(__builtin_bit_cast(u32x4, o[j]), ors, ov + (unsigned)(((tr[r] + j) * DM + d_base) * 4), 0, WT);
        }
        STAMPT(dbg, 7, 0);
        STAMPT(dbg, 16, 384);
        if (tb == 15 && nb == 0 && wid == 0 && lane == 0) __hip_atomic_store((gu32*)(ws + WS_EPOCH), tag, RLX_AGENT);
    }
}

extern "C" void kernel_launch(void* const* d_in, const int* in_sizes, int n_in,
                              void* d_out, int out_size, void* d_ws, size_t ws_size,
                              hipStream_t stream) {
    const u16* x = (const u16*)d_in[0];
    const u16* lre = (const u16*)d_in[1];
    const u16* lim = (const u16*)d_in[2];
    const u16* Bre = (const u16*)d_in[3];
    const u16* Bim = (const u16*)d_in[4];
    const u16* Cre = (const u16*)d_in[5];
    const u16* Cim = (const u16*)d_in[6];
    const u16* logdt = (const u16*)d_in[7];
    const u16* dskip = (const u16*)d_in[8];
    const u16* kin = (const u16*)d_in[10];
    const u16* vin = (const u16*)d_in[11];
    const u16* shiftk = (const u16*)d_in[12];
    const u16* ssmk = (const u16*)d_in[13];
    const u16* ssmD = (const u16*)d_in[14];
    const u16* gatew = (const u16*)d_in[15];
    float* out = (float*)d_out;

    char* ws = (char*)d_ws;

    hipFuncSetAttribute((const void*)k1_kernel, hipFuncAttributeMaxDynamicSharedMemorySize, K1_LDS);

    k1_kernel<<<256, 512, K1_LDS, stream>>>(x, Bre, Bim, kin, vin, shiftk, gatew, ws, lre, lim, logdt, ssmk, ssmD, Cre, Cim, dskip, out);
}
#if defined(__HIP_DEVICE_COMPILE__)
#pragma clang attribute pop
#endif
```

```cpp
#define PROBE 0
#if defined(__HIP_DEVICE_COMPILE__)
#pragma clang attribute push(__attribute__((target("no-packed-fp32-ops"))), apply_to = function)
#endif
#include <hip/hip_runtime.h>
#include <hip/hip_bf16.h>
#include <stdint.h>

#define TIDX ((int)__builtin_amdgcn_workitem_id_x())
#define BIDX ((int)__builtin_amdgcn_workgroup_id_x())
typedef unsigned short u16;
typedef __attribute__((ext_vector_type(8))) short bf16x8;
typedef __attribute__((ext_vector_type(4))) float f32x4;
typedef __attribute__((ext_vector_type(2))) float f32x2;
typedef __attribute__((ext_vector_type(2))) __bf16 bf16x2;
typedef __attribute__((ext_vector_type(4))) unsigned u32x4;
typedef __attribute__((ext_vector_type(2))) unsigned u32x2;
typedef __attribute__((address_space(1))) unsigned long long gu64;
typedef __attribute__((address_space(1))) unsigned gu32;
typedef __attribute__((address_space(3))) void lds_void;
typedef __attribute__((address_space(3))) unsigned lds_u32;
typedef __attribute__((ext_vector_type(4))) short s16x4;
typedef __attribute__((address_space(3))) s16x4 lds_s16x4;
#define TRD(a) __builtin_amdgcn_ds_read_tr16_b64_v4i16((lds_s16x4*)(size_t)(a))

#define DBGON 0
#define STAMPT(buf, k, t) do { } while (0)
#define CODE_LINES 262
#define SEQ 2048
#define DM 1024
#define SPIN_MAX (1u << 18)
#define PRIO_G 2
#define PSLEEP 4
#define PRIO_L 3
#define WT 16

__device__ __forceinline__ float bf2f(u16 v) { return __uint_as_float(((unsigned)v) << 16); }
__device__ __forceinline__ u16 f2bf(float f) { __bf16 b = (__bf16)f; return __builtin_bit_cast(u16, b); }
__device__ __forceinline__ float rbf(float f) { return bf2f(f2bf(f)); }
__device__ __forceinline__ unsigned pkbf(float lo, float hi) { return __builtin_bit_cast(unsigned, __builtin_convertvector((f32x2){lo, hi}, bf16x2)); }
__device__ __forceinline__ float lo16(unsigned u) { return __uint_as_float(u << 16); }
__device__ __forceinline__ float hi16(unsigned u) { return __uint_as_float(u & 0xffff0000u); }

struct cf { float r, i; };
__device__ __forceinline__ cf cmul(cf a, cf b) { return {a.r * b.r - a.i * b.i, a.r * b.i + a.i * b.r}; }
__device__ __forceinline__ cf cfma(cf a, cf b, cf c) {
    return {fmaf(a.r, b.r, fmaf(-a.i, b.i, c.r)), fmaf(a.r, b.i, fmaf(a.i, b.r, c.i))};
}
__device__ __forceinline__ f32x4 splat4(float v) { return (f32x4){v, v, v, v}; }
__device__ __forceinline__ f32x4 fma4(f32x4 a, f32x4 b, f32x4 c) { return __builtin_elementwise_fma(a, b, c); }
__device__ __forceinline__ f32x4 mul4(f32x4 a, f32x4 b) { return a * b; }

__device__ __forceinline__ float red32(float a, float b) {
    const auto r = __builtin_amdgcn_permlane32_swap(__float_as_uint(a), __float_as_uint(b), false, false);
    return __uint_as_float(r[0]) + __uint_as_float(r[1]);
}
__device__ __forceinline__ float red16(float x) {
    const auto r = __builtin_amdgcn_permlane16_swap(__float_as_uint(x), __float_as_uint(x), false, false);
    return __uint_as_float(r[0]) + __uint_as_float(r[1]);
}
__device__ __forceinline__ int fresh_lane() {
    int l;
    asm volatile("v_mbcnt_lo_u32_b32 %0, -1, 0\n\tv_mbcnt_hi_u32_b32 %0, -1, %0" : "=v"(l));
    return l;
}
#define BAR() __builtin_amdgcn_s_barrier()
#define WAIT_VM(n) asm volatile("s_waitcnt vmcnt(" #n ")" ::: "memory")
#define WAIT_LGKM0() __builtin_amdgcn_s_waitcnt(0xC07F)
#define SCHED() __builtin_amdgcn_sched_barrier(0)
#define MF(c, a, b) asm volatile("v_mfma_f32_16x16x32_bf16 %0, %1, %2, %0" : "+v"(c) : "v"(a), "v"(b))
#define PAD8(c0, c1, c2, c3) asm volatile("s_nop 7" : "+v"(c0), "+v"(c1), "+v"(c2), "+v"(c3))
#define PIN4(c0, c1, c2, c3) asm volatile("" : "+v"(c0), "+v"(c1), "+v"(c2), "+v"(c3))
#define GLDS_S(sbase, voff, sl, IMM) \
    asm volatile("s_add_u32 m0, %2, %3\n\ts_nop 0\n\tglobal_load_lds_dwordx4 %0, %1" \
                 :: "v"(voff), "s"(sbase), "s"(sl), "i"(IMM) : "memory", "scc")
#define GLDS_K(sbase, voff, sl, IMM) \
    asm volatile("s_add_u32 m0, %2, %3\n\ts_nop 0\n\tglobal_load_lds_dwordx4 %0, %1 nt" \
                 :: "v"(voff), "s"(sbase), "s"(sl), "i"(IMM) : "memory", "scc")
#define RLX_AGENT __ATOMIC_RELAXED, __HIP_MEMORY_SCOPE_AGENT

#define UNITS_A 0x0756075607560560ull
#define UNITS_B 0x1432143214321432ull
#define RA(t) ((int)((UNITS_A >> (4 * (t))) & 15) * 16384)
#define RB(t) ((int)((UNITS_B >> (4 * (t))) & 15) * 16384)
#define LA_OFF 0
#define GA_OFF 32768
#define CV_COPY 4160
#define CV_W (4 * CV_COPY)
#define KC_OFF 65536
#define ST_OFF 114688
#define ST_V 8832
#define ST_SZ (ST_V + 8 * 1056)
#define U_OFF (KC_OFF + 16 * CV_COPY)
#define HT_OFF (U_OFF + 256 + 16384)
#define GW_OFF (HT_OFF + 8192)
#define TAG_OFF (GW_OFF + 2048)
#define UFLAG_OFF (TAG_OFF + 16)
#define G1D_OFF (UFLAG_OFF + 16)
#define MISC_OFF (G1D_OFF + 16)
#define YFLAG_OFF (MISC_OFF + 16)
#define RF_OFF (YFLAG_OFF + 16)
#define EF_OFF (RF_OFF + 64)
#define COK_OFF (EF_OFF + 64)
#define NFLAGW 52
#define DUMMY_OFF (COK_OFF + 16)
#define K1_LDS (DUMMY_OFF + 256)
#define WS_GRAN 0
#define WS_KVFLAG (256u << 10)
#define WS_EPOCH (WS_KVFLAG + 65536)
#define WS_CFLAG (WS_EPOCH + 4096)
#define WS_HFLAG (384u << 10)
#define WS_GATE (512u << 10)

#define CODE_TOUCH(vl) asm volatile("s_getpc_b64 s[98:99]\n\ts_add_u32 s98, s98, k1_kernel@rel32@lo+4\n\ts_addc_u32 s99, s99, k1_kernel@rel32@hi+12\n\t" \
                                    "s_mov_b32 m0, %1\n\ts_nop 0\n\tglobal_load_lds_dword %0, s[98:99]" :: "v"((unsigned)(vl) * 128u), "i"(DUMMY_OFF) : "s98", "s99", "scc", "memory")
#define RD128(a, off) (*(const __attribute__((address_space(3))) bf16x8*)(size_t)(unsigned)((a) + (off)))
#define RDA(dst, A, F) do { const s16x4 lo_ = TRD((A) + 512 * (F) + 8); SCHED(); const s16x4 hi_ = TRD((A) + 512 * (F)); SCHED(); \
                            dst = __builtin_shufflevector(lo_, hi_, 0, 1, 2, 3, 4, 5, 6, 7); } while (0)
#define TOUCH(sbase, voff) asm volatile("s_mov_b32 m0, %2\n\ts_nop 0\n\tglobal_load_lds_dword %0, %1" :: "v"(voff), "s"(sbase), "i"(DUMMY_OFF) : "memory")
#define FLAGD(off) do { unsigned long long pc_; asm volatile("s_getpc_b64 %0\n\ts_mov_b32 m0, %2\n\ts_mov_b64 exec, 1\n\tglobal_load_lds_dword %1, %0\n\ts_mov_b64 exec, -1" \
                                                             : "=&s"(pc_) : "v"(0u), "s"(__builtin_amdgcn_readfirstlane(off)) : "memory"); } while (0)
#define GLDS_SC1(sbase, voff, sl, IMM) \
    asm volatile("s_add_u32 m0, %2, %3\n\ts_nop 0\n\tglobal_load_lds_dwordx4 %0, %1 sc1" \
                 :: "v"(voff), "s"(sbase), "s"(sl), "i"(IMM) : "memory", "scc")
__device__ __forceinline__ unsigned mv(unsigned v) { unsigned r; asm("v_mov_b32 %0, %1" : "=v"(r) : "v"(v)); return r; }
__device__ __forceinline__ unsigned al(unsigned hi, unsigned lo) { unsigned r; asm volatile("v_alignbit_b32 %0, %1, %2, 16" : "=v"(r) : "v"(hi), "v"(lo)); return r; }
__device__ __forceinline__ u32x2 pred2(const u32x4& a, unsigned f0, unsigned f1) {
    return (u32x2){(unsigned)__builtin_amdgcn_update_dpp((int)f0, (int)a.z, 0x138, 0xf, 0xf, false), (unsigned)__builtin_amdgcn_update_dpp((int)f1, (int)a.w, 0x138, 0xf, 0xf, false)};
}
#define RL63(v) ((unsigned)__builtin_amdgcn_readlane((int)(v), 63))
__device__ __forceinline__ void wait_lds4(char* smem, int off) {
    const unsigned a = (unsigned)(size_t)(lds_void*)smem + off;
    unsigned spins = 0;
    for (;;) {
        u32x4 f;
        asm volatile("ds_read_b128 %0, %1\n\ts_waitcnt lgkmcnt(0)" : "=&v"(f) : "v"(a) : "memory");
        if (__builtin_amdgcn_readfirstlane((f.x != 0u) & (f.y != 0u) & (f.z != 0u) & (f.w != 0u))) break;
        if (++spins > SPIN_MAX * 16u) break;
        __builtin_amdgcn_s_sleep(1);
    }
}
__device__ __forceinline__ unsigned lds_word(char* smem, int off) {
    const unsigned a = (unsigned)(size_t)(lds_void*)smem + off;
    unsigned f;
    asm volatile("ds_read_b32 %0, %1\n\ts_waitcnt lgkmcnt(0)" : "=&v"(f) : "v"(a) : "memory");
    return __builtin_amdgcn_readfirstlane(f);
}
__device__ __forceinline__ void wait_lds1(char* smem, int off) {
    unsigned spins = 0;
    while (lds_word(smem, off) == 0u) {
        if (++spins > SPIN_MAX * 16u) break;
        __builtin_amdgcn_s_sleep(1);
    }
}
#define SETW(off, i, v) __hip_atomic_store((lds_u32*)(smem + (off)) + (i), (v), __ATOMIC_RELAXED, __HIP_MEMORY_SCOPE_WORKGROUP)

#define PRIO_CV 1
#define PRIO_KV 3
#define CS0 1
#define CS1 3
#define CS2 5
#define XE 4
#define YPOLL 32
#define ZF 59
template <int I> struct IC { static constexpr int value = I; };
template <int N, int I = 0, class F> __device__ __forceinline__ void unroll(F& f) { if constexpr (I < N) { f(IC<I>{}); unroll<N, I + 1>(f); } }
#define MF0(c, a, b) asm volatile("v_mfma_f32_16x16x32_bf16 %0, %1, %2, 0" : "=&v"(c) : "v"(a), "v"(b))
#define DEPTH 6
#define PRIO2_G 0
#define PRIO2_L 1
__device__ __forceinline__ cf pick4(int q, cf a1, cf a2, cf a3) {
    return {q == 0 ? 1.f : q == 1 ? a1.r : q == 2 ? a2.r : a3.r, q == 0 ? 0.f : q == 1 ? a1.i : q == 2 ? a2.i : a3.i};
}
__device__ __forceinline__ void idle_mfma(unsigned a, unsigned b, int n) {
    u32x4 p = {a * 2654435761u, b * 40503u, a * 2246822519u, b * 3266489917u};
    p = (p & 0xbfffbfffu) | 0x3c003c00u;
    u32x4 q = p.yzwx ^ 0x01230456u;
    f32x4 w;
    asm volatile("s_nop 1" : "+v"(p), "+v"(q));
    MF0(w, p, q);
#pragma unroll 1
    for (int i = 0; i < n; ++i) { MF(w, q, p); MF(w, p, p); MF(w, q, q); MF(w, p, q); }
    asm volatile("s_nop 7" : "+v"(w));
}
#define PINC(c) asm volatile("" : "+v"((c).r), "+v"((c).i))

extern "C" __global__ __launch_bounds__(512, 2) void k1_kernel(
        const u16* __restrict__ x, const u16* __restrict__ Bre, const u16* __restrict__ Bim,
        const u16* __restrict__ kin, const u16* __restrict__ vin, const u16* __restrict__ shiftk, const u16* __restrict__ gatew, char* ws,
        const u16* __restrict__ lre, const u16* __restrict__ lim, const u16* __restrict__ logdt,
        const u16* __restrict__ ssmk, const u16* __restrict__ ssmD,
        const u16* __restrict__ Cre, const u16* __restrict__ Cim, const u16* __restrict__ dskip, float* __restrict__ out) {
    extern __shared__ __attribute__((aligned(16))) char smem[];
#define h ((u16*)(ws + (1u << 20)))
#define kvT ((u16*)(ws + (9u << 20)))
#define y2T ((u16*)(ws + (13u << 20)))
#define dbg ((unsigned long long*)(ws + (64u << 20)))
    const int tid = TIDX;
    if (tid < 40 && tid + ((BIDX >> 3) & 7) * 40 < CODE_LINES) CODE_TOUCH(tid + ((BIDX >> 3) & 7) * 40);
    const int lane = tid & 63;
    int wid = __builtin_amdgcn_readfirstlane(tid >> 6);
    const int b = BIDX;
    const int xcd = b & 7, idx = b >> 3;
    const int tb = ((xcd >> 1) ^ (xcd >> 2)) * 4 + (idx & 3), nb = (xcd & 1) * 8 + (idx >> 2);
    const int cb = xcd * 32 + idx;
    const int t_base = tb * 128, p_base = nb * 64, d_base = nb * 64;
    const int l15 = lane & 15, q4 = lane >> 4;
    unsigned stv = 0, sth = 0;
    const unsigned long long pr0 = __builtin_amdgcn_s_memrealtime();
    const unsigned lbase = (unsigned)(size_t)(lds_void*)smem;
    const int rot1 = (xcd >> 1) * 4 + (xcd & 1) * 8 + (idx >> 2) + (idx & 1);
    unsigned voA[4] = {0u, 0u, 0u, 0u}, voB[4] = {0u, 0u, 0u, 0u}, lw0 = 0;
    if (wid == 4 || wid == 5) {
        __builtin_amdgcn_s_setprio(PRIO_L);
        const int lh = wid - 4;
        const unsigned ch = (unsigned)(((lane & 7) ^ (lane >> 4)) * 16);
        voA[0] = (unsigned)(t_base + lh * 16 + (lane >> 5) * 32 + ((lane >> 3) & 3)) * 2048u + ch;
        voA[1] = (voA[0] ^ 64u) + (131072u - 1024u);
        voA[2] = voA[0] + 6144u; voA[3] = voA[1] + 6144u;
        voB[0] = (unsigned)(p_base + lh * 32 + (lane >> 3)) * 2048u + ch;
        voB[1] = (voB[0] ^ 64u) + (16384u - 1024u);
        voB[2] = voB[0] + 30720u; voB[3] = voB[1] + 30720u;
        PIN4(voA[0], voA[1], voA[2], voA[3]); PIN4(voB[0], voB[1], voB[2], voB[3]);
#define GLDS4(sbase, v0, v1, v2, v3, sl, IMM) \
            asm volatile("s_add_u32 m0, %5, %6\n\ts_nop 0\n\tglobal_load_lds_dwordx4 %0, %4\n\tglobal_load_lds_dwordx4 %1, %4 offset:1024\n\t" \
                         "global_load_lds_dwordx4 %2, %4 offset:2048\n\tglobal_load_lds_dwordx4 %3, %4 offset:3072" \
                         :: "v"(v0), "v"(v1), "v"(v2), "v"(v3), "s"(sbase), "s"(sl), "i"(IMM) : "memory", "scc")
#define STAGE_A(kt, sl) do { const char* sa_ = (const char*)x + (((kt) + rot1) & 15) * 128; \
            GLDS4(sa_, voA[0], voA[1], voA[2], voA[3], sl, 0); GLDS4(sa_ + 16384, voA[0], voA[1], voA[2], voA[3], sl, 4096); } while (0)
#define STAGE_B(kt, sl) do { const int kr_ = (((kt) + rot1) & 15) * 128; const char* sb_ = (const char*)Bre + kr_; const char* sc_ = (const char*)Bim + kr_; \
            GLDS4(sb_, voB[0], voB[1], voB[2], voB[3], sl, 0); GLDS4(sc_, voB[0], voB[1], voB[2], voB[3], sl, 4096); } while (0)
        lw0 = lbase + lh * 8192;
        STAGE_A(0, lw0 + RA(0)); STAGE_B(0, lw0 + RB(0));
        idle_mfma(voA[0], voB[1], 12);
        STAGE_A(1, lw0 + RA(1)); STAGE_B(1, lw0 + RB(1));
    }
    const int sblk = b >> 3, dblk = b & 7;
    u32x2 tw[4];
    unsigned epoch;
    asm volatile("" : "=v"(tw[0]), "=v"(tw[1]), "=v"(tw[2]), "=v"(tw[3]), "=v"(epoch));
    if (wid >= 6) {
        __builtin_amdgcn_s_setprio(PRIO_L);
        const int vw = wid - 6;
        const int dch = dblk * 128 + vw * 64 + l15;
        const unsigned lk = lbase + ST_OFF + vw * ST_SZ;
        const unsigned vk = (unsigned)((lane >> 3) * DM * 2 + (((lane & 7) ^ ((lane >> 4) & 1)) * 16));
        const char* kb = (const char*)kin + (size_t)(sblk * 64 - 4) * (DM * 2) + dblk * 256 + vw * 128;
#define KLD(n) GLDS_K(kb + (n) * 8 * DM * 2, vk, lk, (n) * 1024 + ((n) >> 1) * 32)
#define VLD(n) GLDS_K(vb + (((n) >> 1) * 16 + ((n) & 1) * 2) * DM * 2, vv_, lk, ST_V + (n) * 1056)
        if (vw == 1) {
            const unsigned vg = (unsigned)((lane * 16 + rot1 * 128) & 2047), lg = lbase + GW_OFF;
            GLDS_S(gatew, vg, lg, 0); GLDS_S(gatew, vg ^ 1024u, lg, 1024);
        }
        asm volatile("global_load_dwordx2 %0, %5, %7\n\tglobal_load_dwordx2 %1, %5, %7 offset:128\n\tglobal_load_dwordx2 %2, %5, %7 offset:256\n\t"
                     "global_load_dwordx2 %3, %5, %7 offset:384\n\tglobal_load_dword %4, %6, %8 sc1"
                     : "+v"(tw[0]), "+v"(tw[1]), "+v"(tw[2]), "+v"(tw[3]), "+v"(epoch) : "v"((unsigned)(dch * 8)), "v"((unsigned)WS_EPOCH), "s"(shiftk), "s"(ws) : "memory");
        if (sblk == 0 && lane < 32) *(u32x4*)(smem + ST_OFF + vw * ST_SZ + lane * 16) = (u32x4){0u, 0u, 0u, 0u};
        if (sblk != 0 || lane >= 32) KLD(0);
        KLD(1); KLD(2); KLD(3); KLD(4); KLD(5); KLD(6); KLD(7);
        if (lane < 32) KLD(8);
        __builtin_amdgcn_s_setprio(0);
    }
    asm volatile("" : "+s"(wid));

#define WSL(w) ((((w) >> 2) ^ (nb >> 3)) * 4 + ((w) & 3))
#define COFF(w, ti) ((2 * WSL(w) + ((ti) >> 1)) * 128 + ((ti) & 1) * 4096)
    bf16x8 cf_[8][4];
    bf16x8 af[DEPTH];
    int cra, crb;
#define CT_SETUP() const unsigned vB0 = (unsigned)((d_base + (lane >> 3) * 4) * 2048 + (((lane & 7) ^ (lane >> 4)) * 16)), vB1 = (vB0 ^ 64u) + 65536u - 1024u
#define CTILE(w, ti, dst) do { const int co_ = COFF(w, ti); const unsigned ds_ = (dst); \
                               GLDS4((const char*)Cre + co_, vB0, vB1, vB0, vB1, ds_, 0); GLDS4((const char*)Cim + co_, vB0, vB1, vB0, vB1, ds_, 4096); } while (0)
#define CF(ti, n) cf_[4 * ((ti) >> 1) + ((n) >> 1)][2 * ((ti) & 1) + ((n) & 1)]
#define CRD(ti, n) CF(ti, n) = RD128((((n) & 4) ? crb : cra) ^ (((n) & 2) * 16), ((n) & 1) * 2048)
#define CREAD(ti) do { _Pragma("unroll") for (int n_ = 0; n_ < 8; ++n_) CRD(ti, n_); } while (0)
#define CPIN(ti) do { PIN4(CF(ti, 0), CF(ti, 1), CF(ti, 2), CF(ti, 3)); PIN4(CF(ti, 4), CF(ti, 5), CF(ti, 6), CF(ti, 7)); } while (0)
#define CRA(base) ((base) + (q4 >> 1) * 4096 + l15 * 128 + (((q4 & 1) ^ ((l15 >> 1) & 7)) * 16))
    const int rot2 = (idx & 16) >> 2;
#define JB(u) (((((u) ^ (rot2 >> 2)) & 1) << 2) | ((wid - ((u) >> 1) + rot2) & 3))
    const __amdgpu_buffer_rsrc_t hs = __builtin_amdgcn_make_buffer_rsrc((char*)h + (size_t)tb * 524288 + WSL(wid) * 8192, 0, 524288, 0x00020000);
#define LOADF(f) do { af[(f) % DEPTH] = __builtin_bit_cast(bf16x8, __builtin_amdgcn_raw_buffer_load_b128(hs, vh + ((f) & 3) * 1024, JB((f) >> 3) * 65536 + ((f) & 4) * 1024, 16)); SCHED(); } while (0)
#define HPOLL_SETUP() gu32* hfp = (gu32*)(ws + WS_HFLAG) + (tb * 16 + 2 * WSL(wid) + (lane & 1)) * 32
#define HWAIT() do { unsigned spins = 0; \
            while (!__all(__hip_atomic_load(hfp, RLX_AGENT) == tag)) { if (++spins > SPIN_MAX) break; __builtin_amdgcn_s_sleep(PSLEEP); } asm volatile("" ::: "memory"); } while (0)

    const __amdgpu_buffer_rsrc_t gs = __builtin_amdgcn_make_buffer_rsrc(ws + WS_GATE, 0, 32768, 0x00020000);
#define GV(q4) ((unsigned)(((q4) >> 1) * 128 + ((q4) & 1) * 32))
#define GSO(r) (tb * 2048 + JB(6 + (r)) * 256 + (wid >> 2) * 16)
#define TR(r) (t_base + JB(6 + (r)) * 4 + (wid >> 2) * 2)
    u32x2 xx[2][2], dk2;
    u32x4 gv[2];
#define XLOAD() do { \
        const __amdgpu_buffer_rsrc_t xs = __builtin_amdgcn_make_buffer_rsrc((void*)x, 0, SEQ * DM * 2, 0x00020000); \
        const __amdgpu_buffer_rsrc_t ks = __builtin_amdgcn_make_buffer_rsrc((void*)dskip, 0, DM * 2, 0x00020000); \
        dk2 = __builtin_amdgcn_raw_buffer_load_b64(ks, (unsigned)(l15 * 8), d_base * 2, 0); \
        _Pragma("unroll") for (int q = 0; q < 4; ++q) \
            xx[q >> 1][q & 1] = __builtin_amdgcn_raw_buffer_load_b64(xs, (unsigned)(q4 * 32 * DM * 2 + l15 * 8 + (q & 1) * DM * 2), (TR(q >> 1) * DM + d_base) * 2, 0); \
        _Pragma("unroll") for (int r = 0; r < 2; ++r) gv[r] = __builtin_amdgcn_raw_buffer_load_b128(gs, GV(q4), GSO(r), 16); \
        SCHED(); } while (0)
#define XPIN() do { PIN4(xx[0][0], xx[0][1], xx[1][0], xx[1][1]); asm volatile("" : "+v"(dk2), "+v"(gv[0]), "+v"(gv[1])); } while (0)

    if (wid < 4) {
        typedef __attribute__((ext_vector_type(8))) unsigned u32x8;
        typedef const __attribute__((address_space(4))) u32x8 cu8;
        const int po = (p_base + wid * 16) * 2;
        u32x8 s0 = *(cu8*)((size_t)logdt + po), s1 = *(cu8*)((size_t)lre + po), s2 = *(cu8*)((size_t)lim + po);
        SCHED();
        const int pq = p_base + wid * 16 + l15;
        unsigned goff[4], gmask[4];
#pragma unroll
        for (int i = 0; i < 4; ++i) {
            goff[i] = (unsigned)(((tb - 1 - q4 - 4 * i) * 1024 + pq) * 16);
            gmask[i] = (tb - 1 - q4 - 4 * i >= 0) ? ~0u : 0u;
            asm volatile("" : "+v"(goff[i]), "+v"(gmask[i]));
        }
        unsigned gpub = q4 == 0 ? (unsigned)((tb * 1024 + pq) * 16) : 0x80000000u;
        unsigned hp = (unsigned)(tb * 524288 + (nb * 4 + wid) * 1024 + lane * 16);
        unsigned wa = (unsigned)(HT_OFF + wid * 2048 + (q4 * 16 + (l15 ^ ((q4 >> 1) * 4))) * 8);
        unsigned ra0 = (unsigned)(HT_OFF + wid * 2048 + ((q4 >> 1) * 64 + (l15 & 3) * 16 + (((q4 & 1) * 8 + (l15 >> 2)) ^ ((l15 & 2) * 2))) * 8), ra1 = ra0 ^ 32u;
        cra = CRA(GA_OFF + wid * 8192); crb = cra ^ 64;
        unsigned vB0 = (unsigned)((d_base + (lane >> 3) * 4) * 2048 + (((lane & 7) ^ (lane >> 4)) * 16)), vB1 = (vB0 ^ 64u) + 65536u - 1024u;
        PIN4(gpub, vB0, hp, wa); PIN4(ra0, ra1, cra, crb); asm volatile("" : "+v"(vB1));
        f32x4 acc[8][2];
#pragma unroll
        for (int m = 0; m < 8; m += 2) {
#pragma unroll
            for (int c = 0; c < 4; ++c) acc[m + (c >> 1)][c & 1] = (f32x4){0.f, 0.f, 0.f, 0.f};
            PIN4(acc[m][0], acc[m][1], acc[m + 1][0], acc[m + 1][1]);
        }
        SCHED();
        cf L1, Lq3, cc, L32, cLq, Lp, L512;
        {
            unsigned w0 = 0, w1 = 0, w2 = 0;
#pragma unroll
            for (int i = 0; i < 8; ++i) {
                asm("v_writelane_b32 %0, %3, %6\n\tv_writelane_b32 %1, %4, %6\n\tv_writelane_b32 %2, %5, %6" : "+v"(w0), "+v"(w1), "+v"(w2) : "s"(s0[i]), "s"(s1[i]), "s"(s2[i]), "i"(2 * i));
            }
            asm("s_nop 1" : "+v"(w0), "+v"(w1), "+v"(w2));
            auto spread = [&](unsigned w) {
                const unsigned e = (unsigned)__builtin_amdgcn_update_dpp(0, (int)w, 0xa0, 0xf, 0xf, false);
                const unsigned a = __builtin_amdgcn_permlane16_swap(e, e, false, false)[0];
                const unsigned c = __builtin_amdgcn_permlane32_swap(a, a, false, false)[0];
                return (l15 & 1) ? hi16(c) : lo16(c);
            };
            const float ldt = spread(w0), re = spread(w1), im = spread(w2);
            if (wid == 3 && lane < NFLAGW) *(unsigned*)(smem + UFLAG_OFF + lane * 4) = 0u;
            const float dt = rbf(expf(ldt));
            const float ea = expf(dt * re);
            float sn, cs;
            sincosf(dt * im, &sn, &cs);
            L1 = {ea * cs, ea * sn};
            const float nr = L1.r - 1.0f, ni = L1.i, den = re * re + im * im;
            cc = {2.f * ((nr * re + ni * im) / den), 2.f * ((ni * re - nr * im) / den)};
            const cf T2 = cmul(L1, L1), T4 = cmul(T2, T2), L8 = cmul(T4, T4), L16 = cmul(L8, L8);
            L32 = cmul(L16, L16);
            const cf L64 = cmul(L32, L32), L96 = cmul(L64, L32), L128 = cmul(L64, L64), L256 = cmul(L128, L128), L384 = cmul(L256, L128);
            L512 = cmul(L256, L256);
            Lp = pick4(q4, L128, L256, L384);
            Lq3 = pick4(3 - q4, L32, L64, L96);
            cLq = cmul(cc, pick4(q4, L32, L64, L96));
            PINC(L1); PINC(Lq3); PINC(cc); PINC(L32); PINC(cLq); PINC(Lp); PINC(L512);
        }
        __builtin_amdgcn_s_setprio(PRIO_G);
        unsigned tagv;
        cf S;
        STAMPT(dbg, 22, 0);
        const int swz = (l15 >> 1) & 7;
        {
            const int a0 = RA(0) + l15 * 128 + ((q4 ^ swz) * 16);
            const int b0 = RB(0) + ((wid >> 1) * 64 + (wid & 1) * 16 + l15) * 128 + ((q4 ^ swz) * 16);
            WAIT_LGKM0();
            BAR();
            asm volatile("" ::: "memory");
            STAMPT(dbg, 19, 64);
            bf16x8 FA[8][2], FB[2][2][2];
            int pa[2] = {a0, a0 ^ 64}, pb[2] = {b0, b0 ^ 64};
            PIN4(pa[0], pa[1], pb[0], pb[1]);
#define LGW(n) __builtin_amdgcn_s_waitcnt(0xC07F | ((n) << 8))
#define RA_(j) FA[(j) >> 1][(j) & 1] = RD128(pa[(j) & 1], ((j) >> 1) * 2048)
#define RB_(bu, j) FB[bu][(j) >> 1][((j) ^ ((j) >> 1)) & 1] = RD128(pb[((j) ^ ((j) >> 1)) & 1], ((j) >> 1) * 4096)
#define RN_(bu, i) do { if ((i) == 0) RB_(bu, 0); else if ((i) < 3) RA_((i) - 1); else if ((i) < 6) RB_(bu, (i) - 2); else RA_((i) - 4); } while (0)
#define G1_H(H, bu, RD, FILL) do { \
            _Pragma("unroll") for (int i = 0; i < 16; ++i) { \
                const int m = (H) * 4 + (i >> 2), n = ((i >> 1) ^ m) & 1, ks = (i ^ (i >> 1)) & 1; \
                if ((H) == 0) { if (i == 0) LGW(6); if (i == 4) LGW(8); if (i == 8) LGW(10); if (i == 12) LGW(8); } \
                SCHED(); \
                MF(acc[m][n], FA[m][ks], FB[bu][n][ks]); SCHED(); \
                if ((H) == 0 && i < 8) RA_(8 + i); \
                if ((H) == 1 && (RD) && i < 12) RN_((bu) ^ 1, i); \
                FILL((H) * 16 + i); \
                SCHED(); \
            } } while (0)
#define NOF(g) do { } while (0)
#pragma unroll
            for (int j = 0; j < 12; ++j) { RN_(0, j); SCHED(); }
            unsigned long long ua_ = UNITS_A;
            int sb = RB(0), sa = RA(0), nx, na;
#define ADV(g) do { if ((g) == 8) { ua_ >>= 4; na = (int)((unsigned)ua_ & 15u) << 14; asm volatile("" : "+s"(na), "+s"(ua_)); } \
                    if ((g) == 9) { const int o_ = sa; sa = na; na -= o_; asm volatile("" : "+s"(na), "+s"(sa)); } \
                    if ((g) == 10) { pa[0] += na; pa[1] += na; asm volatile("" : "+v"(pa[0]), "+v"(pa[1])); } \
                    if ((g) == 11) { nx = (sb == 65536) ? -49152 : 16384; sb += nx; asm volatile("" : "+s"(nx), "+s"(sb)); } \
                    if ((g) == 13) { pb[0] += nx; pb[1] += nx; asm volatile("" : "+v"(pb[0]), "+v"(pb[1])); } } while (0)
#define G1_T(bu) do { \
                G1_H(0, bu, true, ADV); \
                WAIT_LGKM0(); \
                asm volatile("" ::: "memory"); \
                BAR();                     \
                asm volatile("" ::: "memory"); \
                G1_H(1, bu, true, NOF); } while (0)
#pragma unroll 1
            for (int t2 = 0; t2 < 7; ++t2) { G1_T(0); G1_T(1); }
            G1_T(0);
#define AGG(g) do { if ((g) == 8) { asm volatile("" ::: "memory"); SETW(G1D_OFF, wid, 1u); }        \
                    if ((g) == 9) tagv = *(volatile const lds_u32*)(size_t)(unsigned)TAG_OFF; \
                    if ((g) >= 7) { const int t = (g) - 6; \
                        if (t == 1) { asm volatile("s_nop 1" : "+v"(acc[0][0]), "+v"(acc[0][1])); S = {acc[0][0][0], acc[0][1][0]}; } \
                        if ((t & 3) == 0) asm volatile("" : "+v"(acc[t >> 2][0]), "+v"(acc[t >> 2][1])); \
                        S = cfma(L1, S, cf{acc[t >> 2][0][t & 3], acc[t >> 2][1][t & 3]}); PINC(S); } } while (0)
            G1_H(0, 1, false, AGG);
            G1_H(1, 1, false, AGG);
        }
        STAMPT(dbg, 1, 0);
        const unsigned tag = __builtin_amdgcn_readfirstlane(tagv);
        const __amdgpu_buffer_rsrc_t grs = __builtin_amdgcn_make_buffer_rsrc(ws, 0, 15 * 16384, 0x00020000);
        {
#pragma unroll
            for (int t = 26; t < 32; ++t) {
                if (t == 28) { PINC(S); asm volatile("" : "+v"(acc[7][0]), "+v"(acc[7][1])); }
                S = cfma(L1, S, cf{acc[t >> 2][0][t & 3], acc[t >> 2][1][t & 3]});
            }
            const cf W = cmul(S, Lq3);
            const float e2 = red16(red32(W.r, W.i));
            const auto eo = __builtin_amdgcn_permlane32_swap(__float_as_uint(e2), __float_as_uint(e2), false, false);
            __builtin_amdgcn_raw_buffer_store_b128((u32x4){eo[0], tag, eo[1], tag}, grs, gpub, 0, 16);
        }
        SCHED();
        STAMPT(dbg, 2, 0);
        const unsigned cg = lbase + GA_OFF + wid * 8192;
        wait_lds1(smem, COK_OFF + wid * 4);
        CREAD(0);
        WAIT_LGKM0();
        CPIN(0);
        CTILE(wid, 1, cg);
        u32x4 gq[4];
#pragma unroll
        for (int m = 0; m < 8; ++m) {
            const f32x4 R = acc[m][0], I = acc[m][1];
            acc[m][0] = fma4(splat4(cc.r), R, mul4(splat4(-cc.i), I));
            acc[m][1] = fma4(splat4(-cc.r), I, mul4(splat4(-cc.i), R));
            asm volatile("" : "+v"(acc[m][0]), "+v"(acc[m][1]));
            if (m == 3) {
                SCHED();
#pragma unroll
                for (int i = 0; i < 4; ++i) asm volatile("buffer_load_dwordx4 %0, %1, %2, 0 offen sc1" : "=&v"(gq[i]) : "v"(goff[i]), "s"(grs) : "memory");
                SCHED();
            }
        }
        cf Ec;
        {
            const auto sr = __builtin_amdgcn_permlane16_swap(__float_as_uint(S.r), __float_as_uint(S.r), false, false);
            const auto si = __builtin_amdgcn_permlane16_swap(__float_as_uint(S.i), __float_as_uint(S.i), false, false);
            const cf Se = {__uint_as_float(sr[0]), __uint_as_float(si[0])}, So = {__uint_as_float(sr[1]), __uint_as_float(si[1])};
            const cf A = cfma(L32, Se, So);
            const auto ar = __builtin_amdgcn_permlane32_swap(__float_as_uint(A.r), __float_as_uint(A.r), false, false);
            const auto ai = __builtin_amdgcn_permlane32_swap(__float_as_uint(A.i), __float_as_uint(A.i), false, false);
            const cf A01 = {(q4 & 2) ? __uint_as_float(ar[0]) : 0.f, (q4 & 2) ? __uint_as_float(ai[0]) : 0.f};
            const cf kq = {(q4 & 1) ? L32.r : 1.f, (q4 & 1) ? L32.i : 0.f};
            const cf E = cfma(kq, A01, cf{(q4 & 1) ? Se.r : 0.f, (q4 & 1) ? Se.i : 0.f});
            Ec = cmul(cc, E);
            PINC(Ec);
        }
        WAIT_VM(4);
        CREAD(1);
        WAIT_LGKM0();
        CPIN(1);
        CTILE(wid, 2, cg);
        asm volatile("s_waitcnt vmcnt(8)" : "+v"(gq[0]), "+v"(gq[1]), "+v"(gq[2]), "+v"(gq[3]) :: "memory");
        STAMPT(dbg, 27, 0);
        cf carry;
        {
            unsigned spins = 0;
            for (;;) {
                unsigned bad = 0;
#pragma unroll
                for (int i = 0; i < 4; ++i) bad |= ((gq[i].y ^ tag) | (gq[i].w ^ tag)) & gmask[i];
                if (__all(bad == 0u)) break;
                if (++spins > SPIN_MAX) break;
#pragma unroll
                for (int i = 0; i < 4; ++i) asm volatile("buffer_load_dwordx4 %0, %1, %2, 0 offen sc1" : "+v"(gq[i]) : "v"(goff[i]), "s"(grs) : "memory");
                asm volatile("s_waitcnt vmcnt(0)" : "+v"(gq[0]), "+v"(gq[1]), "+v"(gq[2]), "+v"(gq[3]) :: "memory");
            }
            cf E = {__uint_as_float(gq[3].x), __uint_as_float(gq[3].z)};
#pragma unroll
            for (int i = 2; i >= 0; --i) E = cfma(L512, E, cf{__uint_as_float(gq[i].x), __uint_as_float(gq[i].z)});
            E = cmul(E, Lp);
            const float e2 = red16(red32(E.r, E.i));
            const auto eo = __builtin_amdgcn_permlane32_swap(__float_as_uint(e2), __float_as_uint(e2), false, false);
            carry = {__uint_as_float(eo[0]), __uint_as_float(eo[1])};
        }
        STAMPT(dbg, 3, 0);
        {
            const __amdgpu_buffer_rsrc_t hrs = __builtin_amdgcn_make_buffer_rsrc(h, 0, SEQ * 2048 * 2, 0x00020000);
            const cf e = cfma(cLq, carry, Ec);
            float hr = e.r, hn = -e.i;
            s16x4 fr[2][4];
#define HSTORE(m) do { _Pragma("unroll") for (int c = 0; c < 2; ++c) \
                __builtin_amdgcn_raw_buffer_store_b128(__builtin_bit_cast(u32x4, __builtin_shufflevector(fr[(m) & 1][2 * c], fr[(m) & 1][2 * c + 1], 0, 1, 2, 3, 4, 5, 6, 7)), hrs, hp, (2 * (m) + c) * 65536, WT); } while (0)
#pragma unroll
            for (int m = 0; m < 4; ++m) {
#pragma unroll
                for (int j = 0; j < 8; ++j) {
                    const float nr = fmaf(L1.r, hr, fmaf(L1.i, hn, acc[2 * m + (j >> 2)][0][j & 3]));
                    const float nn = fmaf(L1.r, hn, fmaf(-L1.i, hr, acc[2 * m + (j >> 2)][1][j & 3]));
                    hr = nr; hn = nn;
                    acc[2 * m + (j >> 2)][0][j & 3] = nr; acc[2 * m + (j >> 2)][1][j & 3] = nn;
                }
                u32x2 pk[4];
#pragma unroll
                for (int c = 0; c < 4; ++c) { const f32x4 v = acc[2 * m + (c >> 1)][c & 1]; pk[c] = (u32x2){pkbf(v[0], v[1]), pkbf(v[2], v[3])}; }
                PIN4(pk[0], pk[1], pk[2], pk[3]);
                SCHED();
#pragma unroll
                for (int c = 0; c < 4; ++c) { *(volatile __attribute__((address_space(3))) u32x2*)(size_t)(wa + c * 512) = pk[c]; SCHED(); }
                asm volatile("" ::: "memory");
#pragma unroll
                for (int c = 0; c < 2; ++c) { fr[m & 1][2 * c] = TRD(ra0 + c * 1024); SCHED(); fr[m & 1][2 * c + 1] = TRD(ra1 + c * 1024); SCHED(); }
                asm volatile("" ::: "memory");
                if (m > 0) HSTORE(m - 1);
                SCHED();
            }
            HSTORE(3);
        }
        STAMPT(dbg, 4, 0);
        {
            WAIT_VM(8);
            CREAD(2);
            WAIT_VM(0);
            {
                unsigned n;
                asm volatile("s_mov_b64 exec, 1\n\tds_add_rtn_u32 %0, %1, %2\n\ts_mov_b64 exec, -1\n\ts_waitcnt lgkmcnt(0)" : "=&v"(n) : "v"(MISC_OFF), "v"(1u) : "memory");
                if (__builtin_amdgcn_readfirstlane(n) == 3u) {
                    if (lane == 0) __hip_atomic_store((gu32*)(ws + WS_HFLAG) + (tb * 16 + nb) * 32, tag, RLX_AGENT);
                    if (DBGON && lane == 0) dbg[BIDX * 64 + 20] = __builtin_amdgcn_s_memrealtime();
                }
            }
            STAMPT(dbg, 12, 0);
            if (wid == 0 && lane < 7) CODE_TOUCH(CODE_LINES - 1 - lane - (idx & 7) * 7);
            __builtin_amdgcn_s_setprio(0);
            WAIT_LGKM0();
            CPIN(2);
            CTILE(wid, 3, cg);
            XLOAD();
            HPOLL_SETUP();
            HWAIT();
            STAMPT(dbg, 8, 0);
            const unsigned vh = (unsigned)(lane * 16);
#pragma unroll
            for (int f = 0; f < DEPTH; ++f) LOADF(f);
            __builtin_amdgcn_s_setprio(PRIO2_G);
        }
    } else {
        const int lw = wid - 4;
        const int d = cb * 4 + lw;
        const char* Krow = (const char*)(ssmk + (size_t)d * SEQ);
        cra = CRA(LA_OFF + lw * 8192); crb = cra ^ 64;
        const unsigned cst0 = lbase + LA_OFF + lw * 8192;
        CT_SETUP();
        asm volatile("" : "+v"(cra), "+v"(crb));
        u32x4 KA[4];
        unsigned draw;
        unsigned tag;
        float gacc = 0.f;
#define KLOAD() asm volatile("global_load_dwordx4 %0, %5, %7 nt\n\tglobal_load_dwordx4 %1, %5, %7 offset:1024 nt\n\tglobal_load_dwordx4 %2, %5, %7 offset:2048 nt\n\t" \
                             "global_load_dwordx4 %3, %5, %7 offset:3072 nt\n\tglobal_load_ushort %4, %6, %8" \
                             : "=&v"(KA[0]), "=&v"(KA[1]), "=&v"(KA[2]), "=&v"(KA[3]), "=&v"(draw) : "v"((unsigned)(lane * 16)), "v"((unsigned)(d * 2)), "s"(Krow), "s"(ssmD) : "memory")
#define KWAIT(n) asm volatile("s_waitcnt vmcnt(%5)" : "+v"(KA[0]), "+v"(KA[1]), "+v"(KA[2]), "+v"(KA[3]), "+v"(draw) : "i"(n) : "memory")
        if (wid < 6) {
            WAIT_VM(16);
            STAMPT(dbg, 17, 320);
            BAR();
            STAGE_A(2, lw0 + RA(2)); STAGE_B(2, lw0 + RB(2));
            unsigned long long ua_ = UNITS_A >> 12;
            unsigned sla = lw0 + RA(3), slb = lw0 + RB(4);
            int ka = ((3 + rot1) & 15) * 128, kb_ = ka;
            const char* sa = (const char*)x + ka;
            asm volatile("" : "+s"(sa), "+s"(sla), "+s"(ka), "+s"(ua_));
#define NEXT_A() do { ka = (ka + 128) & 2047; sa = (const char*)x + ka; ua_ >>= 4; sla = lw0 + (((unsigned)ua_ & 15u) << 14); asm volatile("" : "+s"(sa), "+s"(sla), "+s"(ka), "+s"(ua_)); } while (0)
#define LSTEP(n) do { \
                if ((n) == 24 && t == 1) { WAIT_VM(16); BAR(); STAGE_B(3, lw0 + RB(3)); } else { WAIT_VM(n); BAR(); } \
                GLDS4(sa, voA[0], voA[1], voA[2], voA[3], sla, 0); \
                GLDS4(sa + 16384, voA[0], voA[1], voA[2], voA[3], sla, 4096); \
                kb_ = (kb_ + 128) & 2047; \
                GLDS4((const char*)Bre + kb_, voB[0], voB[1], voB[2], voB[3], slb, 0); \
                NEXT_A(); \
                const unsigned slb0 = slb; \
                slb = (slb == lw0 + 65536) ? lw0 + 16384 : slb + 16384; \
                asm volatile("" : "+s"(slb), "+s"(kb_), "+s"(t)); \
                GLDS4((const char*)Bim + kb_, voB[0], voB[1], voB[2], voB[3], slb0, 4096); } while (0)
#pragma unroll 1
            for (int t = 1; t < 5; ++t) LSTEP(24);
            GLDS4(sa, voA[0], voA[1], voA[2], voA[3], sla, 0); GLDS4(sa + 16384, voA[0], voA[1], voA[2], voA[3], sla, 4096);
            NEXT_A();
#pragma unroll 1
            for (int t = 5; t < 13; ++t) LSTEP(32);
            KLOAD();
            WAIT_VM(37);
            BAR();
            __builtin_amdgcn_s_setprio(0);
            tag = lds_word(smem, TAG_OFF);
            CTILE(lw, 0, cst0 + GA_OFF);
            FLAGD(COK_OFF + lw * 4);
            WAIT_VM(30);
            BAR();
            CTILE(lw + 2, 0, cst0 + GA_OFF + 16384);
            FLAGD(COK_OFF + lw * 4 + 8);
            WAIT_VM(23);
            BAR();
        } else {
            const int vw = wid - 6;
            const int dch = dblk * 128 + vw * 64 + l15;
            const unsigned lk = lbase + ST_OFF + vw * ST_SZ;
            const unsigned vv_ = (unsigned)(((lane >> 4) * 4 + ((lane >> 3) & 1)) * DM * 2 + (lane & 7) * 16);
            const char* vb = (const char*)vin + (size_t)(sblk * 64) * (DM * 2) + dblk * 256 + vw * 128;
            const int grow = nb * 8 + vw * 4 + q4;
            unsigned gx0 = (unsigned)(grow * 128 + (((l15 >> 1) ^ ((grow >> 1) & 7)) * 16) + (l15 & 1) * 8), gx1 = gx0 + RA(2), gwa = (unsigned)(GW_OFF + l15 * 8);
            asm volatile("" : "+v"(gx0), "+v"(gx1), "+v"(gwa));
            u32x2 gxv, gwv;
            typedef const volatile __attribute__((address_space(3))) u32x2* l2p;
#define VBAR(t) do { SCHED(); asm volatile("" ::: "memory"); BAR(); asm volatile("" ::: "memory"); \
                     gxv = *(l2p)(size_t)(RA(t) == 0 ? gx0 : gx1 + (RA(t) - RA(2))); gwv = *(l2p)(size_t)(gwa + (t) * 128); SCHED(); } while (0)
#define GATE() do { SCHED(); gacc = fmaf(lo16(gxv.x), lo16(gwv.x), gacc); gacc = fmaf(hi16(gxv.x), hi16(gwv.x), gacc); \
                    gacc = fmaf(lo16(gxv.y), lo16(gwv.y), gacc); gacc = fmaf(hi16(gxv.y), hi16(gwv.y), gacc); asm volatile("" : "+v"(gacc)); SCHED(); } while (0)
            unsigned ka[4][2], sel[4];
            const int R0 = 4 + (l15 >> 2) * 16 + (l15 & 3) - (q4 >> 1);
            unsigned kr0 = (unsigned)(2 * R0), kr1 = (unsigned)(ST_OFF + vw * ST_SZ + R0 * 128 + (((q4 ^ (R0 >> 1)) & 1) * 16));
            asm volatile("" : "+v"(kr0), "+v"(kr1));
#pragma unroll
            for (int i = 0; i < 4; ++i)
#pragma unroll
                for (int tp = 0; tp < 2; ++tp) {
                    ka[i][tp] = (kr1 ^ (tp * 16)) + ((kr0 + 8 * i - 4 * tp) & 0xe0) + (4 * i - 2 * tp) * 128;
                    asm volatile("" : "+v"(ka[i][tp]));
                }
#pragma unroll
            for (int r = 0; r < 4; ++r) {
                const unsigned pick = (q4 & 2) ? 0x0302u : 0x0100u;
                sel[r] = ((l15 >> 1) == (q4 & 1) * 4 + r) ? ((l15 & 1) ? (pick << 16) | 0x0c0cu : 0x0c0c0000u | pick) : 0x0c0c0c0cu;
            }
            unsigned va_ = (unsigned)(ST_OFF + vw * ST_SZ + ST_V + (q4 * 2 + (l15 >> 3)) * 1056 + ((l15 >> 2) & 1) * 128 + (l15 & 3) * 8);
            unsigned so = (unsigned)(((dch >> 2) * 32 + sblk) * 512 + q4 * 64 + (dch & 3) * 16);
            PIN4(sel[0], sel[1], sel[2], sel[3]); asm volatile("" : "+v"(so), "+v"(va_));
            asm volatile("s_waitcnt vmcnt(9)" : "+v"(tw[0]), "+v"(tw[1]), "+v"(tw[2]), "+v"(tw[3]), "+v"(epoch) :: "memory");
            STAMPT(dbg, 18, 384);
            VBAR(0);
            bf16x8 wf[4][2];
#pragma unroll
            for (int ps = 0; ps < 4; ++ps)
#pragma unroll
                for (int tp = 0; tp < 2; ++tp) {
                    u32x4 w;
#pragma unroll
                    for (int r = 0; r < 4; ++r) w[r] = __builtin_amdgcn_perm(0u, tw[ps][tp], sel[r]);
                    wf[ps][tp] = __builtin_bit_cast(bf16x8, w);
                    asm volatile("" : "+v"(wf[ps][tp]));
                }
            SCHED();
            GATE(); VBAR(1);
            VLD(0); VLD(1); VLD(2); VLD(3); VLD(4); VLD(5); VLD(6); VLD(7);
            f32x4 ks[4][4];
            auto sconv = [&](int i0) __attribute__((always_inline)) {
                bf16x8 kf[2][4][2];
#pragma unroll
                for (int q = 0; q < 16; ++q) { kf[q >> 3][q & 3][(q >> 2) & 1] = RD128(ka[i0 + (q >> 3)][(q >> 2) & 1], (q & 3) * 32); SCHED(); }
                GATE();
                __builtin_amdgcn_s_setprio(PRIO_KV);
                WAIT_LGKM0();
                SCHED();
#pragma unroll
                for (int i = 0; i < 2; ++i) {
#pragma unroll
                    for (int ps = 0; ps < 4; ++ps) MF0(ks[ps][i0 + i], kf[i][ps][0], wf[ps][0]);
#pragma unroll
                    for (int ps = 0; ps < 4; ++ps) MF(ks[ps][i0 + i], kf[i][ps][1], wf[ps][1]);
                }
                __builtin_amdgcn_s_setprio(0);
                SCHED();
            };
            WAIT_VM(8);
            STAMPT(dbg, 21, 384);
            sconv(0); VBAR(2);
            sconv(2); VBAR(3);
            WAIT_VM(0);
            u32x2 vq[4][4];
#pragma unroll
            for (int ps = 0; ps < 4; ++ps)
#pragma unroll
                for (int j = 0; j < 4; ++j) vq[ps][j] = __builtin_bit_cast(u32x2, TRD(va_ + j * 256 + ps * 32));
            const __amdgpu_buffer_rsrc_t rs = __builtin_amdgcn_make_buffer_rsrc(kvT, 0, DM * SEQ * 2, 0x00020000);
            u32x4 o[8];
            auto kvout = [&](int n) __attribute__((always_inline)) {
                const int hh = n >> 2, ps = n & 3;
                asm volatile("" : "+v"(ks[ps][2 * hh]), "+v"(ks[ps][2 * hh + 1]));
#pragma unroll
                for (int e = 0; e < 4; ++e) {
                    const unsigned vv = vq[ps][2 * hh + (e >> 1)][e & 1];
                    o[n][e] = pkbf(ks[ps][2 * hh + (e >> 1)][2 * (e & 1)] * lo16(vv), ks[ps][2 * hh + (e >> 1)][2 * (e & 1) + 1] * hi16(vv));
                }
                asm volatile("" : "+v"(o[n]));
                SCHED();
            };
            GATE(); kvout(0); kvout(1);
            WAIT_LGKM0();

            VBAR(4);
#define KVST(h) do { _Pragma("unroll") for (int n = 0; n < 4; ++n) __builtin_amdgcn_raw_buffer_store_b128(o[4 * (h) + n], rs, so + (unsigned)((h) * 256), n * 65536, WT); \
                     SCHED(); asm volatile("s_nop 1" ::: "memory"); SCHED(); } while (0)
            kvout(2); kvout(3); GATE(); VBAR(5);
            KVST(0);
            kvout(4); kvout(5); GATE(); VBAR(6);
            kvout(6); kvout(7); GATE(); VBAR(7);
            KVST(1);
            GATE(); VBAR(8);
            if (vw == 1 && lane == 0) *(unsigned*)(smem + TAG_OFF) = (epoch + 1u) ? epoch + 1u : 1u;
            GATE();
            WAIT_LGKM0();
            WAIT_VM(0);
            VBAR(9);
            tag = __builtin_amdgcn_readfirstlane(*(const unsigned*)(smem + TAG_OFF));
            if (vw == 0 && lane == 0) __hip_atomic_store((gu32*)(ws + WS_KVFLAG) + (dblk * 32 + sblk) * 32, tag, RLX_AGENT);
            if (vw == 1 && lane < 4) *(u32x4*)(smem + U_OFF + 192 + lane * 16) = (u32x4){0u, 0u, 0u, 0u};
            gu32* kfp = (gu32*)(ws + WS_KVFLAG) + ((cb >> 5) * 32 + vw * 16 + l15) * 32;
            const char* ub = (const char*)kvT + (size_t)__builtin_amdgcn_readfirstlane(cb * 16384 + vw * 8192);
            const unsigned vu = (unsigned)((lane >> 5) * 512 + ((lane >> 2) & 1) * 256 + ((lane >> 3) & 3) * 64 + ((lane - (lane >> 4)) & 3) * 16);
            const unsigned lu = lbase + U_OFF + 256 + vw * 8192;
#define U_DMA1(o) asm volatile("s_add_u32 m0, %2, %3\n\ts_nop 0\n\tglobal_load_lds_dwordx4 %0, %1 sc1\n\tglobal_load_lds_dwordx4 %0, %1 offset:1024 sc1\n\t" \
                             "global_load_lds_dwordx4 %0, %1 offset:2048 sc1\n\tglobal_load_lds_dwordx4 %0, %1 offset:3072 sc1" \
                             :: "v"(vu), "s"(ub + (o)), "s"(lu), "i"(o) : "memory", "scc")
#define U_DMA() do { U_DMA1(0); U_DMA1(4096); FLAGD(UFLAG_OFF + lw * 4); } while (0)
            unsigned fo = lane < 8 ? (unsigned)((tb * 1024 + p_base) * 16 + lane * 128) : lane == 8 ? (unsigned)(WS_HFLAG + (tb * 16 + nb) * 128)
                        : lane == 9 ? (unsigned)(WS_CFLAG + cb * 128) : (unsigned)(WS_GATE + (tb * 16 + nb) * 128);
            asm volatile("" : "+v"(fo));
            GATE(); VBAR(10);
            TOUCH((const char*)(vw ? Cim : Cre) + (size_t)b * 8192, (unsigned)(lane * 128));
            if (vw == 0) TOUCH(ws, fo); else TOUCH(dskip, (unsigned)(d_base * 2));
            {
                const int cl = 64 + lane + ((idx & 15) * 2 + vw) * 7;
                if (lane < 7 && cl < CODE_LINES) CODE_TOUCH(cl);
            }
            GATE(); VBAR(11);
            GATE(); VBAR(12);
            GATE(); VBAR(13);
            unsigned pv;
            asm volatile("global_load_dword %0, %1, off sc1" : "=&v"(pv) : "v"(kfp) : "memory");
            KLOAD();
            GATE(); VBAR(14);
            GATE(); VBAR(15);
            asm volatile("s_waitcnt vmcnt(5)" : "+v"(pv) :: "memory");
            const bool done = __all(pv == tag);
            if (done) U_DMA();
            if (DBGON && TIDX == 384) dbg[BIDX * 64 + 13] = (done ? 100ull : 0ull) | (1ull << 62);
            GATE();
            if (lane == 0) SETW(UFLAG_OFF, vw, 1u);
            WAIT_VM(9);
            if (!done) {
                unsigned spins = 0;
                for (;;) {
                    const unsigned v = __hip_atomic_load(kfp, RLX_AGENT);
                    if (__all(v == tag)) break;
                    if (++spins > SPIN_MAX) break;
                    __builtin_amdgcn_s_sleep(1);
                }
                U_DMA();
            }
        }
        KWAIT(18);
        STAMPT(dbg, 14, 320);
        __builtin_amdgcn_s_sleep(6);
        {
            const unsigned cp = (unsigned)(KC_OFF + lw * CV_W + 64 + lane * 16);
            if (lane == 0) KA[0].x = (KA[0].x & 0xffff0000u) | f2bf(lo16(KA[0].x) + bf2f((u16)draw));
#define WR128(v, off) asm volatile("ds_write_b128 %0, %1 offset:%2" :: "v"(cp), "v"(v), "i"(off) : "memory")
#pragma unroll
            for (int q = 3; q >= 0; --q) WR128(KA[q], q * 1024);
#pragma unroll
            for (int q = 3; q >= 0; --q) {
                const u32x2 p = q ? pred2(KA[q], RL63(KA[q ? q - 1 : 0].z), RL63(KA[q ? q - 1 : 0].w)) : pred2(KA[0], 0u, 0u);
                const u32x4 a = KA[q];
                const u32x4 s1 = {al(a.x, p.y), al(a.y, a.x), al(a.z, a.y), al(a.w, a.z)};
                WR128(s1, q * 1024 + CV_COPY);
                const u32x4 s2 = {p.y, mv(a.x), mv(a.y), mv(a.z)};
                WR128(s2, q * 1024 + 2 * CV_COPY);
                const u32x4 s3 = {al(p.y, p.x), mv(s1.x), mv(s1.y), mv(s1.z)};
                WR128(s3, q * 1024 + 3 * CV_COPY);
            }
            if (lane < 16) *(u32x4*)(smem + KC_OFF + lw * CV_W + (lane >> 2) * CV_COPY + (lane & 3) * 16) = (u32x4){0u, 0u, 0u, 0u};
            SCHED();
        }
        STAMPT(dbg, 26, 320);
        wait_lds4(smem, G1D_OFF);
        wait_lds4(smem, UFLAG_OFF);
        STAMPT(dbg, 9, 256);
        __builtin_amdgcn_s_setprio(PRIO_CV);
        STAMPT(dbg, 10, 384);
        f32x4 cv[8];
#pragma unroll
        for (int g = 0; g < 8; ++g) cv[g] = (f32x4){0.f, 0.f, 0.f, 0.f};
        PIN4(cv[0], cv[1], cv[2], cv[3]); PIN4(cv[4], cv[5], cv[6], cv[7]);
        const int uz = U_OFF + 192;
        const int ua0 = U_OFF + 256 + (2 * l15 + q4 - 2) * 64, rv0 = (2 * l15 + q4 - 2) * 4 + lw * 16;
        const unsigned ka0 = (unsigned)((l15 >> 2) * CV_COPY - 16 * q4 + 8 * (l15 & 3) - 8);
        int ua, rv, pu, pz;
        unsigned adr;
#define UNEXT1() do { pu = ua | (rv & 0x30); asm volatile("" : "+v"(pu)); } while (0)
#define UNEXT2() do { pz = (ua > uz ? ua : uz) | (rv & 0x30); asm volatile("" : "+v"(pz)); } while (0)
#define UNEXT3() do { ua -= 256; rv -= 16; asm volatile("" : "+v"(ua), "+v"(rv)); } while (0)
#define ANEXT() do { adr += 64; asm volatile("" : "+v"(adr)); } while (0)
#define RDU(dst, c) do { dst = (c) ? RD128(pu, (c) * 2048) : RD128(pz, 0); SCHED(); } while (0)
        HPOLL_SETUP();
        unsigned hpv = 0;
        {
            bf16x8 A[8], UB[8];
            ua = ua0; rv = rv0; adr = lbase + KC_OFF + lw * CV_W + ka0 + 96;
            asm volatile("" : "+v"(adr));
            UNEXT1(); UNEXT2(); UNEXT3();
#pragma unroll
            for (int i = 0; i < 8; ++i) { RDA(A[7 - i], adr, 7 - i); RDU(UB[i], i); }
            unsigned gb = lw >= 2 ? (unsigned)((tb * 16 + nb) * 128 + (lw - 2) * 32) : 0x80000000u;
            asm volatile("" : "+s"(gb));
            const unsigned gso = (l15 == 0) ? gb + (unsigned)(q4 * 8) : 0x80000000u;
            float gt = 0.f;
            unsigned vxo = 0, vdo = 0, vgo = 0;
            auto gate_out = [&](int i) __attribute__((always_inline)) {
                if (i == 0) gacc += __uint_as_float(__builtin_amdgcn_update_dpp(0u, __float_as_uint(gacc), 0xb1, 0xf, 0xf, false));
                if (i == 1) gacc += __uint_as_float(__builtin_amdgcn_update_dpp(0u, __float_as_uint(gacc), 0x4e, 0xf, 0xf, false));
                if (i == 2) gacc += __uint_as_float(__builtin_amdgcn_update_dpp(0u, __float_as_uint(gacc), 0x141, 0xf, 0xf, false));
                if (i == 3) gacc += __uint_as_float(__builtin_amdgcn_update_dpp(0u, __float_as_uint(gacc), 0x140, 0xf, 0xf, false));
                if (i == 4) gacc = rbf(gacc);
                if (i == 5) gacc = __builtin_amdgcn_exp2f(gacc * -1.44269504088896f);
                if (i == 6) gacc = __builtin_amdgcn_rcpf(1.0f + gacc);
                if (i == 7) gacc = rbf(gacc);
                if (i == 8) gt = __uint_as_float(pkbf(gacc, 1.0f - gacc));
                if (i == 9) __builtin_amdgcn_raw_buffer_store_b64((u32x2){__float_as_uint(gt), tag}, gs, gso, 0, WT);
                asm volatile("" : "+v"(gacc), "+v"(gt));
            };
            auto step = [&](auto E) __attribute__((always_inline)) {
                constexpr int e = decltype(E)::value;
                constexpr int ti = (e == CS0) ? 0 : (e == CS1) ? 1 : (e == CS2) ? 2 : -1, tj = ti < 0 ? 0 : ti;
                constexpr unsigned char GA[36] = {0x66, 0x77, 0x76, 0x44, 0x55, 0x54, 0x65, 0x64, 0x75, 0x74, 0x22, 0x33, 0x32, 0x43, 0x42, 0x53, 0x52, 0x63, 0x62, 0x73, 0x72,
                                                  0x00, 0x11, 0x10, 0x21, 0x20, 0x31, 0x30, 0x41, 0x40, 0x51, 0x50, 0x61, 0x60, 0x71, 0x70};
                const char* sb_ = (const char*)Cre + COFF(wid, tj + 1);
                const char* sc_ = (const char*)Cim + COFF(wid, tj + 1);
#pragma unroll
                for (int q = 0; q < 36; ++q) {
                    const int g = GA[q] >> 4, a = GA[q] & 15;
                    MF(cv[g], A[a], UB[g - a]); SCHED();
                    if (e < 7) {
                        if (a == 1) RDU(UB[g - 1], g - 1);
                        if (q == 35) RDU(UB[7], 7);
                        if (g == 7) RDA(A[a], adr + 64 * (e + 1), a);
                        if (q == 11) UNEXT1();
                        if (q == 13) UNEXT2();
                        if (q == 15) UNEXT3();
                    }
                    if (ti >= 0) {
                        const int n = (q >= 3 && q <= 7) ? q - 3 : q == 10 ? 5 : q == 12 ? 6 : q == 14 ? 7 : -1;
                        if (q == 3) { if (ti == 0) WAIT_VM(1); else if (e == XE + 1) WAIT_VM(7); else WAIT_VM(0); }
                        if (n >= 0) CRD(tj, n & 7);
                        if (q == 18) CPIN(tj);
                        if (q == 18 || q == 23) asm volatile("s_add_u32 m0, %3, %4\n\ts_nop 0\n\tglobal_load_lds_dwordx4 %0, %2\n\tglobal_load_lds_dwordx4 %1, %2 offset:1024"
                                                             :: "v"(vB0), "v"(vB1), "s"(q == 18 ? sb_ : sc_), "s"(cst0), "i"(q == 18 ? 0 : 4096) : "memory", "scc");
                        if (q == 21 || q == 25) asm volatile("global_load_lds_dwordx4 %0, %2 offset:2048\n\tglobal_load_lds_dwordx4 %1, %2 offset:3072"
                                                             :: "v"(vB0), "v"(vB1), "s"(q == 21 ? sb_ : sc_) : "memory");
                    }
                    if (e == 0 && q >= 3 && q <= 7) gate_out(q - 3);
                    if (e == 0 && q >= 10 && q <= 18 && !(q & 1)) gate_out(5 + (q - 10) / 2);
                    if (e == 0 && q == 4) CTILE(wid, 0, cst0);
                    if (e == XE && q >= 23 && q <= 35 && (q & 1)) {
                        const int i = (q - 23) / 2;
                        const __amdgpu_buffer_rsrc_t xs = __builtin_amdgcn_make_buffer_rsrc((void*)x, 0, SEQ * DM * 2, 0x00020000);
                        const __amdgpu_buffer_rsrc_t ks = __builtin_amdgcn_make_buffer_rsrc((void*)dskip, 0, DM * 2, 0x00020000);
                        if (i < 4) asm volatile("buffer_load_dwordx2 %0, %1, %2, %3 offen offset:%4" : "=&v"(xx[(i >> 1) & 1][i & 1]) : "v"(vxo), "s"(xs), "s"((TR((i >> 1) & 1) * DM + d_base) * 2), "i"((i & 1) * 2048) : "memory");
                        if (i == 4) asm volatile("buffer_load_dwordx2 %0, %1, %2, %3 offen" : "=&v"(dk2) : "v"(vdo), "s"(ks), "s"(d_base * 2) : "memory");
                        if (i >= 5) asm volatile("buffer_load_dwordx4 %0, %1, %2, %3 offen sc1" : "=&v"(gv[(i - 5) & 1]) : "v"(vgo), "s"(gs), "s"(GSO((i - 5) & 1)) : "memory");
                    }
                    if (e == XE && q == 16) { vxo = (unsigned)(q4 * 32 * DM * 2 + l15 * 8); vdo = (unsigned)(l15 * 8); asm volatile("" : "+v"(vxo), "+v"(vdo)); }
                    if (e == XE && q == 17) { vgo = GV(q4); asm volatile("" : "+v"(vgo)); }
                    if (e == 6 && q == 30) hpv = __hip_atomic_load(hfp, RLX_AGENT);
                    SCHED();
                }
            };
            unroll<8>(step);
            WAIT_VM(0);
            XPIN();
        }
        PAD8(cv[0], cv[1], cv[2], cv[3]); PIN4(cv[4], cv[5], cv[6], cv[7]);
        STAMPT(dbg, 11, 384);
        const unsigned vh = (unsigned)((lane ^ 2) * 16);
        if (!__all(hpv == tag)) HWAIT();
        asm volatile("" ::: "memory");
#pragma unroll
        for (int f = 0; f < DEPTH; ++f) LOADF(f);
        STAMPT(dbg, 25, 384);
        {
            const __amdgpu_buffer_rsrc_t yrs = __builtin_amdgcn_make_buffer_rsrc(y2T, 0, DM * SEQ * 2, 0x00020000);
            const unsigned yo = (unsigned)(d * 4096 + (q4 & 1) * 512 + (l15 >> 3) * 256 + (q4 >> 1) * 128 + (l15 & 7) * 16);
#pragma unroll
            for (int g = 0; g < 8; g += 2) {
                const auto r0 = __builtin_amdgcn_permlane16_swap(pkbf(cv[g][0], cv[g][1]), pkbf(cv[g + 1][0], cv[g + 1][1]), false, false);
                const auto r1 = __builtin_amdgcn_permlane16_swap(pkbf(cv[g][2], cv[g][3]), pkbf(cv[g + 1][2], cv[g + 1][3]), false, false);
                __builtin_amdgcn_raw_buffer_store_b128((u32x4){r0[0], r1[0], r0[1], r1[1]}, yrs, yo + (unsigned)(g * 512), 0, WT);
            }
        }
        asm volatile("" ::: "memory");
        STAMPT(dbg, 15, 384);
        __builtin_amdgcn_s_setprio(PRIO2_L);
    }
    {
        const int lane = fresh_lane(), l15 = lane & 15, q4 = lane >> 4;
        const int ring = wid >> 2;
        const unsigned va = (unsigned)((lane ^ (ring * 2)) * 16), vh = va;
        const unsigned tag = lds_word(smem, TAG_OFF);
        const int cb0 = ring ? KC_OFF : GA_OFF, cst = ring ? CV_W : 8192;
        f32x4 ac[2][4];
        unsigned ca[2], fa[2], fl = 0, fv = 0;
#define CHAIN(u) do { const int c_ = (wid - ((u) >> 1)) & 3; ca[(u) & 1] = va + (unsigned)(cb0 + c_ * cst + ((u) & 1) * 4096); \
                      fa[(u) & 1] = (unsigned)(RF_OFF + ring * 32 + ((u) & 1) * 16 + c_ * 4); asm volatile("" : "+v"(ca[(u) & 1]), "+v"(fa[(u) & 1])); } while (0)
#define RDIN(dst, a, f_) asm volatile("ds_read_b32 %0, %5\n\tds_read_b128 %1, %6\n\tds_read_b128 %2, %6 offset:1024\n\tds_read_b128 %3, %6 offset:2048\n\tds_read_b128 %4, %6 offset:3072" \
                     : "=&v"(fl), "=&v"(dst[0]), "=&v"(dst[1]), "=&v"(dst[2]), "=&v"(dst[3]) : "v"(f_), "v"(a) : "memory")
#define LWAIT(dst) asm volatile("s_waitcnt lgkmcnt(0)" : "+v"(fl), "+v"(dst[0]), "+v"(dst[1]), "+v"(dst[2]), "+v"(dst[3]) :: "memory")
        CHAIN(0); CHAIN(1);
        WAIT_LGKM0();
        CPIN(0); CPIN(1); CPIN(2);
        CREAD(3);
        int tr[2] = {TR(0), TR(1)};
        unsigned gw[2][2];
        {
            unsigned spins = 0;
            while (__builtin_expect(!__all((gv[0].y == tag) & (gv[0].w == tag) & (gv[1].y == tag) & (gv[1].w == tag)), 0)) {
                if (++spins > SPIN_MAX) break;
#pragma unroll
                for (int r = 0; r < 2; ++r) asm volatile("s_nop 4\n\tbuffer_load_dwordx4 %0, %1, %2, %3 offen sc1\n\ts_waitcnt vmcnt(0)" : "=&v"(gv[r]) : "v"(GV(q4)), "s"(gs), "s"(__builtin_amdgcn_readfirstlane(GSO(r))) : "memory");
            }
            gw[0][0] = gv[0].x; gw[0][1] = gv[0].z; gw[1][0] = gv[1].x; gw[1][1] = gv[1].z;
            PIN4(gw[0][0], gw[0][1], gw[1][0], gw[1][1]);
        }
        unsigned cpv = 0, yy[2][4];
        f32x2 zz[2][4];
        unsigned exo = 0, exi = 0, efo = 0, efi = 0;
        auto send = [&](int r) __attribute__((always_inline)) {
            const unsigned one = 1u;
#pragma unroll
            for (int n = 0; n < 4; ++n) { const f32x2 v = {ac[r][n][2], ac[r][n][3]}; asm volatile("ds_write_b64 %0, %1 offset:%2" :: "v"(exo), "v"(v), "i"(r * 4096 + n * 512) : "memory"); }
            asm volatile("ds_write_b32 %0, %1 offset:%2" :: "v"(efo), "v"(one), "i"(r * 4) : "memory");
        };
        STAMPT(dbg, 5, 0);
        auto body = [&](auto F) __attribute__((always_inline)) {
            constexpr int f = decltype(F)::value;
            constexpr int u = f >> 3, k = f & 7;
            f32x4* an = ac[u & 1];
            f32x4* ao = ac[(u + 1) & 1];
#pragma unroll
            for (int n = 0; n < 4; ++n) {
                if (k == 0 && u < 2) MF0(an[n], af[f % DEPTH], cf_[k][n]); else MF(an[n], af[f % DEPTH], cf_[k][n]);
                SCHED();
                const int g = k * 4 + n;
                if (u >= 1 && u < 7) {
                    if (g == 1) { fv = ((u - 1) >> 1) + 1; asm volatile("" : "+v"(fv)); }
                    if (g == 4 || g == 6 || g == 8 || g == 10) asm volatile("ds_write_b128 %0, %1 offset:%2" :: "v"(ca[(u + 1) & 1]), "v"(ao[(g - 4) / 2]), "i"((g - 4) * 512) : "memory");
                    if (g == 12) asm volatile("ds_write_b32 %0, %1" :: "v"(fa[(u + 1) & 1]), "v"(fv) : "memory");
                    if (g == 18) CHAIN(u + 1);
                    if (g == 20) asm volatile("ds_read_b32 %0, %1" : "=&v"(fl) : "v"(fa[(u + 1) & 1]) : "memory");
                    if (g > 20 && g <= 24) asm volatile("ds_read_b128 %0, %1 offset:%2" : "=&v"(ao[g - 21]) : "v"(ca[(u + 1) & 1]), "i"((g - 21) * 1024) : "memory");
                    if (g == 30) {
                        LWAIT(ao);
                        if (__builtin_expect(__builtin_amdgcn_readfirstlane(fl) != (unsigned)((u + 1) >> 1), 0)) {
                            unsigned spins = 0;
#pragma unroll 1
                            do {
                                __builtin_amdgcn_s_sleep(1);
                                RDIN(ao, ca[(u + 1) & 1], fa[(u + 1) & 1]); LWAIT(ao);
                                if (++spins > SPIN_MAX * 16u) break;
                            } while (__builtin_amdgcn_readfirstlane(fl) != (unsigned)((u + 1) >> 1));
                        }
                    }
                }
                if (u == 7 && g == 6) send(0);
                if (f >= ZF && f < ZF + 4) {
                    constexpr int r = ((f - ZF) >> 1) & 1, j = (f - ZF) & 1;
                    const unsigned xw = xx[r][j][n >> 1], dw = dk2[n >> 1];
                    const float xf = (n & 1) ? hi16(xw) : lo16(xw), df = (n & 1) ? hi16(dw) : lo16(dw), yf = j ? hi16(yy[r][n]) : lo16(yy[r][n]);
                    zz[r][n][j] = xf * fmaf(hi16(gw[r][j]), yf, lo16(gw[r][j]) * df);
                    asm volatile("" : "+v"(zz[r][n]));
                }
                if (f == ZF + 4) {
                    zz[0][n] = (f32x2){fmaf(lo16(gw[0][0]), ac[0][n][0], zz[0][n][0]), fmaf(lo16(gw[0][1]), ac[0][n][1], zz[0][n][1])};
                    asm volatile("" : "+v"(zz[0][n]));
                }
                SCHED();
            }
            if (f + DEPTH < 64) LOADF(f + DEPTH);
            if (f == DEPTH + 1 && ring) {
                WAIT_VM(6);
                if (lane == 0) __hip_atomic_store((gu32*)(ws + WS_CFLAG) + cb * 32 + (wid & 3), tag, RLX_AGENT);
                SCHED();
            }
            if (f == YPOLL && ring) { const int l15 = fresh_lane() & 15; cpv = __hip_atomic_load((gu32*)(ws + WS_CFLAG) + (nb * 16 + (wid & 3) * 4 + (l15 >> 2)) * 32 + (l15 & 3), RLX_AGENT); SCHED(); }
            if (f == YPOLL + DEPTH && ring) {
                const int ln = fresh_lane(), l15 = ln & 15, q4 = ln >> 4;
                if (__builtin_expect(!__all(cpv == tag), 0)) {
                    unsigned spins = 0;
                    while (!__all(__hip_atomic_load((gu32*)(ws + WS_CFLAG) + (nb * 16 + (wid & 3) * 4 + (l15 >> 2)) * 32 + (l15 & 3), RLX_AGENT) == tag)) {
                        if (++spins > SPIN_MAX) break;
                        __builtin_amdgcn_s_sleep(1);
                    }
                }
                const unsigned lx = lbase + LA_OFF + (wid & 3) * 8192;
#pragma unroll
                for (int i = 0; i < 4; ++i) {
                    const unsigned vo = (unsigned)((d_base + (wid & 3) * 16 + i * 4 + q4) * 4096 + tb * 256 + ((l15 ^ ((wid & 3) * 4 + i)) * 16));
                    GLDS_SC1(y2T, vo, lx, i * 1024);
                }
                SCHED();
            }
            if (f == YPOLL + 2 * DEPTH + 1 && ring) {
                WAIT_VM(6);
                SETW(YFLAG_OFF, wid & 3, 1u);
                SCHED();
            }
            if (f == 55) {
                const int ln = fresh_lane(), c_ = (wid - 3) & 3;
                const int eg = GA_OFF + c_ * 8192, el = KC_OFF + c_ * CV_W;
                exo = (unsigned)((ring ? el : eg) + ln * 8);
                exi = (unsigned)((ring ? eg : el) + ln * 8);
                efo = (unsigned)(EF_OFF + wid * 8); efi = (unsigned)(EF_OFF + (wid ^ 4) * 8);
                PIN4(exo, exi, efo, efi);
                SCHED();
            }
            if (f == ZF - 2) {
                const int ln = fresh_lane(), l15 = ln & 15, q4 = ln >> 4;
                wait_lds4(smem, YFLAG_OFF);
#pragma unroll
                for (int r = 0; r < 2; ++r) {
                    const int jb = JB(6 + r);
                    const int ya = LA_OFF + (l15 >> 2) * 8192 + (l15 & 3) * 1024 + (((((jb >> 1) & 1) * 8 + (jb >> 2) + q4 * 2) ^ l15) * 16) + (jb & 1) * 8 + ring * 4;
#pragma unroll
                    for (int e = 0; e < 4; ++e) yy[r][e] = *(const unsigned*)(smem + ya + e * 256);
                }
                SCHED();
            }
        };
        unroll<64>(body);
        PAD8(ac[1][0], ac[1][1], ac[1][2], ac[1][3]);
        send(1);
        STAMPT(dbg, 6, 0);
        const __amdgpu_buffer_rsrc_t ors = __builtin_amdgcn_make_buffer_rsrc(out, 0, SEQ * DM * 4, 0x00020000);
        const unsigned ov = (unsigned)((lane >> 4) * (32 * DM * 4) + (lane & 15) * 16);
#pragma unroll
        for (int r = 0; r < 2; ++r) {
            f32x2 in[4];
#define EXRD() asm volatile("ds_read_b32 %0, %5 offset:%7\n\tds_read_b64 %1, %6 offset:%8\n\tds_read_b64 %2, %6 offset:%9\n\tds_read_b64 %3, %6 offset:%10\n\tds_read_b64 %4, %6 offset:%11" \
                             : "=&v"(fl), "=&v"(in[0]), "=&v"(in[1]), "=&v"(in[2]), "=&v"(in[3]) \
                             : "v"(efi), "v"(exi), "i"(r * 4), "i"(r * 4096), "i"(r * 4096 + 512), "i"(r * 4096 + 1024), "i"(r * 4096 + 1536) : "memory")
            EXRD();
            if (r == 0) {
#pragma unroll
                for (int n = 0; n < 4; ++n) {
                    zz[1][n] = (f32x2){fmaf(lo16(gw[1][0]), ac[1][n][0], zz[1][n][0]), fmaf(lo16(gw[1][1]), ac[1][n][1], zz[1][n][1])};
                    asm volatile("" : "+v"(zz[1][n]));
                }
            }
            LWAIT(in);
            if (__builtin_expect(__builtin_amdgcn_readfirstlane(fl) == 0u, 0)) {
                unsigned spins = 0;
#pragma unroll 1
                do {
                    EXRD(); LWAIT(in);
                    if (++spins > SPIN_MAX * 16u) break;
                } while (__builtin_amdgcn_readfirstlane(fl) == 0u);
            }
            f32x4 o[2];
#pragma unroll
            for (int j = 0; j < 2; ++j)
#pragma unroll
                for (int e = 0; e < 4; ++e) o[j][e] = fmaf(lo16(gw[r][j]), in[e][j], zz[r][e][j]);
#pragma unroll
            for (int j = 0; j < 2; ++j)
                __builtin_amdgcn_raw_buffer_store_b128(__builtin_bit_cast(u32x4, o[j]), ors, ov + (unsigned)(((tr[r] + j) * DM + d_base) * 4), 0, WT);
        }
        STAMPT(dbg, 7, 0);
        if (wid == 0) {
            WAIT_VM(0);
            if (lane == 0) { __hip_atomic_store((gu64*)dbg + b * 16, pr0, RLX_AGENT); __hip_atomic_store((gu64*)dbg + b * 16 + 1, __builtin_amdgcn_s_memrealtime(), RLX_AGENT); }
        }
        if (b == 100 && wid == 0) {
            while (__builtin_amdgcn_s_memrealtime() - pr0 < 3600) __builtin_amdgcn_s_sleep(1);
            unsigned long long mn = ~0ull, mx = 0, me = 0, md = 0;
            for (int i = 0; i < 256; ++i) { const unsigned long long v = __hip_atomic_load((gu64*)dbg + i * 16, RLX_AGENT), e = __hip_atomic_load((gu64*)dbg + i * 16 + 1, RLX_AGENT);
                mn = v < mn ? v : mn; mx = v > mx ? v : mx; me = e > me ? e : me; md += e - v; }
            unsigned long long T = (PROBE == 1) ? 10 * (mx - mn) : (PROBE == 2) ? 10 * (me - mn - 2000) : (PROBE == 3) ? 10 * (md / 256 - 2000) : 0;
            if (T > 40000) T = 40000;
            while (__builtin_amdgcn_s_memrealtime() - mn < 30000 + T) __builtin_amdgcn_s_sleep(1);
        }
        if (tb == 15 && nb == 0 && wid == 0 && lane == 0) __hip_atomic_store((gu32*)(ws + WS_EPOCH), tag, RLX_AGENT);
    }
}

extern "C" void kernel_launch(void* const* d_in, const int* in_sizes, int n_in,
                              void* d_out, int out_size, void* d_ws, size_t ws_size,
                              hipStream_t stream) {
    const u16* x = (const u16*)d_in[0];
    const u16* lre = (const u16*)d_in[1];
    const u16* lim = (const u16*)d_in[2];
    const u16* Bre = (const u16*)d_in[3];
    const u16* Bim = (const u16*)d_in[4];
    const u16* Cre = (const u16*)d_in[5];
    const u16* Cim = (const u16*)d_in[6];
    const u16* logdt = (const u16*)d_in[7];
    const u16* dskip = (const u16*)d_in[8];
    const u16* kin = (const u16*)d_in[10];
    const u16* vin = (const u16*)d_in[11];
    const u16* shiftk = (const u16*)d_in[12];
    const u16* ssmk = (const u16*)d_in[13];
    const u16* ssmD = (const u16*)d_in[14];
    const u16* gatew = (const u16*)d_in[15];
    float* out = (float*)d_out;

    char* ws = (char*)d_ws;

    hipFuncSetAttribute((const void*)k1_kernel, hipFuncAttributeMaxDynamicSharedMemorySize, K1_LDS);

    k1_kernel<<<256, 512, K1_LDS, stream>>>(x, Bre, Bim, kin, vin, shiftk, gatew, ws, lre, lim, logdt, ssmk, ssmD, Cre, Cim, dskip, out);
}
#if defined(__HIP_DEVICE_COMPILE__)
#pragma clang attribute pop
#endif
```

```cpp
#if defined(__HIP_DEVICE_COMPILE__)
#pragma clang attribute push(__attribute__((target("no-packed-fp32-ops"))), apply_to = function)
#endif
#include <hip/hip_runtime.h>
#include <hip/hip_bf16.h>
#include <stdint.h>

#define TIDX ((int)__builtin_amdgcn_workitem_id_x())
#define BIDX ((int)__builtin_amdgcn_workgroup_id_x())
typedef unsigned short u16;
typedef __attribute__((ext_vector_type(8))) short bf16x8;
typedef __attribute__((ext_vector_type(4))) float f32x4;
typedef __attribute__((ext_vector_type(2))) float f32x2;
typedef __attribute__((ext_vector_type(2))) __bf16 bf16x2;
typedef __attribute__((ext_vector_type(4))) unsigned u32x4;
typedef __attribute__((ext_vector_type(2))) unsigned u32x2;
typedef __attribute__((address_space(1))) unsigned long long gu64;
typedef __attribute__((address_space(1))) unsigned gu32;
typedef __attribute__((address_space(3))) void lds_void;
typedef __attribute__((address_space(3))) unsigned lds_u32;
typedef __attribute__((ext_vector_type(4))) short s16x4;
typedef __attribute__((address_space(3))) s16x4 lds_s16x4;
#define TRD(a) __builtin_amdgcn_ds_read_tr16_b64_v4i16((lds_s16x4*)(size_t)(a))

#define DBGON 0
#define STAMPT(buf, k, t) do { } while (0)
#define CODE_LINES 262
#define SEQ 2048
#define DM 1024
#define SPIN_MAX (1u << 18)
#define PRIO_G 2
#define PSLEEP 4
#define PRIO_L 3
#define WT 16

__device__ __forceinline__ float bf2f(u16 v) { return __uint_as_float(((unsigned)v) << 16); }
__device__ __forceinline__ u16 f2bf(float f) { __bf16 b = (__bf16)f; return __builtin_bit_cast(u16, b); }
__device__ __forceinline__ float rbf(float f) { return bf2f(f2bf(f)); }
__device__ __forceinline__ unsigned pkbf(float lo, float hi) { return __builtin_bit_cast(unsigned, __builtin_convertvector((f32x2){lo, hi}, bf16x2)); }
__device__ __forceinline__ float lo16(unsigned u) { return __uint_as_float(u << 16); }
__device__ __forceinline__ float hi16(unsigned u) { return __uint_as_float(u & 0xffff0000u); }

struct cf { float r, i; };
__device__ __forceinline__ cf cmul(cf a, cf b) { return {a.r * b.r - a.i * b.i, a.r * b.i + a.i * b.r}; }
__device__ __forceinline__ cf cfma(cf a, cf b, cf c) {
    return {fmaf(a.r, b.r, fmaf(-a.i, b.i, c.r)), fmaf(a.r, b.i, fmaf(a.i, b.r, c.i))};
}
__device__ __forceinline__ f32x4 splat4(float v) { return (f32x4){v, v, v, v}; }
__device__ __forceinline__ f32x4 fma4(f32x4 a, f32x4 b, f32x4 c) { return __builtin_elementwise_fma(a, b, c); }
__device__ __forceinline__ f32x4 mul4(f32x4 a, f32x4 b) { return a * b; }

__device__ __forceinline__ float red32(float a, float b) {
    const auto r = __builtin_amdgcn_permlane32_swap(__float_as_uint(a), __float_as_uint(b), false, false);
    return __uint_as_float(r[0]) + __uint_as_float(r[1]);
}
__device__ __forceinline__ float red16(float x) {
    const auto r = __builtin_amdgcn_permlane16_swap(__float_as_uint(x), __float_as_uint(x), false, false);
    return __uint_as_float(r[0]) + __uint_as_float(r[1]);
}
__device__ __forceinline__ int fresh_lane() {
    int l;
    asm volatile("v_mbcnt_lo_u32_b32 %0, -1, 0\n\tv_mbcnt_hi_u32_b32 %0, -1, %0" : "=v"(l));
    return l;
}
#define BAR() __builtin_amdgcn_s_barrier()
#define WAIT_VM(n) asm volatile("s_waitcnt vmcnt(" #n ")" ::: "memory")
#define WAIT_LGKM0() __builtin_amdgcn_s_waitcnt(0xC07F)
#define SCHED() __builtin_amdgcn_sched_barrier(0)
#define MF(c, a, b) asm volatile("v_mfma_f32_16x16x32_bf16 %0, %1, %2, %0" : "+v"(c) : "v"(a), "v"(b))
#define PAD8(c0, c1, c2, c3) asm volatile("s_nop 7" : "+v"(c0), "+v"(c1), "+v"(c2), "+v"(c3))
#define PIN4(c0, c1, c2, c3) asm volatile("" : "+v"(c0), "+v"(c1), "+v"(c2), "+v"(c3))
#define GLDS_S(sbase, voff, sl, IMM) \
    asm volatile("s_add_u32 m0, %2, %3\n\ts_nop 0\n\tglobal_load_lds_dwordx4 %0, %1" \
                 :: "v"(voff), "s"(sbase), "s"(sl), "i"(IMM) : "memory", "scc")
#define GLDS_K(sbase, voff, sl, IMM) \
    asm volatile("s_add_u32 m0, %2, %3\n\ts_nop 0\n\tglobal_load_lds_dwordx4 %0, %1 nt" \
                 :: "v"(voff), "s"(sbase), "s"(sl), "i"(IMM) : "memory", "scc")
#define RLX_AGENT __ATOMIC_RELAXED, __HIP_MEMORY_SCOPE_AGENT

#define UNITS_A 0x0756075607560560ull
#define UNITS_B 0x1432143214321432ull
#define RA(t) ((int)((UNITS_A >> (4 * (t))) & 15) * 16384)
#define RB(t) ((int)((UNITS_B >> (4 * (t))) & 15) * 16384)
#define LA_OFF 0
#define GA_OFF 32768
#define CV_COPY 4160
#define CV_W (4 * CV_COPY)
#define KC_OFF 65536
#define ST_OFF 114688
#define ST_V 8832
#define ST_SZ (ST_V + 8 * 1056)
#define U_OFF (KC_OFF + 16 * CV_COPY)
#define HT_OFF (U_OFF + 256 + 16384)
#define GW_OFF (HT_OFF + 8192)
#define TAG_OFF (GW_OFF + 2048)
#define UFLAG_OFF (TAG_OFF + 16)
#define G1D_OFF (UFLAG_OFF + 16)
#define MISC_OFF (G1D_OFF + 16)
#define YFLAG_OFF (MISC_OFF + 16)
#define RF_OFF (YFLAG_OFF + 16)
#define EF_OFF (RF_OFF + 64)
#define COK_OFF (EF_OFF + 64)
#define NFLAGW 52
#define DUMMY_OFF (COK_OFF + 16)
#define K1_LDS (DUMMY_OFF + 256)
#define WS_GRAN 0
#define WS_KVFLAG (256u << 10)
#define WS_EPOCH (WS_KVFLAG + 65536)
#define WS_CFLAG (WS_EPOCH + 4096)
#define WS_HFLAG (384u << 10)
#define WS_GATE (512u << 10)

#define CODE_TOUCH(vl) asm volatile("s_getpc_b64 s[98:99]\n\ts_add_u32 s98, s98, k1_kernel@rel32@lo+4\n\ts_addc_u32 s99, s99, k1_kernel@rel32@hi+12\n\t" \
                                    "s_mov_b32 m0, %1\n\ts_nop 0\n\tglobal_load_lds_dword %0, s[98:99]" :: "v"((unsigned)(vl) * 128u), "i"(DUMMY_OFF) : "s98", "s99", "scc", "memory")
#define RD128(a, off) (*(const __attribute__((address_space(3))) bf16x8*)(size_t)(unsigned)((a) + (off)))
#define RDA(dst, A, F) do { const s16x4 lo_ = TRD((A) + 512 * (F) + 8); SCHED(); const s16x4 hi_ = TRD((A) + 512 * (F)); SCHED(); \
                            dst = __builtin_shufflevector(lo_, hi_, 0, 1, 2, 3, 4, 5, 6, 7); } while (0)
#define TOUCH(sbase, voff) asm volatile("s_mov_b32 m0, %2\n\ts_nop 0\n\tglobal_load_lds_dword %0, %1" :: "v"(voff), "s"(sbase), "i"(DUMMY_OFF) : "memory")
#define FLAGD(off) do { unsigned long long pc_; asm volatile("s_getpc_b64 %0\n\ts_mov_b32 m0, %2\n\ts_mov_b64 exec, 1\n\tglobal_load_lds_dword %1, %0\n\ts_mov_b64 exec, -1" \
                                                             : "=&s"(pc_) : "v"(0u), "s"(__builtin_amdgcn_readfirstlane(off)) : "memory"); } while (0)
#define GLDS_SC1(sbase, voff, sl, IMM) \
    asm volatile("s_add_u32 m0, %2, %3\n\ts_nop 0\n\tglobal_load_lds_dwordx4 %0, %1 sc1" \
                 :: "v"(voff), "s"(sbase), "s"(sl), "i"(IMM) : "memory", "scc")
__device__ __forceinline__ unsigned mv(unsigned v) { unsigned r; asm("v_mov_b32 %0, %1" : "=v"(r) : "v"(v)); return r; }
__device__ __forceinline__ unsigned al(unsigned hi, unsigned lo) { unsigned r; asm volatile("v_alignbit_b32 %0, %1, %2, 16" : "=v"(r) : "v"(hi), "v"(lo)); return r; }
__device__ __forceinline__ u32x2 pred2(const u32x4& a, unsigned f0, unsigned f1) {
    return (u32x2){(unsigned)__builtin_amdgcn_update_dpp((int)f0, (int)a.z, 0x138, 0xf, 0xf, false), (unsigned)__builtin_amdgcn_update_dpp((int)f1, (int)a.w, 0x138, 0xf, 0xf, false)};
}
#define RL63(v) ((unsigned)__builtin_amdgcn_readlane((int)(v), 63))
__device__ __forceinline__ void wait_lds4(char* smem, int off) {
    const unsigned a = (unsigned)(size_t)(lds_void*)smem + off;
    unsigned spins = 0;
    for (;;) {
        u32x4 f;
        asm volatile("ds_read_b128 %0, %1\n\ts_waitcnt lgkmcnt(0)" : "=&v"(f) : "v"(a) : "memory");
        if (__builtin_amdgcn_readfirstlane((f.x != 0u) & (f.y != 0u) & (f.z != 0u) & (f.w != 0u))) break;
        if (++spins > SPIN_MAX * 16u) break;
        __builtin_amdgcn_s_sleep(1);
    }
}
__device__ __forceinline__ unsigned lds_word(char* smem, int off) {
    const unsigned a = (unsigned)(size_t)(lds_void*)smem + off;
    unsigned f;
    asm volatile("ds_read_b32 %0, %1\n\ts_waitcnt lgkmcnt(0)" : "=&v"(f) : "v"(a) : "memory");
    return __builtin_amdgcn_readfirstlane(f);
}
__device__ __forceinline__ void wait_lds1(char* smem, int off) {
    unsigned spins = 0;
    while (lds_word(smem, off) == 0u) {
        if (++spins > SPIN_MAX * 16u) break;
        __builtin_amdgcn_s_sleep(1);
    }
}
#define SETW(off, i, v) __hip_atomic_store((lds_u32*)(smem + (off)) + (i), (v), __ATOMIC_RELAXED, __HIP_MEMORY_SCOPE_WORKGROUP)

#define PRIO_CV 1
#define PRIO_KV 3
#define CS0 1
#define CS1 3
#define CS2 5
#define XE 4
#define YPOLL 32
#define ZF 59
template <int I> struct IC { static constexpr int value = I; };
template <int N, int I = 0, class F> __device__ __forceinline__ void unroll(F& f) { if constexpr (I < N) { f(IC<I>{}); unroll<N, I + 1>(f); } }
#define MF0(c, a, b) asm volatile("v_mfma_f32_16x16x32_bf16 %0, %1, %2, 0" : "=&v"(c) : "v"(a), "v"(b))
#define DEPTH 6
#define PRIO2_G 0
#define PRIO2_L 1
__device__ __forceinline__ cf pick4(int q, cf a1, cf a2, cf a3) {
    return {q == 0 ? 1.f : q == 1 ? a1.r : q == 2 ? a2.r : a3.r, q == 0 ? 0.f : q == 1 ? a1.i : q == 2 ? a2.i : a3.i};
}
__device__ __forceinline__ void idle_mfma(unsigned a, unsigned b, int n) {
    u32x4 p = {a * 2654435761u, b * 40503u, a * 2246822519u, b * 3266489917u};
    p = (p & 0xbfffbfffu) | 0x3c003c00u;
    u32x4 q = p.yzwx ^ 0x01230456u;
    f32x4 w;
    asm volatile("s_nop 1" : "+v"(p), "+v"(q));
    MF0(w, p, q);
#pragma unroll 1
    for (int i = 0; i < n; ++i) { MF(w, q, p); MF(w, p, p); MF(w, q, q); MF(w, p, q); }
    asm volatile("s_nop 7" : "+v"(w));
}
#define PINC(c) asm volatile("" : "+v"((c).r), "+v"((c).i))

extern "C" __global__ __launch_bounds__(512, 2) void k1_kernel(
        const u16* __restrict__ x, const u16* __restrict__ Bre, const u16* __restrict__ Bim,
        const u16* __restrict__ kin, const u16* __restrict__ vin, const u16* __restrict__ shiftk, const u16* __restrict__ gatew, char* ws,
        const u16* __restrict__ lre, const u16* __restrict__ lim, const u16* __restrict__ logdt,
        const u16* __restrict__ ssmk, const u16* __restrict__ ssmD,
        const u16* __restrict__ Cre, const u16* __restrict__ Cim, const u16* __restrict__ dskip, float* __restrict__ out) {
    extern __shared__ __attribute__((aligned(16))) char smem[];
#define h ((u16*)(ws + (1u << 20)))
#define kvT ((u16*)(ws + (9u << 20)))
#define y2T ((u16*)(ws + (13u << 20)))
#define dbg ((unsigned long long*)(ws + (64u << 20)))
    const int tid = TIDX;
    if (tid < 40 && tid + ((BIDX >> 3) & 7) * 40 < CODE_LINES) CODE_TOUCH(tid + ((BIDX >> 3) & 7) * 40);
    const int lane = tid & 63;
    int wid = __builtin_amdgcn_readfirstlane(tid >> 6);
    const int b = BIDX;
    const int xcd = b & 7, idx = b >> 3;
    const int tb = ((xcd >> 1) ^ (xcd >> 2)) * 4 + (idx & 3), nb = (xcd & 1) * 8 + (idx >> 2);
    const int cb = xcd * 32 + idx;
    const int t_base = tb * 128, p_base = nb * 64, d_base = nb * 64;
    const int l15 = lane & 15, q4 = lane >> 4;
    unsigned stv = 0, sth = 0;
    STAMPT(dbg, 0, 0);
    const unsigned lbase = (unsigned)(size_t)(lds_void*)smem;
    const int rot1 = (xcd >> 1) * 4 + (xcd & 1) * 8 + (idx >> 2) + (idx & 1);
    unsigned voA[4] = {0u, 0u, 0u, 0u}, voB[4] = {0u, 0u, 0u, 0u}, lw0 = 0;
    if (wid == 4 || wid == 5) {
        __builtin_amdgcn_s_setprio(PRIO_L);
        const int lh = wid - 4;
        const unsigned ch = (unsigned)(((lane & 7) ^ (lane >> 4)) * 16);
        voA[0] = (unsigned)(t_base + lh * 16 + (lane >> 5) * 32 + ((lane >> 3) & 3)) * 2048u + ch;
        voA[1] = (voA[0] ^ 64u) + (131072u - 1024u);
        voA[2] = voA[0] + 6144u; voA[3] = voA[1] + 6144u;
        voB[0] = (unsigned)(p_base + lh * 32 + (lane >> 3)) * 2048u + ch;
        voB[1] = (voB[0] ^ 64u) + (16384u - 1024u);
        voB[2] = voB[0] + 30720u; voB[3] = voB[1] + 30720u;
        PIN4(voA[0], voA[1], voA[2], voA[3]); PIN4(voB[0], voB[1], voB[2], voB[3]);
#define GLDS4(sbase, v0, v1, v2, v3, sl, IMM) \
            asm volatile("s_add_u32 m0, %5, %6\n\ts_nop 0\n\tglobal_load_lds_dwordx4 %0, %4\n\tglobal_load_lds_dwordx4 %1, %4 offset:1024\n\t" \
                         "global_load_lds_dwordx4 %2, %4 offset:2048\n\tglobal_load_lds_dwordx4 %3, %4 offset:3072" \
                         :: "v"(v0), "v"(v1), "v"(v2), "v"(v3), "s"(sbase), "s"(sl), "i"(IMM) : "memory", "scc")
#define STAGE_A(kt, sl) do { const char* sa_ = (const char*)x + (((kt) + rot1) & 15) * 128; \
            GLDS4(sa_, voA[0], voA[1], voA[2], voA[3], sl, 0); GLDS4(sa_ + 16384, voA[0], voA[1], voA[2], voA[3], sl, 4096); } while (0)
#define STAGE_B(kt, sl) do { const int kr_ = (((kt) + rot1) & 15) * 128; const char* sb_ = (const char*)Bre + kr_; const char* sc_ = (const char*)Bim + kr_; \
            GLDS4(sb_, voB[0], voB[1], voB[2], voB[3], sl, 0); GLDS4(sc_, voB[0], voB[1], voB[2], voB[3], sl, 4096); } while (0)
        lw0 = lbase + lh * 8192;
        STAGE_A(0, lw0 + RA(0)); STAGE_B(0, lw0 + RB(0));
        idle_mfma(voA[0], voB[1], 12);
        STAGE_A(1, lw0 + RA(1)); STAGE_B(1, lw0 + RB(1));
    }
    const int sblk = b >> 3, dblk = b & 7;
    u32x2 tw[4];
    unsigned epoch;
    asm volatile("" : "=v"(tw[0]), "=v"(tw[1]), "=v"(tw[2]), "=v"(tw[3]), "=v"(epoch));
    if (wid >= 6) {
        __builtin_amdgcn_s_setprio(PRIO_L);
        const int vw = wid - 6;
        const int dch = dblk * 128 + vw * 64 + l15;
        const unsigned lk = lbase + ST_OFF + vw * ST_SZ;
        const unsigned vk = (unsigned)((lane >> 3) * DM * 2 + (((lane & 7) ^ ((lane >> 4) & 1)) * 16));
        const char* kb = (const char*)kin + (size_t)(sblk * 64 - 4) * (DM * 2) + dblk * 256 + vw * 128;
#define KLD(n) GLDS_K(kb + (n) * 8 * DM * 2, vk, lk, (n) * 1024 + ((n) >> 1) * 32)
#define VLD(n) GLDS_K(vb + (((n) >> 1) * 16 + ((n) & 1) * 2) * DM * 2, vv_, lk, ST_V + (n) * 1056)
        if (vw == 1) {
            const unsigned vg = (unsigned)((lane * 16 + rot1 * 128) & 2047), lg = lbase + GW_OFF;
            GLDS_S(gatew, vg, lg, 0); GLDS_S(gatew, vg ^ 1024u, lg, 1024);
        }
        asm volatile("global_load_dwordx2 %0, %5, %7\n\tglobal_load_dwordx2 %1, %5, %7 offset:128\n\tglobal_load_dwordx2 %2, %5, %7 offset:256\n\t"
                     "global_load_dwordx2 %3, %5, %7 offset:384\n\tglobal_load_dword %4, %6, %8 sc1"
                     : "+v"(tw[0]), "+v"(tw[1]), "+v"(tw[2]), "+v"(tw[3]), "+v"(epoch) : "v"((unsigned)(dch * 8)), "v"((unsigned)WS_EPOCH), "s"(shiftk), "s"(ws) : "memory");
        if (sblk == 0 && lane < 32) *(u32x4*)(smem + ST_OFF + vw * ST_SZ + lane * 16) = (u32x4){0u, 0u, 0u, 0u};
        if (sblk != 0 || lane >= 32) KLD(0);
        KLD(1); KLD(2); KLD(3); KLD(4); KLD(5); KLD(6); KLD(7);
        if (lane < 32) KLD(8);
        __builtin_amdgcn_s_setprio(0);
    }
    asm volatile("" : "+s"(wid));

    const int wf_ = (nb >> 3) ^ (xcd >> 2), tq = (xcd >> 1) & 1;
#define WSL(w) ((((w) >> 2) ^ wf_) * 4 + ((w) & 3))
#define COFF(w, ti) ((2 * WSL(w) + (((ti) >> 1) ^ tq)) * 128 + ((ti) & 1) * 4096)
    bf16x8 cf_[8][4];
    bf16x8 af[DEPTH];
    int cra, crb;
#define CT_SETUP() const unsigned vB0 = (unsigned)((d_base + (lane >> 3) * 4) * 2048 + (((lane & 7) ^ (lane >> 4)) * 16)), vB1 = (vB0 ^ 64u) + 65536u - 1024u
#define CTILE(w, ti, dst) do { const int co_ = COFF(w, ti); const unsigned ds_ = (dst); \
                               GLDS4((const char*)Cre + co_, vB0, vB1, vB0, vB1, ds_, 0); GLDS4((const char*)Cim + co_, vB0, vB1, vB0, vB1, ds_, 4096); } while (0)
#define CF(ti, n) cf_[4 * ((ti) >> 1) + ((n) >> 1)][2 * ((ti) & 1) + ((n) & 1)]
#define CRD(ti, n) CF(ti, n) = RD128((((n) & 4) ? crb : cra) ^ (((n) & 2) * 16), ((n) & 1) * 2048)
#define CREAD(ti) do { _Pragma("unroll") for (int n_ = 0; n_ < 8; ++n_) CRD(ti, n_); } while (0)
#define CPIN(ti) do { PIN4(CF(ti, 0), CF(ti, 1), CF(ti, 2), CF(ti, 3)); PIN4(CF(ti, 4), CF(ti, 5), CF(ti, 6), CF(ti, 7)); } while (0)
#define CRA(base) ((base) + (q4 >> 1) * 4096 + l15 * 128 + (((q4 & 1) ^ ((l15 >> 1) & 7)) * 16))
    const int rot2 = (idx & 16) >> 2;
#define JB(u) (((((u) ^ (rot2 >> 2)) & 1) << 2) | ((wid - ((u) >> 1) + rot2) & 3))
    const __amdgpu_buffer_rsrc_t hs = __builtin_amdgcn_make_buffer_rsrc((char*)h + (size_t)tb * 524288 + WSL(wid) * 8192, 0, 524288, 0x00020000);
#define LOADF(f) do { af[(f) % DEPTH] = __builtin_bit_cast(bf16x8, __builtin_amdgcn_raw_buffer_load_b128(hs, vh + ((f) & 3) * 1024, JB((f) >> 3) * 65536 + ((((f) >> 2) & 1) ^ tq) * 4096, 16)); SCHED(); } while (0)
#define HPOLL_SETUP() gu32* hfp = (gu32*)(ws + WS_HFLAG) + (tb * 16 + 2 * WSL(wid) + (lane & 1)) * 32
#define HWAIT() do { unsigned spins = 0; \
            while (!__all(__hip_atomic_load(hfp, RLX_AGENT) == tag)) { if (++spins > SPIN_MAX) break; __builtin_amdgcn_s_sleep(PSLEEP); } asm volatile("" ::: "memory"); } while (0)

    const __amdgpu_buffer_rsrc_t gs = __builtin_amdgcn_make_buffer_rsrc(ws + WS_GATE, 0, 32768, 0x00020000);
#define GV(q4) ((unsigned)(((q4) >> 1) * 128 + ((q4) & 1) * 32))
#define GSO(r) (tb * 2048 + JB(6 + (r)) * 256 + (wid >> 2) * 16)
#define TR(r) (t_base + JB(6 + (r)) * 4 + (wid >> 2) * 2)
    u32x2 xx[2][2], dk2;
    u32x4 gv[2];
#define XLOAD() do { \
        const __amdgpu_buffer_rsrc_t xs = __builtin_amdgcn_make_buffer_rsrc((void*)x, 0, SEQ * DM * 2, 0x00020000); \
        const __amdgpu_buffer_rsrc_t ks = __builtin_amdgcn_make_buffer_rsrc((void*)dskip, 0, DM * 2, 0x00020000); \
        dk2 = __builtin_amdgcn_raw_buffer_load_b64(ks, (unsigned)(l15 * 8), d_base * 2, 0); \
        _Pragma("unroll") for (int q = 0; q < 4; ++q) \
            xx[q >> 1][q & 1] = __builtin_amdgcn_raw_buffer_load_b64(xs, (unsigned)(q4 * 32 * DM * 2 + l15 * 8 + (q & 1) * DM * 2), (TR(q >> 1) * DM + d_base) * 2, 0); \
        _Pragma("unroll") for (int r = 0; r < 2; ++r) gv[r] = __builtin_amdgcn_raw_buffer_load_b128(gs, GV(q4), GSO(r), 16); \
        SCHED(); } while (0)
#define XPIN() do { PIN4(xx[0][0], xx[0][1], xx[1][0], xx[1][1]); asm volatile("" : "+v"(dk2), "+v"(gv[0]), "+v"(gv[1])); } while (0)

    if (wid < 4) {
        typedef __attribute__((ext_vector_type(8))) unsigned u32x8;
        typedef const __attribute__((address_space(4))) u32x8 cu8;
        const int po = (p_base + wid * 16) * 2;
        u32x8 s0 = *(cu8*)((size_t)logdt + po), s1 = *(cu8*)((size_t)lre + po), s2 = *(cu8*)((size_t)lim + po);
        SCHED();
        const int pq = p_base + wid * 16 + l15;
        unsigned goff[4], gmask[4];
#pragma unroll
        for (int i = 0; i < 4; ++i) {
            goff[i] = (unsigned)(((tb - 1 - q4 - 4 * i) * 1024 + pq) * 16);
            gmask[i] = (tb - 1 - q4 - 4 * i >= 0) ? ~0u : 0u;
            asm volatile("" : "+v"(goff[i]), "+v"(gmask[i]));
        }
        unsigned gpub = q4 == 0 ? (unsigned)((tb * 1024 + pq) * 16) : 0x80000000u;
        unsigned hp = (unsigned)(tb * 524288 + (nb * 4 + wid) * 1024 + lane * 16);
        unsigned wa = (unsigned)(HT_OFF + wid * 2048 + (q4 * 16 + (l15 ^ ((q4 >> 1) * 4))) * 8);
        unsigned ra0 = (unsigned)(HT_OFF + wid * 2048 + ((q4 >> 1) * 64 + (l15 & 3) * 16 + (((q4 & 1) * 8 + (l15 >> 2)) ^ ((l15 & 2) * 2))) * 8), ra1 = ra0 ^ 32u;
        cra = CRA(GA_OFF + wid * 8192); crb = cra ^ 64;
        unsigned vB0 = (unsigned)((d_base + (lane >> 3) * 4) * 2048 + (((lane & 7) ^ (lane >> 4)) * 16)), vB1 = (vB0 ^ 64u) + 65536u - 1024u;
        PIN4(gpub, vB0, hp, wa); PIN4(ra0, ra1, cra, crb); asm volatile("" : "+v"(vB1));
        f32x4 acc[8][2];
#pragma unroll
        for (int m = 0; m < 8; m += 2) {
#pragma unroll
            for (int c = 0; c < 4; ++c) acc[m + (c >> 1)][c & 1] = (f32x4){0.f, 0.f, 0.f, 0.f};
            PIN4(acc[m][0], acc[m][1], acc[m + 1][0], acc[m + 1][1]);
        }
        SCHED();
        cf L1, Lq3, cc, L32, cLq, Lp, L512;
        {
            unsigned w0 = 0, w1 = 0, w2 = 0;
#pragma unroll
            for (int i = 0; i < 8; ++i) {
                asm("v_writelane_b32 %0, %3, %6\n\tv_writelane_b32 %1, %4, %6\n\tv_writelane_b32 %2, %5, %6" : "+v"(w0), "+v"(w1), "+v"(w2) : "s"(s0[i]), "s"(s1[i]), "s"(s2[i]), "i"(2 * i));
            }
            asm("s_nop 1" : "+v"(w0), "+v"(w1), "+v"(w2));
            auto spread = [&](unsigned w) {
                const unsigned e = (unsigned)__builtin_amdgcn_update_dpp(0, (int)w, 0xa0, 0xf, 0xf, false);
                const unsigned a = __builtin_amdgcn_permlane16_swap(e, e, false, false)[0];
                const unsigned c = __builtin_amdgcn_permlane32_swap(a, a, false, false)[0];
                return (l15 & 1) ? hi16(c) : lo16(c);
            };
            const float ldt = spread(w0), re = spread(w1), im = spread(w2);
            if (wid == 3 && lane < NFLAGW) *(unsigned*)(smem + UFLAG_OFF + lane * 4) = 0u;
            const float dt = rbf(expf(ldt));
            const float ea = expf(dt * re);
            float sn, cs;
            sincosf(dt * im, &sn, &cs);
            L1 = {ea * cs, ea * sn};
            const float nr = L1.r - 1.0f, ni = L1.i, den = re * re + im * im;
            cc = {2.f * ((nr * re + ni * im) / den), 2.f * ((ni * re - nr * im) / den)};
            const cf T2 = cmul(L1, L1), T4 = cmul(T2, T2), L8 = cmul(T4, T4), L16 = cmul(L8, L8);
            L32 = cmul(L16, L16);
            const cf L64 = cmul(L32, L32), L96 = cmul(L64, L32), L128 = cmul(L64, L64), L256 = cmul(L128, L128), L384 = cmul(L256, L128);
            L512 = cmul(L256, L256);
            Lp = pick4(q4, L128, L256, L384);
            Lq3 = pick4(3 - q4, L32, L64, L96);
            cLq = cmul(cc, pick4(q4, L32, L64, L96));
            PINC(L1); PINC(Lq3); PINC(cc); PINC(L32); PINC(cLq); PINC(Lp); PINC(L512);
        }
        __builtin_amdgcn_s_setprio(PRIO_G);
        unsigned tagv;
        cf S;
        STAMPT(dbg, 22, 0);
        const int swz = (l15 >> 1) & 7;
        {
            const int a0 = RA(0) + l15 * 128 + ((q4 ^ swz) * 16);
            const int b0 = RB(0) + ((wid >> 1) * 64 + (wid & 1) * 16 + l15) * 128 + ((q4 ^ swz) * 16);
            WAIT_LGKM0();
            BAR();
            asm volatile("" ::: "memory");
            STAMPT(dbg, 19, 64);
            bf16x8 FA[8][2], FB[2][2][2];
            int pa[2] = {a0, a0 ^ 64}, pb[2] = {b0, b0 ^ 64};
            PIN4(pa[0], pa[1], pb[0], pb[1]);
#define LGW(n) __builtin_amdgcn_s_waitcnt(0xC07F | ((n) << 8))
#define RA_(j) FA[(j) >> 1][(j) & 1] = RD128(pa[(j) & 1], ((j) >> 1) * 2048)
#define RB_(bu, j) FB[bu][(j) >> 1][((j) ^ ((j) >> 1)) & 1] = RD128(pb[((j) ^ ((j) >> 1)) & 1], ((j) >> 1) * 4096)
#define RN_(bu, i) do { if ((i) == 0) RB_(bu, 0); else if ((i) < 3) RA_((i) - 1); else if ((i) < 6) RB_(bu, (i) - 2); else RA_((i) - 4); } while (0)
#define G1_H(H, bu, RD, FILL) do { \
            _Pragma("unroll") for (int i = 0; i < 16; ++i) { \
                const int m = (H) * 4 + (i >> 2), n = ((i >> 1) ^ m) & 1, ks = (i ^ (i >> 1)) & 1; \
                if ((H) == 0) { if (i == 0) LGW(6); if (i == 4) LGW(8); if (i == 8) LGW(10); if (i == 12) LGW(8); } \
                SCHED(); \
                MF(acc[m][n], FA[m][ks], FB[bu][n][ks]); SCHED(); \
                if ((H) == 0 && i < 8) RA_(8 + i); \
                if ((H) == 1 && (RD) && i < 12) RN_((bu) ^ 1, i); \
                FILL((H) * 16 + i); \
                SCHED(); \
            } } while (0)
#define NOF(g) do { } while (0)
#pragma unroll
            for (int j = 0; j < 12; ++j) { RN_(0, j); SCHED(); }
            unsigned long long ua_ = UNITS_A;
            int sb = RB(0), sa = RA(0), nx, na;
#define ADV(g) do { if ((g) == 8) { ua_ >>= 4; na = (int)((unsigned)ua_ & 15u) << 14; asm volatile("" : "+s"(na), "+s"(ua_)); } \
                    if ((g) == 9) { const int o_ = sa; sa = na; na -= o_; asm volatile("" : "+s"(na), "+s"(sa)); } \
                    if ((g) == 10) { pa[0] += na; pa[1] += na; asm volatile("" : "+v"(pa[0]), "+v"(pa[1])); } \
                    if ((g) == 11) { nx = (sb == 65536) ? -49152 : 16384; sb += nx; asm volatile("" : "+s"(nx), "+s"(sb)); } \
                    if ((g) == 13) { pb[0] += nx; pb[1] += nx; asm volatile("" : "+v"(pb[0]), "+v"(pb[1])); } } while (0)
#define G1_T(bu) do { \
                G1_H(0, bu, true, ADV); \
                WAIT_LGKM0(); \
                asm volatile("" ::: "memory"); \
                BAR();                     \
                asm volatile("" ::: "memory"); \
                G1_H(1, bu, true, NOF); } while (0)
#pragma unroll 1
            for (int t2 = 0; t2 < 7; ++t2) { G1_T(0); G1_T(1); }
            G1_T(0);
#define AGG(g) do { if ((g) == 8) { asm volatile("" ::: "memory"); SETW(G1D_OFF, wid, 1u); }        \
                    if ((g) == 9) tagv = *(volatile const lds_u32*)(size_t)(unsigned)TAG_OFF; \
                    if ((g) >= 7) { const int t = (g) - 6; \
                        if (t == 1) { asm volatile("s_nop 1" : "+v"(acc[0][0]), "+v"(acc[0][1])); S = {acc[0][0][0], acc[0][1][0]}; } \
                        if ((t & 3) == 0) asm volatile("" : "+v"(acc[t >> 2][0]), "+v"(acc[t >> 2][1])); \
                        S = cfma(L1, S, cf{acc[t >> 2][0][t & 3], acc[t >> 2][1][t & 3]}); PINC(S); } } while (0)
            G1_H(0, 1, false, AGG);
            G1_H(1, 1, false, AGG);
        }
        STAMPT(dbg, 1, 0);
        const unsigned tag = __builtin_amdgcn_readfirstlane(tagv);
        const __amdgpu_buffer_rsrc_t grs = __builtin_amdgcn_make_buffer_rsrc(ws, 0, 15 * 16384, 0x00020000);
        {
#pragma unroll
            for (int t = 26; t < 32; ++t) {
                if (t == 28) { PINC(S); asm volatile("" : "+v"(acc[7][0]), "+v"(acc[7][1])); }
                S = cfma(L1, S, cf{acc[t >> 2][0][t & 3], acc[t >> 2][1][t & 3]});
            }
            const cf W = cmul(S, Lq3);
            const float e2 = red16(red32(W.r, W.i));
            const auto eo = __builtin_amdgcn_permlane32_swap(__float_as_uint(e2), __float_as_uint(e2), false, false);
            __builtin_amdgcn_raw_buffer_store_b128((u32x4){eo[0], tag, eo[1], tag}, grs, gpub, 0, 16);
        }
        SCHED();
        STAMPT(dbg, 2, 0);
        const unsigned cg = lbase + GA_OFF + wid * 8192;
        wait_lds1(smem, COK_OFF + wid * 4);
        CREAD(0);
        WAIT_LGKM0();
        CPIN(0);
        CTILE(wid, 1, cg);
        u32x4 gq[4];
#pragma unroll
        for (int m = 0; m < 8; ++m) {
            const f32x4 R = acc[m][0], I = acc[m][1];
            acc[m][0] = fma4(splat4(cc.r), R, mul4(splat4(-cc.i), I));
            acc[m][1] = fma4(splat4(-cc.r), I, mul4(splat4(-cc.i), R));
            asm volatile("" : "+v"(acc[m][0]), "+v"(acc[m][1]));
            if (m == 3) {
                SCHED();
#pragma unroll
                for (int i = 0; i < 4; ++i) asm volatile("buffer_load_dwordx4 %0, %1, %2, 0 offen sc1" : "=&v"(gq[i]) : "v"(goff[i]), "s"(grs) : "memory");
                SCHED();
            }
        }
        cf Ec;
        {
            const auto sr = __builtin_amdgcn_permlane16_swap(__float_as_uint(S.r), __float_as_uint(S.r), false, false);
            const auto si = __builtin_amdgcn_permlane16_swap(__float_as_uint(S.i), __float_as_uint(S.i), false, false);
            const cf Se = {__uint_as_float(sr[0]), __uint_as_float(si[0])}, So = {__uint_as_float(sr[1]), __uint_as_float(si[1])};
            const cf A = cfma(L32, Se, So);
            const auto ar = __builtin_amdgcn_permlane32_swap(__float_as_uint(A.r), __float_as_uint(A.r), false, false);
            const auto ai = __builtin_amdgcn_permlane32_swap(__float_as_uint(A.i), __float_as_uint(A.i), false, false);
            const cf A01 = {(q4 & 2) ? __uint_as_float(ar[0]) : 0.f, (q4 & 2) ? __uint_as_float(ai[0]) : 0.f};
            const cf kq = {(q4 & 1) ? L32.r : 1.f, (q4 & 1) ? L32.i : 0.f};
            const cf E = cfma(kq, A01, cf{(q4 & 1) ? Se.r : 0.f, (q4 & 1) ? Se.i : 0.f});
            Ec = cmul(cc, E);
            PINC(Ec);
        }
        WAIT_VM(4);
        CREAD(1);
        WAIT_LGKM0();
        CPIN(1);
        CTILE(wid, 2, cg);
        asm volatile("s_waitcnt vmcnt(8)" : "+v"(gq[0]), "+v"(gq[1]), "+v"(gq[2]), "+v"(gq[3]) :: "memory");
        STAMPT(dbg, 27, 0);
        cf carry;
        {
            unsigned spins = 0;
            for (;;) {
                unsigned bad = 0;
#pragma unroll
                for (int i = 0; i < 4; ++i) bad |= ((gq[i].y ^ tag) | (gq[i].w ^ tag)) & gmask[i];
                if (__all(bad == 0u)) break;
                if (++spins > SPIN_MAX) break;
#pragma unroll
                for (int i = 0; i < 4; ++i) asm volatile("buffer_load_dwordx4 %0, %1, %2, 0 offen sc1" : "+v"(gq[i]) : "v"(goff[i]), "s"(grs) : "memory");
                asm volatile("s_waitcnt vmcnt(0)" : "+v"(gq[0]), "+v"(gq[1]), "+v"(gq[2]), "+v"(gq[3]) :: "memory");
            }
            cf E = {__uint_as_float(gq[3].x), __uint_as_float(gq[3].z)};
#pragma unroll
            for (int i = 2; i >= 0; --i) E = cfma(L512, E, cf{__uint_as_float(gq[i].x), __uint_as_float(gq[i].z)});
            E = cmul(E, Lp);
            const float e2 = red16(red32(E.r, E.i));
            const auto eo = __builtin_amdgcn_permlane32_swap(__float_as_uint(e2), __float_as_uint(e2), false, false);
            carry = {__uint_as_float(eo[0]), __uint_as_float(eo[1])};
        }
        STAMPT(dbg, 3, 0);
        {
            const __amdgpu_buffer_rsrc_t hrs = __builtin_amdgcn_make_buffer_rsrc(h, 0, SEQ * 2048 * 2, 0x00020000);
            const cf e = cfma(cLq, carry, Ec);
            float hr = e.r, hn = -e.i;
            s16x4 fr[2][4];
#define HSTORE(m) do { _Pragma("unroll") for (int c = 0; c < 2; ++c) \
                __builtin_amdgcn_raw_buffer_store_b128(__builtin_bit_cast(u32x4, __builtin_shufflevector(fr[(m) & 1][2 * c], fr[(m) & 1][2 * c + 1], 0, 1, 2, 3, 4, 5, 6, 7)), hrs, hp, (2 * (m) + c) * 65536, WT); } while (0)
#pragma unroll
            for (int m = 0; m < 4; ++m) {
#pragma unroll
                for (int j = 0; j < 8; ++j) {
                    const float nr = fmaf(L1.r, hr, fmaf(L1.i, hn, acc[2 * m + (j >> 2)][0][j & 3]));
                    const float nn = fmaf(L1.r, hn, fmaf(-L1.i, hr, acc[2 * m + (j >> 2)][1][j & 3]));
                    hr = nr; hn = nn;
                    acc[2 * m + (j >> 2)][0][j & 3] = nr; acc[2 * m + (j >> 2)][1][j & 3] = nn;
                }
                u32x2 pk[4];
#pragma unroll
                for (int c = 0; c < 4; ++c) { const f32x4 v = acc[2 * m + (c >> 1)][c & 1]; pk[c] = (u32x2){pkbf(v[0], v[1]), pkbf(v[2], v[3])}; }
                PIN4(pk[0], pk[1], pk[2], pk[3]);
                SCHED();
#pragma unroll
                for (int c = 0; c < 4; ++c) { *(volatile __attribute__((address_space(3))) u32x2*)(size_t)(wa + c * 512) = pk[c]; SCHED(); }
                asm volatile("" ::: "memory");
#pragma unroll
                for (int c = 0; c < 2; ++c) { fr[m & 1][2 * c] = TRD(ra0 + c * 1024); SCHED(); fr[m & 1][2 * c + 1] = TRD(ra1 + c * 1024); SCHED(); }
                asm volatile("" ::: "memory");
                if (m > 0) HSTORE(m - 1);
                SCHED();
            }
            HSTORE(3);
        }
        STAMPT(dbg, 4, 0);
        {
            WAIT_VM(8);
            CREAD(2);
            WAIT_VM(0);
            {
                unsigned n;
                asm volatile("s_mov_b64 exec, 1\n\tds_add_rtn_u32 %0, %1, %2\n\ts_mov_b64 exec, -1\n\ts_waitcnt lgkmcnt(0)" : "=&v"(n) : "v"(MISC_OFF), "v"(1u) : "memory");
                if (__builtin_amdgcn_readfirstlane(n) == 3u) {
                    if (lane == 0) __hip_atomic_store((gu32*)(ws + WS_HFLAG) + (tb * 16 + nb) * 32, tag, RLX_AGENT);
                    if (DBGON && lane == 0) dbg[BIDX * 64 + 20] = __builtin_amdgcn_s_memrealtime();
                }
            }
            STAMPT(dbg, 12, 0);
            if (wid == 0 && lane < 7) CODE_TOUCH(CODE_LINES - 1 - lane - (idx & 7) * 7);
            __builtin_amdgcn_s_setprio(0);
            WAIT_LGKM0();
            CPIN(2);
            CTILE(wid, 3, cg);
            XLOAD();
            HPOLL_SETUP();
            HWAIT();
            STAMPT(dbg, 8, 0);
            const unsigned vh = (unsigned)(lane * 16);
#pragma unroll
            for (int f = 0; f < DEPTH; ++f) LOADF(f);
            __builtin_amdgcn_s_setprio(PRIO2_G);
        }
    } else {
        const int lw = wid - 4;
        const int d = cb * 4 + lw;
        const char* Krow = (const char*)(ssmk + (size_t)d * SEQ);
        cra = CRA(LA_OFF + lw * 8192); crb = cra ^ 64;
        const unsigned cst0 = lbase + LA_OFF + lw * 8192;
        CT_SETUP();
        asm volatile("" : "+v"(cra), "+v"(crb));
        u32x4 KA[4];
        unsigned draw;
        unsigned tag;
        float gacc = 0.f;
#define KLOAD() asm volatile("global_load_dwordx4 %0, %5, %7 nt\n\tglobal_load_dwordx4 %1, %5, %7 offset:1024 nt\n\tglobal_load_dwordx4 %2, %5, %7 offset:2048 nt\n\t" \
                             "global_load_dwordx4 %3, %5, %7 offset:3072 nt\n\tglobal_load_ushort %4, %6, %8" \
                             : "=&v"(KA[0]), "=&v"(KA[1]), "=&v"(KA[2]), "=&v"(KA[3]), "=&v"(draw) : "v"((unsigned)(lane * 16)), "v"((unsigned)(d * 2)), "s"(Krow), "s"(ssmD) : "memory")
#define KWAIT(n) asm volatile("s_waitcnt vmcnt(%5)" : "+v"(KA[0]), "+v"(KA[1]), "+v"(KA[2]), "+v"(KA[3]), "+v"(draw) : "i"(n) : "memory")
        if (wid < 6) {
            WAIT_VM(16);
            STAMPT(dbg, 17, 320);
            BAR();
            STAGE_A(2, lw0 + RA(2)); STAGE_B(2, lw0 + RB(2));
            unsigned long long ua_ = UNITS_A >> 12;
            unsigned sla = lw0 + RA(3), slb = lw0 + RB(4);
            int ka = ((3 + rot1) & 15) * 128, kb_ = ka;
            const char* sa = (const char*)x + ka;
            asm volatile("" : "+s"(sa), "+s"(sla), "+s"(ka), "+s"(ua_));
#define NEXT_A() do { ka = (ka + 128) & 2047; sa = (const char*)x + ka; ua_ >>= 4; sla = lw0 + (((unsigned)ua_ & 15u) << 14); asm volatile("" : "+s"(sa), "+s"(sla), "+s"(ka), "+s"(ua_)); } while (0)
#define LSTEP(n) do { \
                if ((n) == 24 && t == 1) { WAIT_VM(16); BAR(); STAGE_B(3, lw0 + RB(3)); } else { WAIT_VM(n); BAR(); } \
                GLDS4(sa, voA[0], voA[1], voA[2], voA[3], sla, 0); \
                GLDS4(sa + 16384, voA[0], voA[1], voA[2], voA[3], sla, 4096); \
                kb_ = (kb_ + 128) & 2047; \
                GLDS4((const char*)Bre + kb_, voB[0], voB[1], voB[2], voB[3], slb, 0); \
                NEXT_A(); \
                const unsigned slb0 = slb; \
                slb = (slb == lw0 + 65536) ? lw0 + 16384 : slb + 16384; \
                asm volatile("" : "+s"(slb), "+s"(kb_), "+s"(t)); \
                GLDS4((const char*)Bim + kb_, voB[0], voB[1], voB[2], voB[3], slb0, 4096); } while (0)
#pragma unroll 1
            for (int t = 1; t < 5; ++t) LSTEP(24);
            GLDS4(sa, voA[0], voA[1], voA[2], voA[3], sla, 0); GLDS4(sa + 16384, voA[0], voA[1], voA[2], voA[3], sla, 4096);
            NEXT_A();
#pragma unroll 1
            for (int t = 5; t < 13; ++t) LSTEP(32);
            KLOAD();
            WAIT_VM(37);
            BAR();
            __builtin_amdgcn_s_setprio(0);
            tag = lds_word(smem, TAG_OFF);
            CTILE(lw, 0, cst0 + GA_OFF);
            FLAGD(COK_OFF + lw * 4);
            WAIT_VM(30);
            BAR();
            CTILE(lw + 2, 0, cst0 + GA_OFF + 16384);
            FLAGD(COK_OFF + lw * 4 + 8);
            WAIT_VM(23);
            BAR();
        } else {
            const int vw = wid - 6;
            const int dch = dblk * 128 + vw * 64 + l15;
            const unsigned lk = lbase + ST_OFF + vw * ST_SZ;
            const unsigned vv_ = (unsigned)(((lane >> 4) * 4 + ((lane >> 3) & 1)) * DM * 2 + (lane & 7) * 16);
            const char* vb = (const char*)vin + (size_t)(sblk * 64) * (DM * 2) + dblk * 256 + vw * 128;
            const int grow = nb * 8 + vw * 4 + q4;
            unsigned gx0 = (unsigned)(grow * 128 + (((l15 >> 1) ^ ((grow >> 1) & 7)) * 16) + (l15 & 1) * 8), gx1 = gx0 + RA(2), gwa = (unsigned)(GW_OFF + l15 * 8);
            asm volatile("" : "+v"(gx0), "+v"(gx1), "+v"(gwa));
            u32x2 gxv, gwv;
            typedef const volatile __attribute__((address_space(3))) u32x2* l2p;
#define VBAR(t) do { SCHED(); asm volatile("" ::: "memory"); BAR(); asm volatile("" ::: "memory"); \
                     gxv = *(l2p)(size_t)(RA(t) == 0 ? gx0 : gx1 + (RA(t) - RA(2))); gwv = *(l2p)(size_t)(gwa + (t) * 128); SCHED(); } while (0)
#define GATE() do { SCHED(); gacc = fmaf(lo16(gxv.x), lo16(gwv.x), gacc); gacc = fmaf(hi16(gxv.x), hi16(gwv.x), gacc); \
                    gacc = fmaf(lo16(gxv.y), lo16(gwv.y), gacc); gacc = fmaf(hi16(gxv.y), hi16(gwv.y), gacc); asm volatile("" : "+v"(gacc)); SCHED(); } while (0)
            unsigned ka[4][2], sel[4];
            const int R0 = 4 + (l15 >> 2) * 16 + (l15 & 3) - (q4 >> 1);
            unsigned kr0 = (unsigned)(2 * R0), kr1 = (unsigned)(ST_OFF + vw * ST_SZ + R0 * 128 + (((q4 ^ (R0 >> 1)) & 1) * 16));
            asm volatile("" : "+v"(kr0), "+v"(kr1));
#pragma unroll
            for (int i = 0; i < 4; ++i)
#pragma unroll
                for (int tp = 0; tp < 2; ++tp) {
                    ka[i][tp] = (kr1 ^ (tp * 16)) + ((kr0 + 8 * i - 4 * tp) & 0xe0) + (4 * i - 2 * tp) * 128;
                    asm volatile("" : "+v"(ka[i][tp]));
                }
#pragma unroll
            for (int r = 0; r < 4; ++r) {
                const unsigned pick = (q4 & 2) ? 0x0302u : 0x0100u;
                sel[r] = ((l15 >> 1) == (q4 & 1) * 4 + r) ? ((l15 & 1) ? (pick << 16) | 0x0c0cu : 0x0c0c0000u | pick) : 0x0c0c0c0cu;
            }
            unsigned va_ = (unsigned)(ST_OFF + vw * ST_SZ + ST_V + (q4 * 2 + (l15 >> 3)) * 1056 + ((l15 >> 2) & 1) * 128 + (l15 & 3) * 8);
            unsigned so = (unsigned)(((dch >> 2) * 32 + sblk) * 512 + q4 * 64 + (dch & 3) * 16);
            PIN4(sel[0], sel[1], sel[2], sel[3]); asm volatile("" : "+v"(so), "+v"(va_));
            asm volatile("s_waitcnt vmcnt(9)" : "+v"(tw[0]), "+v"(tw[1]), "+v"(tw[2]), "+v"(tw[3]), "+v"(epoch) :: "memory");
            STAMPT(dbg, 18, 384);
            VBAR(0);
            bf16x8 wf[4][2];
#pragma unroll
            for (int ps = 0; ps < 4; ++ps)
#pragma unroll
                for (int tp = 0; tp < 2; ++tp) {
                    u32x4 w;
#pragma unroll
                    for (int r = 0; r < 4; ++r) w[r] = __builtin_amdgcn_perm(0u, tw[ps][tp], sel[r]);
                    wf[ps][tp] = __builtin_bit_cast(bf16x8, w);
                    asm volatile("" : "+v"(wf[ps][tp]));
                }
            SCHED();
            GATE(); VBAR(1);
            VLD(0); VLD(1); VLD(2); VLD(3); VLD(4); VLD(5); VLD(6); VLD(7);
            f32x4 ks[4][4];
            auto sconv = [&](int i0) __attribute__((always_inline)) {
                bf16x8 kf[2][4][2];
#pragma unroll
                for (int q = 0; q < 16; ++q) { kf[q >> 3][q & 3][(q >> 2) & 1] = RD128(ka[i0 + (q >> 3)][(q >> 2) & 1], (q & 3) * 32); SCHED(); }
                GATE();
                __builtin_amdgcn_s_setprio(PRIO_KV);
                WAIT_LGKM0();
                SCHED();
#pragma unroll
                for (int i = 0; i < 2; ++i) {
#pragma unroll
                    for (int ps = 0; ps < 4; ++ps) MF0(ks[ps][i0 + i], kf[i][ps][0], wf[ps][0]);
#pragma unroll
                    for (int ps = 0; ps < 4; ++ps) MF(ks[ps][i0 + i], kf[i][ps][1], wf[ps][1]);
                }
                __builtin_amdgcn_s_setprio(0);
                SCHED();
            };
            WAIT_VM(8);
            STAMPT(dbg, 21, 384);
            sconv(0); VBAR(2);
            sconv(2); VBAR(3);
            WAIT_VM(0);
            u32x2 vq[4][4];
#pragma unroll
            for (int ps = 0; ps < 4; ++ps)
#pragma unroll
                for (int j = 0; j < 4; ++j) vq[ps][j] = __builtin_bit_cast(u32x2, TRD(va_ + j * 256 + ps * 32));
            const __amdgpu_buffer_rsrc_t rs = __builtin_amdgcn_make_buffer_rsrc(kvT, 0, DM * SEQ * 2, 0x00020000);
            u32x4 o[8];
            auto kvout = [&](int n) __attribute__((always_inline)) {
                const int hh = n >> 2, ps = n & 3;
                asm volatile("" : "+v"(ks[ps][2 * hh]), "+v"(ks[ps][2 * hh + 1]));
#pragma unroll
                for (int e = 0; e < 4; ++e) {
                    const unsigned vv = vq[ps][2 * hh + (e >> 1)][e & 1];
                    o[n][e] = pkbf(ks[ps][2 * hh + (e >> 1)][2 * (e & 1)] * lo16(vv), ks[ps][2 * hh + (e >> 1)][2 * (e & 1) + 1] * hi16(vv));
                }
                asm volatile("" : "+v"(o[n]));
                SCHED();
            };
            GATE(); kvout(0); kvout(1);
            WAIT_LGKM0();

            VBAR(4);
#define KVST(h) do { _Pragma("unroll") for (int n = 0; n < 4; ++n) __builtin_amdgcn_raw_buffer_store_b128(o[4 * (h) + n], rs, so + (unsigned)((h) * 256), n * 65536, WT); \
                     SCHED(); asm volatile("s_nop 1" ::: "memory"); SCHED(); } while (0)
            kvout(2); kvout(3); GATE(); VBAR(5);
            KVST(0);
            kvout(4); kvout(5); GATE(); VBAR(6);
            kvout(6); kvout(7); GATE(); VBAR(7);
            KVST(1);
            GATE(); VBAR(8);
            if (vw == 1 && lane == 0) *(unsigned*)(smem + TAG_OFF) = (epoch + 1u) ? epoch + 1u : 1u;
            GATE();
            WAIT_LGKM0();
            WAIT_VM(0);
            VBAR(9);
            tag = __builtin_amdgcn_readfirstlane(*(const unsigned*)(smem + TAG_OFF));
            if (vw == 0 && lane == 0) __hip_atomic_store((gu32*)(ws + WS_KVFLAG) + (dblk * 32 + sblk) * 32, tag, RLX_AGENT);
            if (vw == 1 && lane < 4) *(u32x4*)(smem + U_OFF + 192 + lane * 16) = (u32x4){0u, 0u, 0u, 0u};
            gu32* kfp = (gu32*)(ws + WS_KVFLAG) + ((cb >> 5) * 32 + vw * 16 + l15) * 32;
            const char* ub = (const char*)kvT + (size_t)__builtin_amdgcn_readfirstlane(cb * 16384 + vw * 8192);
            const unsigned vu = (unsigned)((lane >> 5) * 512 + ((lane >> 2) & 1) * 256 + ((lane >> 3) & 3) * 64 + ((lane - (lane >> 4)) & 3) * 16);
            const unsigned lu = lbase + U_OFF + 256 + vw * 8192;
#define U_DMA1(o) asm volatile("s_add_u32 m0, %2, %3\n\ts_nop 0\n\tglobal_load_lds_dwordx4 %0, %1 sc1\n\tglobal_load_lds_dwordx4 %0, %1 offset:1024 sc1\n\t" \
                             "global_load_lds_dwordx4 %0, %1 offset:2048 sc1\n\tglobal_load_lds_dwordx4 %0, %1 offset:3072 sc1" \
                             :: "v"(vu), "s"(ub + (o)), "s"(lu), "i"(o) : "memory", "scc")
#define U_DMA() do { U_DMA1(0); U_DMA1(4096); FLAGD(UFLAG_OFF + lw * 4); } while (0)
            unsigned fo = lane < 8 ? (unsigned)((tb * 1024 + p_base) * 16 + lane * 128) : lane == 8 ? (unsigned)(WS_HFLAG + (tb * 16 + nb) * 128)
                        : lane == 9 ? (unsigned)(WS_CFLAG + cb * 128) : (unsigned)(WS_GATE + (tb * 16 + nb) * 128);
            asm volatile("" : "+v"(fo));
            GATE(); VBAR(10);
            TOUCH((const char*)(vw ? Cim : Cre) + (d_base + (idx & 3) * 16) * 2048 + COFF(0, 2), (unsigned)((lane >> 2) * 2048 + (lane & 3) * 256));
            if (vw == 0) TOUCH(ws, fo); else TOUCH(dskip, (unsigned)(d_base * 2));
            {
                const int cl = 64 + lane + ((idx & 15) * 2 + vw) * 7;
                if (lane < 7 && cl < CODE_LINES) CODE_TOUCH(cl);
            }
            GATE(); VBAR(11);
            GATE(); VBAR(12);
            GATE(); VBAR(13);
            unsigned pv;
            asm volatile("global_load_dword %0, %1, off sc1" : "=&v"(pv) : "v"(kfp) : "memory");
            KLOAD();
            GATE(); VBAR(14);
            GATE(); VBAR(15);
            asm volatile("s_waitcnt vmcnt(5)" : "+v"(pv) :: "memory");
            const bool done = __all(pv == tag);
            if (done) U_DMA();
            if (DBGON && TIDX == 384) dbg[BIDX * 64 + 13] = (done ? 100ull : 0ull) | (1ull << 62);
            GATE();
            if (lane == 0) SETW(UFLAG_OFF, vw, 1u);
            WAIT_VM(9);
            if (!done) {
                unsigned spins = 0;
                for (;;) {
                    const unsigned v = __hip_atomic_load(kfp, RLX_AGENT);
                    if (__all(v == tag)) break;
                    if (++spins > SPIN_MAX) break;
                    __builtin_amdgcn_s_sleep(1);
                }
                U_DMA();
            }
        }
        KWAIT(18);
        STAMPT(dbg, 14, 320);
        __builtin_amdgcn_s_sleep(6);
        {
            const unsigned cp = (unsigned)(KC_OFF + lw * CV_W + 64 + lane * 16);
            if (lane == 0) KA[0].x = (KA[0].x & 0xffff0000u) | f2bf(lo16(KA[0].x) + bf2f((u16)draw));
#define WR128(v, off) asm volatile("ds_write_b128 %0, %1 offset:%2" :: "v"(cp), "v"(v), "i"(off) : "memory")
#pragma unroll
            for (int q = 3; q >= 0; --q) WR128(KA[q], q * 1024);
#pragma unroll
            for (int q = 3; q >= 0; --q) {
                const u32x2 p = q ? pred2(KA[q], RL63(KA[q ? q - 1 : 0].z), RL63(KA[q ? q - 1 : 0].w)) : pred2(KA[0], 0u, 0u);
                const u32x4 a = KA[q];
                const u32x4 s1 = {al(a.x, p.y), al(a.y, a.x), al(a.z, a.y), al(a.w, a.z)};
                WR128(s1, q * 1024 + CV_COPY);
                const u32x4 s2 = {p.y, mv(a.x), mv(a.y), mv(a.z)};
                WR128(s2, q * 1024 + 2 * CV_COPY);
                const u32x4 s3 = {al(p.y, p.x), mv(s1.x), mv(s1.y), mv(s1.z)};
                WR128(s3, q * 1024 + 3 * CV_COPY);
            }
            if (lane < 16) *(u32x4*)(smem + KC_OFF + lw * CV_W + (lane >> 2) * CV_COPY + (lane & 3) * 16) = (u32x4){0u, 0u, 0u, 0u};
            SCHED();
        }
        STAMPT(dbg, 26, 320);
        wait_lds4(smem, G1D_OFF);
        wait_lds4(smem, UFLAG_OFF);
        STAMPT(dbg, 9, 256);
        __builtin_amdgcn_s_setprio(PRIO_CV);
        STAMPT(dbg, 10, 384);
        f32x4 cv[8];
#pragma unroll
        for (int g = 0; g < 8; ++g) cv[g] = (f32x4){0.f, 0.f, 0.f, 0.f};
        PIN4(cv[0], cv[1], cv[2], cv[3]); PIN4(cv[4], cv[5], cv[6], cv[7]);
        const int uz = U_OFF + 192;
        const int ua0 = U_OFF + 256 + (2 * l15 + q4 - 2) * 64, rv0 = (2 * l15 + q4 - 2) * 4 + lw * 16;
        const unsigned ka0 = (unsigned)((l15 >> 2) * CV_COPY - 16 * q4 + 8 * (l15 & 3) - 8);
        int ua, rv, pu, pz;
        unsigned adr;
#define UNEXT1() do { pu = ua | (rv & 0x30); asm volatile("" : "+v"(pu)); } while (0)
#define UNEXT2() do { pz = (ua > uz ? ua : uz) | (rv & 0x30); asm volatile("" : "+v"(pz)); } while (0)
#define UNEXT3() do { ua -= 256; rv -= 16; asm volatile("" : "+v"(ua), "+v"(rv)); } while (0)
#define ANEXT() do { adr += 64; asm volatile("" : "+v"(adr)); } while (0)
#define RDU(dst, c) do { dst = (c) ? RD128(pu, (c) * 2048) : RD128(pz, 0); SCHED(); } while (0)
        HPOLL_SETUP();
        unsigned hpv = 0;
        {
            bf16x8 A[8], UB[8];
            ua = ua0; rv = rv0; adr = lbase + KC_OFF + lw * CV_W + ka0 + 96;
            asm volatile("" : "+v"(adr));
            UNEXT1(); UNEXT2(); UNEXT3();
#pragma unroll
            for (int i = 0; i < 8; ++i) { RDA(A[7 - i], adr, 7 - i); RDU(UB[i], i); }
            unsigned gb = lw >= 2 ? (unsigned)((tb * 16 + nb) * 128 + (lw - 2) * 32) : 0x80000000u;
            asm volatile("" : "+s"(gb));
            const unsigned gso = (l15 == 0) ? gb + (unsigned)(q4 * 8) : 0x80000000u;
            float gt = 0.f;
            unsigned vxo = 0, vdo = 0, vgo = 0;
            auto gate_out = [&](int i) __attribute__((always_inline)) {
                if (i == 0) gacc += __uint_as_float(__builtin_amdgcn_update_dpp(0u, __float_as_uint(gacc), 0xb1, 0xf, 0xf, false));
                if (i == 1) gacc += __uint_as_float(__builtin_amdgcn_update_dpp(0u, __float_as_uint(gacc), 0x4e, 0xf, 0xf, false));
                if (i == 2) gacc += __uint_as_float(__builtin_amdgcn_update_dpp(0u, __float_as_uint(gacc), 0x141, 0xf, 0xf, false));
                if (i == 3) gacc += __uint_as_float(__builtin_amdgcn_update_dpp(0u, __float_as_uint(gacc), 0x140, 0xf, 0xf, false));
                if (i == 4) gacc = rbf(gacc);
                if (i == 5) gacc = __builtin_amdgcn_exp2f(gacc * -1.44269504088896f);
                if (i == 6) gacc = __builtin_amdgcn_rcpf(1.0f + gacc);
                if (i == 7) gacc = rbf(gacc);
                if (i == 8) gt = __uint_as_float(pkbf(gacc, 1.0f - gacc));
                if (i == 9) __builtin_amdgcn_raw_buffer_store_b64((u32x2){__float_as_uint(gt), tag}, gs, gso, 0, WT);
                asm volatile("" : "+v"(gacc), "+v"(gt));
            };
            auto step = [&](auto E) __attribute__((always_inline)) {
                constexpr int e = decltype(E)::value;
                constexpr int ti = (e == CS0) ? 0 : (e == CS1) ? 1 : (e == CS2) ? 2 : -1, tj = ti < 0 ? 0 : ti;
                constexpr unsigned char GA[36] = {0x66, 0x77, 0x76, 0x44, 0x55, 0x54, 0x65, 0x64, 0x75, 0x74, 0x22, 0x33, 0x32, 0x43, 0x42, 0x53, 0x52, 0x63, 0x62, 0x73, 0x72,
                                                  0x00, 0x11, 0x10, 0x21, 0x20, 0x31, 0x30, 0x41, 0x40, 0x51, 0x50, 0x61, 0x60, 0x71, 0x70};
                const char* sb_ = (const char*)Cre + COFF(wid, tj + 1);
                const char* sc_ = (const char*)Cim + COFF(wid, tj + 1);
#pragma unroll
                for (int q = 0; q < 36; ++q) {
                    const int g = GA[q] >> 4, a = GA[q] & 15;
                    MF(cv[g], A[a], UB[g - a]); SCHED();
                    if (e < 7) {
                        if (a == 1) RDU(UB[g - 1], g - 1);
                        if (q == 35) RDU(UB[7], 7);
                        if (g == 7) RDA(A[a], adr + 64 * (e + 1), a);
                        if (q == 11) UNEXT1();
                        if (q == 13) UNEXT2();
                        if (q == 15) UNEXT3();
                    }
                    if (ti >= 0) {
                        const int n = (q >= 3 && q <= 7) ? q - 3 : q == 10 ? 5 : q == 12 ? 6 : q == 14 ? 7 : -1;
                        if (q == 3) { if (ti == 0) WAIT_VM(1); else if (e == XE + 1) WAIT_VM(7); else WAIT_VM(0); }
                        if (n >= 0) CRD(tj, n & 7);
                        if (q == 18) CPIN(tj);
                        if (q == 18 || q == 23) asm volatile("s_add_u32 m0, %3, %4\n\ts_nop 0\n\tglobal_load_lds_dwordx4 %0, %2\n\tglobal_load_lds_dwordx4 %1, %2 offset:1024"
                                                             :: "v"(vB0), "v"(vB1), "s"(q == 18 ? sb_ : sc_), "s"(cst0), "i"(q == 18 ? 0 : 4096) : "memory", "scc");
                        if (q == 21 || q == 25) asm volatile("global_load_lds_dwordx4 %0, %2 offset:2048\n\tglobal_load_lds_dwordx4 %1, %2 offset:3072"
                                                             :: "v"(vB0), "v"(vB1), "s"(q == 21 ? sb_ : sc_) : "memory");
                    }
                    if (e == 0 && q >= 3 && q <= 7) gate_out(q - 3);
                    if (e == 0 && q >= 10 && q <= 18 && !(q & 1)) gate_out(5 + (q - 10) / 2);
                    if (e == 0 && q == 4) CTILE(wid, 0, cst0);
                    if (e == XE && q >= 23 && q <= 35 && (q & 1)) {
                        const int i = (q - 23) / 2;
                        const __amdgpu_buffer_rsrc_t xs = __builtin_amdgcn_make_buffer_rsrc((void*)x, 0, SEQ * DM * 2, 0x00020000);
                        const __amdgpu_buffer_rsrc_t ks = __builtin_amdgcn_make_buffer_rsrc((void*)dskip, 0, DM * 2, 0x00020000);
                        if (i < 4) asm volatile("buffer_load_dwordx2 %0, %1, %2, %3 offen offset:%4" : "=&v"(xx[(i >> 1) & 1][i & 1]) : "v"(vxo), "s"(xs), "s"((TR((i >> 1) & 1) * DM + d_base) * 2), "i"((i & 1) * 2048) : "memory");
                        if (i == 4) asm volatile("buffer_load_dwordx2 %0, %1, %2, %3 offen" : "=&v"(dk2) : "v"(vdo), "s"(ks), "s"(d_base * 2) : "memory");
                        if (i >= 5) asm volatile("buffer_load_dwordx4 %0, %1, %2, %3 offen sc1" : "=&v"(gv[(i - 5) & 1]) : "v"(vgo), "s"(gs), "s"(GSO((i - 5) & 1)) : "memory");
                    }
                    if (e == XE && q == 16) { vxo = (unsigned)(q4 * 32 * DM * 2 + l15 * 8); vdo = (unsigned)(l15 * 8); asm volatile("" : "+v"(vxo), "+v"(vdo)); }
                    if (e == XE && q == 17) { vgo = GV(q4); asm volatile("" : "+v"(vgo)); }
                    if (e == 6 && q == 30) hpv = __hip_atomic_load(hfp, RLX_AGENT);
                    SCHED();
                }
            };
            unroll<8>(step);
            WAIT_VM(0);
            XPIN();
        }
        PAD8(cv[0], cv[1], cv[2], cv[3]); PIN4(cv[4], cv[5], cv[6], cv[7]);
        STAMPT(dbg, 11, 384);
        const unsigned vh = (unsigned)((lane ^ 2) * 16);
        if (!__all(hpv == tag)) HWAIT();
        asm volatile("" ::: "memory");
#pragma unroll
        for (int f = 0; f < DEPTH; ++f) LOADF(f);
        STAMPT(dbg, 25, 384);
        {
            const __amdgpu_buffer_rsrc_t yrs = __builtin_amdgcn_make_buffer_rsrc(y2T, 0, DM * SEQ * 2, 0x00020000);
            const unsigned yo = (unsigned)(d * 4096 + (q4 & 1) * 512 + (l15 >> 3) * 256 + (q4 >> 1) * 128 + (l15 & 7) * 16);
#pragma unroll
            for (int g = 0; g < 8; g += 2) {
                const auto r0 = __builtin_amdgcn_permlane16_swap(pkbf(cv[g][0], cv[g][1]), pkbf(cv[g + 1][0], cv[g + 1][1]), false, false);
                const auto r1 = __builtin_amdgcn_permlane16_swap(pkbf(cv[g][2], cv[g][3]), pkbf(cv[g + 1][2], cv[g + 1][3]), false, false);
                __builtin_amdgcn_raw_buffer_store_b128((u32x4){r0[0], r1[0], r0[1], r1[1]}, yrs, yo + (unsigned)(g * 512), 0, WT);
            }
        }
        asm volatile("" ::: "memory");
        STAMPT(dbg, 15, 384);
        __builtin_amdgcn_s_setprio(PRIO2_L);
    }
    {
        const int lane = fresh_lane(), l15 = lane & 15, q4 = lane >> 4;
        const int ring = wid >> 2;
        const unsigned va = (unsigned)((lane ^ (ring * 2)) * 16), vh = va;
        const unsigned tag = lds_word(smem, TAG_OFF);
        const int cb0 = ring ? KC_OFF : GA_OFF, cst = ring ? CV_W : 8192;
        f32x4 ac[2][4];
        unsigned ca[2], fa[2], fl = 0, fv = 0;
#define CHAIN(u) do { const int c_ = (wid - ((u) >> 1)) & 3; ca[(u) & 1] = va + (unsigned)(cb0 + c_ * cst + ((u) & 1) * 4096); \
                      fa[(u) & 1] = (unsigned)(RF_OFF + ring * 32 + ((u) & 1) * 16 + c_ * 4); asm volatile("" : "+v"(ca[(u) & 1]), "+v"(fa[(u) & 1])); } while (0)
#define RDIN(dst, a, f_) asm volatile("ds_read_b32 %0, %5\n\tds_read_b128 %1, %6\n\tds_read_b128 %2, %6 offset:1024\n\tds_read_b128 %3, %6 offset:2048\n\tds_read_b128 %4, %6 offset:3072" \
                     : "=&v"(fl), "=&v"(dst[0]), "=&v"(dst[1]), "=&v"(dst[2]), "=&v"(dst[3]) : "v"(f_), "v"(a) : "memory")
#define LWAIT(dst) asm volatile("s_waitcnt lgkmcnt(0)" : "+v"(fl), "+v"(dst[0]), "+v"(dst[1]), "+v"(dst[2]), "+v"(dst[3]) :: "memory")
        CHAIN(0); CHAIN(1);
        WAIT_LGKM0();
        CPIN(0); CPIN(1); CPIN(2);
        CREAD(3);
        int tr[2] = {TR(0), TR(1)};
        unsigned gw[2][2];
        {
            unsigned spins = 0;
            while (__builtin_expect(!__all((gv[0].y == tag) & (gv[0].w == tag) & (gv[1].y == tag) & (gv[1].w == tag)), 0)) {
                if (++spins > SPIN_MAX) break;
#pragma unroll
                for (int r = 0; r < 2; ++r) asm volatile("s_nop 4\n\tbuffer_load_dwordx4 %0, %1, %2, %3 offen sc1\n\ts_waitcnt vmcnt(0)" : "=&v"(gv[r]) : "v"(GV(q4)), "s"(gs), "s"(__builtin_amdgcn_readfirstlane(GSO(r))) : "memory");
            }
            gw[0][0] = gv[0].x; gw[0][1] = gv[0].z; gw[1][0] = gv[1].x; gw[1][1] = gv[1].z;
            PIN4(gw[0][0], gw[0][1], gw[1][0], gw[1][1]);
        }
        unsigned cpv = 0, yy[2][4];
        f32x2 zz[2][4];
        unsigned exo = 0, exi = 0, efo = 0, efi = 0;
        auto send = [&](int r) __attribute__((always_inline)) {
            const unsigned one = 1u;
#pragma unroll
            for (int n = 0; n < 4; ++n) { const f32x2 v = {ac[r][n][2], ac[r][n][3]}; asm volatile("ds_write_b64 %0, %1 offset:%2" :: "v"(exo), "v"(v), "i"(r * 4096 + n * 512) : "memory"); }
            asm volatile("ds_write_b32 %0, %1 offset:%2" :: "v"(efo), "v"(one), "i"(r * 4) : "memory");
        };
        STAMPT(dbg, 5, 0);
        auto body = [&](auto F) __attribute__((always_inline)) {
            constexpr int f = decltype(F)::value;
            constexpr int u = f >> 3, k = f & 7;
            f32x4* an = ac[u & 1];
            f32x4* ao = ac[(u + 1) & 1];
#pragma unroll
            for (int n = 0; n < 4; ++n) {
                if (k == 0 && u < 2) MF0(an[n], af[f % DEPTH], cf_[k][n]); else MF(an[n], af[f % DEPTH], cf_[k][n]);
                SCHED();
                const int g = k * 4 + n;
                if (u >= 1 && u < 7) {
                    if (g == 1) { fv = ((u - 1) >> 1) + 1; asm volatile("" : "+v"(fv)); }
                    if (g == 4 || g == 6 || g == 8 || g == 10) asm volatile("ds_write_b128 %0, %1 offset:%2" :: "v"(ca[(u + 1) & 1]), "v"(ao[(g - 4) / 2]), "i"((g - 4) * 512) : "memory");
                    if (g == 12) asm volatile("ds_write_b32 %0, %1" :: "v"(fa[(u + 1) & 1]), "v"(fv) : "memory");
                    if (g == 18) CHAIN(u + 1);
                    if (g == 20) asm volatile("ds_read_b32 %0, %1" : "=&v"(fl) : "v"(fa[(u + 1) & 1]) : "memory");
                    if (g > 20 && g <= 24) asm volatile("ds_read_b128 %0, %1 offset:%2" : "=&v"(ao[g - 21]) : "v"(ca[(u + 1) & 1]), "i"((g - 21) * 1024) : "memory");
                    if (g == 30) {
                        LWAIT(ao);
                        if (__builtin_expect(__builtin_amdgcn_readfirstlane(fl) != (unsigned)((u + 1) >> 1), 0)) {
                            unsigned spins = 0;
#pragma unroll 1
                            do {
                                __builtin_amdgcn_s_sleep(1);
                                RDIN(ao, ca[(u + 1) & 1], fa[(u + 1) & 1]); LWAIT(ao);
                                if (++spins > SPIN_MAX * 16u) break;
                            } while (__builtin_amdgcn_readfirstlane(fl) != (unsigned)((u + 1) >> 1));
                        }
                    }
                }
                if (u == 7 && g == 6) send(0);
                if (f >= ZF && f < ZF + 4) {
                    constexpr int r = ((f - ZF) >> 1) & 1, j = (f - ZF) & 1;
                    const unsigned xw = xx[r][j][n >> 1], dw = dk2[n >> 1];
                    const float xf = (n & 1) ? hi16(xw) : lo16(xw), df = (n & 1) ? hi16(dw) : lo16(dw), yf = j ? hi16(yy[r][n]) : lo16(yy[r][n]);
                    zz[r][n][j] = xf * fmaf(hi16(gw[r][j]), yf, lo16(gw[r][j]) * df);
                    asm volatile("" : "+v"(zz[r][n]));
                }
                if (f == ZF + 4) {
                    zz[0][n] = (f32x2){fmaf(lo16(gw[0][0]), ac[0][n][0], zz[0][n][0]), fmaf(lo16(gw[0][1]), ac[0][n][1], zz[0][n][1])};
                    asm volatile("" : "+v"(zz[0][n]));
                }
                SCHED();
            }
            if (f + DEPTH < 64) LOADF(f + DEPTH);
            if (f == DEPTH + 1 && ring) {
                WAIT_VM(6);
                if (lane == 0) __hip_atomic_store((gu32*)(ws + WS_CFLAG) + cb * 32 + (wid & 3), tag, RLX_AGENT);
                SCHED();
            }
            if (f == YPOLL && ring) { const int l15 = fresh_lane() & 15; cpv = __hip_atomic_load((gu32*)(ws + WS_CFLAG) + (nb * 16 + (wid & 3) * 4 + (l15 >> 2)) * 32 + (l15 & 3), RLX_AGENT); SCHED(); }
            if (f == YPOLL + DEPTH && ring) {
                const int ln = fresh_lane(), l15 = ln & 15, q4 = ln >> 4;
                if (__builtin_expect(!__all(cpv == tag), 0)) {
                    unsigned spins = 0;
                    while (!__all(__hip_atomic_load((gu32*)(ws + WS_CFLAG) + (nb * 16 + (wid & 3) * 4 + (l15 >> 2)) * 32 + (l15 & 3), RLX_AGENT) == tag)) {
                        if (++spins > SPIN_MAX) break;
                        __builtin_amdgcn_s_sleep(1);
                    }
                }
                const unsigned lx = lbase + LA_OFF + (wid & 3) * 8192;
#pragma unroll
                for (int i = 0; i < 4; ++i) {
                    const unsigned vo = (unsigned)((d_base + (wid & 3) * 16 + i * 4 + q4) * 4096 + tb * 256 + ((l15 ^ ((wid & 3) * 4 + i)) * 16));
                    GLDS_SC1(y2T, vo, lx, i * 1024);
                }
                SCHED();
            }
            if (f == YPOLL + 2 * DEPTH + 1 && ring) {
                WAIT_VM(6);
                SETW(YFLAG_OFF, wid & 3, 1u);
                SCHED();
            }
            if (f == 55) {
                const int ln = fresh_lane(), c_ = (wid - 3) & 3;
                const int eg = GA_OFF + c_ * 8192, el = KC_OFF + c_ * CV_W;
                exo = (unsigned)((ring ? el : eg) + ln * 8);
                exi = (unsigned)((ring ? eg : el) + ln * 8);
                efo = (unsigned)(EF_OFF + wid * 8); efi = (unsigned)(EF_OFF + (wid ^ 4) * 8);
                PIN4(exo, exi, efo, efi);
                SCHED();
            }
            if (f == ZF - 2) {
                const int ln = fresh_lane(), l15 = ln & 15, q4 = ln >> 4;
                wait_lds4(smem, YFLAG_OFF);
#pragma unroll
                for (int r = 0; r < 2; ++r) {
                    const int jb = JB(6 + r);
                    const int ya = LA_OFF + (l15 >> 2) * 8192 + (l15 & 3) * 1024 + (((((jb >> 1) & 1) * 8 + (jb >> 2) + q4 * 2) ^ l15) * 16) + (jb & 1) * 8 + ring * 4;
#pragma unroll
                    for (int e = 0; e < 4; ++e) yy[r][e] = *(const unsigned*)(smem + ya + e * 256);
                }
                SCHED();
            }
        };
        unroll<64>(body);
        PAD8(ac[1][0], ac[1][1], ac[1][2], ac[1][3]);
        send(1);
        STAMPT(dbg, 6, 0);
        const __amdgpu_buffer_rsrc_t ors = __builtin_amdgcn_make_buffer_rsrc(out, 0, SEQ * DM * 4, 0x00020000);
        const unsigned ov = (unsigned)((lane >> 4) * (32 * DM * 4) + (lane & 15) * 16);
#pragma unroll
        for (int r = 0; r < 2; ++r) {
            f32x2 in[4];
#define EXRD() asm volatile("ds_read_b32 %0, %5 offset:%7\n\tds_read_b64 %1, %6 offset:%8\n\tds_read_b64 %2, %6 offset:%9\n\tds_read_b64 %3, %6 offset:%10\n\tds_read_b64 %4, %6 offset:%11" \
                             : "=&v"(fl), "=&v"(in[0]), "=&v"(in[1]), "=&v"(in[2]), "=&v"(in[3]) \
                             : "v"(efi), "v"(exi), "i"(r * 4), "i"(r * 4096), "i"(r * 4096 + 512), "i"(r * 4096 + 1024), "i"(r * 4096 + 1536) : "memory")
            EXRD();
            if (r == 0) {
#pragma unroll
                for (int n = 0; n < 4; ++n) {
                    zz[1][n] = (f32x2){fmaf(lo16(gw[1][0]), ac[1][n][0], zz[1][n][0]), fmaf(lo16(gw[1][1]), ac[1][n][1], zz[1][n][1])};
                    asm volatile("" : "+v"(zz[1][n]));
                }
            }
            LWAIT(in);
            if (__builtin_expect(__builtin_amdgcn_readfirstlane(fl) == 0u, 0)) {
                unsigned spins = 0;
#pragma unroll 1
                do {
                    EXRD(); LWAIT(in);
                    if (++spins > SPIN_MAX * 16u) break;
                } while (__builtin_amdgcn_readfirstlane(fl) == 0u);
            }
            f32x4 o[2];
#pragma unroll
            for (int j = 0; j < 2; ++j)
#pragma unroll
                for (int e = 0; e < 4; ++e) o[j][e] = fmaf(lo16(gw[r][j]), in[e][j], zz[r][e][j]);
#pragma unroll
            for (int j = 0; j < 2; ++j)
                __builtin_amdgcn_raw_buffer_store_b128(__builtin_bit_cast(u32x4, o[j]), ors, ov + (unsigned)(((tr[r] + j) * DM + d_base) * 4), 0, WT);
        }
        STAMPT(dbg, 7, 0);
        STAMPT(dbg, 16, 384);
        if (tb == 15 && nb == 0 && wid == 0 && lane == 0) __hip_atomic_store((gu32*)(ws + WS_EPOCH), tag, RLX_AGENT);
    }
}

extern "C" void kernel_launch(void* const* d_in, const int* in_sizes, int n_in,
                              void* d_out, int out_size, void* d_ws, size_t ws_size,
                              hipStream_t stream) {
    const u16* x = (const u16*)d_in[0];
    const u16* lre = (const u16*)d_in[1];
    const u16* lim = (const u16*)d_in[2];
    const u16* Bre = (const u16*)d_in[3];
    const u16* Bim = (const u16*)d_in[4];
    const u16* Cre = (const u16*)d_in[5];
    const u16* Cim = (const u16*)d_in[6];
    const u16* logdt = (const u16*)d_in[7];
    const u16* dskip = (const u16*)d_in[8];
    const u16* kin = (const u16*)d_in[10];
    const u16* vin = (const u16*)d_in[11];
    const u16* shiftk = (const u16*)d_in[12];
    const u16* ssmk = (const u16*)d_in[13];
    const u16* ssmD = (const u16*)d_in[14];
    const u16* gatew = (const u16*)d_in[15];
    float* out = (float*)d_out;

    char* ws = (char*)d_ws;

    hipFuncSetAttribute((const void*)k1_kernel, hipFuncAttributeMaxDynamicSharedMemorySize, K1_LDS);

    k1_kernel<<<256, 512, K1_LDS, stream>>>(x, Bre, Bim, kin, vin, shiftk, gatew, ws, lre, lim, logdt, ssmk, ssmD, Cre, Cim, dskip, out);
}
#if defined(__HIP_DEVICE_COMPILE__)
#pragma clang attribute pop
#endif
```

```cpp
#if defined(__HIP_DEVICE_COMPILE__)
#pragma clang attribute push(__attribute__((target("no-packed-fp32-ops"))), apply_to = function)
#endif
#include <hip/hip_runtime.h>
#include <hip/hip_bf16.h>
#include <stdint.h>

#define TIDX ((int)__builtin_amdgcn_workitem_id_x())
#define BIDX ((int)__builtin_amdgcn_workgroup_id_x())
typedef unsigned short u16;
typedef __attribute__((ext_vector_type(8))) short bf16x8;
typedef __attribute__((ext_vector_type(4))) float f32x4;
typedef __attribute__((ext_vector_type(2))) float f32x2;
typedef __attribute__((ext_vector_type(2))) __bf16 bf16x2;
typedef __attribute__((ext_vector_type(4))) unsigned u32x4;
typedef __attribute__((ext_vector_type(2))) unsigned u32x2;
typedef __attribute__((address_space(1))) unsigned long long gu64;
typedef __attribute__((address_space(1))) unsigned gu32;
typedef __attribute__((address_space(3))) void lds_void;
typedef __attribute__((address_space(3))) unsigned lds_u32;
typedef __attribute__((ext_vector_type(4))) short s16x4;
typedef __attribute__((address_space(3))) s16x4 lds_s16x4;
#define TRD(a) __builtin_amdgcn_ds_read_tr16_b64_v4i16((lds_s16x4*)(size_t)(a))

#define DBGON 0
#define STAMPT(buf, k, t) do { } while (0)
#define CODE_LINES 262
#define SEQ 2048
#define DM 1024
#define SPIN_MAX (1u << 18)
#define PRIO_G 2
#define PSLEEP 4
#define PRIO_L 3
#define WT 16

__device__ __forceinline__ float bf2f(u16 v) { return __uint_as_float(((unsigned)v) << 16); }
__device__ __forceinline__ u16 f2bf(float f) { __bf16 b = (__bf16)f; return __builtin_bit_cast(u16, b); }
__device__ __forceinline__ float rbf(float f) { return bf2f(f2bf(f)); }
__device__ __forceinline__ unsigned pkbf(float lo, float hi) { return __builtin_bit_cast(unsigned, __builtin_convertvector((f32x2){lo, hi}, bf16x2)); }
__device__ __forceinline__ float lo16(unsigned u) { return __uint_as_float(u << 16); }
__device__ __forceinline__ float hi16(unsigned u) { return __uint_as_float(u & 0xffff0000u); }

struct cf { float r, i; };
__device__ __forceinline__ cf cmul(cf a, cf b) { return {a.r * b.r - a.i * b.i, a.r * b.i + a.i * b.r}; }
__device__ __forceinline__ cf cfma(cf a, cf b, cf c) {
    return {fmaf(a.r, b.r, fmaf(-a.i, b.i, c.r)), fmaf(a.r, b.i, fmaf(a.i, b.r, c.i))};
}
__device__ __forceinline__ f32x4 splat4(float v) { return (f32x4){v, v, v, v}; }
__device__ __forceinline__ f32x4 fma4(f32x4 a, f32x4 b, f32x4 c) { return __builtin_elementwise_fma(a, b, c); }
__device__ __forceinline__ f32x4 mul4(f32x4 a, f32x4 b) { return a * b; }

__device__ __forceinline__ float red32(float a, float b) {
    const auto r = __builtin_amdgcn_permlane32_swap(__float_as_uint(a), __float_as_uint(b), false, false);
    return __uint_as_float(r[0]) + __uint_as_float(r[1]);
}
__device__ __forceinline__ float red16(float x) {
    const auto r = __builtin_amdgcn_permlane16_swap(__float_as_uint(x), __float_as_uint(x), false, false);
    return __uint_as_float(r[0]) + __uint_as_float(r[1]);
}
__device__ __forceinline__ int fresh_lane() {
    int l;
    asm volatile("v_mbcnt_lo_u32_b32 %0, -1, 0\n\tv_mbcnt_hi_u32_b32 %0, -1, %0" : "=v"(l));
    return l;
}
#define BAR() __builtin_amdgcn_s_barrier()
#define WAIT_VM(n) asm volatile("s_waitcnt vmcnt(" #n ")" ::: "memory")
#define WAIT_LGKM0() __builtin_amdgcn_s_waitcnt(0xC07F)
#define SCHED() __builtin_amdgcn_sched_barrier(0)
#define MF(c, a, b) asm volatile("v_mfma_f32_16x16x32_bf16 %0, %1, %2, %0" : "+v"(c) : "v"(a), "v"(b))
#define PAD8(c0, c1, c2, c3) asm volatile("s_nop 7" : "+v"(c0), "+v"(c1), "+v"(c2), "+v"(c3))
#define PIN4(c0, c1, c2, c3) asm volatile("" : "+v"(c0), "+v"(c1), "+v"(c2), "+v"(c3))
#define GLDS_S(sbase, voff, sl, IMM) \
    asm volatile("s_add_u32 m0, %2, %3\n\ts_nop 0\n\tglobal_load_lds_dwordx4 %0, %1" \
                 :: "v"(voff), "s"(sbase), "s"(sl), "i"(IMM) : "memory", "scc")
#define GLDS_K(sbase, voff, sl, IMM) \
    asm volatile("s_add_u32 m0, %2, %3\n\ts_nop 0\n\tglobal_load_lds_dwordx4 %0, %1 nt" \
                 :: "v"(voff), "s"(sbase), "s"(sl), "i"(IMM) : "memory", "scc")
#define RLX_AGENT __ATOMIC_RELAXED, __HIP_MEMORY_SCOPE_AGENT

#define UNITS_A 0x0756075607560560ull
#define UNITS_B 0x1432143214321432ull
#define RA(t) ((int)((UNITS_A >> (4 * (t))) & 15) * 16384)
#define RB(t) ((int)((UNITS_B >> (4 * (t))) & 15) * 16384)
#define LA_OFF 0
#define GA_OFF 32768
#define CV_COPY 4160
#define CV_W (4 * CV_COPY)
#define KC_OFF 65536
#define ST_OFF 114688
#define ST_V 8832
#define ST_SZ (ST_V + 8 * 1056)
#define U_OFF (KC_OFF + 16 * CV_COPY)
#define HT_OFF (U_OFF + 256 + 16384)
#define GW_OFF (HT_OFF + 8192)
#define TAG_OFF (GW_OFF + 2048)
#define UFLAG_OFF (TAG_OFF + 16)
#define G1D_OFF (UFLAG_OFF + 16)
#define MISC_OFF (G1D_OFF + 16)
#define YFLAG_OFF (MISC_OFF + 16)
#define RF_OFF (YFLAG_OFF + 16)
#define EF_OFF (RF_OFF + 64)
#define COK_OFF (EF_OFF + 64)
#define NFLAGW 52
#define DUMMY_OFF (COK_OFF + 16)
#define K1_LDS (DUMMY_OFF + 256)
#define WS_GRAN 0
#define WS_KVFLAG (256u << 10)
#define WS_EPOCH (WS_KVFLAG + 65536)
#define WS_CFLAG (WS_EPOCH + 4096)
#define WS_HFLAG (384u << 10)
#define WS_GATE (512u << 10)

#define CODE_TOUCH(vl) asm volatile("s_getpc_b64 s[98:99]\n\ts_add_u32 s98, s98, k1_kernel@rel32@lo+4\n\ts_addc_u32 s99, s99, k1_kernel@rel32@hi+12\n\t" \
                                    "s_mov_b32 m0, %1\n\ts_nop 0\n\tglobal_load_lds_dword %0, s[98:99]" :: "v"((unsigned)(vl) * 128u), "i"(DUMMY_OFF) : "s98", "s99", "scc", "memory")
#define RD128(a, off) (*(const __attribute__((address_space(3))) bf16x8*)(size_t)(unsigned)((a) + (off)))
#define RDA(dst, A, F) do { const s16x4 lo_ = TRD((A) + 512 * (F) + 8); SCHED(); const s16x4 hi_ = TRD((A) + 512 * (F)); SCHED(); \
                            dst = __builtin_shufflevector(lo_, hi_, 0, 1, 2, 3, 4, 5, 6, 7); } while (0)
#define TOUCH(sbase, voff) asm volatile("s_mov_b32 m0, %2\n\ts_nop 0\n\tglobal_load_lds_dword %0, %1" :: "v"(voff), "s"(sbase), "i"(DUMMY_OFF) : "memory")
#define FLAGD(off) do { unsigned long long pc_; asm volatile("s_getpc_b64 %0\n\ts_mov_b32 m0, %2\n\ts_mov_b64 exec, 1\n\tglobal_load_lds_dword %1, %0\n\ts_mov_b64 exec, -1" \
                                                             : "=&s"(pc_) : "v"(0u), "s"(__builtin_amdgcn_readfirstlane(off)) : "memory"); } while (0)
#define GLDS_SC1(sbase, voff, sl, IMM) \
    asm volatile("s_add_u32 m0, %2, %3\n\ts_nop 0\n\tglobal_load_lds_dwordx4 %0, %1 sc1" \
                 :: "v"(voff), "s"(sbase), "s"(sl), "i"(IMM) : "memory", "scc")
__device__ __forceinline__ unsigned mv(unsigned v) { unsigned r; asm("v_mov_b32 %0, %1" : "=v"(r) : "v"(v)); return r; }
__device__ __forceinline__ unsigned al(unsigned hi, unsigned lo) { unsigned r; asm volatile("v_alignbit_b32 %0, %1, %2, 16" : "=v"(r) : "v"(hi), "v"(lo)); return r; }
__device__ __forceinline__ u32x2 pred2(const u32x4& a, unsigned f0, unsigned f1) {
    return (u32x2){(unsigned)__builtin_amdgcn_update_dpp((int)f0, (int)a.z, 0x138, 0xf, 0xf, false), (unsigned)__builtin_amdgcn_update_dpp((int)f1, (int)a.w, 0x138, 0xf, 0xf, false)};
}
#define RL63(v) ((unsigned)__builtin_amdgcn_readlane((int)(v), 63))
__device__ __forceinline__ void wait_lds4(char* smem, int off) {
    const unsigned a = (unsigned)(size_t)(lds_void*)smem + off;
    unsigned spins = 0;
    for (;;) {
        u32x4 f;
        asm volatile("ds_read_b128 %0, %1\n\ts_waitcnt lgkmcnt(0)" : "=&v"(f) : "v"(a) : "memory");
        if (__builtin_amdgcn_readfirstlane((f.x != 0u) & (f.y != 0u) & (f.z != 0u) & (f.w != 0u))) break;
        if (++spins > SPIN_MAX * 16u) break;
        __builtin_amdgcn_s_sleep(1);
    }
}
__device__ __forceinline__ unsigned lds_word(char* smem, int off) {
    const unsigned a = (unsigned)(size_t)(lds_void*)smem + off;
    unsigned f;
    asm volatile("ds_read_b32 %0, %1\n\ts_waitcnt lgkmcnt(0)" : "=&v"(f) : "v"(a) : "memory");
    return __builtin_amdgcn_readfirstlane(f);
}
__device__ __forceinline__ void wait_lds1(char* smem, int off) {
    unsigned spins = 0;
    while (lds_word(smem, off) == 0u) {
        if (++spins > SPIN_MAX * 16u) break;
        __builtin_amdgcn_s_sleep(1);
    }
}
#define SETW(off, i, v) __hip_atomic_store((lds_u32*)(smem + (off)) + (i), (v), __ATOMIC_RELAXED, __HIP_MEMORY_SCOPE_WORKGROUP)

#define PRIO_CV 1
#define PRIO_KV 3
#define CS0 2
#define CS1 4
#define CS2 6
#define XE 5
#define YPOLL 32
#define ZF 59
template <int I> struct IC { static constexpr int value = I; };
template <int N, int I = 0, class F> __device__ __forceinline__ void unroll(F& f) { if constexpr (I < N) { f(IC<I>{}); unroll<N, I + 1>(f); } }
#define MF0(c, a, b) asm volatile("v_mfma_f32_16x16x32_bf16 %0, %1, %2, 0" : "=&v"(c) : "v"(a), "v"(b))
#define DEPTH 6
#define PRIO2_G 0
#define PRIO2_L 1
__device__ __forceinline__ cf pick4(int q, cf a1, cf a2, cf a3) {
    return {q == 0 ? 1.f : q == 1 ? a1.r : q == 2 ? a2.r : a3.r, q == 0 ? 0.f : q == 1 ? a1.i : q == 2 ? a2.i : a3.i};
}
__device__ __forceinline__ void idle_mfma(unsigned a, unsigned b, int n) {
    u32x4 p = {a * 2654435761u, b * 40503u, a * 2246822519u, b * 3266489917u};
    p = (p & 0xbfffbfffu) | 0x3c003c00u;
    u32x4 q = p.yzwx ^ 0x01230456u;
    f32x4 w;
    asm volatile("s_nop 1" : "+v"(p), "+v"(q));
    MF0(w, p, q);
#pragma unroll 1
    for (int i = 0; i < n; ++i) { MF(w, q, p); MF(w, p, p); MF(w, q, q); MF(w, p, q); }
    asm volatile("s_nop 7" : "+v"(w));
}
#define PINC(c) asm volatile("" : "+v"((c).r), "+v"((c).i))

extern "C" __global__ __launch_bounds__(512, 2) void k1_kernel(
        const u16* __restrict__ x, const u16* __restrict__ Bre, const u16* __restrict__ Bim,
        const u16* __restrict__ kin, const u16* __restrict__ vin, const u16* __restrict__ shiftk, const u16* __restrict__ gatew, char* ws,
        const u16* __restrict__ lre, const u16* __restrict__ lim, const u16* __restrict__ logdt,
        const u16* __restrict__ ssmk, const u16* __restrict__ ssmD,
        const u16* __restrict__ Cre, const u16* __restrict__ Cim, const u16* __restrict__ dskip, float* __restrict__ out) {
    extern __shared__ __attribute__((aligned(16))) char smem[];
#define h ((u16*)(ws + (1u << 20)))
#define kvT ((u16*)(ws + (9u << 20)))
#define y2T ((u16*)(ws + (13u << 20)))
#define dbg ((unsigned long long*)(ws + (64u << 20)))
    const int tid = TIDX;
    if (tid < 40 && tid + ((BIDX >> 3) & 7) * 40 < CODE_LINES) CODE_TOUCH(tid + ((BIDX >> 3) & 7) * 40);
    const int lane = tid & 63;
    int wid = __builtin_amdgcn_readfirstlane(tid >> 6);
    const int b = BIDX;
    const int xcd = b & 7, idx = b >> 3;
    const int tb = ((xcd >> 1) ^ (xcd >> 2)) * 4 + (idx & 3), nb = (xcd & 1) * 8 + (idx >> 2);
    const int cb = xcd * 32 + idx;
    const int t_base = tb * 128, p_base = nb * 64, d_base = nb * 64;
    const int l15 = lane & 15, q4 = lane >> 4;
    unsigned stv = 0, sth = 0;
    STAMPT(dbg, 0, 0);
    const unsigned lbase = (unsigned)(size_t)(lds_void*)smem;
    const int rot1 = (xcd >> 1) * 4 + (xcd & 1) * 8 + (idx >> 2) + (idx & 1);
    unsigned voA[4] = {0u, 0u, 0u, 0u}, voB[4] = {0u, 0u, 0u, 0u}, lw0 = 0;
    if (wid == 4 || wid == 5) {
        __builtin_amdgcn_s_setprio(PRIO_L);
        const int lh = wid - 4;
        const unsigned ch = (unsigned)(((lane & 7) ^ (lane >> 4)) * 16);
        voA[0] = (unsigned)(t_base + lh * 16 + (lane >> 5) * 32 + ((lane >> 3) & 3)) * 2048u + ch;
        voA[1] = (voA[0] ^ 64u) + (131072u - 1024u);
        voA[2] = voA[0] + 6144u; voA[3] = voA[1] + 6144u;
        voB[0] = (unsigned)(p_base + lh * 32 + (lane >> 3)) * 2048u + ch;
        voB[1] = (voB[0] ^ 64u) + (16384u - 1024u);
        voB[2] = voB[0] + 30720u; voB[3] = voB[1] + 30720u;
        PIN4(voA[0], voA[1], voA[2], voA[3]); PIN4(voB[0], voB[1], voB[2], voB[3]);
#define GLDS4(sbase, v0, v1, v2, v3, sl, IMM) \
            asm volatile("s_add_u32 m0, %5, %6\n\ts_nop 0\n\tglobal_load_lds_dwordx4 %0, %4\n\tglobal_load_lds_dwordx4 %1, %4 offset:1024\n\t" \
                         "global_load_lds_dwordx4 %2, %4 offset:2048\n\tglobal_load_lds_dwordx4 %3, %4 offset:3072" \
                         :: "v"(v0), "v"(v1), "v"(v2), "v"(v3), "s"(sbase), "s"(sl), "i"(IMM) : "memory", "scc")
#define STAGE_A(kt, sl) do { const char* sa_ = (const char*)x + (((kt) + rot1) & 15) * 128; \
            GLDS4(sa_, voA[0], voA[1], voA[2], voA[3], sl, 0); GLDS4(sa_ + 16384, voA[0], voA[1], voA[2], voA[3], sl, 4096); } while (0)
#define STAGE_B(kt, sl) do { const int kr_ = (((kt) + rot1) & 15) * 128; const char* sb_ = (const char*)Bre + kr_; const char* sc_ = (const char*)Bim + kr_; \
            GLDS4(sb_, voB[0], voB[1], voB[2], voB[3], sl, 0); GLDS4(sc_, voB[0], voB[1], voB[2], voB[3], sl, 4096); } while (0)
        lw0 = lbase + lh * 8192;
        STAGE_A(0, lw0 + RA(0)); STAGE_B(0, lw0 + RB(0));
        idle_mfma(voA[0], voB[1], 12);
        STAGE_A(1, lw0 + RA(1)); STAGE_B(1, lw0 + RB(1));
    }
    const int sblk = b >> 3, dblk = b & 7;
    u32x2 tw[4];
    unsigned epoch;
    asm volatile("" : "=v"(tw[0]), "=v"(tw[1]), "=v"(tw[2]), "=v"(tw[3]), "=v"(epoch));
    if (wid >= 6) {
        __builtin_amdgcn_s_setprio(PRIO_L);
        const int vw = wid - 6;
        const int dch = dblk * 128 + vw * 64 + l15;
        const unsigned lk = lbase + ST_OFF + vw * ST_SZ;
        const unsigned vk = (unsigned)((lane >> 3) * DM * 2 + (((lane & 7) ^ ((lane >> 4) & 1)) * 16));
        const char* kb = (const char*)kin + (size_t)(sblk * 64 - 4) * (DM * 2) + dblk * 256 + vw * 128;
#define KLD(n) GLDS_K(kb + (n) * 8 * DM * 2, vk, lk, (n) * 1024 + ((n) >> 1) * 32)
#define VLD(n) GLDS_K(vb + (((n) >> 1) * 16 + ((n) & 1) * 2) * DM * 2, vv_, lk, ST_V + (n) * 1056)
        if (vw == 1) {
            const unsigned vg = (unsigned)((lane * 16 + rot1 * 128) & 2047), lg = lbase + GW_OFF;
            GLDS_S(gatew, vg, lg, 0); GLDS_S(gatew, vg ^ 1024u, lg, 1024);
        }
        asm volatile("global_load_dwordx2 %0, %5, %7\n\tglobal_load_dwordx2 %1, %5, %7 offset:128\n\tglobal_load_dwordx2 %2, %5, %7 offset:256\n\t"
                     "global_load_dwordx2 %3, %5, %7 offset:384\n\tglobal_load_dword %4, %6, %8 sc1"
                     : "+v"(tw[0]), "+v"(tw[1]), "+v"(tw[2]), "+v"(tw[3]), "+v"(epoch) : "v"((unsigned)(dch * 8)), "v"((unsigned)WS_EPOCH), "s"(shiftk), "s"(ws) : "memory");
        if (sblk == 0 && lane < 32) *(u32x4*)(smem + ST_OFF + vw * ST_SZ + lane * 16) = (u32x4){0u, 0u, 0u, 0u};
        if (sblk != 0 || lane >= 32) KLD(0);
        KLD(1); KLD(2); KLD(3); KLD(4); KLD(5); KLD(6); KLD(7);
        if (lane < 32) KLD(8);
        __builtin_amdgcn_s_setprio(0);
    }
    asm volatile("" : "+s"(wid));

    const int wf_ = (nb >> 3) ^ (xcd >> 2), tq = (xcd >> 1) & 1;
#define WSL(w) ((((w) >> 2) ^ wf_) * 4 + ((w) & 3))
#define COFF(w, ti) ((2 * WSL(w) + (((ti) >> 1) ^ tq)) * 128 + ((ti) & 1) * 4096)
    bf16x8 cf_[8][4];
    bf16x8 af[DEPTH];
    int cra, crb;
#define CT_SETUP() const unsigned vB0 = (unsigned)((d_base + (lane >> 3) * 4) * 2048 + (((lane & 7) ^ (lane >> 4)) * 16)), vB1 = (vB0 ^ 64u) + 65536u - 1024u
#define CTILE(w, ti, dst) do { const int co_ = COFF(w, ti); const unsigned ds_ = (dst); \
                               GLDS4((const char*)Cre + co_, vB0, vB1, vB0, vB1, ds_, 0); GLDS4((const char*)Cim + co_, vB0, vB1, vB0, vB1, ds_, 4096); } while (0)
#define CF(ti, n) cf_[4 * ((ti) >> 1) + ((n) >> 1)][2 * ((ti) & 1) + ((n) & 1)]
#define CRD(ti, n) CF(ti, n) = RD128((((n) & 4) ? crb : cra) ^ (((n) & 2) * 16), ((n) & 1) * 2048)
#define CREAD(ti) do { _Pragma("unroll") for (int n_ = 0; n_ < 8; ++n_) CRD(ti, n_); } while (0)
#define CPIN(ti) do { PIN4(CF(ti, 0), CF(ti, 1), CF(ti, 2), CF(ti, 3)); PIN4(CF(ti, 4), CF(ti, 5), CF(ti, 6), CF(ti, 7)); } while (0)
#define CRA(base) ((base) + (q4 >> 1) * 4096 + l15 * 128 + (((q4 & 1) ^ ((l15 >> 1) & 7)) * 16))
    const int rot2 = (idx & 16) >> 2;
#define JB(u) (((((u) ^ (rot2 >> 2)) & 1) << 2) | ((wid - ((u) >> 1) + rot2) & 3))
    const __amdgpu_buffer_rsrc_t hs = __builtin_amdgcn_make_buffer_rsrc((char*)h + (size_t)tb * 524288 + WSL(wid) * 8192, 0, 524288, 0x00020000);
#define LOADF(f) do { af[(f) % DEPTH] = __builtin_bit_cast(bf16x8, __builtin_amdgcn_raw_buffer_load_b128(hs, vh + ((f) & 3) * 1024, JB((f) >> 3) * 65536 + ((((f) >> 2) & 1) ^ tq) * 4096, 16)); SCHED(); } while (0)
#define HPOLL_SETUP() gu32* hfp = (gu32*)(ws + WS_HFLAG) + (tb * 16 + 2 * WSL(wid) + (lane & 1)) * 32
#define HWAIT() do { unsigned spins = 0; \
            while (!__all(__hip_atomic_load(hfp, RLX_AGENT) == tag)) { if (++spins > SPIN_MAX) break; __builtin_amdgcn_s_sleep(PSLEEP); } asm volatile("" ::: "memory"); } while (0)

    const __amdgpu_buffer_rsrc_t gs = __builtin_amdgcn_make_buffer_rsrc(ws + WS_GATE, 0, 32768, 0x00020000);
#define GV(q4) ((unsigned)(((q4) >> 1) * 128 + ((q4) & 1) * 32))
#define GSO(r) (tb * 2048 + JB(6 + (r)) * 256 + (wid >> 2) * 16)
#define TR(r) (t_base + JB(6 + (r)) * 4 + (wid >> 2) * 2)
    u32x2 xx[2][2], dk2;
    u32x4 gv[2];
#define XLOAD() do { \
        const __amdgpu_buffer_rsrc_t xs = __builtin_amdgcn_make_buffer_rsrc((void*)x, 0, SEQ * DM * 2, 0x00020000); \
        const __amdgpu_buffer_rsrc_t ks = __builtin_amdgcn_make_buffer_rsrc((void*)dskip, 0, DM * 2, 0x00020000); \
        dk2 = __builtin_amdgcn_raw_buffer_load_b64(ks, (unsigned)(l15 * 8), d_base * 2, 0); \
        _Pragma("unroll") for (int q = 0; q < 4; ++q) \
            xx[q >> 1][q & 1] = __builtin_amdgcn_raw_buffer_load_b64(xs, (unsigned)(q4 * 32 * DM * 2 + l15 * 8 + (q & 1) * DM * 2), (TR(q >> 1) * DM + d_base) * 2, 0); \
        _Pragma("unroll") for (int r = 0; r < 2; ++r) gv[r] = __builtin_amdgcn_raw_buffer_load_b128(gs, GV(q4), GSO(r), 16); \
        SCHED(); } while (0)
#define XPIN() do { PIN4(xx[0][0], xx[0][1], xx[1][0], xx[1][1]); asm volatile("" : "+v"(dk2), "+v"(gv[0]), "+v"(gv[1])); } while (0)

    if (wid < 4) {
        typedef __attribute__((ext_vector_type(8))) unsigned u32x8;
        typedef const __attribute__((address_space(4))) u32x8 cu8;
        const int po = (p_base + wid * 16) * 2;
        u32x8 s0 = *(cu8*)((size_t)logdt + po), s1 = *(cu8*)((size_t)lre + po), s2 = *(cu8*)((size_t)lim + po);
        SCHED();
        const int pq = p_base + wid * 16 + l15;
        unsigned goff[4], gmask[4];
#pragma unroll
        for (int i = 0; i < 4; ++i) {
            goff[i] = (unsigned)(((tb - 1 - q4 - 4 * i) * 1024 + pq) * 16);
            gmask[i] = (tb - 1 - q4 - 4 * i >= 0) ? ~0u : 0u;
            asm volatile("" : "+v"(goff[i]), "+v"(gmask[i]));
        }
        unsigned gpub = q4 == 0 ? (unsigned)((tb * 1024 + pq) * 16) : 0x80000000u;
        unsigned hp = (unsigned)(tb * 524288 + (nb * 4 + wid) * 1024 + lane * 16);
        unsigned wa = (unsigned)(HT_OFF + wid * 2048 + (q4 * 16 + (l15 ^ ((q4 >> 1) * 4))) * 8);
        unsigned ra0 = (unsigned)(HT_OFF + wid * 2048 + ((q4 >> 1) * 64 + (l15 & 3) * 16 + (((q4 & 1) * 8 + (l15 >> 2)) ^ ((l15 & 2) * 2))) * 8), ra1 = ra0 ^ 32u;
        cra = CRA(GA_OFF + wid * 8192); crb = cra ^ 64;
        unsigned vB0 = (unsigned)((d_base + (lane >> 3) * 4) * 2048 + (((lane & 7) ^ (lane >> 4)) * 16)), vB1 = (vB0 ^ 64u) + 65536u - 1024u;
        PIN4(gpub, vB0, hp, wa); PIN4(ra0, ra1, cra, crb); asm volatile("" : "+v"(vB1));
        f32x4 acc[8][2];
#pragma unroll
        for (int m = 0; m < 8; m += 2) {
#pragma unroll
            for (int c = 0; c < 4; ++c) acc[m + (c >> 1)][c & 1] = (f32x4){0.f, 0.f, 0.f, 0.f};
            PIN4(acc[m][0], acc[m][1], acc[m + 1][0], acc[m + 1][1]);
        }
        SCHED();
        cf L1, Lq3, cc, L32, cLq, Lp, L512;
        {
            unsigned w0 = 0, w1 = 0, w2 = 0;
#pragma unroll
            for (int i = 0; i < 8; ++i) {
                asm("v_writelane_b32 %0, %3, %6\n\tv_writelane_b32 %1, %4, %6\n\tv_writelane_b32 %2, %5, %6" : "+v"(w0), "+v"(w1), "+v"(w2) : "s"(s0[i]), "s"(s1[i]), "s"(s2[i]), "i"(2 * i));
            }
            asm("s_nop 1" : "+v"(w0), "+v"(w1), "+v"(w2));
            auto spread = [&](unsigned w) {
                const unsigned e = (unsigned)__builtin_amdgcn_update_dpp(0, (int)w, 0xa0, 0xf, 0xf, false);
                const unsigned a = __builtin_amdgcn_permlane16_swap(e, e, false, false)[0];
                const unsigned c = __builtin_amdgcn_permlane32_swap(a, a, false, false)[0];
                return (l15 & 1) ? hi16(c) : lo16(c);
            };
            const float ldt = spread(w0), re = spread(w1), im = spread(w2);
            if (wid == 3 && lane < NFLAGW) *(unsigned*)(smem + UFLAG_OFF + lane * 4) = 0u;
            const float dt = rbf(expf(ldt));
            const float ea = expf(dt * re);
            float sn, cs;
            sincosf(dt * im, &sn, &cs);
            L1 = {ea * cs, ea * sn};
            const float nr = L1.r - 1.0f, ni = L1.i, den = re * re + im * im;
            cc = {2.f * ((nr * re + ni * im) / den), 2.f * ((ni * re - nr * im) / den)};
            const cf T2 = cmul(L1, L1), T4 = cmul(T2, T2), L8 = cmul(T4, T4), L16 = cmul(L8, L8);
            L32 = cmul(L16, L16);
            const cf L64 = cmul(L32, L32), L96 = cmul(L64, L32), L128 = cmul(L64, L64), L256 = cmul(L128, L128), L384 = cmul(L256, L128);
            L512 = cmul(L256, L256);
            Lp = pick4(q4, L128, L256, L384);
            Lq3 = pick4(3 - q4, L32, L64, L96);
            cLq = cmul(cc, pick4(q4, L32, L64, L96));
            PINC(L1); PINC(Lq3); PINC(cc); PINC(L32); PINC(cLq); PINC(Lp); PINC(L512);
        }
        __builtin_amdgcn_s_setprio(PRIO_G);
        unsigned tagv;
        cf S;
        STAMPT(dbg, 22, 0);
        const int swz = (l15 >> 1) & 7;
        {
            const int a0 = RA(0) + l15 * 128 + ((q4 ^ swz) * 16);
            const int b0 = RB(0) + ((wid >> 1) * 64 + (wid & 1) * 16 + l15) * 128 + ((q4 ^ swz) * 16);
            WAIT_LGKM0();
            BAR();
            asm volatile("" ::: "memory");
            STAMPT(dbg, 19, 64);
            bf16x8 FA[8][2], FB[2][2][2];
            int pa[2] = {a0, a0 ^ 64}, pb[2] = {b0, b0 ^ 64};
            PIN4(pa[0], pa[1], pb[0], pb[1]);
#define LGW(n) __builtin_amdgcn_s_waitcnt(0xC07F | ((n) << 8))
#define RA_(j) FA[(j) >> 1][(j) & 1] = RD128(pa[(j) & 1], ((j) >> 1) * 2048)
#define RB_(bu, j) FB[bu][(j) >> 1][((j) ^ ((j) >> 1)) & 1] = RD128(pb[((j) ^ ((j) >> 1)) & 1], ((j) >> 1) * 4096)
#define RN_(bu, i) do { if ((i) == 0) RB_(bu, 0); else if ((i) < 3) RA_((i) - 1); else if ((i) < 6) RB_(bu, (i) - 2); else RA_((i) - 4); } while (0)
#define G1_H(H, bu, RD, FILL) do { \
            _Pragma("unroll") for (int i = 0; i < 16; ++i) { \
                const int m = (H) * 4 + (i >> 2), n = ((i >> 1) ^ m) & 1, ks = (i ^ (i >> 1)) & 1; \
                if ((H) == 0) { if (i == 0) LGW(6); if (i == 4) LGW(8); if (i == 8) LGW(10); if (i == 12) LGW(8); } \
                SCHED(); \
                MF(acc[m][n], FA[m][ks], FB[bu][n][ks]); SCHED(); \
                if ((H) == 0 && i < 8) RA_(8 + i); \
                if ((H) == 1 && (RD) && i < 12) RN_((bu) ^ 1, i); \
                FILL((H) * 16 + i); \
                SCHED(); \
            } } while (0)
#define NOF(g) do { } while (0)
#pragma unroll
            for (int j = 0; j < 12; ++j) { RN_(0, j); SCHED(); }
            unsigned long long ua_ = UNITS_A;
            int sb = RB(0), sa = RA(0), nx, na;
#define ADV(g) do { if ((g) == 8) { ua_ >>= 4; na = (int)((unsigned)ua_ & 15u) << 14; asm volatile("" : "+s"(na), "+s"(ua_)); } \
                    if ((g) == 9) { const int o_ = sa; sa = na; na -= o_; asm volatile("" : "+s"(na), "+s"(sa)); } \
                    if ((g) == 10) { pa[0] += na; pa[1] += na; asm volatile("" : "+v"(pa[0]), "+v"(pa[1])); } \
                    if ((g) == 11) { nx = (sb == 65536) ? -49152 : 16384; sb += nx; asm volatile("" : "+s"(nx), "+s"(sb)); } \
                    if ((g) == 13) { pb[0] += nx; pb[1] += nx; asm volatile("" : "+v"(pb[0]), "+v"(pb[1])); } } while (0)
#define G1_T(bu) do { \
                G1_H(0, bu, true, ADV); \
                WAIT_LGKM0(); \
                asm volatile("" ::: "memory"); \
                BAR();                     \
                asm volatile("" ::: "memory"); \
                G1_H(1, bu, true, NOF); } while (0)
#pragma unroll 1
            for (int t2 = 0; t2 < 7; ++t2) { G1_T(0); G1_T(1); }
            G1_T(0);
#define AGG(g) do { if ((g) == 8) { asm volatile("" ::: "memory"); SETW(G1D_OFF, wid, 1u); }        \
                    if ((g) == 9) tagv = *(volatile const lds_u32*)(size_t)(unsigned)TAG_OFF; \
                    if ((g) >= 7) { const int t = (g) - 6; \
                        if (t == 1) { asm volatile("s_nop 1" : "+v"(acc[0][0]), "+v"(acc[0][1])); S = {acc[0][0][0], acc[0][1][0]}; } \
                        if ((t & 3) == 0) asm volatile("" : "+v"(acc[t >> 2][0]), "+v"(acc[t >> 2][1])); \
                        S = cfma(L1, S, cf{acc[t >> 2][0][t & 3], acc[t >> 2][1][t & 3]}); PINC(S); } } while (0)
            G1_H(0, 1, false, AGG);
            G1_H(1, 1, false, AGG);
        }
        STAMPT(dbg, 1, 0);
        const unsigned tag = __builtin_amdgcn_readfirstlane(tagv);
        const __amdgpu_buffer_rsrc_t grs = __builtin_amdgcn_make_buffer_rsrc(ws, 0, 15 * 16384, 0x00020000);
        {
#pragma unroll
            for (int t = 26; t < 32; ++t) {
                if (t == 28) { PINC(S); asm volatile("" : "+v"(acc[7][0]), "+v"(acc[7][1])); }
                S = cfma(L1, S, cf{acc[t >> 2][0][t & 3], acc[t >> 2][1][t & 3]});
            }
            const cf W = cmul(S, Lq3);
            const float e2 = red16(red32(W.r, W.i));
            const auto eo = __builtin_amdgcn_permlane32_swap(__float_as_uint(e2), __float_as_uint(e2), false, false);
            __builtin_amdgcn_raw_buffer_store_b128((u32x4){eo[0], tag, eo[1], tag}, grs, gpub, 0, 16);
        }
        SCHED();
        STAMPT(dbg, 2, 0);
        const unsigned cg = lbase + GA_OFF + wid * 8192;
        wait_lds1(smem, COK_OFF + wid * 4);
        CREAD(0);
        WAIT_LGKM0();
        CPIN(0);
        CTILE(wid, 1, cg);
        u32x4 gq[4];
#pragma unroll
        for (int m = 0; m < 8; ++m) {
            const f32x4 R = acc[m][0], I = acc[m][1];
            acc[m][0] = fma4(splat4(cc.r), R, mul4(splat4(-cc.i), I));
            acc[m][1] = fma4(splat4(-cc.r), I, mul4(splat4(-cc.i), R));
            asm volatile("" : "+v"(acc[m][0]), "+v"(acc[m][1]));
            if (m == 3) {
                SCHED();
#pragma unroll
                for (int i = 0; i < 4; ++i) asm volatile("buffer_load_dwordx4 %0, %1, %2, 0 offen sc1" : "=&v"(gq[i]) : "v"(goff[i]), "s"(grs) : "memory");
                SCHED();
            }
        }
        cf Ec;
        {
            const auto sr = __builtin_amdgcn_permlane16_swap(__float_as_uint(S.r), __float_as_uint(S.r), false, false);
            const auto si = __builtin_amdgcn_permlane16_swap(__float_as_uint(S.i), __float_as_uint(S.i), false, false);
            const cf Se = {__uint_as_float(sr[0]), __uint_as_float(si[0])}, So = {__uint_as_float(sr[1]), __uint_as_float(si[1])};
            const cf A = cfma(L32, Se, So);
            const auto ar = __builtin_amdgcn_permlane32_swap(__float_as_uint(A.r), __float_as_uint(A.r), false, false);
            const auto ai = __builtin_amdgcn_permlane32_swap(__float_as_uint(A.i), __float_as_uint(A.i), false, false);
            const cf A01 = {(q4 & 2) ? __uint_as_float(ar[0]) : 0.f, (q4 & 2) ? __uint_as_float(ai[0]) : 0.f};
            const cf kq = {(q4 & 1) ? L32.r : 1.f, (q4 & 1) ? L32.i : 0.f};
            const cf E = cfma(kq, A01, cf{(q4 & 1) ? Se.r : 0.f, (q4 & 1) ? Se.i : 0.f});
            Ec = cmul(cc, E);
            PINC(Ec);
        }
        WAIT_VM(4);
        CREAD(1);
        WAIT_LGKM0();
        CPIN(1);
        CTILE(wid, 2, cg);
        asm volatile("s_waitcnt vmcnt(8)" : "+v"(gq[0]), "+v"(gq[1]), "+v"(gq[2]), "+v"(gq[3]) :: "memory");
        STAMPT(dbg, 27, 0);
        cf carry;
        {
            unsigned spins = 0;
            for (;;) {
                unsigned bad = 0;
#pragma unroll
                for (int i = 0; i < 4; ++i) bad |= ((gq[i].y ^ tag) | (gq[i].w ^ tag)) & gmask[i];
                if (__all(bad == 0u)) break;
                if (++spins > SPIN_MAX) break;
#pragma unroll
                for (int i = 0; i < 4; ++i) asm volatile("buffer_load_dwordx4 %0, %1, %2, 0 offen sc1" : "+v"(gq[i]) : "v"(goff[i]), "s"(grs) : "memory");
                asm volatile("s_waitcnt vmcnt(0)" : "+v"(gq[0]), "+v"(gq[1]), "+v"(gq[2]), "+v"(gq[3]) :: "memory");
            }
            cf E = {__uint_as_float(gq[3].x), __uint_as_float(gq[3].z)};
#pragma unroll
            for (int i = 2; i >= 0; --i) E = cfma(L512, E, cf{__uint_as_float(gq[i].x), __uint_as_float(gq[i].z)});
            E = cmul(E, Lp);
            const float e2 = red16(red32(E.r, E.i));
            const auto eo = __builtin_amdgcn_permlane32_swap(__float_as_uint(e2), __float_as_uint(e2), false, false);
            carry = {__uint_as_float(eo[0]), __uint_as_float(eo[1])};
        }
        STAMPT(dbg, 3, 0);
        {
            const __amdgpu_buffer_rsrc_t hrs = __builtin_amdgcn_make_buffer_rsrc(h, 0, SEQ * 2048 * 2, 0x00020000);
            const cf e = cfma(cLq, carry, Ec);
            float hr = e.r, hn = -e.i;
            s16x4 fr[2][4];
#define HSTORE(m) do { _Pragma("unroll") for (int c = 0; c < 2; ++c) \
                __builtin_amdgcn_raw_buffer_store_b128(__builtin_bit_cast(u32x4, __builtin_shufflevector(fr[(m) & 1][2 * c], fr[(m) & 1][2 * c + 1], 0, 1, 2, 3, 4, 5, 6, 7)), hrs, hp, (2 * (m) + c) * 65536, WT); } while (0)
#pragma unroll
            for (int m = 0; m < 4; ++m) {
#pragma unroll
                for (int j = 0; j < 8; ++j) {
                    const float nr = fmaf(L1.r, hr, fmaf(L1.i, hn, acc[2 * m + (j >> 2)][0][j & 3]));
                    const float nn = fmaf(L1.r, hn, fmaf(-L1.i, hr, acc[2 * m + (j >> 2)][1][j & 3]));
                    hr = nr; hn = nn;
                    acc[2 * m + (j >> 2)][0][j & 3] = nr; acc[2 * m + (j >> 2)][1][j & 3] = nn;
                }
                u32x2 pk[4];
#pragma unroll
                for (int c = 0; c < 4; ++c) { const f32x4 v = acc[2 * m + (c >> 1)][c & 1]; pk[c] = (u32x2){pkbf(v[0], v[1]), pkbf(v[2], v[3])}; }
                PIN4(pk[0], pk[1], pk[2], pk[3]);
                SCHED();
#pragma unroll
                for (int c = 0; c < 4; ++c) { *(volatile __attribute__((address_space(3))) u32x2*)(size_t)(wa + c * 512) = pk[c]; SCHED(); }
                asm volatile("" ::: "memory");
#pragma unroll
                for (int c = 0; c < 2; ++c) { fr[m & 1][2 * c] = TRD(ra0 + c * 1024); SCHED(); fr[m & 1][2 * c + 1] = TRD(ra1 + c * 1024); SCHED(); }
                asm volatile("" ::: "memory");
                if (m > 0) HSTORE(m - 1);
                SCHED();
            }
            HSTORE(3);
        }
        STAMPT(dbg, 4, 0);
        {
            WAIT_VM(8);
            CREAD(2);
            WAIT_VM(0);
            {
                unsigned n;
                asm volatile("s_mov_b64 exec, 1\n\tds_add_rtn_u32 %0, %1, %2\n\ts_mov_b64 exec, -1\n\ts_waitcnt lgkmcnt(0)" : "=&v"(n) : "v"(MISC_OFF), "v"(1u) : "memory");
                if (__builtin_amdgcn_readfirstlane(n) == 3u) {
                    if (lane == 0) __hip_atomic_store((gu32*)(ws + WS_HFLAG) + (tb * 16 + nb) * 32, tag, RLX_AGENT);
                    if (DBGON && lane == 0) dbg[BIDX * 64 + 20] = __builtin_amdgcn_s_memrealtime();
                }
            }
            STAMPT(dbg, 12, 0);
            if (wid == 0 && lane < 7) CODE_TOUCH(CODE_LINES - 1 - lane - (idx & 7) * 7);
            __builtin_amdgcn_s_setprio(0);
            WAIT_LGKM0();
            CPIN(2);
            CTILE(wid, 3, cg);
            XLOAD();
            HPOLL_SETUP();
            HWAIT();
            STAMPT(dbg, 8, 0);
            const unsigned vh = (unsigned)(lane * 16);
#pragma unroll
            for (int f = 0; f < DEPTH; ++f) LOADF(f);
            __builtin_amdgcn_s_setprio(PRIO2_G);
        }
    } else {
        const int lw = wid - 4;
        const int d = cb * 4 + lw;
        const char* Krow = (const char*)(ssmk + (size_t)d * SEQ);
        cra = CRA(LA_OFF + lw * 8192); crb = cra ^ 64;
        const unsigned cst0 = lbase + LA_OFF + lw * 8192;
        CT_SETUP();
        asm volatile("" : "+v"(cra), "+v"(crb));
        u32x4 KA[4];
        unsigned draw;
        unsigned tag;
        float gacc = 0.f;
#define KLOAD() asm volatile("global_load_dwordx4 %0, %5, %7 nt\n\tglobal_load_dwordx4 %1, %5, %7 offset:1024 nt\n\tglobal_load_dwordx4 %2, %5, %7 offset:2048 nt\n\t" \
                             "global_load_dwordx4 %3, %5, %7 offset:3072 nt\n\tglobal_load_ushort %4, %6, %8" \
                             : "=&v"(KA[0]), "=&v"(KA[1]), "=&v"(KA[2]), "=&v"(KA[3]), "=&v"(draw) : "v"((unsigned)(lane * 16)), "v"((unsigned)(d * 2)), "s"(Krow), "s"(ssmD) : "memory")
#define KWAIT(n) asm volatile("s_waitcnt vmcnt(%5)" : "+v"(KA[0]), "+v"(KA[1]), "+v"(KA[2]), "+v"(KA[3]), "+v"(draw) : "i"(n) : "memory")
        if (wid < 6) {
            WAIT_VM(16);
            STAMPT(dbg, 17, 320);
            BAR();
            STAGE_A(2, lw0 + RA(2)); STAGE_B(2, lw0 + RB(2));
            unsigned long long ua_ = UNITS_A >> 12;
            unsigned sla = lw0 + RA(3), slb = lw0 + RB(4);
            int ka = ((3 + rot1) & 15) * 128, kb_ = ka;
            const char* sa = (const char*)x + ka;
            asm volatile("" : "+s"(sa), "+s"(sla), "+s"(ka), "+s"(ua_));
#define NEXT_A() do { ka = (ka + 128) & 2047; sa = (const char*)x + ka; ua_ >>= 4; sla = lw0 + (((unsigned)ua_ & 15u) << 14); asm volatile("" : "+s"(sa), "+s"(sla), "+s"(ka), "+s"(ua_)); } while (0)
#define LSTEP(n) do { \
                if ((n) == 24 && t == 1) { WAIT_VM(16); BAR(); STAGE_B(3, lw0 + RB(3)); } else { WAIT_VM(n); BAR(); } \
                GLDS4(sa, voA[0], voA[1], voA[2], voA[3], sla, 0); \
                GLDS4(sa + 16384, voA[0], voA[1], voA[2], voA[3], sla, 4096); \
                kb_ = (kb_ + 128) & 2047; \
                GLDS4((const char*)Bre + kb_, voB[0], voB[1], voB[2], voB[3], slb, 0); \
                NEXT_A(); \
                const unsigned slb0 = slb; \
                slb = (slb == lw0 + 65536) ? lw0 + 16384 : slb + 16384; \
                asm volatile("" : "+s"(slb), "+s"(kb_), "+s"(t)); \
                GLDS4((const char*)Bim + kb_, voB[0], voB[1], voB[2], voB[3], slb0, 4096); } while (0)
#pragma unroll 1
            for (int t = 1; t < 5; ++t) LSTEP(24);
            GLDS4(sa, voA[0], voA[1], voA[2], voA[3], sla, 0); GLDS4(sa + 16384, voA[0], voA[1], voA[2], voA[3], sla, 4096);
            NEXT_A();
#pragma unroll 1
            for (int t = 5; t < 13; ++t) LSTEP(32);
            KLOAD();
            WAIT_VM(37);
            BAR();
            __builtin_amdgcn_s_setprio(0);
            tag = lds_word(smem, TAG_OFF);
            CTILE(lw, 0, cst0 + GA_OFF);
            FLAGD(COK_OFF + lw * 4);
            WAIT_VM(30);
            BAR();
            CTILE(lw + 2, 0, cst0 + GA_OFF + 16384);
            FLAGD(COK_OFF + lw * 4 + 8);
            WAIT_VM(23);
            BAR();
        } else {
            const int vw = wid - 6;
            const int dch = dblk * 128 + vw * 64 + l15;
            const unsigned lk = lbase + ST_OFF + vw * ST_SZ;
            const unsigned vv_ = (unsigned)(((lane >> 4) * 4 + ((lane >> 3) & 1)) * DM * 2 + (lane & 7) * 16);
            const char* vb = (const char*)vin + (size_t)(sblk * 64) * (DM * 2) + dblk * 256 + vw * 128;
            const int grow = nb * 8 + vw * 4 + q4;
            unsigned gx0 = (unsigned)(grow * 128 + (((l15 >> 1) ^ ((grow >> 1) & 7)) * 16) + (l15 & 1) * 8), gx1 = gx0 + RA(2), gwa = (unsigned)(GW_OFF + l15 * 8);
            asm volatile("" : "+v"(gx0), "+v"(gx1), "+v"(gwa));
            u32x2 gxv, gwv;
            typedef const volatile __attribute__((address_space(3))) u32x2* l2p;
#define VBAR(t) do { SCHED(); asm volatile("" ::: "memory"); BAR(); asm volatile("" ::: "memory"); \
                     gxv = *(l2p)(size_t)(RA(t) == 0 ? gx0 : gx1 + (RA(t) - RA(2))); gwv = *(l2p)(size_t)(gwa + (t) * 128); SCHED(); } while (0)
#define GATE() do { SCHED(); gacc = fmaf(lo16(gxv.x), lo16(gwv.x), gacc); gacc = fmaf(hi16(gxv.x), hi16(gwv.x), gacc); \
                    gacc = fmaf(lo16(gxv.y), lo16(gwv.y), gacc); gacc = fmaf(hi16(gxv.y), hi16(gwv.y), gacc); asm volatile("" : "+v"(gacc)); SCHED(); } while (0)
            unsigned ka[4][2], sel[4];
            const int R0 = 4 + (l15 >> 2) * 16 + (l15 & 3) - (q4 >> 1);
            unsigned kr0 = (unsigned)(2 * R0), kr1 = (unsigned)(ST_OFF + vw * ST_SZ + R0 * 128 + (((q4 ^ (R0 >> 1)) & 1) * 16));
            asm volatile("" : "+v"(kr0), "+v"(kr1));
#pragma unroll
            for (int i = 0; i < 4; ++i)
#pragma unroll
                for (int tp = 0; tp < 2; ++tp) {
                    ka[i][tp] = (kr1 ^ (tp * 16)) + ((kr0 + 8 * i - 4 * tp) & 0xe0) + (4 * i - 2 * tp) * 128;
                    asm volatile("" : "+v"(ka[i][tp]));
                }
#pragma unroll
            for (int r = 0; r < 4; ++r) {
                const unsigned pick = (q4 & 2) ? 0x0302u : 0x0100u;
                sel[r] = ((l15 >> 1) == (q4 & 1) * 4 + r) ? ((l15 & 1) ? (pick << 16) | 0x0c0cu : 0x0c0c0000u | pick) : 0x0c0c0c0cu;
            }
            unsigned va_ = (unsigned)(ST_OFF + vw * ST_SZ + ST_V + (q4 * 2 + (l15 >> 3)) * 1056 + ((l15 >> 2) & 1) * 128 + (l15 & 3) * 8);
            unsigned so = (unsigned)(((dch >> 2) * 32 + sblk) * 512 + q4 * 64 + (dch & 3) * 16);
            PIN4(sel[0], sel[1], sel[2], sel[3]); asm volatile("" : "+v"(so), "+v"(va_));
            asm volatile("s_waitcnt vmcnt(9)" : "+v"(tw[0]), "+v"(tw[1]), "+v"(tw[2]), "+v"(tw[3]), "+v"(epoch) :: "memory");
            STAMPT(dbg, 18, 384);
            VBAR(0);
            bf16x8 wf[4][2];
#pragma unroll
            for (int ps = 0; ps < 4; ++ps)
#pragma unroll
                for (int tp = 0; tp < 2; ++tp) {
                    u32x4 w;
#pragma unroll
                    for (int r = 0; r < 4; ++r) w[r] = __builtin_amdgcn_perm(0u, tw[ps][tp], sel[r]);
                    wf[ps][tp] = __builtin_bit_cast(bf16x8, w);
                    asm volatile("" : "+v"(wf[ps][tp]));
                }
            SCHED();
            GATE(); VBAR(1);
            VLD(0); VLD(1); VLD(2); VLD(3); VLD(4); VLD(5); VLD(6); VLD(7);
            f32x4 ks[4][4];
            auto sconv = [&](int i0) __attribute__((always_inline)) {
                bf16x8 kf[2][4][2];
#pragma unroll
                for (int q = 0; q < 16; ++q) { kf[q >> 3][q & 3][(q >> 2) & 1] = RD128(ka[i0 + (q >> 3)][(q >> 2) & 1], (q & 3) * 32); SCHED(); }
                GATE();
                __builtin_amdgcn_s_setprio(PRIO_KV);
                WAIT_LGKM0();
                SCHED();
#pragma unroll
                for (int i = 0; i < 2; ++i) {
#pragma unroll
                    for (int ps = 0; ps < 4; ++ps) MF0(ks[ps][i0 + i], kf[i][ps][0], wf[ps][0]);
#pragma unroll
                    for (int ps = 0; ps < 4; ++ps) MF(ks[ps][i0 + i], kf[i][ps][1], wf[ps][1]);
                }
                __builtin_amdgcn_s_setprio(0);
                __builtin_amdgcn_s_sleep(1);
                SCHED();
            };
            WAIT_VM(8);
            STAMPT(dbg, 21, 384);
            sconv(0); VBAR(2);
            sconv(2); VBAR(3);
            WAIT_VM(0);
            u32x2 vq[4][4];
#pragma unroll
            for (int ps = 0; ps < 4; ++ps)
#pragma unroll
                for (int j = 0; j < 4; ++j) vq[ps][j] = __builtin_bit_cast(u32x2, TRD(va_ + j * 256 + ps * 32));
            const __amdgpu_buffer_rsrc_t rs = __builtin_amdgcn_make_buffer_rsrc(kvT, 0, DM * SEQ * 2, 0x00020000);
            u32x4 o[8];
            auto kvout = [&](int n) __attribute__((always_inline)) {
                const int hh = n >> 2, ps = n & 3;
                asm volatile("" : "+v"(ks[ps][2 * hh]), "+v"(ks[ps][2 * hh + 1]));
#pragma unroll
                for (int e = 0; e < 4; ++e) {
                    const unsigned vv = vq[ps][2 * hh + (e >> 1)][e & 1];
                    o[n][e] = pkbf(ks[ps][2 * hh + (e >> 1)][2 * (e & 1)] * lo16(vv), ks[ps][2 * hh + (e >> 1)][2 * (e & 1) + 1] * hi16(vv));
                }
                asm volatile("" : "+v"(o[n]));
                SCHED();
            };
            GATE(); kvout(0); kvout(1);
            WAIT_LGKM0();

            VBAR(4);
#define KVST(h) do { _Pragma("unroll") for (int n = 0; n < 4; ++n) __builtin_amdgcn_raw_buffer_store_b128(o[4 * (h) + n], rs, so + (unsigned)((h) * 256), n * 65536, WT); \
                     SCHED(); asm volatile("s_nop 1" ::: "memory"); SCHED(); } while (0)
            kvout(2); kvout(3); GATE(); VBAR(5);
            KVST(0);
            kvout(4); kvout(5); GATE(); VBAR(6);
            kvout(6); kvout(7); GATE(); VBAR(7);
            KVST(1);
            GATE(); VBAR(8);
            if (vw == 1 && lane == 0) *(unsigned*)(smem + TAG_OFF) = (epoch + 1u) ? epoch + 1u : 1u;
            GATE();
            WAIT_LGKM0();
            WAIT_VM(0);
            VBAR(9);
            tag = __builtin_amdgcn_readfirstlane(*(const unsigned*)(smem + TAG_OFF));
            if (vw == 0 && lane == 0) __hip_atomic_store((gu32*)(ws + WS_KVFLAG) + (dblk * 32 + sblk) * 32, tag, RLX_AGENT);
            if (vw == 1 && lane < 4) *(u32x4*)(smem + U_OFF + 192 + lane * 16) = (u32x4){0u, 0u, 0u, 0u};
            gu32* kfp = (gu32*)(ws + WS_KVFLAG) + ((cb >> 5) * 32 + vw * 16 + l15) * 32;
            const char* ub = (const char*)kvT + (size_t)__builtin_amdgcn_readfirstlane(cb * 16384 + vw * 8192);
            const unsigned vu = (unsigned)((lane >> 5) * 512 + ((lane >> 2) & 1) * 256 + ((lane >> 3) & 3) * 64 + ((lane - (lane >> 4)) & 3) * 16);
            const unsigned lu = lbase + U_OFF + 256 + vw * 8192;
#define U_DMA1(o) asm volatile("s_add_u32 m0, %2, %3\n\ts_nop 0\n\tglobal_load_lds_dwordx4 %0, %1 sc1\n\tglobal_load_lds_dwordx4 %0, %1 offset:1024 sc1\n\t" \
                             "global_load_lds_dwordx4 %0, %1 offset:2048 sc1\n\tglobal_load_lds_dwordx4 %0, %1 offset:3072 sc1" \
                             :: "v"(vu), "s"(ub + (o)), "s"(lu), "i"(o) : "memory", "scc")
#define U_DMA() do { U_DMA1(0); U_DMA1(4096); FLAGD(UFLAG_OFF + lw * 4); } while (0)
            unsigned fo = lane < 8 ? (unsigned)((tb * 1024 + p_base) * 16 + lane * 128) : lane == 8 ? (unsigned)(WS_HFLAG + (tb * 16 + nb) * 128)
                        : lane == 9 ? (unsigned)(WS_CFLAG + cb * 128) : (unsigned)(WS_GATE + (tb * 16 + nb) * 128);
            asm volatile("" : "+v"(fo));
            GATE(); VBAR(10);
            TOUCH((const char*)(vw ? Cim : Cre) + (d_base + (idx & 3) * 16) * 2048 + COFF(0, 2), (unsigned)((lane >> 2) * 2048 + (lane & 3) * 256));
            if (vw == 0) TOUCH(ws, fo); else TOUCH(dskip, (unsigned)(d_base * 2));
            {
                const int cl = 64 + lane + ((idx & 15) * 2 + vw) * 7;
                if (lane < 7 && cl < CODE_LINES) CODE_TOUCH(cl);
            }
            GATE(); VBAR(11);
            GATE(); VBAR(12);
            GATE(); VBAR(13);
            unsigned pv;
            asm volatile("global_load_dword %0, %1, off sc1" : "=&v"(pv) : "v"(kfp) : "memory");
            KLOAD();
            GATE(); VBAR(14);
            GATE(); VBAR(15);
            asm volatile("s_waitcnt vmcnt(5)" : "+v"(pv) :: "memory");
            const bool done = __all(pv == tag);
            if (done) U_DMA();
            if (DBGON && TIDX == 384) dbg[BIDX * 64 + 13] = (done ? 100ull : 0ull) | (1ull << 62);
            GATE();
            if (lane == 0) SETW(UFLAG_OFF, vw, 1u);
            WAIT_VM(9);
            if (!done) {
                unsigned spins = 0;
                for (;;) {
                    const unsigned v = __hip_atomic_load(kfp, RLX_AGENT);
                    if (__all(v == tag)) break;
                    if (++spins > SPIN_MAX) break;
                    __builtin_amdgcn_s_sleep(1);
                }
                U_DMA();
            }
        }
        KWAIT(18);
        STAMPT(dbg, 14, 320);
        __builtin_amdgcn_s_sleep(3);
        if (lw < 2) __builtin_amdgcn_s_sleep(3);
        {
            const unsigned cp = (unsigned)(KC_OFF + lw * CV_W + 64 + lane * 16);
            if (lane == 0) KA[0].x = (KA[0].x & 0xffff0000u) | f2bf(lo16(KA[0].x) + bf2f((u16)draw));
#define WR128(v, off) asm volatile("ds_write_b128 %0, %1 offset:%2" :: "v"(cp), "v"(v), "i"(off) : "memory")
#pragma unroll
            for (int q = 3; q >= 0; --q) WR128(KA[q], q * 1024);
#pragma unroll
            for (int q = 3; q >= 0; --q) {
                const u32x2 p = q ? pred2(KA[q], RL63(KA[q ? q - 1 : 0].z), RL63(KA[q ? q - 1 : 0].w)) : pred2(KA[0], 0u, 0u);
                const u32x4 a = KA[q];
                const u32x4 s1 = {al(a.x, p.y), al(a.y, a.x), al(a.z, a.y), al(a.w, a.z)};
                WR128(s1, q * 1024 + CV_COPY);
                const u32x4 s2 = {p.y, mv(a.x), mv(a.y), mv(a.z)};
                WR128(s2, q * 1024 + 2 * CV_COPY);
                const u32x4 s3 = {al(p.y, p.x), mv(s1.x), mv(s1.y), mv(s1.z)};
                WR128(s3, q * 1024 + 3 * CV_COPY);
            }
            if (lane < 16) *(u32x4*)(smem + KC_OFF + lw * CV_W + (lane >> 2) * CV_COPY + (lane & 3) * 16) = (u32x4){0u, 0u, 0u, 0u};
            SCHED();
        }
        STAMPT(dbg, 26, 320);
        wait_lds4(smem, G1D_OFF);
        wait_lds4(smem, UFLAG_OFF);
        STAMPT(dbg, 9, 256);
        __builtin_amdgcn_s_setprio(PRIO_CV);
        STAMPT(dbg, 10, 384);
        f32x4 cv[8];
#pragma unroll
        for (int g = 0; g < 8; ++g) cv[g] = (f32x4){0.f, 0.f, 0.f, 0.f};
        PIN4(cv[0], cv[1], cv[2], cv[3]); PIN4(cv[4], cv[5], cv[6], cv[7]);
        const int uz = U_OFF + 192;
        const int ua0 = U_OFF + 256 + (2 * l15 + q4 - 2) * 64, rv0 = (2 * l15 + q4 - 2) * 4 + lw * 16;
        const unsigned ka0 = (unsigned)((l15 >> 2) * CV_COPY - 16 * q4 + 8 * (l15 & 3) - 8);
        int ua, rv, pu, pz;
        unsigned adr;
#define UNEXT1() do { pu = ua | (rv & 0x30); asm volatile("" : "+v"(pu)); } while (0)
#define UNEXT2() do { pz = (ua > uz ? ua : uz) | (rv & 0x30); asm volatile("" : "+v"(pz)); } while (0)
#define UNEXT3() do { ua -= 256; rv -= 16; asm volatile("" : "+v"(ua), "+v"(rv)); } while (0)
#define ANEXT() do { adr += 64; asm volatile("" : "+v"(adr)); } while (0)
#define RDU(dst, c) do { dst = (c) ? RD128(pu, (c) * 2048) : RD128(pz, 0); SCHED(); } while (0)
        HPOLL_SETUP();
        unsigned hpv = 0;
        {
            bf16x8 A[8], UB[8];
            ua = ua0; rv = rv0; adr = lbase + KC_OFF + lw * CV_W + ka0 + 96;
            asm volatile("" : "+v"(adr));
            UNEXT1(); UNEXT2(); UNEXT3();
#pragma unroll
            for (int i = 0; i < 8; ++i) { RDA(A[7 - i], adr, 7 - i); RDU(UB[i], i); }
            unsigned gb = lw >= 2 ? (unsigned)((tb * 16 + nb) * 128 + (lw - 2) * 32) : 0x80000000u;
            asm volatile("" : "+s"(gb));
            const unsigned gso = (l15 == 0) ? gb + (unsigned)(q4 * 8) : 0x80000000u;
            float gt = 0.f;
            unsigned vxo = 0, vdo = 0, vgo = 0;
            auto gate_out = [&](int i) __attribute__((always_inline)) {
                if (i == 0) gacc += __uint_as_float(__builtin_amdgcn_update_dpp(0u, __float_as_uint(gacc), 0xb1, 0xf, 0xf, false));
                if (i == 1) gacc += __uint_as_float(__builtin_amdgcn_update_dpp(0u, __float_as_uint(gacc), 0x4e, 0xf, 0xf, false));
                if (i == 2) gacc += __uint_as_float(__builtin_amdgcn_update_dpp(0u, __float_as_uint(gacc), 0x141, 0xf, 0xf, false));
                if (i == 3) gacc += __uint_as_float(__builtin_amdgcn_update_dpp(0u, __float_as_uint(gacc), 0x140, 0xf, 0xf, false));
                if (i == 4) gacc = rbf(gacc);
                if (i == 5) gacc = __builtin_amdgcn_exp2f(gacc * -1.44269504088896f);
                if (i == 6) gacc = __builtin_amdgcn_rcpf(1.0f + gacc);
                if (i == 7) gacc = rbf(gacc);
                if (i == 8) gt = __uint_as_float(pkbf(gacc, 1.0f - gacc));
                if (i == 9) __builtin_amdgcn_raw_buffer_store_b64((u32x2){__float_as_uint(gt), tag}, gs, gso, 0, WT);
                asm volatile("" : "+v"(gacc), "+v"(gt));
            };
            auto step = [&](auto E) __attribute__((always_inline)) {
                constexpr int e = decltype(E)::value;
                constexpr int ti = (e == CS0) ? 0 : (e == CS1) ? 1 : (e == CS2) ? 2 : -1, tj = ti < 0 ? 0 : ti;
                constexpr unsigned char GA[36] = {0x66, 0x77, 0x76, 0x44, 0x55, 0x54, 0x65, 0x64, 0x75, 0x74, 0x22, 0x33, 0x32, 0x43, 0x42, 0x53, 0x52, 0x63, 0x62, 0x73, 0x72,
                                                  0x00, 0x11, 0x10, 0x21, 0x20, 0x31, 0x30, 0x41, 0x40, 0x51, 0x50, 0x61, 0x60, 0x71, 0x70};
                const char* sb_ = (const char*)Cre + COFF(wid, tj + 1);
                const char* sc_ = (const char*)Cim + COFF(wid, tj + 1);
#pragma unroll
                for (int q = 0; q < 36; ++q) {
                    const int g = GA[q] >> 4, a = GA[q] & 15;
                    MF(cv[g], A[a], UB[g - a]); SCHED();
                    if (e < 7) {
                        if (a == 1) RDU(UB[g - 1], g - 1);
                        if (q == 35) RDU(UB[7], 7);
                        if (g == 7) RDA(A[a], adr + 64 * (e + 1), a);
                        if (q == 11) UNEXT1();
                        if (q == 13) UNEXT2();
                        if (q == 15) UNEXT3();
                    }
                    if (ti >= 0) {
                        const int n = (q >= 3 && q <= 7) ? q - 3 : q == 10 ? 5 : q == 12 ? 6 : q == 14 ? 7 : -1;
                        if (q == 3) { if (ti == 0) WAIT_VM(1); else if (e == XE + 1) WAIT_VM(7); else WAIT_VM(0); }
                        if (n >= 0) CRD(tj, n & 7);
                        if (q == 18) CPIN(tj);
                        if (q == 18 || q == 23) asm volatile("s_add_u32 m0, %3, %4\n\ts_nop 0\n\tglobal_load_lds_dwordx4 %0, %2\n\tglobal_load_lds_dwordx4 %1, %2 offset:1024"
                                                             :: "v"(vB0), "v"(vB1), "s"(q == 18 ? sb_ : sc_), "s"(cst0), "i"(q == 18 ? 0 : 4096) : "memory", "scc");
                        if (q == 21 || q == 25) asm volatile("global_load_lds_dwordx4 %0, %2 offset:2048\n\tglobal_load_lds_dwordx4 %1, %2 offset:3072"
                                                             :: "v"(vB0), "v"(vB1), "s"(q == 21 ? sb_ : sc_) : "memory");
                    }
                    if (e == 0 && q >= 3 && q <= 7) gate_out(q - 3);
                    if (e == 0 && q >= 10 && q <= 18 && !(q & 1)) gate_out(5 + (q - 10) / 2);
                    if (e == 0 && q == 4) CTILE(wid, 0, cst0);
                    if (e == XE && q >= 23 && q <= 35 && (q & 1)) {
                        const int i = (q - 23) / 2;
                        const __amdgpu_buffer_rsrc_t xs = __builtin_amdgcn_make_buffer_rsrc((void*)x, 0, SEQ * DM * 2, 0x00020000);
                        const __amdgpu_buffer_rsrc_t ks = __builtin_amdgcn_make_buffer_rsrc((void*)dskip, 0, DM * 2, 0x00020000);
                        if (i < 4) asm volatile("buffer_load_dwordx2 %0, %1, %2, %3 offen offset:%4" : "=&v"(xx[(i >> 1) & 1][i & 1]) : "v"(vxo), "s"(xs), "s"((TR((i >> 1) & 1) * DM + d_base) * 2), "i"((i & 1) * 2048) : "memory");
                        if (i == 4) asm volatile("buffer_load_dwordx2 %0, %1, %2, %3 offen" : "=&v"(dk2) : "v"(vdo), "s"(ks), "s"(d_base * 2) : "memory");
                        if (i >= 5) asm volatile("buffer_load_dwordx4 %0, %1, %2, %3 offen sc1" : "=&v"(gv[(i - 5) & 1]) : "v"(vgo), "s"(gs), "s"(GSO((i - 5) & 1)) : "memory");
                    }
                    if (e == XE && q == 16) { vxo = (unsigned)(q4 * 32 * DM * 2 + l15 * 8); vdo = (unsigned)(l15 * 8); asm volatile("" : "+v"(vxo), "+v"(vdo)); }
                    if (e == XE && q == 17) { vgo = GV(q4); asm volatile("" : "+v"(vgo)); }
                    if (e == 6 && q == 30) hpv = __hip_atomic_load(hfp, RLX_AGENT);
                    SCHED();
                }
            };
            unroll<8>(step);
            WAIT_VM(0);
            XPIN();
        }
        PAD8(cv[0], cv[1], cv[2], cv[3]); PIN4(cv[4], cv[5], cv[6], cv[7]);
        STAMPT(dbg, 11, 384);
        const unsigned vh = (unsigned)((lane ^ 2) * 16);
        if (!__all(hpv == tag)) HWAIT();
        asm volatile("" ::: "memory");
#pragma unroll
        for (int f = 0; f < DEPTH; ++f) LOADF(f);
        STAMPT(dbg, 25, 384);
        {
            const __amdgpu_buffer_rsrc_t yrs = __builtin_amdgcn_make_buffer_rsrc(y2T, 0, DM * SEQ * 2, 0x00020000);
            const unsigned yo = (unsigned)(d * 4096 + (q4 & 1) * 512 + (l15 >> 3) * 256 + (q4 >> 1) * 128 + (l15 & 7) * 16);
#pragma unroll
            for (int g = 0; g < 8; g += 2) {
                const auto r0 = __builtin_amdgcn_permlane16_swap(pkbf(cv[g][0], cv[g][1]), pkbf(cv[g + 1][0], cv[g + 1][1]), false, false);
                const auto r1 = __builtin_amdgcn_permlane16_swap(pkbf(cv[g][2], cv[g][3]), pkbf(cv[g + 1][2], cv[g + 1][3]), false, false);
                __builtin_amdgcn_raw_buffer_store_b128((u32x4){r0[0], r1[0], r0[1], r1[1]}, yrs, yo + (unsigned)(g * 512), 0, WT);
            }
        }
        asm volatile("" ::: "memory");
        STAMPT(dbg, 15, 384);
        __builtin_amdgcn_s_setprio(PRIO2_L);
    }
    {
        const int lane = fresh_lane(), l15 = lane & 15, q4 = lane >> 4;
        const int ring = wid >> 2;
        const unsigned va = (unsigned)((lane ^ (ring * 2)) * 16), vh = va;
        const unsigned tag = lds_word(smem, TAG_OFF);
        const int cb0 = ring ? KC_OFF : GA_OFF, cst = ring ? CV_W : 8192;
        f32x4 ac[2][4];
        unsigned ca[2], fa[2], fl = 0, fv = 0;
#define CHAIN(u) do { const int c_ = (wid - ((u) >> 1)) & 3; ca[(u) & 1] = va + (unsigned)(cb0 + c_ * cst + ((u) & 1) * 4096); \
                      fa[(u) & 1] = (unsigned)(RF_OFF + ring * 32 + ((u) & 1) * 16 + c_ * 4); asm volatile("" : "+v"(ca[(u) & 1]), "+v"(fa[(u) & 1])); } while (0)
#define RDIN(dst, a, f_) asm volatile("ds_read_b32 %0, %5\n\tds_read_b128 %1, %6\n\tds_read_b128 %2, %6 offset:1024\n\tds_read_b128 %3, %6 offset:2048\n\tds_read_b128 %4, %6 offset:3072" \
                     : "=&v"(fl), "=&v"(dst[0]), "=&v"(dst[1]), "=&v"(dst[2]), "=&v"(dst[3]) : "v"(f_), "v"(a) : "memory")
#define LWAIT(dst) asm volatile("s_waitcnt lgkmcnt(0)" : "+v"(fl), "+v"(dst[0]), "+v"(dst[1]), "+v"(dst[2]), "+v"(dst[3]) :: "memory")
        CHAIN(0); CHAIN(1);
        WAIT_LGKM0();
        CPIN(0); CPIN(1); CPIN(2);
        CREAD(3);
        int tr[2] = {TR(0), TR(1)};
        unsigned gw[2][2];
        {
            unsigned spins = 0;
            while (__builtin_expect(!__all((gv[0].y == tag) & (gv[0].w == tag) & (gv[1].y == tag) & (gv[1].w == tag)), 0)) {
                if (++spins > SPIN_MAX) break;
#pragma unroll
                for (int r = 0; r < 2; ++r) asm volatile("s_nop 4\n\tbuffer_load_dwordx4 %0, %1, %2, %3 offen sc1\n\ts_waitcnt vmcnt(0)" : "=&v"(gv[r]) : "v"(GV(q4)), "s"(gs), "s"(__builtin_amdgcn_readfirstlane(GSO(r))) : "memory");
            }
            gw[0][0] = gv[0].x; gw[0][1] = gv[0].z; gw[1][0] = gv[1].x; gw[1][1] = gv[1].z;
            PIN4(gw[0][0], gw[0][1], gw[1][0], gw[1][1]);
        }
        unsigned cpv = 0, yy[2][4];
        f32x2 zz[2][4];
        unsigned exo = 0, exi = 0, efo = 0, efi = 0;
        auto send = [&](int r) __attribute__((always_inline)) {
            const unsigned one = 1u;
#pragma unroll
            for (int n = 0; n < 4; ++n) { const f32x2 v = {ac[r][n][2], ac[r][n][3]}; asm volatile("ds_write_b64 %0, %1 offset:%2" :: "v"(exo), "v"(v), "i"(r * 4096 + n * 512) : "memory"); }
            asm volatile("ds_write_b32 %0, %1 offset:%2" :: "v"(efo), "v"(one), "i"(r * 4) : "memory");
        };
        STAMPT(dbg, 5, 0);
        auto body = [&](auto F) __attribute__((always_inline)) {
            constexpr int f = decltype(F)::value;
            constexpr int u = f >> 3, k = f & 7;
            f32x4* an = ac[u & 1];
            f32x4* ao = ac[(u + 1) & 1];
#pragma unroll
            for (int n = 0; n < 4; ++n) {
                if (k == 0 && u < 2) MF0(an[n], af[f % DEPTH], cf_[k][n]); else MF(an[n], af[f % DEPTH], cf_[k][n]);
                SCHED();
                const int g = k * 4 + n;
                if (u >= 1 && u < 7) {
                    if (g == 1) { fv = ((u - 1) >> 1) + 1; asm volatile("" : "+v"(fv)); }
                    if (g == 4 || g == 6 || g == 8 || g == 10) asm volatile("ds_write_b128 %0, %1 offset:%2" :: "v"(ca[(u + 1) & 1]), "v"(ao[(g - 4) / 2]), "i"((g - 4) * 512) : "memory");
                    if (g == 12) asm volatile("ds_write_b32 %0, %1" :: "v"(fa[(u + 1) & 1]), "v"(fv) : "memory");
                    if (g == 18) CHAIN(u + 1);
                    if (g == 20) asm volatile("ds_read_b32 %0, %1" : "=&v"(fl) : "v"(fa[(u + 1) & 1]) : "memory");
                    if (g > 20 && g <= 24) asm volatile("ds_read_b128 %0, %1 offset:%2" : "=&v"(ao[g - 21]) : "v"(ca[(u + 1) & 1]), "i"((g - 21) * 1024) : "memory");
                    if (g == 30) {
                        LWAIT(ao);
                        if (__builtin_expect(__builtin_amdgcn_readfirstlane(fl) != (unsigned)((u + 1) >> 1), 0)) {
                            unsigned spins = 0;
#pragma unroll 1
                            do {
                                __builtin_amdgcn_s_sleep(1);
                                RDIN(ao, ca[(u + 1) & 1], fa[(u + 1) & 1]); LWAIT(ao);
                                if (++spins > SPIN_MAX * 16u) break;
                            } while (__builtin_amdgcn_readfirstlane(fl) != (unsigned)((u + 1) >> 1));
                        }
                    }
                }
                if (u == 7 && g == 6) send(0);
                if (f >= ZF && f < ZF + 4) {
                    constexpr int r = ((f - ZF) >> 1) & 1, j = (f - ZF) & 1;
                    const unsigned xw = xx[r][j][n >> 1], dw = dk2[n >> 1];
                    const float xf = (n & 1) ? hi16(xw) : lo16(xw), df = (n & 1) ? hi16(dw) : lo16(dw), yf = j ? hi16(yy[r][n]) : lo16(yy[r][n]);
                    zz[r][n][j] = xf * fmaf(hi16(gw[r][j]), yf, lo16(gw[r][j]) * df);
                    asm volatile("" : "+v"(zz[r][n]));
                }
                if (f == ZF + 4) {
                    zz[0][n] = (f32x2){fmaf(lo16(gw[0][0]), ac[0][n][0], zz[0][n][0]), fmaf(lo16(gw[0][1]), ac[0][n][1], zz[0][n][1])};
                    asm volatile("" : "+v"(zz[0][n]));
                }
                SCHED();
            }
            if (f + DEPTH < 64) LOADF(f + DEPTH);
            if (f == DEPTH + 1 && ring) {
                WAIT_VM(6);
                if (lane == 0) __hip_atomic_store((gu32*)(ws + WS_CFLAG) + cb * 32 + (wid & 3), tag, RLX_AGENT);
                SCHED();
            }
            if (f == YPOLL && ring) { const int l15 = fresh_lane() & 15; cpv = __hip_atomic_load((gu32*)(ws + WS_CFLAG) + (nb * 16 + (wid & 3) * 4 + (l15 >> 2)) * 32 + (l15 & 3), RLX_AGENT); SCHED(); }
            if (f == YPOLL + DEPTH && ring) {
                const int ln = fresh_lane(), l15 = ln & 15, q4 = ln >> 4;
                if (__builtin_expect(!__all(cpv == tag), 0)) {
                    unsigned spins = 0;
                    while (!__all(__hip_atomic_load((gu32*)(ws + WS_CFLAG) + (nb * 16 + (wid & 3) * 4 + (l15 >> 2)) * 32 + (l15 & 3), RLX_AGENT) == tag)) {
                        if (++spins > SPIN_MAX) break;
                        __builtin_amdgcn_s_sleep(1);
                    }
                }
                const unsigned lx = lbase + LA_OFF + (wid & 3) * 8192;
#pragma unroll
                for (int i = 0; i < 4; ++i) {
                    const unsigned vo = (unsigned)((d_base + (wid & 3) * 16 + i * 4 + q4) * 4096 + tb * 256 + ((l15 ^ ((wid & 3) * 4 + i)) * 16));
                    GLDS_SC1(y2T, vo, lx, i * 1024);
                }
                SCHED();
            }
            if (f == YPOLL + 2 * DEPTH + 1 && ring) {
                WAIT_VM(6);
                SETW(YFLAG_OFF, wid & 3, 1u);
                SCHED();
            }
            if (f == 55) {
                const int ln = fresh_lane(), c_ = (wid - 3) & 3;
                const int eg = GA_OFF + c_ * 8192, el = KC_OFF + c_ * CV_W;
                exo = (unsigned)((ring ? el : eg) + ln * 8);
                exi = (unsigned)((ring ? eg : el) + ln * 8);
                efo = (unsigned)(EF_OFF + wid * 8); efi = (unsigned)(EF_OFF + (wid ^ 4) * 8);
                PIN4(exo, exi, efo, efi);
                SCHED();
            }
            if (f == ZF - 2) {
                const int ln = fresh_lane(), l15 = ln & 15, q4 = ln >> 4;
                wait_lds4(smem, YFLAG_OFF);
#pragma unroll
                for (int r = 0; r < 2; ++r) {
                    const int jb = JB(6 + r);
                    const int ya = LA_OFF + (l15 >> 2) * 8192 + (l15 & 3) * 1024 + (((((jb >> 1) & 1) * 8 + (jb >> 2) + q4 * 2) ^ l15) * 16) + (jb & 1) * 8 + ring * 4;
#pragma unroll
                    for (int e = 0; e < 4; ++e) yy[r][e] = *(const unsigned*)(smem + ya + e * 256);
                }
                SCHED();
            }
        };
        unroll<64>(body);
        PAD8(ac[1][0], ac[1][1], ac[1][2], ac[1][3]);
        send(1);
        STAMPT(dbg, 6, 0);
        const __amdgpu_buffer_rsrc_t ors = __builtin_amdgcn_make_buffer_rsrc(out, 0, SEQ * DM * 4, 0x00020000);
        const unsigned ov = (unsigned)((lane >> 4) * (32 * DM * 4) + (lane & 15) * 16);
#pragma unroll
        for (int r = 0; r < 2; ++r) {
            f32x2 in[4];
#define EXRD() asm volatile("ds_read_b32 %0, %5 offset:%7\n\tds_read_b64 %1, %6 offset:%8\n\tds_read_b64 %2, %6 offset:%9\n\tds_read_b64 %3, %6 offset:%10\n\tds_read_b64 %4, %6 offset:%11" \
                             : "=&v"(fl), "=&v"(in[0]), "=&v"(in[1]), "=&v"(in[2]), "=&v"(in[3]) \
                             : "v"(efi), "v"(exi), "i"(r * 4), "i"(r * 4096), "i"(r * 4096 + 512), "i"(r * 4096 + 1024), "i"(r * 4096 + 1536) : "memory")
            EXRD();
            if (r == 0) {
#pragma unroll
                for (int n = 0; n < 4; ++n) {
                    zz[1][n] = (f32x2){fmaf(lo16(gw[1][0]), ac[1][n][0], zz[1][n][0]), fmaf(lo16(gw[1][1]), ac[1][n][1], zz[1][n][1])};
                    asm volatile("" : "+v"(zz[1][n]));
                }
            }
            LWAIT(in);
            if (__builtin_expect(__builtin_amdgcn_readfirstlane(fl) == 0u, 0)) {
                unsigned spins = 0;
#pragma unroll 1
                do {
                    EXRD(); LWAIT(in);
                    if (++spins > SPIN_MAX * 16u) break;
                } while (__builtin_amdgcn_readfirstlane(fl) == 0u);
            }
            f32x4 o[2];
#pragma unroll
            for (int j = 0; j < 2; ++j)
#pragma unroll
                for (int e = 0; e < 4; ++e) o[j][e] = fmaf(lo16(gw[r][j]), in[e][j], zz[r][e][j]);
#pragma unroll
            for (int j = 0; j < 2; ++j)
                __builtin_amdgcn_raw_buffer_store_b128(__builtin_bit_cast(u32x4, o[j]), ors, ov + (unsigned)(((tr[r] + j) * DM + d_base) * 4), 0, WT);
        }
        STAMPT(dbg, 7, 0);
        STAMPT(dbg, 16, 384);
        if (tb == 15 && nb == 0 && wid == 0 && lane == 0) __hip_atomic_store((gu32*)(ws + WS_EPOCH), tag, RLX_AGENT);
    }
}

extern "C" void kernel_launch(void* const* d_in, const int* in_sizes, int n_in,
                              void* d_out, int out_size, void* d_ws, size_t ws_size,
                              hipStream_t stream) {
    const u16* x = (const u16*)d_in[0];
    const u16* lre = (const u16*)d_in[1];
    const u16* lim = (const u16*)d_in[2];
    const u16* Bre = (const u16*)d_in[3];
    const u16* Bim = (const u16*)d_in[4];
    const u16* Cre = (const u16*)d_in[5];
    const u16* Cim = (const u16*)d_in[6];
    const u16* logdt = (const u16*)d_in[7];
    const u16* dskip = (const u16*)d_in[8];
    const u16* kin = (const u16*)d_in[10];
    const u16* vin = (const u16*)d_in[11];
    const u16* shiftk = (const u16*)d_in[12];
    const u16* ssmk = (const u16*)d_in[13];
    const u16* ssmD = (const u16*)d_in[14];
    const u16* gatew = (const u16*)d_in[15];
    float* out = (float*)d_out;

    char* ws = (char*)d_ws;

    hipFuncSetAttribute((const void*)k1_kernel, hipFuncAttributeMaxDynamicSharedMemorySize, K1_LDS);

    k1_kernel<<<256, 512, K1_LDS, stream>>>(x, Bre, Bim, kin, vin, shiftk, gatew, ws, lre, lim, logdt, ssmk, ssmD, Cre, Cim, dskip, out);
}
#if defined(__HIP_DEVICE_COMPILE__)
#pragma clang attribute pop
#endif
```

```cpp
#if defined(__HIP_DEVICE_COMPILE__)
#pragma clang attribute push(__attribute__((target("no-packed-fp32-ops"))), apply_to = function)
#endif
#include <hip/hip_runtime.h>
#include <hip/hip_bf16.h>
#include <stdint.h>

#define TIDX ((int)__builtin_amdgcn_workitem_id_x())
#define BIDX ((int)__builtin_amdgcn_workgroup_id_x())
typedef unsigned short u16;
typedef __attribute__((ext_vector_type(8))) short bf16x8;
typedef __attribute__((ext_vector_type(4))) float f32x4;
typedef __attribute__((ext_vector_type(2))) float f32x2;
typedef __attribute__((ext_vector_type(2))) __bf16 bf16x2;
typedef __attribute__((ext_vector_type(4))) unsigned u32x4;
typedef __attribute__((ext_vector_type(2))) unsigned u32x2;
typedef __attribute__((address_space(1))) unsigned long long gu64;
typedef __attribute__((address_space(1))) unsigned gu32;
typedef __attribute__((address_space(3))) void lds_void;
typedef __attribute__((address_space(3))) unsigned lds_u32;
typedef __attribute__((ext_vector_type(4))) short s16x4;
typedef __attribute__((address_space(3))) s16x4 lds_s16x4;
#define TRD(a) __builtin_amdgcn_ds_read_tr16_b64_v4i16((lds_s16x4*)(size_t)(a))

#define DBGON 0
#define STAMPT(buf, k, t) do { } while (0)
#define CODE_LINES 263
#define LATE (xcd >= 4)
#define EARLY (xcd < 4)
#define SEQ 2048
#define DM 1024
#define SPIN_MAX (1u << 18)
#define PRIO_G 2
#define PSLEEP 4
#define PRIO_L 3
#define WT 16

__device__ __forceinline__ float bf2f(u16 v) { return __uint_as_float(((unsigned)v) << 16); }
__device__ __forceinline__ u16 f2bf(float f) { __bf16 b = (__bf16)f; return __builtin_bit_cast(u16, b); }
__device__ __forceinline__ float rbf(float f) { return bf2f(f2bf(f)); }
__device__ __forceinline__ unsigned pkbf(float lo, float hi) { return __builtin_bit_cast(unsigned, __builtin_convertvector((f32x2){lo, hi}, bf16x2)); }
__device__ __forceinline__ float lo16(unsigned u) { return __uint_as_float(u << 16); }
__device__ __forceinline__ float hi16(unsigned u) { return __uint_as_float(u & 0xffff0000u); }

struct cf { float r, i; };
__device__ __forceinline__ cf cmul(cf a, cf b) { return {a.r * b.r - a.i * b.i, a.r * b.i + a.i * b.r}; }
__device__ __forceinline__ cf cfma(cf a, cf b, cf c) {
    return {fmaf(a.r, b.r, fmaf(-a.i, b.i, c.r)), fmaf(a.r, b.i, fmaf(a.i, b.r, c.i))};
}
__device__ __forceinline__ f32x4 splat4(float v) { return (f32x4){v, v, v, v}; }
__device__ __forceinline__ f32x4 fma4(f32x4 a, f32x4 b, f32x4 c) { return __builtin_elementwise_fma(a, b, c); }
__device__ __forceinline__ f32x4 mul4(f32x4 a, f32x4 b) { return a * b; }

__device__ __forceinline__ float red32(float a, float b) {
    const auto r = __builtin_amdgcn_permlane32_swap(__float_as_uint(a), __float_as_uint(b), false, false);
    return __uint_as_float(r[0]) + __uint_as_float(r[1]);
}
__device__ __forceinline__ float red16(float x) {
    const auto r = __builtin_amdgcn_permlane16_swap(__float_as_uint(x), __float_as_uint(x), false, false);
    return __uint_as_float(r[0]) + __uint_as_float(r[1]);
}
__device__ __forceinline__ int fresh_lane() {
    int l;
    asm volatile("v_mbcnt_lo_u32_b32 %0, -1, 0\n\tv_mbcnt_hi_u32_b32 %0, -1, %0" : "=v"(l));
    return l;
}
#define BAR() __builtin_amdgcn_s_barrier()
#define WAIT_VM(n) asm volatile("s_waitcnt vmcnt(" #n ")" ::: "memory")
#define WAIT_LGKM0() __builtin_amdgcn_s_waitcnt(0xC07F)
#define SCHED() __builtin_amdgcn_sched_barrier(0)
#define MF(c, a, b) asm volatile("v_mfma_f32_16x16x32_bf16 %0, %1, %2, %0" : "+v"(c) : "v"(a), "v"(b))
#define PAD8(c0, c1, c2, c3) asm volatile("s_nop 7" : "+v"(c0), "+v"(c1), "+v"(c2), "+v"(c3))
#define PIN4(c0, c1, c2, c3) asm volatile("" : "+v"(c0), "+v"(c1), "+v"(c2), "+v"(c3))
#define GLDS_S(sbase, voff, sl, IMM) \
    asm volatile("s_add_u32 m0, %2, %3\n\ts_nop 0\n\tglobal_load_lds_dwordx4 %0, %1" \
                 :: "v"(voff), "s"(sbase), "s"(sl), "i"(IMM) : "memory", "scc")
#define GLDS_K(sbase, voff, sl, IMM) \
    asm volatile("s_add_u32 m0, %2, %3\n\ts_nop 0\n\tglobal_load_lds_dwordx4 %0, %1 nt" \
                 :: "v"(voff), "s"(sbase), "s"(sl), "i"(IMM) : "memory", "scc")
#define RLX_AGENT __ATOMIC_RELAXED, __HIP_MEMORY_SCOPE_AGENT

#define UNITS_A 0x0756075607560560ull
#define UNITS_B 0x1432143214321432ull
#define RA(t) ((int)((UNITS_A >> (4 * (t))) & 15) * 16384)
#define RB(t) ((int)((UNITS_B >> (4 * (t))) & 15) * 16384)
#define LA_OFF 0
#define GA_OFF 32768
#define CV_COPY 4160
#define CV_W (4 * CV_COPY)
#define KC_OFF 65536
#define ST_OFF 114688
#define ST_V 8832
#define ST_SZ (ST_V + 8 * 1056)
#define U_OFF (KC_OFF + 16 * CV_COPY)
#define HT_OFF (U_OFF + 256 + 16384)
#define GW_OFF (HT_OFF + 8192)
#define TAG_OFF (GW_OFF + 2048)
#define UFLAG_OFF (TAG_OFF + 16)
#define G1D_OFF (UFLAG_OFF + 16)
#define MISC_OFF (G1D_OFF + 16)
#define YFLAG_OFF (MISC_OFF + 16)
#define RF_OFF (YFLAG_OFF + 16)
#define EF_OFF (RF_OFF + 64)
#define COK_OFF (EF_OFF + 64)
#define NFLAGW 52
#define DUMMY_OFF (COK_OFF + 16)
#define K1_LDS (DUMMY_OFF + 256)
#define WS_GRAN 0
#define WS_KVFLAG (256u << 10)
#define WS_EPOCH (WS_KVFLAG + 65536)
#define WS_CFLAG (WS_EPOCH + 4096)
#define WS_HFLAG (384u << 10)
#define WS_GATE (512u << 10)

#define CODE_TOUCH(vl) asm volatile("s_getpc_b64 s[98:99]\n\ts_add_u32 s98, s98, k1_kernel@rel32@lo+4\n\ts_addc_u32 s99, s99, k1_kernel@rel32@hi+12\n\t" \
                                    "s_mov_b32 m0, %1\n\ts_nop 0\n\tglobal_load_lds_dword %0, s[98:99]" :: "v"((unsigned)(vl) * 128u), "i"(DUMMY_OFF) : "s98", "s99", "scc", "memory")
#define RD128(a, off) (*(const __attribute__((address_space(3))) bf16x8*)(size_t)(unsigned)((a) + (off)))
#define RDA(dst, A, F) do { const s16x4 lo_ = TRD((A) + 512 * (F) + 8); SCHED(); const s16x4 hi_ = TRD((A) + 512 * (F)); SCHED(); \
                            dst = __builtin_shufflevector(lo_, hi_, 0, 1, 2, 3, 4, 5, 6, 7); } while (0)
#define TOUCH(sbase, voff) asm volatile("s_mov_b32 m0, %2\n\ts_nop 0\n\tglobal_load_lds_dword %0, %1" :: "v"(voff), "s"(sbase), "i"(DUMMY_OFF) : "memory")
#define FLAGD(off) do { unsigned long long pc_; asm volatile("s_getpc_b64 %0\n\ts_mov_b32 m0, %2\n\ts_mov_b64 exec, 1\n\tglobal_load_lds_dword %1, %0\n\ts_mov_b64 exec, -1" \
                                                             : "=&s"(pc_) : "v"(0u), "s"(__builtin_amdgcn_readfirstlane(off)) : "memory"); } while (0)
#define GLDS_SC1(sbase, voff, sl, IMM) \
    asm volatile("s_add_u32 m0, %2, %3\n\ts_nop 0\n\tglobal_load_lds_dwordx4 %0, %1 sc1" \
                 :: "v"(voff), "s"(sbase), "s"(sl), "i"(IMM) : "memory", "scc")
__device__ __forceinline__ unsigned mv(unsigned v) { unsigned r; asm("v_mov_b32 %0, %1" : "=v"(r) : "v"(v)); return r; }
__device__ __forceinline__ unsigned al(unsigned hi, unsigned lo) { unsigned r; asm volatile("v_alignbit_b32 %0, %1, %2, 16" : "=v"(r) : "v"(hi), "v"(lo)); return r; }
__device__ __forceinline__ u32x2 pred2(const u32x4& a, unsigned f0, unsigned f1) {
    return (u32x2){(unsigned)__builtin_amdgcn_update_dpp((int)f0, (int)a.z, 0x138, 0xf, 0xf, false), (unsigned)__builtin_amdgcn_update_dpp((int)f1, (int)a.w, 0x138, 0xf, 0xf, false)};
}
#define RL63(v) ((unsigned)__builtin_amdgcn_readlane((int)(v), 63))
__device__ __forceinline__ void wait_lds4(char* smem, int off) {
    const unsigned a = (unsigned)(size_t)(lds_void*)smem + off;
    unsigned spins = 0;
    for (;;) {
        u32x4 f;
        asm volatile("ds_read_b128 %0, %1\n\ts_waitcnt lgkmcnt(0)" : "=&v"(f) : "v"(a) : "memory");
        if (__builtin_amdgcn_readfirstlane((f.x != 0u) & (f.y != 0u) & (f.z != 0u) & (f.w != 0u))) break;
        if (++spins > SPIN_MAX * 16u) break;
        __builtin_amdgcn_s_sleep(1);
    }
}
__device__ __forceinline__ unsigned lds_word(char* smem, int off) {
    const unsigned a = (unsigned)(size_t)(lds_void*)smem + off;
    unsigned f;
    asm volatile("ds_read_b32 %0, %1\n\ts_waitcnt lgkmcnt(0)" : "=&v"(f) : "v"(a) : "memory");
    return __builtin_amdgcn_readfirstlane(f);
}
__device__ __forceinline__ void wait_lds1(char* smem, int off) {
    unsigned spins = 0;
    while (lds_word(smem, off) == 0u) {
        if (++spins > SPIN_MAX * 16u) break;
        __builtin_amdgcn_s_sleep(1);
    }
}
#define SETW(off, i, v) __hip_atomic_store((lds_u32*)(smem + (off)) + (i), (v), __ATOMIC_RELAXED, __HIP_MEMORY_SCOPE_WORKGROUP)

#define PRIO_CV 1
#define PRIO_KV 3
#define CS0 2
#define CS1 4
#define CS2 6
#define XE 5
#define YPOLL 32
#define ZF 59
template <int I> struct IC { static constexpr int value = I; };
template <int N, int I = 0, class F> __device__ __forceinline__ void unroll(F& f) { if constexpr (I < N) { f(IC<I>{}); unroll<N, I + 1>(f); } }
#define MF0(c, a, b) asm volatile("v_mfma_f32_16x16x32_bf16 %0, %1, %2, 0" : "=&v"(c) : "v"(a), "v"(b))
#define DEPTH 6
#define PRIO2_G 0
#define PRIO2_L 1
__device__ __forceinline__ cf pick4(int q, cf a1, cf a2, cf a3) {
    return {q == 0 ? 1.f : q == 1 ? a1.r : q == 2 ? a2.r : a3.r, q == 0 ? 0.f : q == 1 ? a1.i : q == 2 ? a2.i : a3.i};
}
__device__ __forceinline__ void idle_mfma(unsigned a, unsigned b, int n) {
    u32x4 p = {a * 2654435761u, b * 40503u, a * 2246822519u, b * 3266489917u};
    p = (p & 0xbfffbfffu) | 0x3c003c00u;
    u32x4 q = p.yzwx ^ 0x01230456u;
    f32x4 w;
    asm volatile("s_nop 1" : "+v"(p), "+v"(q));
    MF0(w, p, q);
#pragma unroll 1
    for (int i = 0; i < n; ++i) { MF(w, q, p); MF(w, p, p); MF(w, q, q); MF(w, p, q); }
    asm volatile("s_nop 7" : "+v"(w));
}
#define PINC(c) asm volatile("" : "+v"((c).r), "+v"((c).i))

extern "C" __global__ __launch_bounds__(512, 2) void k1_kernel(
        const u16* __restrict__ x, const u16* __restrict__ Bre, const u16* __restrict__ Bim,
        const u16* __restrict__ kin, const u16* __restrict__ vin, const u16* __restrict__ shiftk, const u16* __restrict__ gatew, char* ws,
        const u16* __restrict__ lre, const u16* __restrict__ lim, const u16* __restrict__ logdt,
        const u16* __restrict__ ssmk, const u16* __restrict__ ssmD,
        const u16* __restrict__ Cre, const u16* __restrict__ Cim, const u16* __restrict__ dskip, float* __restrict__ out) {
    extern __shared__ __attribute__((aligned(16))) char smem[];
#define h ((u16*)(ws + (1u << 20)))
#define kvT ((u16*)(ws + (9u << 20)))
#define y2T ((u16*)(ws + (13u << 20)))
#define dbg ((unsigned long long*)(ws + (64u << 20)))
    const int tid = TIDX;
    if (tid < 40 && tid + ((BIDX >> 3) & 7) * 40 < CODE_LINES) CODE_TOUCH(tid + ((BIDX >> 3) & 7) * 40);
    const int lane = tid & 63;
    int wid = __builtin_amdgcn_readfirstlane(tid >> 6);
    const int b = BIDX;
    const int xcd = b & 7, idx = b >> 3;
    const int tb = ((xcd >> 1) ^ (xcd >> 2)) * 4 + (idx & 3), nb = (xcd & 1) * 8 + (idx >> 2);
    const int cb = xcd * 32 + idx;
    const int t_base = tb * 128, p_base = nb * 64, d_base = nb * 64;
    const int l15 = lane & 15, q4 = lane >> 4;
    unsigned stv = 0, sth = 0;
    STAMPT(dbg, 0, 0);
    const unsigned lbase = (unsigned)(size_t)(lds_void*)smem;
    const int rot1 = (xcd >> 1) * 4 + (xcd & 1) * 8 + (idx >> 2) + (idx & 1);
    unsigned voA[4] = {0u, 0u, 0u, 0u}, voB[4] = {0u, 0u, 0u, 0u}, lw0 = 0;
    if (wid == 4 || wid == 5) {
        __builtin_amdgcn_s_setprio(PRIO_L);
        const int lh = wid - 4;
        const unsigned ch = (unsigned)(((lane & 7) ^ (lane >> 4)) * 16);
        voA[0] = (unsigned)(t_base + lh * 16 + (lane >> 5) * 32 + ((lane >> 3) & 3)) * 2048u + ch;
        voA[1] = (voA[0] ^ 64u) + (131072u - 1024u);
        voA[2] = voA[0] + 6144u; voA[3] = voA[1] + 6144u;
        voB[0] = (unsigned)(p_base + lh * 32 + (lane >> 3)) * 2048u + ch;
        voB[1] = (voB[0] ^ 64u) + (16384u - 1024u);
        voB[2] = voB[0] + 30720u; voB[3] = voB[1] + 30720u;
        PIN4(voA[0], voA[1], voA[2], voA[3]); PIN4(voB[0], voB[1], voB[2], voB[3]);
#define GLDS4(sbase, v0, v1, v2, v3, sl, IMM) \
            asm volatile("s_add_u32 m0, %5, %6\n\ts_nop 0\n\tglobal_load_lds_dwordx4 %0, %4\n\tglobal_load_lds_dwordx4 %1, %4 offset:1024\n\t" \
                         "global_load_lds_dwordx4 %2, %4 offset:2048\n\tglobal_load_lds_dwordx4 %3, %4 offset:3072" \
                         :: "v"(v0), "v"(v1), "v"(v2), "v"(v3), "s"(sbase), "s"(sl), "i"(IMM) : "memory", "scc")
#define STAGE_A(kt, sl) do { const char* sa_ = (const char*)x + (((kt) + rot1) & 15) * 128; \
            GLDS4(sa_, voA[0], voA[1], voA[2], voA[3], sl, 0); GLDS4(sa_ + 16384, voA[0], voA[1], voA[2], voA[3], sl, 4096); } while (0)
#define STAGE_B(kt, sl) do { const int kr_ = (((kt) + rot1) & 15) * 128; const char* sb_ = (const char*)Bre + kr_; const char* sc_ = (const char*)Bim + kr_; \
            GLDS4(sb_, voB[0], voB[1], voB[2], voB[3], sl, 0); GLDS4(sc_, voB[0], voB[1], voB[2], voB[3], sl, 4096); } while (0)
        lw0 = lbase + lh * 8192;
        STAGE_A(0, lw0 + RA(0)); STAGE_B(0, lw0 + RB(0));
        idle_mfma(voA[0], voB[1], 12);
        STAGE_A(1, lw0 + RA(1)); STAGE_B(1, lw0 + RB(1));
    }
    const int sblk = b >> 3, dblk = b & 7;
    u32x2 tw[4];
    unsigned epoch;
    asm volatile("" : "=v"(tw[0]), "=v"(tw[1]), "=v"(tw[2]), "=v"(tw[3]), "=v"(epoch));
    if (wid >= 6) {
        __builtin_amdgcn_s_setprio(PRIO_L);
        const int vw = wid - 6;
        const int dch = dblk * 128 + vw * 64 + l15;
        const unsigned lk = lbase + ST_OFF + vw * ST_SZ;
        const unsigned vk = (unsigned)((lane >> 3) * DM * 2 + (((lane & 7) ^ ((lane >> 4) & 1)) * 16));
        const char* kb = (const char*)kin + (size_t)(sblk * 64 - 4) * (DM * 2) + dblk * 256 + vw * 128;
#define KLD(n) GLDS_K(kb + (n) * 8 * DM * 2, vk, lk, (n) * 1024 + ((n) >> 1) * 32)
#define VLD(n) GLDS_K(vb + (((n) >> 1) * 16 + ((n) & 1) * 2) * DM * 2, vv_, lk, ST_V + (n) * 1056)
        if (vw == 1) {
            const unsigned vg = (unsigned)((lane * 16 + rot1 * 128) & 2047), lg = lbase + GW_OFF;
            GLDS_S(gatew, vg, lg, 0); GLDS_S(gatew, vg ^ 1024u, lg, 1024);
        }
        asm volatile("global_load_dwordx2 %0, %5, %7\n\tglobal_load_dwordx2 %1, %5, %7 offset:128\n\tglobal_load_dwordx2 %2, %5, %7 offset:256\n\t"
                     "global_load_dwordx2 %3, %5, %7 offset:384\n\tglobal_load_dword %4, %6, %8 sc1"
                     : "+v"(tw[0]), "+v"(tw[1]), "+v"(tw[2]), "+v"(tw[3]), "+v"(epoch) : "v"((unsigned)(dch * 8)), "v"((unsigned)WS_EPOCH), "s"(shiftk), "s"(ws) : "memory");
        if (sblk == 0 && lane < 32) *(u32x4*)(smem + ST_OFF + vw * ST_SZ + lane * 16) = (u32x4){0u, 0u, 0u, 0u};
        if (sblk != 0 || lane >= 32) KLD(0);
        KLD(1); KLD(2); KLD(3); KLD(4); KLD(5); KLD(6); KLD(7);
        if (lane < 32) KLD(8);
        __builtin_amdgcn_s_setprio(0);
    }
    asm volatile("" : "+s"(wid));

    const int wf_ = (nb >> 3) ^ (xcd >> 2), tq = (xcd >> 1) & 1;
#define WSL(w) ((((w) >> 2) ^ wf_) * 4 + ((w) & 3))
#define COFF(w, ti) ((2 * WSL(w) + (((ti) >> 1) ^ tq)) * 128 + ((ti) & 1) * 4096)
    bf16x8 cf_[8][4];
    bf16x8 af[DEPTH];
    int cra, crb;
#define CT_SETUP() const unsigned vB0 = (unsigned)((d_base + (lane >> 3) * 4) * 2048 + (((lane & 7) ^ (lane >> 4)) * 16)), vB1 = (vB0 ^ 64u) + 65536u - 1024u
#define CTILE(w, ti, dst) do { const int co_ = COFF(w, ti); const unsigned ds_ = (dst); \
                               GLDS4((const char*)Cre + co_, vB0, vB1, vB0, vB1, ds_, 0); GLDS4((const char*)Cim + co_, vB0, vB1, vB0, vB1, ds_, 4096); } while (0)
#define CF(ti, n) cf_[4 * ((ti) >> 1) + ((n) >> 1)][2 * ((ti) & 1) + ((n) & 1)]
#define CRD(ti, n) CF(ti, n) = RD128((((n) & 4) ? crb : cra) ^ (((n) & 2) * 16), ((n) & 1) * 2048)
#define CREAD(ti) do { _Pragma("unroll") for (int n_ = 0; n_ < 8; ++n_) CRD(ti, n_); } while (0)
#define CPIN(ti) do { PIN4(CF(ti, 0), CF(ti, 1), CF(ti, 2), CF(ti, 3)); PIN4(CF(ti, 4), CF(ti, 5), CF(ti, 6), CF(ti, 7)); } while (0)
#define CRA(base) ((base) + (q4 >> 1) * 4096 + l15 * 128 + (((q4 & 1) ^ ((l15 >> 1) & 7)) * 16))
    const int rot2 = (idx & 16) >> 2;
#define JB(u) (((((u) ^ (rot2 >> 2)) & 1) << 2) | ((wid - ((u) >> 1) + rot2) & 3))
    const __amdgpu_buffer_rsrc_t hs = __builtin_amdgcn_make_buffer_rsrc((char*)h + (size_t)tb * 524288 + WSL(wid) * 8192, 0, 524288, 0x00020000);
#define LOADF(f) do { af[(f) % DEPTH] = __builtin_bit_cast(bf16x8, __builtin_amdgcn_raw_buffer_load_b128(hs, vh + ((f) & 3) * 1024, JB((f) >> 3) * 65536 + ((((f) >> 2) & 1) ^ tq) * 4096, 16)); SCHED(); } while (0)
#define HPOLL_SETUP() gu32* hfp = (gu32*)(ws + WS_HFLAG) + (tb * 16 + 2 * WSL(wid) + (lane & 1)) * 32
#define HWAIT() do { unsigned spins = 0; \
            while (!__all(__hip_atomic_load(hfp, RLX_AGENT) == tag)) { if (++spins > SPIN_MAX) break; __builtin_amdgcn_s_sleep(PSLEEP); } asm volatile("" ::: "memory"); } while (0)

    const __amdgpu_buffer_rsrc_t gs = __builtin_amdgcn_make_buffer_rsrc(ws + WS_GATE, 0, 32768, 0x00020000);
#define GV(q4) ((unsigned)(((q4) >> 1) * 128 + ((q4) & 1) * 32))
#define GSO(r) (tb * 2048 + JB(6 + (r)) * 256 + (wid >> 2) * 16)
#define TR(r) (t_base + JB(6 + (r)) * 4 + (wid >> 2) * 2)
    u32x2 xx[2][2], dk2;
    u32x4 gv[2];
#define XLOAD() do { \
        const __amdgpu_buffer_rsrc_t xs = __builtin_amdgcn_make_buffer_rsrc((void*)x, 0, SEQ * DM * 2, 0x00020000); \
        const __amdgpu_buffer_rsrc_t ks = __builtin_amdgcn_make_buffer_rsrc((void*)dskip, 0, DM * 2, 0x00020000); \
        dk2 = __builtin_amdgcn_raw_buffer_load_b64(ks, (unsigned)(l15 * 8), d_base * 2, 0); \
        _Pragma("unroll") for (int q = 0; q < 4; ++q) \
            xx[q >> 1][q & 1] = __builtin_amdgcn_raw_buffer_load_b64(xs, (unsigned)(q4 * 32 * DM * 2 + l15 * 8 + (q & 1) * DM * 2), (TR(q >> 1) * DM + d_base) * 2, 0); \
        _Pragma("unroll") for (int r = 0; r < 2; ++r) gv[r] = __builtin_amdgcn_raw_buffer_load_b128(gs, GV(q4), GSO(r), 16); \
        SCHED(); } while (0)
#define XPIN() do { PIN4(xx[0][0], xx[0][1], xx[1][0], xx[1][1]); asm volatile("" : "+v"(dk2), "+v"(gv[0]), "+v"(gv[1])); } while (0)

    if (wid < 4) {
        typedef __attribute__((ext_vector_type(8))) unsigned u32x8;
        typedef const __attribute__((address_space(4))) u32x8 cu8;
        const int po = (p_base + wid * 16) * 2;
        u32x8 s0 = *(cu8*)((size_t)logdt + po), s1 = *(cu8*)((size_t)lre + po), s2 = *(cu8*)((size_t)lim + po);
        SCHED();
        const int pq = p_base + wid * 16 + l15;
        unsigned goff[4], gmask[4];
#pragma unroll
        for (int i = 0; i < 4; ++i) {
            goff[i] = (unsigned)(((tb - 1 - q4 - 4 * i) * 1024 + pq) * 16);
            gmask[i] = (tb - 1 - q4 - 4 * i >= 0) ? ~0u : 0u;
            asm volatile("" : "+v"(goff[i]), "+v"(gmask[i]));
        }
        unsigned gpub = q4 == 0 ? (unsigned)((tb * 1024 + pq) * 16) : 0x80000000u;
        unsigned hp = (unsigned)(tb * 524288 + (nb * 4 + wid) * 1024 + lane * 16);
        unsigned wa = (unsigned)(HT_OFF + wid * 2048 + (q4 * 16 + (l15 ^ ((q4 >> 1) * 4))) * 8);
        unsigned ra0 = (unsigned)(HT_OFF + wid * 2048 + ((q4 >> 1) * 64 + (l15 & 3) * 16 + (((q4 & 1) * 8 + (l15 >> 2)) ^ ((l15 & 2) * 2))) * 8), ra1 = ra0 ^ 32u;
        cra = CRA(GA_OFF + wid * 8192); crb = cra ^ 64;
        unsigned vB0 = (unsigned)((d_base + (lane >> 3) * 4) * 2048 + (((lane & 7) ^ (lane >> 4)) * 16)), vB1 = (vB0 ^ 64u) + 65536u - 1024u;
        PIN4(gpub, vB0, hp, wa); PIN4(ra0, ra1, cra, crb); asm volatile("" : "+v"(vB1));
        f32x4 acc[8][2];
#pragma unroll
        for (int m = 0; m < 8; m += 2) {
#pragma unroll
            for (int c = 0; c < 4; ++c) acc[m + (c >> 1)][c & 1] = (f32x4){0.f, 0.f, 0.f, 0.f};
            PIN4(acc[m][0], acc[m][1], acc[m + 1][0], acc[m + 1][1]);
        }
        SCHED();
        cf L1, Lq3, cc, L32, cLq, Lp, L512;
        {
            unsigned w0 = 0, w1 = 0, w2 = 0;
#pragma unroll
            for (int i = 0; i < 8; ++i) {
                asm("v_writelane_b32 %0, %3, %6\n\tv_writelane_b32 %1, %4, %6\n\tv_writelane_b32 %2, %5, %6" : "+v"(w0), "+v"(w1), "+v"(w2) : "s"(s0[i]), "s"(s1[i]), "s"(s2[i]), "i"(2 * i));
            }
            asm("s_nop 1" : "+v"(w0), "+v"(w1), "+v"(w2));
            auto spread = [&](unsigned w) {
                const unsigned e = (unsigned)__builtin_amdgcn_update_dpp(0, (int)w, 0xa0, 0xf, 0xf, false);
                const unsigned a = __builtin_amdgcn_permlane16_swap(e, e, false, false)[0];
                const unsigned c = __builtin_amdgcn_permlane32_swap(a, a, false, false)[0];
                return (l15 & 1) ? hi16(c) : lo16(c);
            };
            const float ldt = spread(w0), re = spread(w1), im = spread(w2);
            if (wid == 3 && lane < NFLAGW) *(unsigned*)(smem + UFLAG_OFF + lane * 4) = 0u;
            const float dt = rbf(expf(ldt));
            const float ea = expf(dt * re);
            float sn, cs;
            sincosf(dt * im, &sn, &cs);
            L1 = {ea * cs, ea * sn};
            const float nr = L1.r - 1.0f, ni = L1.i, den = re * re + im * im;
            cc = {2.f * ((nr * re + ni * im) / den), 2.f * ((ni * re - nr * im) / den)};
            const cf T2 = cmul(L1, L1), T4 = cmul(T2, T2), L8 = cmul(T4, T4), L16 = cmul(L8, L8);
            L32 = cmul(L16, L16);
            const cf L64 = cmul(L32, L32), L96 = cmul(L64, L32), L128 = cmul(L64, L64), L256 = cmul(L128, L128), L384 = cmul(L256, L128);
            L512 = cmul(L256, L256);
            Lp = pick4(q4, L128, L256, L384);
            Lq3 = pick4(3 - q4, L32, L64, L96);
            cLq = cmul(cc, pick4(q4, L32, L64, L96));
            PINC(L1); PINC(Lq3); PINC(cc); PINC(L32); PINC(cLq); PINC(Lp); PINC(L512);
        }
        __builtin_amdgcn_s_setprio(PRIO_G);
        unsigned tagv;
        cf S;
        STAMPT(dbg, 22, 0);
        const int swz = (l15 >> 1) & 7;
        {
            const int a0 = RA(0) + l15 * 128 + ((q4 ^ swz) * 16);
            const int b0 = RB(0) + ((wid >> 1) * 64 + (wid & 1) * 16 + l15) * 128 + ((q4 ^ swz) * 16);
            WAIT_LGKM0();
            BAR();
            asm volatile("" ::: "memory");
            STAMPT(dbg, 19, 64);
            bf16x8 FA[8][2], FB[2][2][2];
            int pa[2] = {a0, a0 ^ 64}, pb[2] = {b0, b0 ^ 64};
            PIN4(pa[0], pa[1], pb[0], pb[1]);
#define LGW(n) __builtin_amdgcn_s_waitcnt(0xC07F | ((n) << 8))
#define RA_(j) FA[(j) >> 1][(j) & 1] = RD128(pa[(j) & 1], ((j) >> 1) * 2048)
#define RB_(bu, j) FB[bu][(j) >> 1][((j) ^ ((j) >> 1)) & 1] = RD128(pb[((j) ^ ((j) >> 1)) & 1], ((j) >> 1) * 4096)
#define RN_(bu, i) do { if ((i) == 0) RB_(bu, 0); else if ((i) < 3) RA_((i) - 1); else if ((i) < 6) RB_(bu, (i) - 2); else RA_((i) - 4); } while (0)
#define G1_H(H, bu, RD, FILL) do { \
            _Pragma("unroll") for (int i = 0; i < 16; ++i) { \
                const int m = (H) * 4 + (i >> 2), n = ((i >> 1) ^ m) & 1, ks = (i ^ (i >> 1)) & 1; \
                if ((H) == 0) { if (i == 0) LGW(6); if (i == 4) LGW(8); if (i == 8) LGW(10); if (i == 12) LGW(8); } \
                SCHED(); \
                MF(acc[m][n], FA[m][ks], FB[bu][n][ks]); SCHED(); \
                if ((H) == 0 && i < 8) RA_(8 + i); \
                if ((H) == 1 && (RD) && i < 12) RN_((bu) ^ 1, i); \
                FILL((H) * 16 + i); \
                SCHED(); \
            } } while (0)
#define NOF(g) do { } while (0)
#pragma unroll
            for (int j = 0; j < 12; ++j) { RN_(0, j); SCHED(); }
            unsigned long long ua_ = UNITS_A;
            int sb = RB(0), sa = RA(0), nx, na;
#define ADV(g) do { if ((g) == 8) { ua_ >>= 4; na = (int)((unsigned)ua_ & 15u) << 14; asm volatile("" : "+s"(na), "+s"(ua_)); } \
                    if ((g) == 9) { const int o_ = sa; sa = na; na -= o_; asm volatile("" : "+s"(na), "+s"(sa)); } \
                    if ((g) == 10) { pa[0] += na; pa[1] += na; asm volatile("" : "+v"(pa[0]), "+v"(pa[1])); } \
                    if ((g) == 11) { nx = (sb == 65536) ? -49152 : 16384; sb += nx; asm volatile("" : "+s"(nx), "+s"(sb)); } \
                    if ((g) == 13) { pb[0] += nx; pb[1] += nx; asm volatile("" : "+v"(pb[0]), "+v"(pb[1])); } } while (0)
#define G1_T(bu) do { \
                G1_H(0, bu, true, ADV); \
                WAIT_LGKM0(); \
                asm volatile("" ::: "memory"); \
                BAR();                     \
                asm volatile("" ::: "memory"); \
                G1_H(1, bu, true, NOF); } while (0)
#pragma unroll 1
            for (int t2 = 0; t2 < 7; ++t2) { G1_T(0); G1_T(1); }
            G1_T(0);
#define AGG(g) do { if ((g) == 8) { asm volatile("" ::: "memory"); SETW(G1D_OFF, wid, 1u); }        \
                    if ((g) == 9) tagv = *(volatile const lds_u32*)(size_t)(unsigned)TAG_OFF; \
                    if ((g) >= 7) { const int t = (g) - 6; \
                        if (t == 1) { asm volatile("s_nop 1" : "+v"(acc[0][0]), "+v"(acc[0][1])); S = {acc[0][0][0], acc[0][1][0]}; } \
                        if ((t & 3) == 0) asm volatile("" : "+v"(acc[t >> 2][0]), "+v"(acc[t >> 2][1])); \
                        S = cfma(L1, S, cf{acc[t >> 2][0][t & 3], acc[t >> 2][1][t & 3]}); PINC(S); } } while (0)
            G1_H(0, 1, false, AGG);
            G1_H(1, 1, false, AGG);
        }
        STAMPT(dbg, 1, 0);
        const unsigned tag = __builtin_amdgcn_readfirstlane(tagv);
        const __amdgpu_buffer_rsrc_t grs = __builtin_amdgcn_make_buffer_rsrc(ws, 0, 15 * 16384, 0x00020000);
        {
#pragma unroll
            for (int t = 26; t < 32; ++t) {
                if (t == 28) { PINC(S); asm volatile("" : "+v"(acc[7][0]), "+v"(acc[7][1])); }
                S = cfma(L1, S, cf{acc[t >> 2][0][t & 3], acc[t >> 2][1][t & 3]});
            }
            const cf W = cmul(S, Lq3);
            const float e2 = red16(red32(W.r, W.i));
            const auto eo = __builtin_amdgcn_permlane32_swap(__float_as_uint(e2), __float_as_uint(e2), false, false);
            __builtin_amdgcn_raw_buffer_store_b128((u32x4){eo[0], tag, eo[1], tag}, grs, gpub, 0, 16);
        }
        SCHED();
        STAMPT(dbg, 2, 0);
        const unsigned cg = lbase + GA_OFF + wid * 8192;
        wait_lds1(smem, COK_OFF + wid * 4);
        CREAD(0);
        WAIT_LGKM0();
        CPIN(0);
        CTILE(wid, 1, cg);
        u32x4 gq[4];
#pragma unroll
        for (int m = 0; m < 8; ++m) {
            const f32x4 R = acc[m][0], I = acc[m][1];
            acc[m][0] = fma4(splat4(cc.r), R, mul4(splat4(-cc.i), I));
            acc[m][1] = fma4(splat4(-cc.r), I, mul4(splat4(-cc.i), R));
            asm volatile("" : "+v"(acc[m][0]), "+v"(acc[m][1]));
            if (m == 3) {
                SCHED();
#pragma unroll
                for (int i = 0; i < 4; ++i) asm volatile("buffer_load_dwordx4 %0, %1, %2, 0 offen sc1" : "=&v"(gq[i]) : "v"(goff[i]), "s"(grs) : "memory");
                SCHED();
            }
        }
        cf Ec;
        {
            const auto sr = __builtin_amdgcn_permlane16_swap(__float_as_uint(S.r), __float_as_uint(S.r), false, false);
            const auto si = __builtin_amdgcn_permlane16_swap(__float_as_uint(S.i), __float_as_uint(S.i), false, false);
            const cf Se = {__uint_as_float(sr[0]), __uint_as_float(si[0])}, So = {__uint_as_float(sr[1]), __uint_as_float(si[1])};
            const cf A = cfma(L32, Se, So);
            const auto ar = __builtin_amdgcn_permlane32_swap(__float_as_uint(A.r), __float_as_uint(A.r), false, false);
            const auto ai = __builtin_amdgcn_permlane32_swap(__float_as_uint(A.i), __float_as_uint(A.i), false, false);
            const cf A01 = {(q4 & 2) ? __uint_as_float(ar[0]) : 0.f, (q4 & 2) ? __uint_as_float(ai[0]) : 0.f};
            const cf kq = {(q4 & 1) ? L32.r : 1.f, (q4 & 1) ? L32.i : 0.f};
            const cf E = cfma(kq, A01, cf{(q4 & 1) ? Se.r : 0.f, (q4 & 1) ? Se.i : 0.f});
            Ec = cmul(cc, E);
            PINC(Ec);
        }
        WAIT_VM(4);
        CREAD(1);
        WAIT_LGKM0();
        CPIN(1);
        CTILE(wid, 2, cg);
        asm volatile("s_waitcnt vmcnt(8)" : "+v"(gq[0]), "+v"(gq[1]), "+v"(gq[2]), "+v"(gq[3]) :: "memory");
        STAMPT(dbg, 27, 0);
        cf carry;
        {
            unsigned spins = 0;
            for (;;) {
                unsigned bad = 0;
#pragma unroll
                for (int i = 0; i < 4; ++i) bad |= ((gq[i].y ^ tag) | (gq[i].w ^ tag)) & gmask[i];
                if (__all(bad == 0u)) break;
                if (++spins > SPIN_MAX) break;
#pragma unroll
                for (int i = 0; i < 4; ++i) asm volatile("buffer_load_dwordx4 %0, %1, %2, 0 offen sc1" : "+v"(gq[i]) : "v"(goff[i]), "s"(grs) : "memory");
                asm volatile("s_waitcnt vmcnt(0)" : "+v"(gq[0]), "+v"(gq[1]), "+v"(gq[2]), "+v"(gq[3]) :: "memory");
            }
            cf E = {__uint_as_float(gq[3].x), __uint_as_float(gq[3].z)};
#pragma unroll
            for (int i = 2; i >= 0; --i) E = cfma(L512, E, cf{__uint_as_float(gq[i].x), __uint_as_float(gq[i].z)});
            E = cmul(E, Lp);
            const float e2 = red16(red32(E.r, E.i));
            const auto eo = __builtin_amdgcn_permlane32_swap(__float_as_uint(e2), __float_as_uint(e2), false, false);
            carry = {__uint_as_float(eo[0]), __uint_as_float(eo[1])};
        }
        STAMPT(dbg, 3, 0);
        {
            const __amdgpu_buffer_rsrc_t hrs = __builtin_amdgcn_make_buffer_rsrc(h, 0, SEQ * 2048 * 2, 0x00020000);
            const cf e = cfma(cLq, carry, Ec);
            float hr = e.r, hn = -e.i;
            s16x4 fr[2][4];
#define HSTORE(m) do { _Pragma("unroll") for (int c = 0; c < 2; ++c) \
                __builtin_amdgcn_raw_buffer_store_b128(__builtin_bit_cast(u32x4, __builtin_shufflevector(fr[(m) & 1][2 * c], fr[(m) & 1][2 * c + 1], 0, 1, 2, 3, 4, 5, 6, 7)), hrs, hp, (2 * (m) + c) * 65536, WT); } while (0)
#pragma unroll
            for (int m = 0; m < 4; ++m) {
#pragma unroll
                for (int j = 0; j < 8; ++j) {
                    const float nr = fmaf(L1.r, hr, fmaf(L1.i, hn, acc[2 * m + (j >> 2)][0][j & 3]));
                    const float nn = fmaf(L1.r, hn, fmaf(-L1.i, hr, acc[2 * m + (j >> 2)][1][j & 3]));
                    hr = nr; hn = nn;
                    acc[2 * m + (j >> 2)][0][j & 3] = nr; acc[2 * m + (j >> 2)][1][j & 3] = nn;
                }
                u32x2 pk[4];
#pragma unroll
                for (int c = 0; c < 4; ++c) { const f32x4 v = acc[2 * m + (c >> 1)][c & 1]; pk[c] = (u32x2){pkbf(v[0], v[1]), pkbf(v[2], v[3])}; }
                PIN4(pk[0], pk[1], pk[2], pk[3]);
                SCHED();
#pragma unroll
                for (int c = 0; c < 4; ++c) { *(volatile __attribute__((address_space(3))) u32x2*)(size_t)(wa + c * 512) = pk[c]; SCHED(); }
                asm volatile("" ::: "memory");
#pragma unroll
                for (int c = 0; c < 2; ++c) { fr[m & 1][2 * c] = TRD(ra0 + c * 1024); SCHED(); fr[m & 1][2 * c + 1] = TRD(ra1 + c * 1024); SCHED(); }
                asm volatile("" ::: "memory");
                if (m > 0) HSTORE(m - 1);
                SCHED();
            }
            HSTORE(3);
        }
        STAMPT(dbg, 4, 0);
        {
            WAIT_VM(8);
            CREAD(2);
            WAIT_VM(0);
            {
                unsigned n;
                asm volatile("s_mov_b64 exec, 1\n\tds_add_rtn_u32 %0, %1, %2\n\ts_mov_b64 exec, -1\n\ts_waitcnt lgkmcnt(0)" : "=&v"(n) : "v"(MISC_OFF), "v"(1u) : "memory");
                if (__builtin_amdgcn_readfirstlane(n) == 3u) {
                    if (lane == 0) __hip_atomic_store((gu32*)(ws + WS_HFLAG) + (tb * 16 + nb) * 32, tag, RLX_AGENT);
                    if (DBGON && lane == 0) dbg[BIDX * 64 + 20] = __builtin_amdgcn_s_memrealtime();
                }
            }
            STAMPT(dbg, 12, 0);
            if (wid == 0 && lane < 7) CODE_TOUCH(CODE_LINES - 1 - lane - (idx & 7) * 7);
            __builtin_amdgcn_s_setprio(0);
            WAIT_LGKM0();
            CPIN(2);
            CTILE(wid, 3, cg);
            XLOAD();
            HPOLL_SETUP();
            HWAIT();
            STAMPT(dbg, 8, 0);
            const unsigned vh = (unsigned)(lane * 16);
#pragma unroll
            for (int f = 0; f < DEPTH; ++f) LOADF(f);
            __builtin_amdgcn_s_setprio(PRIO2_G);
        }
    } else {
        const int lw = wid - 4;
        const int d = cb * 4 + lw;
        const char* Krow = (const char*)(ssmk + (size_t)d * SEQ);
        cra = CRA(LA_OFF + lw * 8192); crb = cra ^ 64;
        const unsigned cst0 = lbase + LA_OFF + lw * 8192;
        CT_SETUP();
        asm volatile("" : "+v"(cra), "+v"(crb));
        u32x4 KA[4];
        unsigned draw;
        unsigned tag;
        float gacc = 0.f;
#define KLOAD() asm volatile("global_load_dwordx4 %0, %5, %7 nt\n\tglobal_load_dwordx4 %1, %5, %7 offset:1024 nt\n\tglobal_load_dwordx4 %2, %5, %7 offset:2048 nt\n\t" \
                             "global_load_dwordx4 %3, %5, %7 offset:3072 nt\n\tglobal_load_ushort %4, %6, %8" \
                             : "=&v"(KA[0]), "=&v"(KA[1]), "=&v"(KA[2]), "=&v"(KA[3]), "=&v"(draw) : "v"((unsigned)(lane * 16)), "v"((unsigned)(d * 2)), "s"(Krow), "s"(ssmD) : "memory")
#define KWAIT(n) asm volatile("s_waitcnt vmcnt(%5)" : "+v"(KA[0]), "+v"(KA[1]), "+v"(KA[2]), "+v"(KA[3]), "+v"(draw) : "i"(n) : "memory")
        if (wid < 6) {
            WAIT_VM(16);
            STAMPT(dbg, 17, 320);
            BAR();
            STAGE_A(2, lw0 + RA(2)); STAGE_B(2, lw0 + RB(2));
            unsigned long long ua_ = UNITS_A >> 12;
            unsigned sla = lw0 + RA(3), slb = lw0 + RB(4);
            int ka = ((3 + rot1) & 15) * 128, kb_ = ka;
            const char* sa = (const char*)x + ka;
            asm volatile("" : "+s"(sa), "+s"(sla), "+s"(ka), "+s"(ua_));
#define NEXT_A() do { ka = (ka + 128) & 2047; sa = (const char*)x + ka; ua_ >>= 4; sla = lw0 + (((unsigned)ua_ & 15u) << 14); asm volatile("" : "+s"(sa), "+s"(sla), "+s"(ka), "+s"(ua_)); } while (0)
#define LSTEP(n) do { \
                if ((n) == 24 && t == 1) { WAIT_VM(16); BAR(); STAGE_B(3, lw0 + RB(3)); } else { WAIT_VM(n); BAR(); } \
                GLDS4(sa, voA[0], voA[1], voA[2], voA[3], sla, 0); \
                GLDS4(sa + 16384, voA[0], voA[1], voA[2], voA[3], sla, 4096); \
                kb_ = (kb_ + 128) & 2047; \
                GLDS4((const char*)Bre + kb_, voB[0], voB[1], voB[2], voB[3], slb, 0); \
                NEXT_A(); \
                const unsigned slb0 = slb; \
                slb = (slb == lw0 + 65536) ? lw0 + 16384 : slb + 16384; \
                asm volatile("" : "+s"(slb), "+s"(kb_), "+s"(t)); \
                GLDS4((const char*)Bim + kb_, voB[0], voB[1], voB[2], voB[3], slb0, 4096); } while (0)
#pragma unroll 1
            for (int t = 1; t < 5; ++t) LSTEP(24);
            GLDS4(sa, voA[0], voA[1], voA[2], voA[3], sla, 0); GLDS4(sa + 16384, voA[0], voA[1], voA[2], voA[3], sla, 4096);
            NEXT_A();
#pragma unroll 1
            for (int t = 5; t < 13; ++t) LSTEP(32);
            KLOAD();
            WAIT_VM(37);
            BAR();
            __builtin_amdgcn_s_setprio(0);
            tag = lds_word(smem, TAG_OFF);
            CTILE(lw, 0, cst0 + GA_OFF);
            FLAGD(COK_OFF + lw * 4);
            WAIT_VM(30);
            BAR();
            CTILE(lw + 2, 0, cst0 + GA_OFF + 16384);
            FLAGD(COK_OFF + lw * 4 + 8);
            WAIT_VM(23);
            BAR();
        } else {
            const int vw = wid - 6;
            const int dch = dblk * 128 + vw * 64 + l15;
            const unsigned lk = lbase + ST_OFF + vw * ST_SZ;
            const unsigned vv_ = (unsigned)(((lane >> 4) * 4 + ((lane >> 3) & 1)) * DM * 2 + (lane & 7) * 16);
            const char* vb = (const char*)vin + (size_t)(sblk * 64) * (DM * 2) + dblk * 256 + vw * 128;
            const int grow = nb * 8 + vw * 4 + q4;
            unsigned gx0 = (unsigned)(grow * 128 + (((l15 >> 1) ^ ((grow >> 1) & 7)) * 16) + (l15 & 1) * 8), gx1 = gx0 + RA(2), gwa = (unsigned)(GW_OFF + l15 * 8);
            asm volatile("" : "+v"(gx0), "+v"(gx1), "+v"(gwa));
            u32x2 gxv, gwv;
            typedef const volatile __attribute__((address_space(3))) u32x2* l2p;
#define VBAR(t) do { SCHED(); asm volatile("" ::: "memory"); BAR(); asm volatile("" ::: "memory"); \
                     gxv = *(l2p)(size_t)(RA(t) == 0 ? gx0 : gx1 + (RA(t) - RA(2))); gwv = *(l2p)(size_t)(gwa + (t) * 128); SCHED(); } while (0)
#define GATE() do { SCHED(); gacc = fmaf(lo16(gxv.x), lo16(gwv.x), gacc); gacc = fmaf(hi16(gxv.x), hi16(gwv.x), gacc); \
                    gacc = fmaf(lo16(gxv.y), lo16(gwv.y), gacc); gacc = fmaf(hi16(gxv.y), hi16(gwv.y), gacc); asm volatile("" : "+v"(gacc)); SCHED(); } while (0)
            unsigned ka[4][2], sel[4];
            const int R0 = 4 + (l15 >> 2) * 16 + (l15 & 3) - (q4 >> 1);
            unsigned kr0 = (unsigned)(2 * R0), kr1 = (unsigned)(ST_OFF + vw * ST_SZ + R0 * 128 + (((q4 ^ (R0 >> 1)) & 1) * 16));
            asm volatile("" : "+v"(kr0), "+v"(kr1));
#pragma unroll
            for (int i = 0; i < 4; ++i)
#pragma unroll
                for (int tp = 0; tp < 2; ++tp) {
                    ka[i][tp] = (kr1 ^ (tp * 16)) + ((kr0 + 8 * i - 4 * tp) & 0xe0) + (4 * i - 2 * tp) * 128;
                    asm volatile("" : "+v"(ka[i][tp]));
                }
#pragma unroll
            for (int r = 0; r < 4; ++r) {
                const unsigned pick = (q4 & 2) ? 0x0302u : 0x0100u;
                sel[r] = ((l15 >> 1) == (q4 & 1) * 4 + r) ? ((l15 & 1) ? (pick << 16) | 0x0c0cu : 0x0c0c0000u | pick) : 0x0c0c0c0cu;
            }
            unsigned va_ = (unsigned)(ST_OFF + vw * ST_SZ + ST_V + (q4 * 2 + (l15 >> 3)) * 1056 + ((l15 >> 2) & 1) * 128 + (l15 & 3) * 8);
            unsigned so = (unsigned)(((dch >> 2) * 32 + sblk) * 512 + q4 * 64 + (dch & 3) * 16);
            PIN4(sel[0], sel[1], sel[2], sel[3]); asm volatile("" : "+v"(so), "+v"(va_));
            asm volatile("s_waitcnt vmcnt(9)" : "+v"(tw[0]), "+v"(tw[1]), "+v"(tw[2]), "+v"(tw[3]), "+v"(epoch) :: "memory");
            STAMPT(dbg, 18, 384);
            VBAR(0);
            bf16x8 wf[4][2];
#pragma unroll
            for (int ps = 0; ps < 4; ++ps)
#pragma unroll
                for (int tp = 0; tp < 2; ++tp) {
                    u32x4 w;
#pragma unroll
                    for (int r = 0; r < 4; ++r) w[r] = __builtin_amdgcn_perm(0u, tw[ps][tp], sel[r]);
                    wf[ps][tp] = __builtin_bit_cast(bf16x8, w);
                    asm volatile("" : "+v"(wf[ps][tp]));
                }
            SCHED();
            GATE(); VBAR(1);
            VLD(0); VLD(1); VLD(2); VLD(3); VLD(4); VLD(5); VLD(6); VLD(7);
            f32x4 ks[4][4];
            auto sconv = [&](int i0) __attribute__((always_inline)) {
                bf16x8 kf[2][4][2];
#pragma unroll
                for (int q = 0; q < 16; ++q) { kf[q >> 3][q & 3][(q >> 2) & 1] = RD128(ka[i0 + (q >> 3)][(q >> 2) & 1], (q & 3) * 32); SCHED(); }
                GATE();
                __builtin_amdgcn_s_setprio(PRIO_KV);
                WAIT_LGKM0();
                SCHED();
#pragma unroll
                for (int i = 0; i < 2; ++i) {
#pragma unroll
                    for (int ps = 0; ps < 4; ++ps) MF0(ks[ps][i0 + i], kf[i][ps][0], wf[ps][0]);
#pragma unroll
                    for (int ps = 0; ps < 4; ++ps) MF(ks[ps][i0 + i], kf[i][ps][1], wf[ps][1]);
                }
                __builtin_amdgcn_s_setprio(0);
                __builtin_amdgcn_s_sleep(1);
                SCHED();
            };
            WAIT_VM(8);
            STAMPT(dbg, 21, 384);
            sconv(0); VBAR(2);
            sconv(2); VBAR(3);
            WAIT_VM(0);
            u32x2 vq[4][4];
#pragma unroll
            for (int ps = 0; ps < 4; ++ps)
#pragma unroll
                for (int j = 0; j < 4; ++j) vq[ps][j] = __builtin_bit_cast(u32x2, TRD(va_ + j * 256 + ps * 32));
            const __amdgpu_buffer_rsrc_t rs = __builtin_amdgcn_make_buffer_rsrc(kvT, 0, DM * SEQ * 2, 0x00020000);
            u32x4 o[8];
            auto kvout = [&](int n) __attribute__((always_inline)) {
                const int hh = n >> 2, ps = n & 3;
                asm volatile("" : "+v"(ks[ps][2 * hh]), "+v"(ks[ps][2 * hh + 1]));
#pragma unroll
                for (int e = 0; e < 4; ++e) {
                    const unsigned vv = vq[ps][2 * hh + (e >> 1)][e & 1];
                    o[n][e] = pkbf(ks[ps][2 * hh + (e >> 1)][2 * (e & 1)] * lo16(vv), ks[ps][2 * hh + (e >> 1)][2 * (e & 1) + 1] * hi16(vv));
                }
                asm volatile("" : "+v"(o[n]));
                SCHED();
            };
            GATE(); kvout(0); kvout(1);
            WAIT_LGKM0();

            VBAR(4);
#define KVST(h) do { _Pragma("unroll") for (int n = 0; n < 4; ++n) __builtin_amdgcn_raw_buffer_store_b128(o[4 * (h) + n], rs, so + (unsigned)((h) * 256), n * 65536, WT); \
                     SCHED(); asm volatile("s_nop 1" ::: "memory"); SCHED(); } while (0)
            kvout(2); kvout(3); GATE(); VBAR(5);
            KVST(0);
            kvout(4); kvout(5); GATE(); VBAR(6);
            kvout(6); kvout(7); GATE(); VBAR(7);
            KVST(1);
            GATE(); VBAR(8);
            if (vw == 1 && lane == 0) *(unsigned*)(smem + TAG_OFF) = (epoch + 1u) ? epoch + 1u : 1u;
            GATE();
            WAIT_LGKM0();
            WAIT_VM(0);
            VBAR(9);
            tag = __builtin_amdgcn_readfirstlane(*(const unsigned*)(smem + TAG_OFF));
            if (vw == 0 && lane == 0) __hip_atomic_store((gu32*)(ws + WS_KVFLAG) + (dblk * 32 + sblk) * 32, tag, RLX_AGENT);
            if (vw == 1 && lane < 4) *(u32x4*)(smem + U_OFF + 192 + lane * 16) = (u32x4){0u, 0u, 0u, 0u};
            gu32* kfp = (gu32*)(ws + WS_KVFLAG) + ((cb >> 5) * 32 + vw * 16 + l15) * 32;
            const char* ub = (const char*)kvT + (size_t)__builtin_amdgcn_readfirstlane(cb * 16384 + vw * 8192);
            const unsigned vu = (unsigned)((lane >> 5) * 512 + ((lane >> 2) & 1) * 256 + ((lane >> 3) & 3) * 64 + ((lane - (lane >> 4)) & 3) * 16);
            const unsigned lu = lbase + U_OFF + 256 + vw * 8192;
#define U_DMA1(o) asm volatile("s_add_u32 m0, %2, %3\n\ts_nop 0\n\tglobal_load_lds_dwordx4 %0, %1 sc1\n\tglobal_load_lds_dwordx4 %0, %1 offset:1024 sc1\n\t" \
                             "global_load_lds_dwordx4 %0, %1 offset:2048 sc1\n\tglobal_load_lds_dwordx4 %0, %1 offset:3072 sc1" \
                             :: "v"(vu), "s"(ub + (o)), "s"(lu), "i"(o) : "memory", "scc")
#define U_DMA() do { U_DMA1(0); U_DMA1(4096); FLAGD(UFLAG_OFF + lw * 4); } while (0)
            unsigned fo = lane < 8 ? (unsigned)((tb * 1024 + p_base) * 16 + lane * 128) : lane == 8 ? (unsigned)(WS_HFLAG + (tb * 16 + nb) * 128)
                        : lane == 9 ? (unsigned)(WS_CFLAG + cb * 128) : (unsigned)(WS_GATE + (tb * 16 + nb) * 128);
            asm volatile("" : "+v"(fo));
            GATE(); VBAR(10);
            if (!(LATE)) TOUCH((const char*)(vw ? Cim : Cre) + (d_base + (idx & 3) * 16) * 2048 + COFF(0, 2), (unsigned)((lane >> 2) * 2048 + (lane & 3) * 256));
            if (EARLY) TOUCH((const char*)(vw ? Cim : Cre) + (d_base + (idx & 3) * 16) * 2048 + COFF(4, 2), (unsigned)((lane >> 2) * 2048 + (lane & 3) * 256));
            if (vw == 0) TOUCH(ws, fo); else TOUCH(dskip, (unsigned)(d_base * 2));
            {
                const int cl = 64 + lane + ((idx & 15) * 2 + vw) * 7;
                if (lane < 7 && cl < CODE_LINES) CODE_TOUCH(cl);
            }
            GATE(); VBAR(11);
            GATE(); VBAR(12);
            GATE(); VBAR(13);
            unsigned pv;
            asm volatile("global_load_dword %0, %1, off sc1" : "=&v"(pv) : "v"(kfp) : "memory");
            KLOAD();
            GATE(); VBAR(14);
            GATE(); VBAR(15);
            asm volatile("s_waitcnt vmcnt(5)" : "+v"(pv) :: "memory");
            const bool done = __all(pv == tag);
            if (done) U_DMA();
            if (DBGON && TIDX == 384) dbg[BIDX * 64 + 13] = (done ? 100ull : 0ull) | (1ull << 62);
            GATE();
            if (lane == 0) SETW(UFLAG_OFF, vw, 1u);
            WAIT_VM(9);
            if (!done) {
                unsigned spins = 0;
                for (;;) {
                    const unsigned v = __hip_atomic_load(kfp, RLX_AGENT);
                    if (__all(v == tag)) break;
                    if (++spins > SPIN_MAX) break;
                    __builtin_amdgcn_s_sleep(1);
                }
                U_DMA();
            }
        }
        KWAIT(18);
        STAMPT(dbg, 14, 320);
        __builtin_amdgcn_s_sleep(3);
        if (lw < 2) __builtin_amdgcn_s_sleep(3);
        {
            const unsigned cp = (unsigned)(KC_OFF + lw * CV_W + 64 + lane * 16);
            if (lane == 0) KA[0].x = (KA[0].x & 0xffff0000u) | f2bf(lo16(KA[0].x) + bf2f((u16)draw));
#define WR128(v, off) asm volatile("ds_write_b128 %0, %1 offset:%2" :: "v"(cp), "v"(v), "i"(off) : "memory")
#pragma unroll
            for (int q = 3; q >= 0; --q) WR128(KA[q], q * 1024);
#pragma unroll
            for (int q = 3; q >= 0; --q) {
                const u32x2 p = q ? pred2(KA[q], RL63(KA[q ? q - 1 : 0].z), RL63(KA[q ? q - 1 : 0].w)) : pred2(KA[0], 0u, 0u);
                const u32x4 a = KA[q];
                const u32x4 s1 = {al(a.x, p.y), al(a.y, a.x), al(a.z, a.y), al(a.w, a.z)};
                WR128(s1, q * 1024 + CV_COPY);
                const u32x4 s2 = {p.y, mv(a.x), mv(a.y), mv(a.z)};
                WR128(s2, q * 1024 + 2 * CV_COPY);
                const u32x4 s3 = {al(p.y, p.x), mv(s1.x), mv(s1.y), mv(s1.z)};
                WR128(s3, q * 1024 + 3 * CV_COPY);
            }
            if (lane < 16) *(u32x4*)(smem + KC_OFF + lw * CV_W + (lane >> 2) * CV_COPY + (lane & 3) * 16) = (u32x4){0u, 0u, 0u, 0u};
            SCHED();
        }
        STAMPT(dbg, 26, 320);
        wait_lds4(smem, G1D_OFF);
        wait_lds4(smem, UFLAG_OFF);
        STAMPT(dbg, 9, 256);
        __builtin_amdgcn_s_setprio(PRIO_CV);
        STAMPT(dbg, 10, 384);
        f32x4 cv[8];
#pragma unroll
        for (int g = 0; g < 8; ++g) cv[g] = (f32x4){0.f, 0.f, 0.f, 0.f};
        PIN4(cv[0], cv[1], cv[2], cv[3]); PIN4(cv[4], cv[5], cv[6], cv[7]);
        const int uz = U_OFF + 192;
        const int ua0 = U_OFF + 256 + (2 * l15 + q4 - 2) * 64, rv0 = (2 * l15 + q4 - 2) * 4 + lw * 16;
        const unsigned ka0 = (unsigned)((l15 >> 2) * CV_COPY - 16 * q4 + 8 * (l15 & 3) - 8);
        int ua, rv, pu, pz;
        unsigned adr;
#define UNEXT1() do { pu = ua | (rv & 0x30); asm volatile("" : "+v"(pu)); } while (0)
#define UNEXT2() do { pz = (ua > uz ? ua : uz) | (rv & 0x30); asm volatile("" : "+v"(pz)); } while (0)
#define UNEXT3() do { ua -= 256; rv -= 16; asm volatile("" : "+v"(ua), "+v"(rv)); } while (0)
#define ANEXT() do { adr += 64; asm volatile("" : "+v"(adr)); } while (0)
#define RDU(dst, c) do { dst = (c) ? RD128(pu, (c) * 2048) : RD128(pz, 0); SCHED(); } while (0)
        HPOLL_SETUP();
        unsigned hpv = 0;
        {
            bf16x8 A[8], UB[8];
            ua = ua0; rv = rv0; adr = lbase + KC_OFF + lw * CV_W + ka0 + 96;
            asm volatile("" : "+v"(adr));
            UNEXT1(); UNEXT2(); UNEXT3();
#pragma unroll
            for (int i = 0; i < 8; ++i) { RDA(A[7 - i], adr, 7 - i); RDU(UB[i], i); }
            unsigned gb = lw >= 2 ? (unsigned)((tb * 16 + nb) * 128 + (lw - 2) * 32) : 0x80000000u;
            asm volatile("" : "+s"(gb));
            const unsigned gso = (l15 == 0) ? gb + (unsigned)(q4 * 8) : 0x80000000u;
            float gt = 0.f;
            unsigned vxo = 0, vdo = 0, vgo = 0;
            auto gate_out = [&](int i) __attribute__((always_inline)) {
                if (i == 0) gacc += __uint_as_float(__builtin_amdgcn_update_dpp(0u, __float_as_uint(gacc), 0xb1, 0xf, 0xf, false));
                if (i == 1) gacc += __uint_as_float(__builtin_amdgcn_update_dpp(0u, __float_as_uint(gacc), 0x4e, 0xf, 0xf, false));
                if (i == 2) gacc += __uint_as_float(__builtin_amdgcn_update_dpp(0u, __float_as_uint(gacc), 0x141, 0xf, 0xf, false));
                if (i == 3) gacc += __uint_as_float(__builtin_amdgcn_update_dpp(0u, __float_as_uint(gacc), 0x140, 0xf, 0xf, false));
                if (i == 4) gacc = rbf(gacc);
                if (i == 5) gacc = __builtin_amdgcn_exp2f(gacc * -1.44269504088896f);
                if (i == 6) gacc = __builtin_amdgcn_rcpf(1.0f + gacc);
                if (i == 7) gacc = rbf(gacc);
                if (i == 8) gt = __uint_as_float(pkbf(gacc, 1.0f - gacc));
                if (i == 9) __builtin_amdgcn_raw_buffer_store_b64((u32x2){__float_as_uint(gt), tag}, gs, gso, 0, WT);
                asm volatile("" : "+v"(gacc), "+v"(gt));
            };
            auto step = [&](auto E) __attribute__((always_inline)) {
                constexpr int e = decltype(E)::value;
                constexpr int ti = (e == CS0) ? 0 : (e == CS1) ? 1 : (e == CS2) ? 2 : -1, tj = ti < 0 ? 0 : ti;
                constexpr unsigned char GA[36] = {0x66, 0x77, 0x76, 0x44, 0x55, 0x54, 0x65, 0x64, 0x75, 0x74, 0x22, 0x33, 0x32, 0x43, 0x42, 0x53, 0x52, 0x63, 0x62, 0x73, 0x72,
                                                  0x00, 0x11, 0x10, 0x21, 0x20, 0x31, 0x30, 0x41, 0x40, 0x51, 0x50, 0x61, 0x60, 0x71, 0x70};
                const char* sb_ = (const char*)Cre + COFF(wid, tj + 1);
                const char* sc_ = (const char*)Cim + COFF(wid, tj + 1);
#pragma unroll
                for (int q = 0; q < 36; ++q) {
                    const int g = GA[q] >> 4, a = GA[q] & 15;
                    MF(cv[g], A[a], UB[g - a]); SCHED();
                    if (e < 7) {
                        if (a == 1) RDU(UB[g - 1], g - 1);
                        if (q == 35) RDU(UB[7], 7);
                        if (g == 7) RDA(A[a], adr + 64 * (e + 1), a);
                        if (q == 11) UNEXT1();
                        if (q == 13) UNEXT2();
                        if (q == 15) UNEXT3();
                    }
                    if (ti >= 0) {
                        const int n = (q >= 3 && q <= 7) ? q - 3 : q == 10 ? 5 : q == 12 ? 6 : q == 14 ? 7 : -1;
                        if (q == 3) { if (ti == 0) WAIT_VM(1); else if (e == XE + 1) WAIT_VM(7); else WAIT_VM(0); }
                        if (n >= 0) CRD(tj, n & 7);
                        if (q == 18) CPIN(tj);
                        if (q == 18 || q == 23) asm volatile("s_add_u32 m0, %3, %4\n\ts_nop 0\n\tglobal_load_lds_dwordx4 %0, %2\n\tglobal_load_lds_dwordx4 %1, %2 offset:1024"
                                                             :: "v"(vB0), "v"(vB1), "s"(q == 18 ? sb_ : sc_), "s"(cst0), "i"(q == 18 ? 0 : 4096) : "memory", "scc");
                        if (q == 21 || q == 25) asm volatile("global_load_lds_dwordx4 %0, %2 offset:2048\n\tglobal_load_lds_dwordx4 %1, %2 offset:3072"
                                                             :: "v"(vB0), "v"(vB1), "s"(q == 21 ? sb_ : sc_) : "memory");
                    }
                    if (e == 0 && q >= 3 && q <= 7) gate_out(q - 3);
                    if (e == 0 && q >= 10 && q <= 18 && !(q & 1)) gate_out(5 + (q - 10) / 2);
                    if (e == 0 && q == 4) CTILE(wid, 0, cst0);
                    if (e == XE && q >= 23 && q <= 35 && (q & 1)) {
                        const int i = (q - 23) / 2;
                        const __amdgpu_buffer_rsrc_t xs = __builtin_amdgcn_make_buffer_rsrc((void*)x, 0, SEQ * DM * 2, 0x00020000);
                        const __amdgpu_buffer_rsrc_t ks = __builtin_amdgcn_make_buffer_rsrc((void*)dskip, 0, DM * 2, 0x00020000);
                        if (i < 4) asm volatile("buffer_load_dwordx2 %0, %1, %2, %3 offen offset:%4" : "=&v"(xx[(i >> 1) & 1][i & 1]) : "v"(vxo), "s"(xs), "s"((TR((i >> 1) & 1) * DM + d_base) * 2), "i"((i & 1) * 2048) : "memory");
                        if (i == 4) asm volatile("buffer_load_dwordx2 %0, %1, %2, %3 offen" : "=&v"(dk2) : "v"(vdo), "s"(ks), "s"(d_base * 2) : "memory");
                        if (i >= 5) asm volatile("buffer_load_dwordx4 %0, %1, %2, %3 offen sc1" : "=&v"(gv[(i - 5) & 1]) : "v"(vgo), "s"(gs), "s"(GSO((i - 5) & 1)) : "memory");
                    }
                    if (e == XE && q == 16) { vxo = (unsigned)(q4 * 32 * DM * 2 + l15 * 8); vdo = (unsigned)(l15 * 8); asm volatile("" : "+v"(vxo), "+v"(vdo)); }
                    if (e == XE && q == 17) { vgo = GV(q4); asm volatile("" : "+v"(vgo)); }
                    if (e == 6 && q == 30) hpv = __hip_atomic_load(hfp, RLX_AGENT);
                    SCHED();
                }
            };
            unroll<8>(step);
            WAIT_VM(0);
            XPIN();
        }
        PAD8(cv[0], cv[1], cv[2], cv[3]); PIN4(cv[4], cv[5], cv[6], cv[7]);
        STAMPT(dbg, 11, 384);
        const unsigned vh = (unsigned)((lane ^ 2) * 16);
        if (!__all(hpv == tag)) HWAIT();
        asm volatile("" ::: "memory");
#pragma unroll
        for (int f = 0; f < DEPTH; ++f) LOADF(f);
        STAMPT(dbg, 25, 384);
        {
            const __amdgpu_buffer_rsrc_t yrs = __builtin_amdgcn_make_buffer_rsrc(y2T, 0, DM * SEQ * 2, 0x00020000);
            const unsigned yo = (unsigned)(d * 4096 + (q4 & 1) * 512 + (l15 >> 3) * 256 + (q4 >> 1) * 128 + (l15 & 7) * 16);
#pragma unroll
            for (int g = 0; g < 8; g += 2) {
                const auto r0 = __builtin_amdgcn_permlane16_swap(pkbf(cv[g][0], cv[g][1]), pkbf(cv[g + 1][0], cv[g + 1][1]), false, false);
                const auto r1 = __builtin_amdgcn_permlane16_swap(pkbf(cv[g][2], cv[g][3]), pkbf(cv[g + 1][2], cv[g + 1][3]), false, false);
                __builtin_amdgcn_raw_buffer_store_b128((u32x4){r0[0], r1[0], r0[1], r1[1]}, yrs, yo + (unsigned)(g * 512), 0, WT);
            }
        }
        asm volatile("" ::: "memory");
        STAMPT(dbg, 15, 384);
        __builtin_amdgcn_s_setprio(PRIO2_L);
    }
    {
        const int lane = fresh_lane(), l15 = lane & 15, q4 = lane >> 4;
        const int ring = wid >> 2;
        const unsigned va = (unsigned)((lane ^ (ring * 2)) * 16), vh = va;
        const unsigned tag = lds_word(smem, TAG_OFF);
        const int cb0 = ring ? KC_OFF : GA_OFF, cst = ring ? CV_W : 8192;
        f32x4 ac[2][4];
        unsigned ca[2], fa[2], fl = 0, fv = 0;
#define CHAIN(u) do { const int c_ = (wid - ((u) >> 1)) & 3; ca[(u) & 1] = va + (unsigned)(cb0 + c_ * cst + ((u) & 1) * 4096); \
                      fa[(u) & 1] = (unsigned)(RF_OFF + ring * 32 + ((u) & 1) * 16 + c_ * 4); asm volatile("" : "+v"(ca[(u) & 1]), "+v"(fa[(u) & 1])); } while (0)
#define RDIN(dst, a, f_) asm volatile("ds_read_b32 %0, %5\n\tds_read_b128 %1, %6\n\tds_read_b128 %2, %6 offset:1024\n\tds_read_b128 %3, %6 offset:2048\n\tds_read_b128 %4, %6 offset:3072" \
                     : "=&v"(fl), "=&v"(dst[0]), "=&v"(dst[1]), "=&v"(dst[2]), "=&v"(dst[3]) : "v"(f_), "v"(a) : "memory")
#define LWAIT(dst) asm volatile("s_waitcnt lgkmcnt(0)" : "+v"(fl), "+v"(dst[0]), "+v"(dst[1]), "+v"(dst[2]), "+v"(dst[3]) :: "memory")
        CHAIN(0); CHAIN(1);
        WAIT_LGKM0();
        CPIN(0); CPIN(1); CPIN(2);
        CREAD(3);
        int tr[2] = {TR(0), TR(1)};
        unsigned gw[2][2];
        {
            unsigned spins = 0;
            while (__builtin_expect(!__all((gv[0].y == tag) & (gv[0].w == tag) & (gv[1].y == tag) & (gv[1].w == tag)), 0)) {
                if (++spins > SPIN_MAX) break;
#pragma unroll
                for (int r = 0; r < 2; ++r) asm volatile("s_nop 4\n\tbuffer_load_dwordx4 %0, %1, %2, %3 offen sc1\n\ts_waitcnt vmcnt(0)" : "=&v"(gv[r]) : "v"(GV(q4)), "s"(gs), "s"(__builtin_amdgcn_readfirstlane(GSO(r))) : "memory");
            }
            gw[0][0] = gv[0].x; gw[0][1] = gv[0].z; gw[1][0] = gv[1].x; gw[1][1] = gv[1].z;
            PIN4(gw[0][0], gw[0][1], gw[1][0], gw[1][1]);
        }
        unsigned cpv = 0, yy[2][4];
        f32x2 zz[2][4];
        unsigned exo = 0, exi = 0, efo = 0, efi = 0;
        auto send = [&](int r) __attribute__((always_inline)) {
            const unsigned one = 1u;
#pragma unroll
            for (int n = 0; n < 4; ++n) { const f32x2 v = {ac[r][n][2], ac[r][n][3]}; asm volatile("ds_write_b64 %0, %1 offset:%2" :: "v"(exo), "v"(v), "i"(r * 4096 + n * 512) : "memory"); }
            asm volatile("ds_write_b32 %0, %1 offset:%2" :: "v"(efo), "v"(one), "i"(r * 4) : "memory");
        };
        STAMPT(dbg, 5, 0);
        auto body = [&](auto F) __attribute__((always_inline)) {
            constexpr int f = decltype(F)::value;
            constexpr int u = f >> 3, k = f & 7;
            f32x4* an = ac[u & 1];
            f32x4* ao = ac[(u + 1) & 1];
#pragma unroll
            for (int n = 0; n < 4; ++n) {
                if (k == 0 && u < 2) MF0(an[n], af[f % DEPTH], cf_[k][n]); else MF(an[n], af[f % DEPTH], cf_[k][n]);
                SCHED();
                const int g = k * 4 + n;
                if (u >= 1 && u < 7) {
                    if (g == 1) { fv = ((u - 1) >> 1) + 1; asm volatile("" : "+v"(fv)); }
                    if (g == 4 || g == 6 || g == 8 || g == 10) asm volatile("ds_write_b128 %0, %1 offset:%2" :: "v"(ca[(u + 1) & 1]), "v"(ao[(g - 4) / 2]), "i"((g - 4) * 512) : "memory");
                    if (g == 12) asm volatile("ds_write_b32 %0, %1" :: "v"(fa[(u + 1) & 1]), "v"(fv) : "memory");
                    if (g == 22) CHAIN(u + 1);
                    if (g == 24) asm volatile("ds_read_b32 %0, %1" : "=&v"(fl) : "v"(fa[(u + 1) & 1]) : "memory");
                    if (g > 24 && g <= 28) asm volatile("ds_read_b128 %0, %1 offset:%2" : "=&v"(ao[g - 25]) : "v"(ca[(u + 1) & 1]), "i"((g - 25) * 1024) : "memory");
                    if (g == 31) {
                        LWAIT(ao);
                        if (__builtin_expect(__builtin_amdgcn_readfirstlane(fl) != (unsigned)((u + 1) >> 1), 0)) {
                            unsigned spins = 0;
#pragma unroll 1
                            do {
                                __builtin_amdgcn_s_sleep(1);
                                RDIN(ao, ca[(u + 1) & 1], fa[(u + 1) & 1]); LWAIT(ao);
                                if (++spins > SPIN_MAX * 16u) break;
                            } while (__builtin_amdgcn_readfirstlane(fl) != (unsigned)((u + 1) >> 1));
                        }
                    }
                }
                if (u == 7 && g == 6) send(0);
                if (f >= ZF && f < ZF + 4) {
                    constexpr int r = ((f - ZF) >> 1) & 1, j = (f - ZF) & 1;
                    const unsigned xw = xx[r][j][n >> 1], dw = dk2[n >> 1];
                    const float xf = (n & 1) ? hi16(xw) : lo16(xw), df = (n & 1) ? hi16(dw) : lo16(dw), yf = j ? hi16(yy[r][n]) : lo16(yy[r][n]);
                    zz[r][n][j] = xf * fmaf(hi16(gw[r][j]), yf, lo16(gw[r][j]) * df);
                    asm volatile("" : "+v"(zz[r][n]));
                }
                if (f == ZF + 4) {
                    zz[0][n] = (f32x2){fmaf(lo16(gw[0][0]), ac[0][n][0], zz[0][n][0]), fmaf(lo16(gw[0][1]), ac[0][n][1], zz[0][n][1])};
                    asm volatile("" : "+v"(zz[0][n]));
                }
                SCHED();
            }
            if (f + DEPTH < 64) LOADF(f + DEPTH);
            if (f == DEPTH + 1 && ring) {
                WAIT_VM(6);
                if (lane == 0) __hip_atomic_store((gu32*)(ws + WS_CFLAG) + cb * 32 + (wid & 3), tag, RLX_AGENT);
                SCHED();
            }
            if (f == YPOLL && ring) { const int l15 = fresh_lane() & 15; cpv = __hip_atomic_load((gu32*)(ws + WS_CFLAG) + (nb * 16 + (wid & 3) * 4 + (l15 >> 2)) * 32 + (l15 & 3), RLX_AGENT); SCHED(); }
            if (f == YPOLL + DEPTH && ring) {
                const int ln = fresh_lane(), l15 = ln & 15, q4 = ln >> 4;
                if (__builtin_expect(!__all(cpv == tag), 0)) {
                    unsigned spins = 0;
                    while (!__all(__hip_atomic_load((gu32*)(ws + WS_CFLAG) + (nb * 16 + (wid & 3) * 4 + (l15 >> 2)) * 32 + (l15 & 3), RLX_AGENT) == tag)) {
                        if (++spins > SPIN_MAX) break;
                        __builtin_amdgcn_s_sleep(1);
                    }
                }
                const unsigned lx = lbase + LA_OFF + (wid & 3) * 8192;
#pragma unroll
                for (int i = 0; i < 4; ++i) {
                    const unsigned vo = (unsigned)((d_base + (wid & 3) * 16 + i * 4 + q4) * 4096 + tb * 256 + ((l15 ^ ((wid & 3) * 4 + i)) * 16));
                    GLDS_SC1(y2T, vo, lx, i * 1024);
                }
                SCHED();
            }
            if (f == YPOLL + 2 * DEPTH + 1 && ring) {
                WAIT_VM(6);
                SETW(YFLAG_OFF, wid & 3, 1u);
                SCHED();
            }
            if (f == 55) {
                const int ln = fresh_lane(), c_ = (wid - 3) & 3;
                const int eg = GA_OFF + c_ * 8192, el = KC_OFF + c_ * CV_W;
                exo = (unsigned)((ring ? el : eg) + ln * 8);
                exi = (unsigned)((ring ? eg : el) + ln * 8);
                efo = (unsigned)(EF_OFF + wid * 8); efi = (unsigned)(EF_OFF + (wid ^ 4) * 8);
                PIN4(exo, exi, efo, efi);
                SCHED();
            }
            if (f == ZF - 2) {
                const int ln = fresh_lane(), l15 = ln & 15, q4 = ln >> 4;
                wait_lds4(smem, YFLAG_OFF);
#pragma unroll
                for (int r = 0; r < 2; ++r) {
                    const int jb = JB(6 + r);
                    const int ya = LA_OFF + (l15 >> 2) * 8192 + (l15 & 3) * 1024 + (((((jb >> 1) & 1) * 8 + (jb >> 2) + q4 * 2) ^ l15) * 16) + (jb & 1) * 8 + ring * 4;
#pragma unroll
                    for (int e = 0; e < 4; ++e) yy[r][e] = *(const unsigned*)(smem + ya + e * 256);
                }
                SCHED();
            }
        };
        unroll<64>(body);
        PAD8(ac[1][0], ac[1][1], ac[1][2], ac[1][3]);
        send(1);
        STAMPT(dbg, 6, 0);
        const __amdgpu_buffer_rsrc_t ors = __builtin_amdgcn_make_buffer_rsrc(out, 0, SEQ * DM * 4, 0x00020000);
        const unsigned ov = (unsigned)((lane >> 4) * (32 * DM * 4) + (lane & 15) * 16);
#pragma unroll
        for (int r = 0; r < 2; ++r) {
            f32x2 in[4];
#define EXRD() asm volatile("ds_read_b32 %0, %5 offset:%7\n\tds_read_b64 %1, %6 offset:%8\n\tds_read_b64 %2, %6 offset:%9\n\tds_read_b64 %3, %6 offset:%10\n\tds_read_b64 %4, %6 offset:%11" \
                             : "=&v"(fl), "=&v"(in[0]), "=&v"(in[1]), "=&v"(in[2]), "=&v"(in[3]) \
                             : "v"(efi), "v"(exi), "i"(r * 4), "i"(r * 4096), "i"(r * 4096 + 512), "i"(r * 4096 + 1024), "i"(r * 4096 + 1536) : "memory")
            EXRD();
            if (r == 0) {
#pragma unroll
                for (int n = 0; n < 4; ++n) {
                    zz[1][n] = (f32x2){fmaf(lo16(gw[1][0]), ac[1][n][0], zz[1][n][0]), fmaf(lo16(gw[1][1]), ac[1][n][1], zz[1][n][1])};
                    asm volatile("" : "+v"(zz[1][n]));
                }
            }
            LWAIT(in);
            if (__builtin_expect(__builtin_amdgcn_readfirstlane(fl) == 0u, 0)) {
                unsigned spins = 0;
#pragma unroll 1
                do {
                    EXRD(); LWAIT(in);
                    if (++spins > SPIN_MAX * 16u) break;
                } while (__builtin_amdgcn_readfirstlane(fl) == 0u);
            }
            f32x4 o[2];
#pragma unroll
            for (int j = 0; j < 2; ++j)
#pragma unroll
                for (int e = 0; e < 4; ++e) o[j][e] = fmaf(lo16(gw[r][j]), in[e][j], zz[r][e][j]);
#pragma unroll
            for (int j = 0; j < 2; ++j)
                __builtin_amdgcn_raw_buffer_store_b128(__builtin_bit_cast(u32x4, o[j]), ors, ov + (unsigned)(((tr[r] + j) * DM + d_base) * 4), 0, WT);
        }
        STAMPT(dbg, 7, 0);
        STAMPT(dbg, 16, 384);
        if (tb == 15 && nb == 0 && wid == 0 && lane == 0) __hip_atomic_store((gu32*)(ws + WS_EPOCH), tag, RLX_AGENT);
    }
}

extern "C" void kernel_launch(void* const* d_in, const int* in_sizes, int n_in,
                              void* d_out, int out_size, void* d_ws, size_t ws_size,
                              hipStream_t stream) {
    const u16* x = (const u16*)d_in[0];
    const u16* lre = (const u16*)d_in[1];
    const u16* lim = (const u16*)d_in[2];
    const u16* Bre = (const u16*)d_in[3];
    const u16* Bim = (const u16*)d_in[4];
    const u16* Cre = (const u16*)d_in[5];
    const u16* Cim = (const u16*)d_in[6];
    const u16* logdt = (const u16*)d_in[7];
    const u16* dskip = (const u16*)d_in[8];
    const u16* kin = (const u16*)d_in[10];
    const u16* vin = (const u16*)d_in[11];
    const u16* shiftk = (const u16*)d_in[12];
    const u16* ssmk = (const u16*)d_in[13];
    const u16* ssmD = (const u16*)d_in[14];
    const u16* gatew = (const u16*)d_in[15];
    float* out = (float*)d_out;

    char* ws = (char*)d_ws;

    hipFuncSetAttribute((const void*)k1_kernel, hipFuncAttributeMaxDynamicSharedMemorySize, K1_LDS);

    k1_kernel<<<256, 512, K1_LDS, stream>>>(x, Bre, Bim, kin, vin, shiftk, gatew, ws, lre, lim, logdt, ssmk, ssmD, Cre, Cim, dskip, out);
}
#if defined(__HIP_DEVICE_COMPILE__)
#pragma clang attribute pop
#endif
```

```cpp
#if defined(__HIP_DEVICE_COMPILE__)
#pragma clang attribute push(__attribute__((target("no-packed-fp32-ops"))), apply_to = function)
#endif
#include <hip/hip_runtime.h>
#include <hip/hip_bf16.h>
#include <stdint.h>

#define TIDX ((int)__builtin_amdgcn_workitem_id_x())
#define BIDX ((int)__builtin_amdgcn_workgroup_id_x())
typedef unsigned short u16;
typedef __attribute__((ext_vector_type(8))) short bf16x8;
typedef __attribute__((ext_vector_type(4))) float f32x4;
typedef __attribute__((ext_vector_type(2))) float f32x2;
typedef __attribute__((ext_vector_type(2))) __bf16 bf16x2;
typedef __attribute__((ext_vector_type(4))) unsigned u32x4;
typedef __attribute__((ext_vector_type(2))) unsigned u32x2;
typedef __attribute__((address_space(1))) unsigned long long gu64;
typedef __attribute__((address_space(1))) unsigned gu32;
typedef __attribute__((address_space(3))) void lds_void;
typedef __attribute__((address_space(3))) unsigned lds_u32;
typedef __attribute__((ext_vector_type(4))) short s16x4;
typedef __attribute__((address_space(3))) s16x4 lds_s16x4;
#define TRD(a) __builtin_amdgcn_ds_read_tr16_b64_v4i16((lds_s16x4*)(size_t)(a))

#define DBGON 0
#define STAMPT(buf, k, t) do { } while (0)
#define CODE_LINES 263
#define SEQ 2048
#define DM 1024
#define SPIN_MAX (1u << 18)
#define PRIO_G 2
#define PSLEEP 4
#define PRIO_L 3
#define WT 16

__device__ __forceinline__ float bf2f(u16 v) { return __uint_as_float(((unsigned)v) << 16); }
__device__ __forceinline__ u16 f2bf(float f) { __bf16 b = (__bf16)f; return __builtin_bit_cast(u16, b); }
__device__ __forceinline__ float rbf(float f) { return bf2f(f2bf(f)); }
__device__ __forceinline__ unsigned pkbf(float lo, float hi) { return __builtin_bit_cast(unsigned, __builtin_convertvector((f32x2){lo, hi}, bf16x2)); }
__device__ __forceinline__ float lo16(unsigned u) { return __uint_as_float(u << 16); }
__device__ __forceinline__ float hi16(unsigned u) { return __uint_as_float(u & 0xffff0000u); }

struct cf { float r, i; };
__device__ __forceinline__ cf cmul(cf a, cf b) { return {a.r * b.r - a.i * b.i, a.r * b.i + a.i * b.r}; }
__device__ __forceinline__ cf cfma(cf a, cf b, cf c) {
    return {fmaf(a.r, b.r, fmaf(-a.i, b.i, c.r)), fmaf(a.r, b.i, fmaf(a.i, b.r, c.i))};
}
__device__ __forceinline__ f32x4 splat4(float v) { return (f32x4){v, v, v, v}; }
__device__ __forceinline__ f32x4 fma4(f32x4 a, f32x4 b, f32x4 c) { return __builtin_elementwise_fma(a, b, c); }
__device__ __forceinline__ f32x4 mul4(f32x4 a, f32x4 b) { return a * b; }

__device__ __forceinline__ float red32(float a, float b) {
    const auto r = __builtin_amdgcn_permlane32_swap(__float_as_uint(a), __float_as_uint(b), false, false);
    return __uint_as_float(r[0]) + __uint_as_float(r[1]);
}
__device__ __forceinline__ float red16(float x) {
    const auto r = __builtin_amdgcn_permlane16_swap(__float_as_uint(x), __float_as_uint(x), false, false);
    return __uint_as_float(r[0]) + __uint_as_float(r[1]);
}
__device__ __forceinline__ int fresh_lane() {
    int l;
    asm volatile("v_mbcnt_lo_u32_b32 %0, -1, 0\n\tv_mbcnt_hi_u32_b32 %0, -1, %0" : "=v"(l));
    return l;
}
#define BAR() __builtin_amdgcn_s_barrier()
#define WAIT_VM(n) asm volatile("s_waitcnt vmcnt(" #n ")" ::: "memory")
#define WAIT_LGKM0() __builtin_amdgcn_s_waitcnt(0xC07F)
#define SCHED() __builtin_amdgcn_sched_barrier(0)
#define MF(c, a, b) asm volatile("v_mfma_f32_16x16x32_bf16 %0, %1, %2, %0" : "+v"(c) : "v"(a), "v"(b))
#define PAD8(c0, c1, c2, c3) asm volatile("s_nop 7" : "+v"(c0), "+v"(c1), "+v"(c2), "+v"(c3))
#define PIN4(c0, c1, c2, c3) asm volatile("" : "+v"(c0), "+v"(c1), "+v"(c2), "+v"(c3))
#define GLDS_S(sbase, voff, sl, IMM) \
    asm volatile("s_add_u32 m0, %2, %3\n\ts_nop 0\n\tglobal_load_lds_dwordx4 %0, %1" \
                 :: "v"(voff), "s"(sbase), "s"(sl), "i"(IMM) : "memory", "scc")
#define GLDS_K(sbase, voff, sl, IMM) \
    asm volatile("s_add_u32 m0, %2, %3\n\ts_nop 0\n\tglobal_load_lds_dwordx4 %0, %1 nt" \
                 :: "v"(voff), "s"(sbase), "s"(sl), "i"(IMM) : "memory", "scc")
#define RLX_AGENT __ATOMIC_RELAXED, __HIP_MEMORY_SCOPE_AGENT

#define UNITS_A 0x0756075607560560ull
#define UNITS_B 0x1432143214321432ull
#define RA(t) ((int)((UNITS_A >> (4 * (t))) & 15) * 16384)
#define RB(t) ((int)((UNITS_B >> (4 * (t))) & 15) * 16384)
#define LA_OFF 0
#define GA_OFF 32768
#define CV_COPY 4160
#define CV_W (4 * CV_COPY)
#define KC_OFF 65536
#define ST_OFF 114688
#define ST_V 8832
#define ST_SZ (ST_V + 8 * 1056)
#define U_OFF (KC_OFF + 16 * CV_COPY)
#define HT_OFF (U_OFF + 256 + 16384)
#define GW_OFF (HT_OFF + 8192)
#define TAG_OFF (GW_OFF + 2048)
#define UFLAG_OFF (TAG_OFF + 16)
#define G1D_OFF (UFLAG_OFF + 16)
#define MISC_OFF (G1D_OFF + 16)
#define YFLAG_OFF (MISC_OFF + 16)
#define RF_OFF (YFLAG_OFF + 16)
#define EF_OFF (RF_OFF + 64)
#define COK_OFF (EF_OFF + 64)
#define NFLAGW 52
#define DUMMY_OFF (COK_OFF + 16)
#define K1_LDS (DUMMY_OFF + 256)
#define WS_GRAN 0
#define WS_KVFLAG (256u << 10)
#define WS_EPOCH (WS_KVFLAG + 65536)
#define WS_CFLAG (WS_EPOCH + 4096)
#define WS_HFLAG (384u << 10)
#define WS_GATE (512u << 10)

#define CODE_TOUCH(vl) asm volatile("s_getpc_b64 s[98:99]\n\ts_add_u32 s98, s98, k1_kernel@rel32@lo+4\n\ts_addc_u32 s99, s99, k1_kernel@rel32@hi+12\n\t" \
                                    "s_mov_b32 m0, %1\n\ts_nop 0\n\tglobal_load_lds_dword %0, s[98:99]" :: "v"((unsigned)(vl) * 128u), "i"(DUMMY_OFF) : "s98", "s99", "scc", "memory")
#define RD128(a, off) (*(const __attribute__((address_space(3))) bf16x8*)(size_t)(unsigned)((a) + (off)))
#define RDA(dst, A, F) do { const s16x4 lo_ = TRD((A) + 512 * (F) + 8); SCHED(); const s16x4 hi_ = TRD((A) + 512 * (F)); SCHED(); \
                            dst = __builtin_shufflevector(lo_, hi_, 0, 1, 2, 3, 4, 5, 6, 7); } while (0)
#define TOUCH(sbase, voff) asm volatile("s_mov_b32 m0, %2\n\ts_nop 0\n\tglobal_load_lds_dword %0, %1" :: "v"(voff), "s"(sbase), "i"(DUMMY_OFF) : "memory")
#define FLAGD(off) do { unsigned long long pc_; asm volatile("s_getpc_b64 %0\n\ts_mov_b32 m0, %2\n\ts_mov_b64 exec, 1\n\tglobal_load_lds_dword %1, %0\n\ts_mov_b64 exec, -1" \
                                                             : "=&s"(pc_) : "v"(0u), "s"(__builtin_amdgcn_readfirstlane(off)) : "memory"); } while (0)
#define GLDS_SC1(sbase, voff, sl, IMM) \
    asm volatile("s_add_u32 m0, %2, %3\n\ts_nop 0\n\tglobal_load_lds_dwordx4 %0, %1 sc1" \
                 :: "v"(voff), "s"(sbase), "s"(sl), "i"(IMM) : "memory", "scc")
__device__ __forceinline__ unsigned mv(unsigned v) { unsigned r; asm("v_mov_b32 %0, %1" : "=v"(r) : "v"(v)); return r; }
__device__ __forceinline__ unsigned al(unsigned hi, unsigned lo) { unsigned r; asm volatile("v_alignbit_b32 %0, %1, %2, 16" : "=v"(r) : "v"(hi), "v"(lo)); return r; }
__device__ __forceinline__ u32x2 pred2(const u32x4& a, unsigned f0, unsigned f1) {
    return (u32x2){(unsigned)__builtin_amdgcn_update_dpp((int)f0, (int)a.z, 0x138, 0xf, 0xf, false), (unsigned)__builtin_amdgcn_update_dpp((int)f1, (int)a.w, 0x138, 0xf, 0xf, false)};
}
#define RL63(v) ((unsigned)__builtin_amdgcn_readlane((int)(v), 63))
__device__ __forceinline__ void wait_lds4(char* smem, int off) {
    const unsigned a = (unsigned)(size_t)(lds_void*)smem + off;
    unsigned spins = 0;
    for (;;) {
        u32x4 f;
        asm volatile("ds_read_b128 %0, %1\n\ts_waitcnt lgkmcnt(0)" : "=&v"(f) : "v"(a) : "memory");
        if (__builtin_amdgcn_readfirstlane((f.x != 0u) & (f.y != 0u) & (f.z != 0u) & (f.w != 0u))) break;
        if (++spins > SPIN_MAX * 16u) break;
        __builtin_amdgcn_s_sleep(1);
    }
}
__device__ __forceinline__ unsigned lds_word(char* smem, int off) {
    const unsigned a = (unsigned)(size_t)(lds_void*)smem + off;
    unsigned f;
    asm volatile("ds_read_b32 %0, %1\n\ts_waitcnt lgkmcnt(0)" : "=&v"(f) : "v"(a) : "memory");
    return __builtin_amdgcn_readfirstlane(f);
}
__device__ __forceinline__ void wait_lds1(char* smem, int off) {
    unsigned spins = 0;
    while (lds_word(smem, off) == 0u) {
        if (++spins > SPIN_MAX * 16u) break;
        __builtin_amdgcn_s_sleep(1);
    }
}
#define SETW(off, i, v) __hip_atomic_store((lds_u32*)(smem + (off)) + (i), (v), __ATOMIC_RELAXED, __HIP_MEMORY_SCOPE_WORKGROUP)

#define PRIO_CV 1
#define PRIO_KV 3
#define CS0 2
#define CS1 4
#define CS2 6
#define XE 5
#define YPOLL 32
#define ZF 59
template <int I> struct IC { static constexpr int value = I; };
template <int N, int I = 0, class F> __device__ __forceinline__ void unroll(F& f) { if constexpr (I < N) { f(IC<I>{}); unroll<N, I + 1>(f); } }
#define MF0(c, a, b) asm volatile("v_mfma_f32_16x16x32_bf16 %0, %1, %2, 0" : "=&v"(c) : "v"(a), "v"(b))
#define DEPTH 6
#define PRIO2_G 0
#define PRIO2_L 1
__device__ __forceinline__ cf pick4(int q, cf a1, cf a2, cf a3) {
    return {q == 0 ? 1.f : q == 1 ? a1.r : q == 2 ? a2.r : a3.r, q == 0 ? 0.f : q == 1 ? a1.i : q == 2 ? a2.i : a3.i};
}
__device__ __forceinline__ void idle_mfma(unsigned a, unsigned b, int n) {
    u32x4 p = {a * 2654435761u, b * 40503u, a * 2246822519u, b * 3266489917u};
    p = (p & 0xbfffbfffu) | 0x3c003c00u;
    u32x4 q = p.yzwx ^ 0x01230456u;
    f32x4 w;
    asm volatile("s_nop 1" : "+v"(p), "+v"(q));
    MF0(w, p, q);
#pragma unroll 1
    for (int i = 0; i < n; ++i) { MF(w, q, p); MF(w, p, p); MF(w, q, q); MF(w, p, q); }
    asm volatile("s_nop 7" : "+v"(w));
}
#define PINC(c) asm volatile("" : "+v"((c).r), "+v"((c).i))

extern "C" __global__ __launch_bounds__(512, 2) void k1_kernel(
        const u16* __restrict__ x, const u16* __restrict__ Bre, const u16* __restrict__ Bim,
        const u16* __restrict__ kin, const u16* __restrict__ vin, const u16* __restrict__ shiftk, const u16* __restrict__ gatew, char* ws,
        const u16* __restrict__ lre, const u16* __restrict__ lim, const u16* __restrict__ logdt,
        const u16* __restrict__ ssmk, const u16* __restrict__ ssmD,
        const u16* __restrict__ Cre, const u16* __restrict__ Cim, const u16* __restrict__ dskip, float* __restrict__ out) {
    extern __shared__ __attribute__((aligned(16))) char smem[];
#define h ((u16*)(ws + (1u << 20)))
#define kvT ((u16*)(ws + (9u << 20)))
#define y2T ((u16*)(ws + (13u << 20)))
#define dbg ((unsigned long long*)(ws + (64u << 20)))
    const int tid = TIDX;
    if (tid < 40 && tid + ((BIDX >> 3) & 7) * 40 < CODE_LINES) CODE_TOUCH(tid + ((BIDX >> 3) & 7) * 40);
    const int lane = tid & 63;
    int wid = __builtin_amdgcn_readfirstlane(tid >> 6);
    const int b = BIDX;
    const int xcd = b & 7, idx = b >> 3;
    const int tb = ((xcd >> 1) ^ (xcd >> 2)) * 4 + (idx & 3), nb = (xcd & 1) * 8 + (idx >> 2);
    const int cb = xcd * 32 + idx;
    const int t_base = tb * 128, p_base = nb * 64, d_base = nb * 64;
    const int l15 = lane & 15, q4 = lane >> 4;
    unsigned stv = 0, sth = 0;
    STAMPT(dbg, 0, 0);
    const unsigned lbase = (unsigned)(size_t)(lds_void*)smem;
    const int rot1 = (xcd >> 1) * 4 + (xcd & 1) * 8 + (idx >> 2) + (idx & 1);
    unsigned voA[4] = {0u, 0u, 0u, 0u}, voB[4] = {0u, 0u, 0u, 0u}, lw0 = 0;
    if (wid == 4 || wid == 5) {
        __builtin_amdgcn_s_setprio(PRIO_L);
        const int lh = wid - 4;
        const unsigned ch = (unsigned)(((lane & 7) ^ (lane >> 4)) * 16);
        voA[0] = (unsigned)(t_base + lh * 16 + (lane >> 5) * 32 + ((lane >> 3) & 3)) * 2048u + ch;
        voA[1] = (voA[0] ^ 64u) + (131072u - 1024u);
        voA[2] = voA[0] + 6144u; voA[3] = voA[1] + 6144u;
        voB[0] = (unsigned)(p_base + lh * 32 + (lane >> 3)) * 2048u + ch;
        voB[1] = (voB[0] ^ 64u) + (16384u - 1024u);
        voB[2] = voB[0] + 30720u; voB[3] = voB[1] + 30720u;
        PIN4(voA[0], voA[1], voA[2], voA[3]); PIN4(voB[0], voB[1], voB[2], voB[3]);
#define GLDS4(sbase, v0, v1, v2, v3, sl, IMM) \
            asm volatile("s_add_u32 m0, %5, %6\n\ts_nop 0\n\tglobal_load_lds_dwordx4 %0, %4\n\tglobal_load_lds_dwordx4 %1, %4 offset:1024\n\t" \
                         "global_load_lds_dwordx4 %2, %4 offset:2048\n\tglobal_load_lds_dwordx4 %3, %4 offset:3072" \
                         :: "v"(v0), "v"(v1), "v"(v2), "v"(v3), "s"(sbase), "s"(sl), "i"(IMM) : "memory", "scc")
#define STAGE_A(kt, sl) do { const char* sa_ = (const char*)x + (((kt) + rot1) & 15) * 128; \
            GLDS4(sa_, voA[0], voA[1], voA[2], voA[3], sl, 0); GLDS4(sa_ + 16384, voA[0], voA[1], voA[2], voA[3], sl, 4096); } while (0)
#define STAGE_B(kt, sl) do { const int kr_ = (((kt) + rot1) & 15) * 128; const char* sb_ = (const char*)Bre + kr_; const char* sc_ = (const char*)Bim + kr_; \
            GLDS4(sb_, voB[0], voB[1], voB[2], voB[3], sl, 0); GLDS4(sc_, voB[0], voB[1], voB[2], voB[3], sl, 4096); } while (0)
        lw0 = lbase + lh * 8192;
        STAGE_A(0, lw0 + RA(0)); STAGE_B(0, lw0 + RB(0));
        idle_mfma(voA[0], voB[1], 12);
        STAGE_A(1, lw0 + RA(1)); STAGE_B(1, lw0 + RB(1));
    }
    const int sblk = b >> 3, dblk = b & 7;
    u32x2 tw[4];
    unsigned epoch;
    asm volatile("" : "=v"(tw[0]), "=v"(tw[1]), "=v"(tw[2]), "=v"(tw[3]), "=v"(epoch));
    if (wid >= 6) {
        __builtin_amdgcn_s_setprio(PRIO_L);
        const int vw = wid - 6;
        const int dch = dblk * 128 + vw * 64 + l15;
        const unsigned lk = lbase + ST_OFF + vw * ST_SZ;
        const unsigned vk = (unsigned)((lane >> 3) * DM * 2 + (((lane & 7) ^ ((lane >> 4) & 1)) * 16));
        const char* kb = (const char*)kin + (size_t)(sblk * 64 - 4) * (DM * 2) + dblk * 256 + vw * 128;
#define KLD(n) GLDS_K(kb + (n) * 8 * DM * 2, vk, lk, (n) * 1024 + ((n) >> 1) * 32)
#define VLD(n) GLDS_K(vb + (((n) >> 1) * 16 + ((n) & 1) * 2) * DM * 2, vv_, lk, ST_V + (n) * 1056)
        if (vw == 1) {
            const unsigned vg = (unsigned)((lane * 16 + rot1 * 128) & 2047), lg = lbase + GW_OFF;
            GLDS_S(gatew, vg, lg, 0); GLDS_S(gatew, vg ^ 1024u, lg, 1024);
        }
        asm volatile("global_load_dwordx2 %0, %5, %7\n\tglobal_load_dwordx2 %1, %5, %7 offset:128\n\tglobal_load_dwordx2 %2, %5, %7 offset:256\n\t"
                     "global_load_dwordx2 %3, %5, %7 offset:384\n\tglobal_load_dword %4, %6, %8 sc1"
                     : "+v"(tw[0]), "+v"(tw[1]), "+v"(tw[2]), "+v"(tw[3]), "+v"(epoch) : "v"((unsigned)(dch * 8)), "v"((unsigned)WS_EPOCH), "s"(shiftk), "s"(ws) : "memory");
        if (sblk == 0 && lane < 32) *(u32x4*)(smem + ST_OFF + vw * ST_SZ + lane * 16) = (u32x4){0u, 0u, 0u, 0u};
        if (sblk != 0 || lane >= 32) KLD(0);
        KLD(1); KLD(2); KLD(3); KLD(4); KLD(5); KLD(6); KLD(7);
        if (lane < 32) KLD(8);
        __builtin_amdgcn_s_setprio(0);
    }
    asm volatile("" : "+s"(wid));

    const int wf_ = (nb >> 3) ^ (xcd >> 2), tq = (xcd >> 1) & 1;
#define WSL(w) ((((w) >> 2) ^ wf_) * 4 + ((w) & 3))
#define COFF(w, ti) ((2 * WSL(w) + (((ti) >> 1) ^ tq)) * 128 + ((ti) & 1) * 4096)
    bf16x8 cf_[8][4];
    bf16x8 af[DEPTH];
    int cra, crb;
#define CT_SETUP() const unsigned vB0 = (unsigned)((d_base + (lane >> 3) * 4) * 2048 + (((lane & 7) ^ (lane >> 4)) * 16)), vB1 = (vB0 ^ 64u) + 65536u - 1024u
#define CTILE(w, ti, dst) do { const int co_ = COFF(w, ti); const unsigned ds_ = (dst); \
                               GLDS4((const char*)Cre + co_, vB0, vB1, vB0, vB1, ds_, 0); GLDS4((const char*)Cim + co_, vB0, vB1, vB0, vB1, ds_, 4096); } while (0)
#define CF(ti, n) cf_[4 * ((ti) >> 1) + ((n) >> 1)][2 * ((ti) & 1) + ((n) & 1)]
#define CRD(ti, n) CF(ti, n) = RD128((((n) & 4) ? crb : cra) ^ (((n) & 2) * 16), ((n) & 1) * 2048)
#define CREAD(ti) do { _Pragma("unroll") for (int n_ = 0; n_ < 8; ++n_) CRD(ti, n_); } while (0)
#define CPIN(ti) do { PIN4(CF(ti, 0), CF(ti, 1), CF(ti, 2), CF(ti, 3)); PIN4(CF(ti, 4), CF(ti, 5), CF(ti, 6), CF(ti, 7)); } while (0)
#define CRA(base) ((base) + (q4 >> 1) * 4096 + l15 * 128 + (((q4 & 1) ^ ((l15 >> 1) & 7)) * 16))
    const int rot2 = (idx & 16) >> 2;
#define JB(u) (((((u) ^ (rot2 >> 2)) & 1) << 2) | ((wid - ((u) >> 1) + rot2) & 3))
    const __amdgpu_buffer_rsrc_t hs = __builtin_amdgcn_make_buffer_rsrc((char*)h + (size_t)tb * 524288 + WSL(wid) * 8192, 0, 524288, 0x00020000);
#define LOADF(f) do { af[(f) % DEPTH] = __builtin_bit_cast(bf16x8, __builtin_amdgcn_raw_buffer_load_b128(hs, vh + ((f) & 3) * 1024, JB((f) >> 3) * 65536 + ((((f) >> 2) & 1) ^ tq) * 4096, 16)); SCHED(); } while (0)
#define HPOLL_SETUP() gu32* hfp = (gu32*)(ws + WS_HFLAG) + (tb * 16 + 2 * WSL(wid) + (lane & 1)) * 32
#define HWAIT() do { unsigned spins = 0; \
            while (!__all(__hip_atomic_load(hfp, RLX_AGENT) == tag)) { if (++spins > SPIN_MAX) break; __builtin_amdgcn_s_sleep(PSLEEP); } asm volatile("" ::: "memory"); } while (0)

    const __amdgpu_buffer_rsrc_t gs = __builtin_amdgcn_make_buffer_rsrc(ws + WS_GATE, 0, 32768, 0x00020000);
#define GV(q4) ((unsigned)(((q4) >> 1) * 128 + ((q4) & 1) * 32))
#define GSO(r) (tb * 2048 + JB(6 + (r)) * 256 + (wid >> 2) * 16)
#define TR(r) (t_base + JB(6 + (r)) * 4 + (wid >> 2) * 2)
    u32x2 xx[2][2], dk2;
    u32x4 gv[2];
#define XLOAD() do { \
        const __amdgpu_buffer_rsrc_t xs = __builtin_amdgcn_make_buffer_rsrc((void*)x, 0, SEQ * DM * 2, 0x00020000); \
        const __amdgpu_buffer_rsrc_t ks = __builtin_amdgcn_make_buffer_rsrc((void*)dskip, 0, DM * 2, 0x00020000); \
        dk2 = __builtin_amdgcn_raw_buffer_load_b64(ks, (unsigned)(l15 * 8), d_base * 2, 0); \
        _Pragma("unroll") for (int q = 0; q < 4; ++q) \
            xx[q >> 1][q & 1] = __builtin_amdgcn_raw_buffer_load_b64(xs, (unsigned)(q4 * 32 * DM * 2 + l15 * 8 + (q & 1) * DM * 2), (TR(q >> 1) * DM + d_base) * 2, 0); \
        _Pragma("unroll") for (int r = 0; r < 2; ++r) gv[r] = __builtin_amdgcn_raw_buffer_load_b128(gs, GV(q4), GSO(r), 16); \
        SCHED(); } while (0)
#define XPIN() do { PIN4(xx[0][0], xx[0][1], xx[1][0], xx[1][1]); asm volatile("" : "+v"(dk2), "+v"(gv[0]), "+v"(gv[1])); } while (0)

    if (wid < 4) {
        typedef __attribute__((ext_vector_type(8))) unsigned u32x8;
        typedef const __attribute__((address_space(4))) u32x8 cu8;
        const int po = (p_base + wid * 16) * 2;
        u32x8 s0 = *(cu8*)((size_t)logdt + po), s1 = *(cu8*)((size_t)lre + po), s2 = *(cu8*)((size_t)lim + po);
        SCHED();
        const int pq = p_base + wid * 16 + l15;
        unsigned goff[4], gmask[4];
#pragma unroll
        for (int i = 0; i < 4; ++i) {
            goff[i] = (unsigned)(((tb - 1 - q4 - 4 * i) * 1024 + pq) * 16);
            gmask[i] = (tb - 1 - q4 - 4 * i >= 0) ? ~0u : 0u;
            asm volatile("" : "+v"(goff[i]), "+v"(gmask[i]));
        }
        unsigned gpub = q4 == 0 ? (unsigned)((tb * 1024 + pq) * 16) : 0x80000000u;
        unsigned hp = (unsigned)(tb * 524288 + (nb * 4 + wid) * 1024 + lane * 16);
        unsigned wa = (unsigned)(HT_OFF + wid * 2048 + (q4 * 16 + (l15 ^ ((q4 >> 1) * 4))) * 8);
        unsigned ra0 = (unsigned)(HT_OFF + wid * 2048 + ((q4 >> 1) * 64 + (l15 & 3) * 16 + (((q4 & 1) * 8 + (l15 >> 2)) ^ ((l15 & 2) * 2))) * 8), ra1 = ra0 ^ 32u;
        cra = CRA(GA_OFF + wid * 8192); crb = cra ^ 64;
        unsigned vB0 = (unsigned)((d_base + (lane >> 3) * 4) * 2048 + (((lane & 7) ^ (lane >> 4)) * 16)), vB1 = (vB0 ^ 64u) + 65536u - 1024u;
        PIN4(gpub, vB0, hp, wa); PIN4(ra0, ra1, cra, crb); asm volatile("" : "+v"(vB1));
        f32x4 acc[8][2];
#pragma unroll
        for (int m = 0; m < 8; m += 2) {
#pragma unroll
            for (int c = 0; c < 4; ++c) acc[m + (c >> 1)][c & 1] = (f32x4){0.f, 0.f, 0.f, 0.f};
            PIN4(acc[m][0], acc[m][1], acc[m + 1][0], acc[m + 1][1]);
        }
        SCHED();
        cf L1, Lq3, cc, L32, cLq, Lp, L512;
        {
            unsigned w0 = 0, w1 = 0, w2 = 0;
#pragma unroll
            for (int i = 0; i < 8; ++i) {
                asm("v_writelane_b32 %0, %3, %6\n\tv_writelane_b32 %1, %4, %6\n\tv_writelane_b32 %2, %5, %6" : "+v"(w0), "+v"(w1), "+v"(w2) : "s"(s0[i]), "s"(s1[i]), "s"(s2[i]), "i"(2 * i));
            }
            asm("s_nop 1" : "+v"(w0), "+v"(w1), "+v"(w2));
            auto spread = [&](unsigned w) {
                const unsigned e = (unsigned)__builtin_amdgcn_update_dpp(0, (int)w, 0xa0, 0xf, 0xf, false);
                const unsigned a = __builtin_amdgcn_permlane16_swap(e, e, false, false)[0];
                const unsigned c = __builtin_amdgcn_permlane32_swap(a, a, false, false)[0];
                return (l15 & 1) ? hi16(c) : lo16(c);
            };
            const float ldt = spread(w0), re = spread(w1), im = spread(w2);
            if (wid == 3 && lane < NFLAGW) *(unsigned*)(smem + UFLAG_OFF + lane * 4) = 0u;
            const float dt = rbf(expf(ldt));
            const float ea = expf(dt * re);
            float sn, cs;
            sincosf(dt * im, &sn, &cs);
            L1 = {ea * cs, ea * sn};
            const float nr = L1.r - 1.0f, ni = L1.i, den = re * re + im * im;
            cc = {2.f * ((nr * re + ni * im) / den), 2.f * ((ni * re - nr * im) / den)};
            const cf T2 = cmul(L1, L1), T4 = cmul(T2, T2), L8 = cmul(T4, T4), L16 = cmul(L8, L8);
            L32 = cmul(L16, L16);
            const cf L64 = cmul(L32, L32), L96 = cmul(L64, L32), L128 = cmul(L64, L64), L256 = cmul(L128, L128), L384 = cmul(L256, L128);
            L512 = cmul(L256, L256);
            Lp = pick4(q4, L128, L256, L384);
            Lq3 = pick4(3 - q4, L32, L64, L96);
            cLq = cmul(cc, pick4(q4, L32, L64, L96));
            PINC(L1); PINC(Lq3); PINC(cc); PINC(L32); PINC(cLq); PINC(Lp); PINC(L512);
        }
        __builtin_amdgcn_s_setprio(PRIO_G);
        unsigned tagv;
        cf S;
        STAMPT(dbg, 22, 0);
        const int swz = (l15 >> 1) & 7;
        {
            const int a0 = RA(0) + l15 * 128 + ((q4 ^ swz) * 16);
            const int b0 = RB(0) + ((wid >> 1) * 64 + (wid & 1) * 16 + l15) * 128 + ((q4 ^ swz) * 16);
            WAIT_LGKM0();
            BAR();
            asm volatile("" ::: "memory");
            STAMPT(dbg, 19, 64);
            bf16x8 FA[8][2], FB[2][2][2];
            int pa[2] = {a0, a0 ^ 64}, pb[2] = {b0, b0 ^ 64};
            PIN4(pa[0], pa[1], pb[0], pb[1]);
#define LGW(n) __builtin_amdgcn_s_waitcnt(0xC07F | ((n) << 8))
#define RA_(j) FA[(j) >> 1][(j) & 1] = RD128(pa[(j) & 1], ((j) >> 1) * 2048)
#define RB_(bu, j) FB[bu][(j) >> 1][((j) ^ ((j) >> 1)) & 1] = RD128(pb[((j) ^ ((j) >> 1)) & 1], ((j) >> 1) * 4096)
#define RN_(bu, i) do { if ((i) == 0) RB_(bu, 0); else if ((i) < 3) RA_((i) - 1); else if ((i) < 6) RB_(bu, (i) - 2); else RA_((i) - 4); } while (0)
#define G1_H(H, bu, RD, FILL) do { \
            _Pragma("unroll") for (int i = 0; i < 16; ++i) { \
                const int m = (H) * 4 + (i >> 2), n = ((i >> 1) ^ m) & 1, ks = (i ^ (i >> 1)) & 1; \
                if ((H) == 0) { if (i == 0) LGW(6); if (i == 4) LGW(8); if (i == 8) LGW(10); if (i == 12) LGW(8); } \
                SCHED(); \
                MF(acc[m][n], FA[m][ks], FB[bu][n][ks]); SCHED(); \
                if ((H) == 0 && i < 8) RA_(8 + i); \
                if ((H) == 1 && (RD) && i < 12) RN_((bu) ^ 1, i); \
                FILL((H) * 16 + i); \
                SCHED(); \
            } } while (0)
#define NOF(g) do { } while (0)
#pragma unroll
            for (int j = 0; j < 12; ++j) { RN_(0, j); SCHED(); }
            unsigned long long ua_ = UNITS_A;
            int sb = RB(0), sa = RA(0), nx, na;
#define ADV(g) do { if ((g) == 8) { ua_ >>= 4; na = (int)((unsigned)ua_ & 15u) << 14; asm volatile("" : "+s"(na), "+s"(ua_)); } \
                    if ((g) == 9) { const int o_ = sa; sa = na; na -= o_; asm volatile("" : "+s"(na), "+s"(sa)); } \
                    if ((g) == 10) { pa[0] += na; pa[1] += na; asm volatile("" : "+v"(pa[0]), "+v"(pa[1])); } \
                    if ((g) == 11) { nx = (sb == 65536) ? -49152 : 16384; sb += nx; asm volatile("" : "+s"(nx), "+s"(sb)); } \
                    if ((g) == 13) { pb[0] += nx; pb[1] += nx; asm volatile("" : "+v"(pb[0]), "+v"(pb[1])); } } while (0)
#define G1_T(bu) do { \
                G1_H(0, bu, true, ADV); \
                WAIT_LGKM0(); \
                asm volatile("" ::: "memory"); \
                BAR();                     \
                asm volatile("" ::: "memory"); \
                G1_H(1, bu, true, NOF); } while (0)
#pragma unroll 1
            for (int t2 = 0; t2 < 7; ++t2) { G1_T(0); G1_T(1); }
            G1_T(0);
#define AGG(g) do { if ((g) == 8) { asm volatile("" ::: "memory"); SETW(G1D_OFF, wid, 1u); }        \
                    if ((g) == 9) tagv = *(volatile const lds_u32*)(size_t)(unsigned)TAG_OFF; \
                    if ((g) >= 7) { const int t = (g) - 6; \
                        if (t == 1) { asm volatile("s_nop 1" : "+v"(acc[0][0]), "+v"(acc[0][1])); S = {acc[0][0][0], acc[0][1][0]}; } \
                        if ((t & 3) == 0) asm volatile("" : "+v"(acc[t >> 2][0]), "+v"(acc[t >> 2][1])); \
                        S = cfma(L1, S, cf{acc[t >> 2][0][t & 3], acc[t >> 2][1][t & 3]}); PINC(S); } } while (0)
            G1_H(0, 1, false, AGG);
            G1_H(1, 1, false, AGG);
        }
        STAMPT(dbg, 1, 0);
        const unsigned tag = __builtin_amdgcn_readfirstlane(tagv);
        const __amdgpu_buffer_rsrc_t grs = __builtin_amdgcn_make_buffer_rsrc(ws, 0, 15 * 16384, 0x00020000);
        {
#pragma unroll
            for (int t = 26; t < 32; ++t) {
                if (t == 28) { PINC(S); asm volatile("" : "+v"(acc[7][0]), "+v"(acc[7][1])); }
                S = cfma(L1, S, cf{acc[t >> 2][0][t & 3], acc[t >> 2][1][t & 3]});
            }
            const cf W = cmul(S, Lq3);
            const float e2 = red16(red32(W.r, W.i));
            const auto eo = __builtin_amdgcn_permlane32_swap(__float_as_uint(e2), __float_as_uint(e2), false, false);
            __builtin_amdgcn_raw_buffer_store_b128((u32x4){eo[0], tag, eo[1], tag}, grs, gpub, 0, 16);
        }
        SCHED();
        STAMPT(dbg, 2, 0);
        const unsigned cg = lbase + GA_OFF + wid * 8192;
        wait_lds1(smem, COK_OFF + wid * 4);
        CREAD(0);
        WAIT_LGKM0();
        CPIN(0);
        CTILE(wid, 1, cg);
        u32x4 gq[4];
#pragma unroll
        for (int m = 0; m < 8; ++m) {
            const f32x4 R = acc[m][0], I = acc[m][1];
            acc[m][0] = fma4(splat4(cc.r), R, mul4(splat4(-cc.i), I));
            acc[m][1] = fma4(splat4(-cc.r), I, mul4(splat4(-cc.i), R));
            asm volatile("" : "+v"(acc[m][0]), "+v"(acc[m][1]));
            if (m == 3) {
                SCHED();
#pragma unroll
                for (int i = 0; i < 4; ++i) asm volatile("buffer_load_dwordx4 %0, %1, %2, 0 offen sc1" : "=&v"(gq[i]) : "v"(goff[i]), "s"(grs) : "memory");
                SCHED();
            }
        }
        cf Ec;
        {
            const auto sr = __builtin_amdgcn_permlane16_swap(__float_as_uint(S.r), __float_as_uint(S.r), false, false);
            const auto si = __builtin_amdgcn_permlane16_swap(__float_as_uint(S.i), __float_as_uint(S.i), false, false);
            const cf Se = {__uint_as_float(sr[0]), __uint_as_float(si[0])}, So = {__uint_as_float(sr[1]), __uint_as_float(si[1])};
            const cf A = cfma(L32, Se, So);
            const auto ar = __builtin_amdgcn_permlane32_swap(__float_as_uint(A.r), __float_as_uint(A.r), false, false);
            const auto ai = __builtin_amdgcn_permlane32_swap(__float_as_uint(A.i), __float_as_uint(A.i), false, false);
            const cf A01 = {(q4 & 2) ? __uint_as_float(ar[0]) : 0.f, (q4 & 2) ? __uint_as_float(ai[0]) : 0.f};
            const cf kq = {(q4 & 1) ? L32.r : 1.f, (q4 & 1) ? L32.i : 0.f};
            const cf E = cfma(kq, A01, cf{(q4 & 1) ? Se.r : 0.f, (q4 & 1) ? Se.i : 0.f});
            Ec = cmul(cc, E);
            PINC(Ec);
        }
        WAIT_VM(4);
        CREAD(1);
        WAIT_LGKM0();
        CPIN(1);
        CTILE(wid, 2, cg);
        asm volatile("s_waitcnt vmcnt(8)" : "+v"(gq[0]), "+v"(gq[1]), "+v"(gq[2]), "+v"(gq[3]) :: "memory");
        STAMPT(dbg, 27, 0);
        cf carry;
        {
            unsigned spins = 0;
            for (;;) {
                unsigned bad = 0;
#pragma unroll
                for (int i = 0; i < 4; ++i) bad |= ((gq[i].y ^ tag) | (gq[i].w ^ tag)) & gmask[i];
                if (__all(bad == 0u)) break;
                if (++spins > SPIN_MAX) break;
#pragma unroll
                for (int i = 0; i < 4; ++i) asm volatile("buffer_load_dwordx4 %0, %1, %2, 0 offen sc1" : "+v"(gq[i]) : "v"(goff[i]), "s"(grs) : "memory");
                asm volatile("s_waitcnt vmcnt(0)" : "+v"(gq[0]), "+v"(gq[1]), "+v"(gq[2]), "+v"(gq[3]) :: "memory");
            }
            cf E = {__uint_as_float(gq[3].x), __uint_as_float(gq[3].z)};
#pragma unroll
            for (int i = 2; i >= 0; --i) E = cfma(L512, E, cf{__uint_as_float(gq[i].x), __uint_as_float(gq[i].z)});
            E = cmul(E, Lp);
            const float e2 = red16(red32(E.r, E.i));
            const auto eo = __builtin_amdgcn_permlane32_swap(__float_as_uint(e2), __float_as_uint(e2), false, false);
            carry = {__uint_as_float(eo[0]), __uint_as_float(eo[1])};
        }
        STAMPT(dbg, 3, 0);
        {
            const __amdgpu_buffer_rsrc_t hrs = __builtin_amdgcn_make_buffer_rsrc(h, 0, SEQ * 2048 * 2, 0x00020000);
            const cf e = cfma(cLq, carry, Ec);
            float hr = e.r, hn = -e.i;
            s16x4 fr[2][4];
#define HSTORE(m) do { _Pragma("unroll") for (int c = 0; c < 2; ++c) \
                __builtin_amdgcn_raw_buffer_store_b128(__builtin_bit_cast(u32x4, __builtin_shufflevector(fr[(m) & 1][2 * c], fr[(m) & 1][2 * c + 1], 0, 1, 2, 3, 4, 5, 6, 7)), hrs, hp, (2 * (m) + c) * 65536, WT); } while (0)
#pragma unroll
            for (int m = 0; m < 4; ++m) {
#pragma unroll
                for (int j = 0; j < 8; ++j) {
                    const float nr = fmaf(L1.r, hr, fmaf(L1.i, hn, acc[2 * m + (j >> 2)][0][j & 3]));
                    const float nn = fmaf(L1.r, hn, fmaf(-L1.i, hr, acc[2 * m + (j >> 2)][1][j & 3]));
                    hr = nr; hn = nn;
                    acc[2 * m + (j >> 2)][0][j & 3] = nr; acc[2 * m + (j >> 2)][1][j & 3] = nn;
                }
                u32x2 pk[4];
#pragma unroll
                for (int c = 0; c < 4; ++c) { const f32x4 v = acc[2 * m + (c >> 1)][c & 1]; pk[c] = (u32x2){pkbf(v[0], v[1]), pkbf(v[2], v[3])}; }
                PIN4(pk[0], pk[1], pk[2], pk[3]);
                SCHED();
#pragma unroll
                for (int c = 0; c < 4; ++c) { *(volatile __attribute__((address_space(3))) u32x2*)(size_t)(wa + c * 512) = pk[c]; SCHED(); }
                asm volatile("" ::: "memory");
#pragma unroll
                for (int c = 0; c < 2; ++c) { fr[m & 1][2 * c] = TRD(ra0 + c * 1024); SCHED(); fr[m & 1][2 * c + 1] = TRD(ra1 + c * 1024); SCHED(); }
                asm volatile("" ::: "memory");
                if (m > 0) HSTORE(m - 1);
                SCHED();
            }
            HSTORE(3);
        }
        STAMPT(dbg, 4, 0);
        {
            WAIT_VM(8);
            CREAD(2);
            WAIT_VM(0);
            {
                unsigned n;
                asm volatile("s_mov_b64 exec, 1\n\tds_add_rtn_u32 %0, %1, %2\n\ts_mov_b64 exec, -1\n\ts_waitcnt lgkmcnt(0)" : "=&v"(n) : "v"(MISC_OFF), "v"(1u) : "memory");
                if (__builtin_amdgcn_readfirstlane(n) == 3u) {
                    if (lane == 0) __hip_atomic_store((gu32*)(ws + WS_HFLAG) + (tb * 16 + nb) * 32, tag, RLX_AGENT);
                    if (DBGON && lane == 0) dbg[BIDX * 64 + 20] = __builtin_amdgcn_s_memrealtime();
                }
            }
            STAMPT(dbg, 12, 0);
            if (wid == 0 && lane < 7) CODE_TOUCH(CODE_LINES - 1 - lane - (idx & 7) * 7);
            __builtin_amdgcn_s_setprio(0);
            WAIT_LGKM0();
            CPIN(2);
            CTILE(wid, 3, cg);
            XLOAD();
            HPOLL_SETUP();
            HWAIT();
            STAMPT(dbg, 8, 0);
            const unsigned vh = (unsigned)(lane * 16);
#pragma unroll
            for (int f = 0; f < DEPTH; ++f) LOADF(f);
            __builtin_amdgcn_s_setprio(PRIO2_G);
        }
    } else {
        const int lw = wid - 4;
        const int d = cb * 4 + lw;
        const char* Krow = (const char*)(ssmk + (size_t)d * SEQ);
        cra = CRA(LA_OFF + lw * 8192); crb = cra ^ 64;
        const unsigned cst0 = lbase + LA_OFF + lw * 8192;
        CT_SETUP();
        asm volatile("" : "+v"(cra), "+v"(crb));
        u32x4 KA[4];
        unsigned draw;
        unsigned tag;
        float gacc = 0.f;
#define KLOAD() asm volatile("global_load_dwordx4 %0, %5, %7 nt\n\tglobal_load_dwordx4 %1, %5, %7 offset:1024 nt\n\tglobal_load_dwordx4 %2, %5, %7 offset:2048 nt\n\t" \
                             "global_load_dwordx4 %3, %5, %7 offset:3072 nt\n\tglobal_load_ushort %4, %6, %8" \
                             : "=&v"(KA[0]), "=&v"(KA[1]), "=&v"(KA[2]), "=&v"(KA[3]), "=&v"(draw) : "v"((unsigned)(lane * 16)), "v"((unsigned)(d * 2)), "s"(Krow), "s"(ssmD) : "memory")
#define KWAIT(n) asm volatile("s_waitcnt vmcnt(%5)" : "+v"(KA[0]), "+v"(KA[1]), "+v"(KA[2]), "+v"(KA[3]), "+v"(draw) : "i"(n) : "memory")
        if (wid < 6) {
            WAIT_VM(16);
            STAMPT(dbg, 17, 320);
            BAR();
            STAGE_A(2, lw0 + RA(2)); STAGE_B(2, lw0 + RB(2));
            unsigned long long ua_ = UNITS_A >> 12;
            unsigned sla = lw0 + RA(3), slb = lw0 + RB(4);
            int ka = ((3 + rot1) & 15) * 128, kb_ = ka;
            const char* sa = (const char*)x + ka;
            asm volatile("" : "+s"(sa), "+s"(sla), "+s"(ka), "+s"(ua_));
#define NEXT_A() do { ka = (ka + 128) & 2047; sa = (const char*)x + ka; ua_ >>= 4; sla = lw0 + (((unsigned)ua_ & 15u) << 14); asm volatile("" : "+s"(sa), "+s"(sla), "+s"(ka), "+s"(ua_)); } while (0)
#define LSTEP(n) do { \
                if ((n) == 24 && t == 1) { WAIT_VM(16); BAR(); STAGE_B(3, lw0 + RB(3)); } else { WAIT_VM(n); BAR(); } \
                GLDS4(sa, voA[0], voA[1], voA[2], voA[3], sla, 0); \
                GLDS4(sa + 16384, voA[0], voA[1], voA[2], voA[3], sla, 4096); \
                kb_ = (kb_ + 128) & 2047; \
                GLDS4((const char*)Bre + kb_, voB[0], voB[1], voB[2], voB[3], slb, 0); \
                NEXT_A(); \
                const unsigned slb0 = slb; \
                slb = (slb == lw0 + 65536) ? lw0 + 16384 : slb + 16384; \
                asm volatile("" : "+s"(slb), "+s"(kb_), "+s"(t)); \
                GLDS4((const char*)Bim + kb_, voB[0], voB[1], voB[2], voB[3], slb0, 4096); } while (0)
#pragma unroll 1
            for (int t = 1; t < 5; ++t) LSTEP(24);
            GLDS4(sa, voA[0], voA[1], voA[2], voA[3], sla, 0); GLDS4(sa + 16384, voA[0], voA[1], voA[2], voA[3], sla, 4096);
            NEXT_A();
#pragma unroll 1
            for (int t = 5; t < 13; ++t) LSTEP(32);
            KLOAD();
            WAIT_VM(37);
            BAR();
            __builtin_amdgcn_s_setprio(0);
            tag = lds_word(smem, TAG_OFF);
            CTILE(lw, 0, cst0 + GA_OFF);
            FLAGD(COK_OFF + lw * 4);
            WAIT_VM(30);
            BAR();
            CTILE(lw + 2, 0, cst0 + GA_OFF + 16384);
            FLAGD(COK_OFF + lw * 4 + 8);
            WAIT_VM(23);
            BAR();
        } else {
            const int vw = wid - 6;
            const int dch = dblk * 128 + vw * 64 + l15;
            const unsigned lk = lbase + ST_OFF + vw * ST_SZ;
            const unsigned vv_ = (unsigned)(((lane >> 4) * 4 + ((lane >> 3) & 1)) * DM * 2 + (lane & 7) * 16);
            const char* vb = (const char*)vin + (size_t)(sblk * 64) * (DM * 2) + dblk * 256 + vw * 128;
            const int grow = nb * 8 + vw * 4 + q4;
            unsigned gx0 = (unsigned)(grow * 128 + (((l15 >> 1) ^ ((grow >> 1) & 7)) * 16) + (l15 & 1) * 8), gx1 = gx0 + RA(2), gwa = (unsigned)(GW_OFF + l15 * 8);
            asm volatile("" : "+v"(gx0), "+v"(gx1), "+v"(gwa));
            u32x2 gxv, gwv;
            typedef const volatile __attribute__((address_space(3))) u32x2* l2p;
#define VBAR(t) do { SCHED(); asm volatile("" ::: "memory"); BAR(); asm volatile("" ::: "memory"); \
                     gxv = *(l2p)(size_t)(RA(t) == 0 ? gx0 : gx1 + (RA(t) - RA(2))); gwv = *(l2p)(size_t)(gwa + (t) * 128); SCHED(); } while (0)
#define GATE() do { SCHED(); gacc = fmaf(lo16(gxv.x), lo16(gwv.x), gacc); gacc = fmaf(hi16(gxv.x), hi16(gwv.x), gacc); \
                    gacc = fmaf(lo16(gxv.y), lo16(gwv.y), gacc); gacc = fmaf(hi16(gxv.y), hi16(gwv.y), gacc); asm volatile("" : "+v"(gacc)); SCHED(); } while (0)
            unsigned ka[4][2], sel[4];
            const int R0 = 4 + (l15 >> 2) * 16 + (l15 & 3) - (q4 >> 1);
            unsigned kr0 = (unsigned)(2 * R0), kr1 = (unsigned)(ST_OFF + vw * ST_SZ + R0 * 128 + (((q4 ^ (R0 >> 1)) & 1) * 16));
            asm volatile("" : "+v"(kr0), "+v"(kr1));
#pragma unroll
            for (int i = 0; i < 4; ++i)
#pragma unroll
                for (int tp = 0; tp < 2; ++tp) {
                    ka[i][tp] = (kr1 ^ (tp * 16)) + ((kr0 + 8 * i - 4 * tp) & 0xe0) + (4 * i - 2 * tp) * 128;
                    asm volatile("" : "+v"(ka[i][tp]));
                }
#pragma unroll
            for (int r = 0; r < 4; ++r) {
                const unsigned pick = (q4 & 2) ? 0x0302u : 0x0100u;
                sel[r] = ((l15 >> 1) == (q4 & 1) * 4 + r) ? ((l15 & 1) ? (pick << 16) | 0x0c0cu : 0x0c0c0000u | pick) : 0x0c0c0c0cu;
            }
            unsigned va_ = (unsigned)(ST_OFF + vw * ST_SZ + ST_V + (q4 * 2 + (l15 >> 3)) * 1056 + ((l15 >> 2) & 1) * 128 + (l15 & 3) * 8);
            unsigned so = (unsigned)(((dch >> 2) * 32 + sblk) * 512 + q4 * 64 + (dch & 3) * 16);
            PIN4(sel[0], sel[1], sel[2], sel[3]); asm volatile("" : "+v"(so), "+v"(va_));
            asm volatile("s_waitcnt vmcnt(9)" : "+v"(tw[0]), "+v"(tw[1]), "+v"(tw[2]), "+v"(tw[3]), "+v"(epoch) :: "memory");
            STAMPT(dbg, 18, 384);
            VBAR(0);
            bf16x8 wf[4][2];
#pragma unroll
            for (int ps = 0; ps < 4; ++ps)
#pragma unroll
                for (int tp = 0; tp < 2; ++tp) {
                    u32x4 w;
#pragma unroll
                    for (int r = 0; r < 4; ++r) w[r] = __builtin_amdgcn_perm(0u, tw[ps][tp], sel[r]);
                    wf[ps][tp] = __builtin_bit_cast(bf16x8, w);
                    asm volatile("" : "+v"(wf[ps][tp]));
                }
            SCHED();
            GATE(); VBAR(1);
            VLD(0); VLD(1); VLD(2); VLD(3); VLD(4); VLD(5); VLD(6); VLD(7);
            f32x4 ks[4][4];
            auto sconv = [&](int i0) __attribute__((always_inline)) {
                bf16x8 kf[2][4][2];
#pragma unroll
                for (int q = 0; q < 16; ++q) { kf[q >> 3][q & 3][(q >> 2) & 1] = RD128(ka[i0 + (q >> 3)][(q >> 2) & 1], (q & 3) * 32); SCHED(); }
                GATE();
                __builtin_amdgcn_s_setprio(PRIO_KV);
                WAIT_LGKM0();
                SCHED();
#pragma unroll
                for (int i = 0; i < 2; ++i) {
#pragma unroll
                    for (int ps = 0; ps < 4; ++ps) MF0(ks[ps][i0 + i], kf[i][ps][0], wf[ps][0]);
#pragma unroll
                    for (int ps = 0; ps < 4; ++ps) MF(ks[ps][i0 + i], kf[i][ps][1], wf[ps][1]);
                }
                __builtin_amdgcn_s_setprio(0);
                __builtin_amdgcn_s_sleep(1);
                SCHED();
            };
            WAIT_VM(8);
            STAMPT(dbg, 21, 384);
            sconv(0); VBAR(2);
            sconv(2); VBAR(3);
            WAIT_VM(0);
            u32x2 vq[4][4];
#pragma unroll
            for (int ps = 0; ps < 4; ++ps)
#pragma unroll
                for (int j = 0; j < 4; ++j) vq[ps][j] = __builtin_bit_cast(u32x2, TRD(va_ + j * 256 + ps * 32));
            const __amdgpu_buffer_rsrc_t rs = __builtin_amdgcn_make_buffer_rsrc(kvT, 0, DM * SEQ * 2, 0x00020000);
            u32x4 o[8];
            auto kvout = [&](int n) __attribute__((always_inline)) {
                const int hh = n >> 2, ps = n & 3;
                asm volatile("" : "+v"(ks[ps][2 * hh]), "+v"(ks[ps][2 * hh + 1]));
#pragma unroll
                for (int e = 0; e < 4; ++e) {
                    const unsigned vv = vq[ps][2 * hh + (e >> 1)][e & 1];
                    o[n][e] = pkbf(ks[ps][2 * hh + (e >> 1)][2 * (e & 1)] * lo16(vv), ks[ps][2 * hh + (e >> 1)][2 * (e & 1) + 1] * hi16(vv));
                }
                asm volatile("" : "+v"(o[n]));
                SCHED();
            };
            GATE(); kvout(0); kvout(1);
            WAIT_LGKM0();

            VBAR(4);
#define KVST(h) do { _Pragma("unroll") for (int n = 0; n < 4; ++n) __builtin_amdgcn_raw_buffer_store_b128(o[4 * (h) + n], rs, so + (unsigned)((h) * 256), n * 65536, WT); \
                     SCHED(); asm volatile("s_nop 1" ::: "memory"); SCHED(); } while (0)
            kvout(2); kvout(3); GATE(); VBAR(5);
            KVST(0);
            kvout(4); kvout(5); GATE(); VBAR(6);
            kvout(6); kvout(7); GATE(); VBAR(7);
            KVST(1);
            GATE(); VBAR(8);
            if (vw == 1 && lane == 0) *(unsigned*)(smem + TAG_OFF) = (epoch + 1u) ? epoch + 1u : 1u;
            GATE();
            WAIT_LGKM0();
            WAIT_VM(0);
            VBAR(9);
            tag = __builtin_amdgcn_readfirstlane(*(const unsigned*)(smem + TAG_OFF));
            if (vw == 0 && lane == 0) __hip_atomic_store((gu32*)(ws + WS_KVFLAG) + (dblk * 32 + sblk) * 32, tag, RLX_AGENT);
            if (vw == 1 && lane < 4) *(u32x4*)(smem + U_OFF + 192 + lane * 16) = (u32x4){0u, 0u, 0u, 0u};
            gu32* kfp = (gu32*)(ws + WS_KVFLAG) + ((cb >> 5) * 32 + vw * 16 + l15) * 32;
            const char* ub = (const char*)kvT + (size_t)__builtin_amdgcn_readfirstlane(cb * 16384 + vw * 8192);
            const unsigned vu = (unsigned)((lane >> 5) * 512 + ((lane >> 2) & 1) * 256 + ((lane >> 3) & 3) * 64 + ((lane - (lane >> 4)) & 3) * 16);
            const unsigned lu = lbase + U_OFF + 256 + vw * 8192;
#define U_DMA1(o) asm volatile("s_add_u32 m0, %2, %3\n\ts_nop 0\n\tglobal_load_lds_dwordx4 %0, %1 sc1\n\tglobal_load_lds_dwordx4 %0, %1 offset:1024 sc1\n\t" \
                             "global_load_lds_dwordx4 %0, %1 offset:2048 sc1\n\tglobal_load_lds_dwordx4 %0, %1 offset:3072 sc1" \
                             :: "v"(vu), "s"(ub + (o)), "s"(lu), "i"(o) : "memory", "scc")
#define U_DMA() do { U_DMA1(0); U_DMA1(4096); FLAGD(UFLAG_OFF + lw * 4); } while (0)
            unsigned fo = lane < 8 ? (unsigned)((tb * 1024 + p_base) * 16 + lane * 128) : lane == 8 ? (unsigned)(WS_HFLAG + (tb * 16 + nb) * 128)
                        : lane == 9 ? (unsigned)(WS_CFLAG + cb * 128) : (unsigned)(WS_GATE + (tb * 16 + nb) * 128);
            asm volatile("" : "+v"(fo));
            GATE(); VBAR(10);
            TOUCH((const char*)(vw ? Cim : Cre) + (d_base + (idx & 3) * 16) * 2048 + COFF(0, 2), (unsigned)((lane >> 2) * 2048 + (lane & 3) * 256));
            if (vw == 0) TOUCH(ws, fo); else TOUCH(dskip, (unsigned)(d_base * 2));
            {
                const int cl = 64 + lane + ((idx & 15) * 2 + vw) * 7;
                if (lane < 7 && cl < CODE_LINES) CODE_TOUCH(cl);
            }
            GATE(); VBAR(11);
            GATE(); VBAR(12);
            GATE(); VBAR(13);
            unsigned pv;
            asm volatile("global_load_dword %0, %1, off sc1" : "=&v"(pv) : "v"(kfp) : "memory");
            KLOAD();
            GATE(); VBAR(14);
            GATE(); VBAR(15);
            asm volatile("s_waitcnt vmcnt(5)" : "+v"(pv) :: "memory");
            const bool done = __all(pv == tag);
            if (done) U_DMA();
            if (DBGON && TIDX == 384) dbg[BIDX * 64 + 13] = (done ? 100ull : 0ull) | (1ull << 62);
            GATE();
            if (lane == 0) SETW(UFLAG_OFF, vw, 1u);
            WAIT_VM(9);
            if (!done) {
                unsigned spins = 0;
                for (;;) {
                    const unsigned v = __hip_atomic_load(kfp, RLX_AGENT);
                    if (__all(v == tag)) break;
                    if (++spins > SPIN_MAX) break;
                    __builtin_amdgcn_s_sleep(1);
                }
                U_DMA();
            }
        }
        KWAIT(18);
        STAMPT(dbg, 14, 320);
        __builtin_amdgcn_s_sleep(3);
        if (lw < 2) __builtin_amdgcn_s_sleep(3);
        {
            const unsigned cp = (unsigned)(KC_OFF + lw * CV_W + 64 + lane * 16);
            if (lane == 0) KA[0].x = (KA[0].x & 0xffff0000u) | f2bf(lo16(KA[0].x) + bf2f((u16)draw));
#define WR128(v, off) asm volatile("ds_write_b128 %0, %1 offset:%2" :: "v"(cp), "v"(v), "i"(off) : "memory")
#pragma unroll
            for (int q = 3; q >= 0; --q) WR128(KA[q], q * 1024);
#pragma unroll
            for (int q = 3; q >= 0; --q) {
                const u32x2 p = q ? pred2(KA[q], RL63(KA[q ? q - 1 : 0].z), RL63(KA[q ? q - 1 : 0].w)) : pred2(KA[0], 0u, 0u);
                const u32x4 a = KA[q];
                const u32x4 s1 = {al(a.x, p.y), al(a.y, a.x), al(a.z, a.y), al(a.w, a.z)};
                WR128(s1, q * 1024 + CV_COPY);
                const u32x4 s2 = {p.y, mv(a.x), mv(a.y), mv(a.z)};
                WR128(s2, q * 1024 + 2 * CV_COPY);
                const u32x4 s3 = {al(p.y, p.x), mv(s1.x), mv(s1.y), mv(s1.z)};
                WR128(s3, q * 1024 + 3 * CV_COPY);
            }
            if (lane < 16) *(u32x4*)(smem + KC_OFF + lw * CV_W + (lane >> 2) * CV_COPY + (lane & 3) * 16) = (u32x4){0u, 0u, 0u, 0u};
            SCHED();
        }
        STAMPT(dbg, 26, 320);
        wait_lds4(smem, G1D_OFF);
        wait_lds4(smem, UFLAG_OFF);
        STAMPT(dbg, 9, 256);
        __builtin_amdgcn_s_setprio(PRIO_CV);
        STAMPT(dbg, 10, 384);
        f32x4 cv[8];
#pragma unroll
        for (int g = 0; g < 8; ++g) cv[g] = (f32x4){0.f, 0.f, 0.f, 0.f};
        PIN4(cv[0], cv[1], cv[2], cv[3]); PIN4(cv[4], cv[5], cv[6], cv[7]);
        const int uz = U_OFF + 192;
        const int ua0 = U_OFF + 256 + (2 * l15 + q4 - 2) * 64, rv0 = (2 * l15 + q4 - 2) * 4 + lw * 16;
        const unsigned ka0 = (unsigned)((l15 >> 2) * CV_COPY - 16 * q4 + 8 * (l15 & 3) - 8);
        int ua, rv, pu, pz;
        unsigned adr;
#define UNEXT1() do { pu = ua | (rv & 0x30); asm volatile("" : "+v"(pu)); } while (0)
#define UNEXT2() do { pz = (ua > uz ? ua : uz) | (rv & 0x30); asm volatile("" : "+v"(pz)); } while (0)
#define UNEXT3() do { ua -= 256; rv -= 16; asm volatile("" : "+v"(ua), "+v"(rv)); } while (0)
#define ANEXT() do { adr += 64; asm volatile("" : "+v"(adr)); } while (0)
#define RDU(dst, c) do { dst = (c) ? RD128(pu, (c) * 2048) : RD128(pz, 0); SCHED(); } while (0)
        HPOLL_SETUP();
        unsigned hpv = 0;
        {
            bf16x8 A[8], UB[8];
            ua = ua0; rv = rv0; adr = lbase + KC_OFF + lw * CV_W + ka0 + 96;
            asm volatile("" : "+v"(adr));
            UNEXT1(); UNEXT2(); UNEXT3();
#pragma unroll
            for (int i = 0; i < 8; ++i) { RDA(A[7 - i], adr, 7 - i); RDU(UB[i], i); }
            unsigned gb = lw >= 2 ? (unsigned)((tb * 16 + nb) * 128 + (lw - 2) * 32) : 0x80000000u;
            asm volatile("" : "+s"(gb));
            const unsigned gso = (l15 == 0) ? gb + (unsigned)(q4 * 8) : 0x80000000u;
            float gt = 0.f;
            unsigned vxo = 0, vdo = 0, vgo = 0;
            auto gate_out = [&](int i) __attribute__((always_inline)) {
                if (i == 0) gacc += __uint_as_float(__builtin_amdgcn_update_dpp(0u, __float_as_uint(gacc), 0xb1, 0xf, 0xf, false));
                if (i == 1) gacc += __uint_as_float(__builtin_amdgcn_update_dpp(0u, __float_as_uint(gacc), 0x4e, 0xf, 0xf, false));
                if (i == 2) gacc += __uint_as_float(__builtin_amdgcn_update_dpp(0u, __float_as_uint(gacc), 0x141, 0xf, 0xf, false));
                if (i == 3) gacc += __uint_as_float(__builtin_amdgcn_update_dpp(0u, __float_as_uint(gacc), 0x140, 0xf, 0xf, false));
                if (i == 4) gacc = rbf(gacc);
                if (i == 5) gacc = __builtin_amdgcn_exp2f(gacc * -1.44269504088896f);
                if (i == 6) gacc = __builtin_amdgcn_rcpf(1.0f + gacc);
                if (i == 7) gacc = rbf(gacc);
                if (i == 8) gt = __uint_as_float(pkbf(gacc, 1.0f - gacc));
                if (i == 9) __builtin_amdgcn_raw_buffer_store_b64((u32x2){__float_as_uint(gt), tag}, gs, gso, 0, WT);
                asm volatile("" : "+v"(gacc), "+v"(gt));
            };
            auto step = [&](auto E) __attribute__((always_inline)) {
                constexpr int e = decltype(E)::value;
                constexpr int ti = (e == CS0) ? 0 : (e == CS1) ? 1 : (e == CS2) ? 2 : -1, tj = ti < 0 ? 0 : ti;
                constexpr unsigned char GA[36] = {0x66, 0x77, 0x76, 0x44, 0x55, 0x54, 0x65, 0x64, 0x75, 0x74, 0x22, 0x33, 0x32, 0x43, 0x42, 0x53, 0x52, 0x63, 0x62, 0x73, 0x72,
                                                  0x00, 0x11, 0x10, 0x21, 0x20, 0x31, 0x30, 0x41, 0x40, 0x51, 0x50, 0x61, 0x60, 0x71, 0x70};
                const char* sb_ = (const char*)Cre + COFF(wid, tj + 1);
                const char* sc_ = (const char*)Cim + COFF(wid, tj + 1);
#pragma unroll
                for (int q = 0; q < 36; ++q) {
                    const int g = GA[q] >> 4, a = GA[q] & 15;
                    MF(cv[g], A[a], UB[g - a]); SCHED();
                    if (e < 7) {
                        if (a == 1) RDU(UB[g - 1], g - 1);
                        if (q == 35) RDU(UB[7], 7);
                        if (g == 7) RDA(A[a], adr + 64 * (e + 1), a);
                        if (q == 11) UNEXT1();
                        if (q == 13) UNEXT2();
                        if (q == 15) UNEXT3();
                    }
                    if (ti >= 0) {
                        const int n = (q >= 3 && q <= 7) ? q - 3 : q == 10 ? 5 : q == 12 ? 6 : q == 14 ? 7 : -1;
                        if (q == 3) { if (ti == 0) WAIT_VM(1); else if (e == XE + 1) WAIT_VM(7); else WAIT_VM(0); }
                        if (n >= 0) CRD(tj, n & 7);
                        if (q == 18) CPIN(tj);
                        if (q == 18 || q == 23) asm volatile("s_add_u32 m0, %3, %4\n\ts_nop 0\n\tglobal_load_lds_dwordx4 %0, %2\n\tglobal_load_lds_dwordx4 %1, %2 offset:1024"
                                                             :: "v"(vB0), "v"(vB1), "s"(q == 18 ? sb_ : sc_), "s"(cst0), "i"(q == 18 ? 0 : 4096) : "memory", "scc");
                        if (q == 21 || q == 25) asm volatile("global_load_lds_dwordx4 %0, %2 offset:2048\n\tglobal_load_lds_dwordx4 %1, %2 offset:3072"
                                                             :: "v"(vB0), "v"(vB1), "s"(q == 21 ? sb_ : sc_) : "memory");
                    }
                    if (e == 0 && q >= 3 && q <= 7) gate_out(q - 3);
                    if (e == 0 && q >= 10 && q <= 18 && !(q & 1)) gate_out(5 + (q - 10) / 2);
                    if (e == 0 && q == 4) CTILE(wid, 0, cst0);
                    if (e == XE && q >= 23 && q <= 35 && (q & 1)) {
                        const int i = (q - 23) / 2;
                        const __amdgpu_buffer_rsrc_t xs = __builtin_amdgcn_make_buffer_rsrc((void*)x, 0, SEQ * DM * 2, 0x00020000);
                        const __amdgpu_buffer_rsrc_t ks = __builtin_amdgcn_make_buffer_rsrc((void*)dskip, 0, DM * 2, 0x00020000);
                        if (i < 4) asm volatile("buffer_load_dwordx2 %0, %1, %2, %3 offen offset:%4" : "=&v"(xx[(i >> 1) & 1][i & 1]) : "v"(vxo), "s"(xs), "s"((TR((i >> 1) & 1) * DM + d_base) * 2), "i"((i & 1) * 2048) : "memory");
                        if (i == 4) asm volatile("buffer_load_dwordx2 %0, %1, %2, %3 offen" : "=&v"(dk2) : "v"(vdo), "s"(ks), "s"(d_base * 2) : "memory");
                        if (i >= 5) asm volatile("buffer_load_dwordx4 %0, %1, %2, %3 offen sc1" : "=&v"(gv[(i - 5) & 1]) : "v"(vgo), "s"(gs), "s"(GSO((i - 5) & 1)) : "memory");
                    }
                    if (e == XE && q == 16) { vxo = (unsigned)(q4 * 32 * DM * 2 + l15 * 8); vdo = (unsigned)(l15 * 8); asm volatile("" : "+v"(vxo), "+v"(vdo)); }
                    if (e == XE && q == 17) { vgo = GV(q4); asm volatile("" : "+v"(vgo)); }
                    if (e == 6 && q == 30) hpv = __hip_atomic_load(hfp, RLX_AGENT);
                    SCHED();
                }
            };
            unroll<8>(step);
            WAIT_VM(0);
            XPIN();
        }
        PAD8(cv[0], cv[1], cv[2], cv[3]); PIN4(cv[4], cv[5], cv[6], cv[7]);
        STAMPT(dbg, 11, 384);
        const unsigned vh = (unsigned)((lane ^ 2) * 16);
        if (!__all(hpv == tag)) HWAIT();
        asm volatile("" ::: "memory");
#pragma unroll
        for (int f = 0; f < DEPTH; ++f) LOADF(f);
        STAMPT(dbg, 25, 384);
        {
            const __amdgpu_buffer_rsrc_t yrs = __builtin_amdgcn_make_buffer_rsrc(y2T, 0, DM * SEQ * 2, 0x00020000);
            const unsigned yo = (unsigned)(d * 4096 + (q4 & 1) * 512 + (l15 >> 3) * 256 + (q4 >> 1) * 128 + (l15 & 7) * 16);
#pragma unroll
            for (int g = 0; g < 8; g += 2) {
                const auto r0 = __builtin_amdgcn_permlane16_swap(pkbf(cv[g][0], cv[g][1]), pkbf(cv[g + 1][0], cv[g + 1][1]), false, false);
                const auto r1 = __builtin_amdgcn_permlane16_swap(pkbf(cv[g][2], cv[g][3]), pkbf(cv[g + 1][2], cv[g + 1][3]), false, false);
                __builtin_amdgcn_raw_buffer_store_b128((u32x4){r0[0], r1[0], r0[1], r1[1]}, yrs, yo + (unsigned)(g * 512), 0, WT);
            }
        }
        asm volatile("" ::: "memory");
        STAMPT(dbg, 15, 384);
        __builtin_amdgcn_s_setprio(PRIO2_L);
    }
    {
        const int lane = fresh_lane(), l15 = lane & 15, q4 = lane >> 4;
        const int ring = wid >> 2;
        const unsigned va = (unsigned)((lane ^ (ring * 2)) * 16), vh = va;
        const unsigned tag = lds_word(smem, TAG_OFF);
        const int cb0 = ring ? KC_OFF : GA_OFF, cst = ring ? CV_W : 8192;
        f32x4 ac[2][4];
        unsigned ca[2], fa[2], fl = 0, fv = 0;
#define CHAIN(u) do { const int c_ = (wid - ((u) >> 1)) & 3; ca[(u) & 1] = va + (unsigned)(cb0 + c_ * cst + ((u) & 1) * 4096); \
                      fa[(u) & 1] = (unsigned)(RF_OFF + ring * 32 + ((u) & 1) * 16 + c_ * 4); asm volatile("" : "+v"(ca[(u) & 1]), "+v"(fa[(u) & 1])); } while (0)
#define RDIN(dst, a, f_) asm volatile("ds_read_b32 %0, %5\n\tds_read_b128 %1, %6\n\tds_read_b128 %2, %6 offset:1024\n\tds_read_b128 %3, %6 offset:2048\n\tds_read_b128 %4, %6 offset:3072" \
                     : "=&v"(fl), "=&v"(dst[0]), "=&v"(dst[1]), "=&v"(dst[2]), "=&v"(dst[3]) : "v"(f_), "v"(a) : "memory")
#define LWAIT(dst) asm volatile("s_waitcnt lgkmcnt(0)" : "+v"(fl), "+v"(dst[0]), "+v"(dst[1]), "+v"(dst[2]), "+v"(dst[3]) :: "memory")
        CHAIN(0); CHAIN(1);
        WAIT_LGKM0();
        CPIN(0); CPIN(1); CPIN(2);
        CREAD(3);
        int tr[2] = {TR(0), TR(1)};
        unsigned gw[2][2];
        {
            unsigned spins = 0;
            while (__builtin_expect(!__all((gv[0].y == tag) & (gv[0].w == tag) & (gv[1].y == tag) & (gv[1].w == tag)), 0)) {
                if (++spins > SPIN_MAX) break;
#pragma unroll
                for (int r = 0; r < 2; ++r) asm volatile("s_nop 4\n\tbuffer_load_dwordx4 %0, %1, %2, %3 offen sc1\n\ts_waitcnt vmcnt(0)" : "=&v"(gv[r]) : "v"(GV(q4)), "s"(gs), "s"(__builtin_amdgcn_readfirstlane(GSO(r))) : "memory");
            }
            gw[0][0] = gv[0].x; gw[0][1] = gv[0].z; gw[1][0] = gv[1].x; gw[1][1] = gv[1].z;
            PIN4(gw[0][0], gw[0][1], gw[1][0], gw[1][1]);
        }
        unsigned cpv = 0, yy[2][4];
        f32x2 zz[2][4];
        unsigned exo = 0, exi = 0, efo = 0, efi = 0;
        auto send = [&](int r) __attribute__((always_inline)) {
            const unsigned one = 1u;
#pragma unroll
            for (int n = 0; n < 4; ++n) { const f32x2 v = {ac[r][n][2], ac[r][n][3]}; asm volatile("ds_write_b64 %0, %1 offset:%2" :: "v"(exo), "v"(v), "i"(r * 4096 + n * 512) : "memory"); }
            asm volatile("ds_write_b32 %0, %1 offset:%2" :: "v"(efo), "v"(one), "i"(r * 4) : "memory");
        };
        STAMPT(dbg, 5, 0);
        auto body = [&](auto F) __attribute__((always_inline)) {
            constexpr int f = decltype(F)::value;
            constexpr int u = f >> 3, k = f & 7;
            f32x4* an = ac[u & 1];
            f32x4* ao = ac[(u + 1) & 1];
#pragma unroll
            for (int n = 0; n < 4; ++n) {
                if (k == 0 && u < 2) MF0(an[n], af[f % DEPTH], cf_[k][n]); else MF(an[n], af[f % DEPTH], cf_[k][n]);
                SCHED();
                const int g = k * 4 + n;
                if (u >= 1 && u < 7) {
                    if (g == 1) { fv = ((u - 1) >> 1) + 1; asm volatile("" : "+v"(fv)); }
                    if (g == 4 || g == 6 || g == 8 || g == 10) asm volatile("ds_write_b128 %0, %1 offset:%2" :: "v"(ca[(u + 1) & 1]), "v"(ao[(g - 4) / 2]), "i"((g - 4) * 512) : "memory");
                    if (g == 12) asm volatile("ds_write_b32 %0, %1" :: "v"(fa[(u + 1) & 1]), "v"(fv) : "memory");
                    if (g == 22) CHAIN(u + 1);
                    if (g == 24) asm volatile("ds_read_b32 %0, %1" : "=&v"(fl) : "v"(fa[(u + 1) & 1]) : "memory");
                    if (g > 24 && g <= 28) asm volatile("ds_read_b128 %0, %1 offset:%2" : "=&v"(ao[g - 25]) : "v"(ca[(u + 1) & 1]), "i"((g - 25) * 1024) : "memory");
                    if (g == 31) {
                        LWAIT(ao);
                        if (__builtin_expect(__builtin_amdgcn_readfirstlane(fl) != (unsigned)((u + 1) >> 1), 0)) {
                            unsigned spins = 0;
#pragma unroll 1
                            do {
                                __builtin_amdgcn_s_sleep(1);
                                RDIN(ao, ca[(u + 1) & 1], fa[(u + 1) & 1]); LWAIT(ao);
                                if (++spins > SPIN_MAX * 16u) break;
                            } while (__builtin_amdgcn_readfirstlane(fl) != (unsigned)((u + 1) >> 1));
                        }
                    }
                }
                if (u == 7 && g == 6) send(0);
                if (f >= ZF && f < ZF + 4) {
                    constexpr int r = ((f - ZF) >> 1) & 1, j = (f - ZF) & 1;
                    const unsigned xw = xx[r][j][n >> 1], dw = dk2[n >> 1];
                    const float xf = (n & 1) ? hi16(xw) : lo16(xw), df = (n & 1) ? hi16(dw) : lo16(dw), yf = j ? hi16(yy[r][n]) : lo16(yy[r][n]);
                    zz[r][n][j] = xf * fmaf(hi16(gw[r][j]), yf, lo16(gw[r][j]) * df);
                    asm volatile("" : "+v"(zz[r][n]));
                }
                if (f == ZF + 4) {
                    zz[0][n] = (f32x2){fmaf(lo16(gw[0][0]), ac[0][n][0], zz[0][n][0]), fmaf(lo16(gw[0][1]), ac[0][n][1], zz[0][n][1])};
                    asm volatile("" : "+v"(zz[0][n]));
                }
                SCHED();
            }
            if (f + DEPTH < 64) LOADF(f + DEPTH);
            if (f == DEPTH + 1 && ring) {
                WAIT_VM(6);
                if (lane == 0) __hip_atomic_store((gu32*)(ws + WS_CFLAG) + cb * 32 + (wid & 3), tag, RLX_AGENT);
                SCHED();
            }
            if (f == YPOLL && ring) { const int l15 = fresh_lane() & 15; cpv = __hip_atomic_load((gu32*)(ws + WS_CFLAG) + (nb * 16 + (wid & 3) * 4 + (l15 >> 2)) * 32 + (l15 & 3), RLX_AGENT); SCHED(); }
            if (f == YPOLL + DEPTH && ring) {
                const int ln = fresh_lane(), l15 = ln & 15, q4 = ln >> 4;
                if (__builtin_expect(!__all(cpv == tag), 0)) {
                    unsigned spins = 0;
                    while (!__all(__hip_atomic_load((gu32*)(ws + WS_CFLAG) + (nb * 16 + (wid & 3) * 4 + (l15 >> 2)) * 32 + (l15 & 3), RLX_AGENT) == tag)) {
                        if (++spins > SPIN_MAX) break;
                        __builtin_amdgcn_s_sleep(1);
                    }
                }
                const unsigned lx = lbase + LA_OFF + (wid & 3) * 8192;
#pragma unroll
                for (int i = 0; i < 4; ++i) {
                    const unsigned vo = (unsigned)((d_base + (wid & 3) * 16 + i * 4 + q4) * 4096 + tb * 256 + ((l15 ^ ((wid & 3) * 4 + i)) * 16));
                    GLDS_SC1(y2T, vo, lx, i * 1024);
                }
                SCHED();
            }
            if (f == YPOLL + 2 * DEPTH + 1 && ring) {
                WAIT_VM(6);
                SETW(YFLAG_OFF, wid & 3, 1u);
                SCHED();
            }
            if (f == 55) {
                const int ln = fresh_lane(), c_ = (wid - 3) & 3;
                const int eg = GA_OFF + c_ * 8192, el = KC_OFF + c_ * CV_W;
                exo = (unsigned)((ring ? el : eg) + ln * 8);
                exi = (unsigned)((ring ? eg : el) + ln * 8);
                efo = (unsigned)(EF_OFF + wid * 8); efi = (unsigned)(EF_OFF + (wid ^ 4) * 8);
                PIN4(exo, exi, efo, efi);
                SCHED();
            }
            if (f == ZF - 2) {
                const int ln = fresh_lane(), l15 = ln & 15, q4 = ln >> 4;
                wait_lds4(smem, YFLAG_OFF);
#pragma unroll
                for (int r = 0; r < 2; ++r) {
                    const int jb = JB(6 + r);
                    const int ya = LA_OFF + (l15 >> 2) * 8192 + (l15 & 3) * 1024 + (((((jb >> 1) & 1) * 8 + (jb >> 2) + q4 * 2) ^ l15) * 16) + (jb & 1) * 8 + ring * 4;
#pragma unroll
                    for (int e = 0; e < 4; ++e) yy[r][e] = *(const unsigned*)(smem + ya + e * 256);
                }
                SCHED();
            }
        };
        unroll<64>(body);
        PAD8(ac[1][0], ac[1][1], ac[1][2], ac[1][3]);
        send(1);
        STAMPT(dbg, 6, 0);
        const __amdgpu_buffer_rsrc_t ors = __builtin_amdgcn_make_buffer_rsrc(out, 0, SEQ * DM * 4, 0x00020000);
        const unsigned ov = (unsigned)((lane >> 4) * (32 * DM * 4) + (lane & 15) * 16);
#pragma unroll
        for (int r = 0; r < 2; ++r) {
            f32x2 in[4];
#define EXRD() asm volatile("ds_read_b32 %0, %5 offset:%7\n\tds_read_b64 %1, %6 offset:%8\n\tds_read_b64 %2, %6 offset:%9\n\tds_read_b64 %3, %6 offset:%10\n\tds_read_b64 %4, %6 offset:%11" \
                             : "=&v"(fl), "=&v"(in[0]), "=&v"(in[1]), "=&v"(in[2]), "=&v"(in[3]) \
                             : "v"(efi), "v"(exi), "i"(r * 4), "i"(r * 4096), "i"(r * 4096 + 512), "i"(r * 4096 + 1024), "i"(r * 4096 + 1536) : "memory")
            EXRD();
            if (r == 0) {
#pragma unroll
                for (int n = 0; n < 4; ++n) {
                    zz[1][n] = (f32x2){fmaf(lo16(gw[1][0]), ac[1][n][0], zz[1][n][0]), fmaf(lo16(gw[1][1]), ac[1][n][1], zz[1][n][1])};
                    asm volatile("" : "+v"(zz[1][n]));
                }
            }
            LWAIT(in);
            if (__builtin_expect(__builtin_amdgcn_readfirstlane(fl) == 0u, 0)) {
                unsigned spins = 0;
#pragma unroll 1
                do {
                    EXRD(); LWAIT(in);
                    if (++spins > SPIN_MAX * 16u) break;
                } while (__builtin_amdgcn_readfirstlane(fl) == 0u);
            }
            f32x4 o[2];
#pragma unroll
            for (int j = 0; j < 2; ++j)
#pragma unroll
                for (int e = 0; e < 4; ++e) o[j][e] = fmaf(lo16(gw[r][j]), in[e][j], zz[r][e][j]);
#pragma unroll
            for (int j = 0; j < 2; ++j)
                __builtin_amdgcn_raw_buffer_store_b128(__builtin_bit_cast(u32x4, o[j]), ors, ov + (unsigned)(((tr[r] + j) * DM + d_base) * 4), 0, WT);
        }
        STAMPT(dbg, 7, 0);
        STAMPT(dbg, 16, 384);
        if (tb == 15 && nb == 0 && wid == 0 && lane == 0) __hip_atomic_store((gu32*)(ws + WS_EPOCH), tag, RLX_AGENT);
    }
}

extern "C" void kernel_launch(void* const* d_in, const int* in_sizes, int n_in,
                              void* d_out, int out_size, void* d_ws, size_t ws_size,
                              hipStream_t stream) {
    const u16* x = (const u16*)d_in[0];
    const u16* lre = (const u16*)d_in[1];
    const u16* lim = (const u16*)d_in[2];
    const u16* Bre = (const u16*)d_in[3];
    const u16* Bim = (const u16*)d_in[4];
    const u16* Cre = (const u16*)d_in[5];
    const u16* Cim = (const u16*)d_in[6];
    const u16* logdt = (const u16*)d_in[7];
    const u16* dskip = (const u16*)d_in[8];
    const u16* kin = (const u16*)d_in[10];
    const u16* vin = (const u16*)d_in[11];
    const u16* shiftk = (const u16*)d_in[12];
    const u16* ssmk = (const u16*)d_in[13];
    const u16* ssmD = (const u16*)d_in[14];
    const u16* gatew = (const u16*)d_in[15];
    float* out = (float*)d_out;

    char* ws = (char*)d_ws;

    hipFuncSetAttribute((const void*)k1_kernel, hipFuncAttributeMaxDynamicSharedMemorySize, K1_LDS);

    k1_kernel<<<256, 512, K1_LDS, stream>>>(x, Bre, Bim, kin, vin, shiftk, gatew, ws, lre, lim, logdt, ssmk, ssmD, Cre, Cim, dskip, out);
}
#if defined(__HIP_DEVICE_COMPILE__)
#pragma clang attribute pop
#endif
```
